```python
import torch

def _softmax(x, axis=-1):
    return torch.softmax(x, dim=axis)

def _gelu(x):
    return torch.nn.functional.gelu(x, approximate='tanh')

def _silu(x):
    return torch.nn.functional.silu(x)

def _sigmoid(x):
    return torch.sigmoid(x)

def _layernorm(x, axis=-1, eps=1e-05):
    m = x.mean(dim=axis, keepdim=True)
    v = x.var(dim=axis, keepdim=True, unbiased=False)
    return (x - m) / torch.sqrt(v + eps)

def _scatter_add(tgt, idx, src, axis=0):
    return tgt.clone().index_add_(axis, idx, src)

def _segment_sum(src, seg, n):
    out = torch.zeros((n,) + src.shape[1:], dtype=src.dtype)
    return out.index_add_(0, seg, src)

def _pad(x, pw):
    flat = tuple((v for pair in reversed(pw) for v in pair))
    return torch.nn.functional.pad(x, flat)
kernel_name = 'parallel_sum__memory_aug_recurrent_mem_tr__mixer_channel_mlp__prompt'

def setup_inputs():

    _w = lambda a: (lambda t: t.to(torch.bfloat16) if t.is_floating_point() else t)(torch.as_tensor(a))

    def _A_setup():
        B, S, Dm = (1, 2048, 1024)
        H, Dh = (32, 128)
        M, seg = (8, 32)
        seg = min(seg, S)
        S = S // seg * seg
        return {'x': _w((lambda *s: torch.randn(*s, dtype=torch.bfloat16) * 0.05)(B, S, Dm)), 'mem0': _w((lambda *s: torch.randn(*s, dtype=torch.bfloat16) * 0.2)(B, M, Dm)), 'Wq': _w((lambda *s: torch.randn(*s, dtype=torch.bfloat16) * 1.6)(Dm, H, Dh) * Dm ** (-0.5)), 'Wk': _w((lambda *s: torch.randn(*s, dtype=torch.bfloat16) * 1.6)(Dm, H, Dh) * Dm ** (-0.5)), 'Wv': _w((lambda *s: torch.randn(*s, dtype=torch.bfloat16) * 0.2)(Dm, H, Dh) * Dm ** (-0.5)), 'Wo': _w((lambda *s: torch.randn(*s, dtype=torch.bfloat16) * 0.2)(H, Dh, Dm) * (H * Dh) ** (-0.5)), 'seg_len': seg}
    _A_in = _A_setup()

    def _B_setup():
        B, S, D, F = (1, 2048, 1024, 3584)
        return {'x': _w((lambda *s: torch.randn(*s, dtype=torch.bfloat16) * 0.05)(B, S, D)), 'w1': _w((lambda *s: torch.randn(*s, dtype=torch.bfloat16) * 1.6)(D, F) * D ** (-0.5)), 'b1': _w((lambda *s: torch.randn(*s, dtype=torch.bfloat16) * 0.4)(F)), 'w2': _w((lambda *s: torch.randn(*s, dtype=torch.bfloat16) * 0.05)(F, D) * F ** (-0.5)), 'b2': _w((lambda *s: torch.randn(*s, dtype=torch.bfloat16) * 0.05)(D))}
    _B_in = _B_setup()
    return {'A_x': _A_in['x'], 'A_mem0': _A_in['mem0'], 'A_Wq': _A_in['Wq'], 'A_Wk': _A_in['Wk'], 'A_Wv': _A_in['Wv'], 'A_Wo': _A_in['Wo'], 'A_seg_len': _A_in['seg_len'], 'B_x': _B_in['x'], 'B_w1': _B_in['w1'], 'B_b1': _B_in['b1'], 'B_w2': _B_in['w2'], 'B_b2': _B_in['b2']}

def reference(A_x, A_mem0, A_Wq, A_Wk, A_Wv, A_Wo, A_seg_len, B_x, B_w1, B_b1, B_w2, B_b2):

    def _A_ref(x, mem0, Wq, Wk, Wv, Wo, seg_len):
        B, S, Dm = x.shape
        H, Dh = (Wq.shape[1], Wq.shape[2])
        M = mem0.shape[1]
        scale = Dh ** (-0.5)
        n_seg = S // seg_len
        L = M + seg_len
        idx = torch.arange(L)
        mask = (idx[:, None] < M) | (idx[:, None] >= idx[None, :])
        mem = mem0
        outs = []
        for s in range(n_seg):
            xs = x[:, s * seg_len:(s + 1) * seg_len, :].clone()
            h = torch.concatenate([mem, xs], axis=1)
            q = torch.einsum('btd,dhe->bhte', h, Wq)
            k = torch.einsum('btd,dhe->bhte', h, Wk)
            v = torch.einsum('btd,dhe->bhte', h, Wv)
            scores = torch.einsum('bhqe,bhke->bhqk', q, k) * scale
            scores = torch.where(mask[None, None], scores, -1000000000.0)
            p = _softmax(scores, axis=-1)
            o = torch.einsum('bhqk,bhke->bhqe', p, v)
            o = torch.einsum('bhte,hed->btd', o, Wo)
            mem = o[:, :M, :].clone()
            outs.append(o[:, M:, :])
        return torch.concatenate(outs, axis=1)

    def _B_ref(x, w1, b1, w2, b2):
        y = _layernorm(x, axis=-1)
        h = _gelu(torch.einsum('bsd,df->bsf', y, w1) + b1)
        return torch.einsum('bsf,fd->bsd', h, w2) + b2
    x = A_x
    xn = _layernorm(x, axis=-1)
    return x + _A_ref(xn, A_mem0, A_Wq, A_Wk, A_Wv, A_Wo, A_seg_len) + _B_ref(xn, B_w1, B_b1, B_w2, B_b2)
```

```cpp
#include <hip/hip_runtime.h>
#include <hip/hip_bf16.h>

#define LAS __attribute__((address_space(3)))
typedef unsigned short bf16_t;
typedef short bf16x8 __attribute__((ext_vector_type(8)));
typedef short bf16x4 __attribute__((ext_vector_type(4)));
typedef float f32x4 __attribute__((ext_vector_type(4)));
typedef float f32x2 __attribute__((ext_vector_type(2)));
typedef unsigned u32x4 __attribute__((ext_vector_type(4)));
typedef unsigned u32x2 __attribute__((ext_vector_type(2)));

constexpr int S_LEN = 2048, DM = 1024, NH = 32, DH = 128, HD = NH * DH, MM = 8, SEG = 32, NSEG = S_LEN / SEG, FF = 3584;
constexpr int AUX_SC1 = 16;
constexpr unsigned SENT = 0xFFFFFFFFu;

__device__ __forceinline__ float bf2f(unsigned b) { return __builtin_bit_cast(float, b << 16); }
__device__ __forceinline__ unsigned pk_bf16(float lo, float hi) { unsigned r; asm volatile("v_cvt_pk_bf16_f32 %0, %1, %2" : "=v"(r) : "v"(lo), "v"(hi)); return r; }
__device__ __forceinline__ float rbf(float x) { return bf2f(pk_bf16(x, x) & 0xffffu); }
__device__ __forceinline__ unsigned desent(unsigned w) { return w == SENT ? 0x7FC07FC0u : w; }
__device__ __forceinline__ bool has_sent(u32x4 v) { return v.x == SENT || v.y == SENT || v.z == SENT || v.w == SENT; }

constexpr size_t WS_XN = 0;
constexpr size_t WS_Y = WS_XN + (size_t)S_LEN * DM * 2;
constexpr size_t WS_Q = WS_Y + (size_t)S_LEN * DM * 2;
constexpr size_t WS_K = WS_Q + (size_t)S_LEN * HD * 2;
constexpr size_t WS_V = WS_K + (size_t)S_LEN * HD * 2;
constexpr size_t WS_H = WS_V + (size_t)S_LEN * HD * 2;
constexpr size_t WS_O = WS_H + (size_t)S_LEN * FF * 2;
constexpr size_t WS_B = WS_O + (size_t)S_LEN * HD * 2;
constexpr size_t WS_SENT0 = WS_B + (size_t)S_LEN * DM * 2;
constexpr size_t MEM_B = (size_t)MM * DM * 2;
constexpr size_t WS_MEM = WS_SENT0;
constexpr size_t QKVM_B = (size_t)NH * 3 * MM * DH * 2;
constexpr size_t WS_QKVM = WS_MEM + (size_t)(NSEG + 1) * MEM_B;
constexpr size_t OM_B = (size_t)MM * HD * 2;
constexpr size_t WS_OM = WS_QKVM + (size_t)NSEG * QKVM_B;
constexpr size_t WS_SENT1 = WS_OM + (size_t)NSEG * OM_B;
constexpr size_t WS_TMO = WS_SENT1;
constexpr size_t WS_END = WS_TMO + 256;

__device__ __forceinline__ float wave_sum(float v) {
#pragma unroll
    for (int o = 32; o > 0; o >>= 1) v += __shfl_xor(v, o);
    return v;
}
__device__ __forceinline__ void ln_row(float (&v)[16]) {
    float s = 0.f;
#pragma unroll
    for (int i = 0; i < 16; ++i) s += v[i];
    const float mean = wave_sum(s) * (1.0f / DM);
    float q = 0.f;
#pragma unroll
    for (int i = 0; i < 16; ++i) { const float d = v[i] - mean; q += d * d; }
    const float var = rbf(wave_sum(q) * (1.0f / DM));
    const float mb = rbf(mean);
    const float den = rbf(sqrtf(rbf(var + 1e-5f)));
#pragma unroll
    for (int i = 0; i < 16; ++i) v[i] = rbf(rbf(v[i] - mb) / den);
}
__global__ void __launch_bounds__(256) k_prep(const bf16_t* __restrict__ x, const bf16_t* __restrict__ mem0, char* __restrict__ ws) {
    const int wave = threadIdx.x >> 6, lane = threadIdx.x & 63, row = blockIdx.x * 4 + wave;
    const u32x4* src = (const u32x4*)(x + (size_t)row * DM);
    u32x4 a = src[lane], b = src[lane + 64];
    float v[16];
    const unsigned w[8] = {a.x, a.y, a.z, a.w, b.x, b.y, b.z, b.w};
#pragma unroll
    for (int i = 0; i < 8; ++i) { v[2 * i] = bf2f(w[i] & 0xffffu); v[2 * i + 1] = bf2f(w[i] >> 16); }
    ln_row(v);
    u32x4 o0, o1;
    o0.x = pk_bf16(v[0], v[1]); o0.y = pk_bf16(v[2], v[3]); o0.z = pk_bf16(v[4], v[5]); o0.w = pk_bf16(v[6], v[7]);
    o1.x = pk_bf16(v[8], v[9]); o1.y = pk_bf16(v[10], v[11]); o1.z = pk_bf16(v[12], v[13]); o1.w = pk_bf16(v[14], v[15]);
    u32x4* dx = (u32x4*)(ws + WS_XN + (size_t)row * DM * 2);
    dx[lane] = o0; dx[lane + 64] = o1;
    ln_row(v);
    o0.x = pk_bf16(v[0], v[1]); o0.y = pk_bf16(v[2], v[3]); o0.z = pk_bf16(v[4], v[5]); o0.w = pk_bf16(v[6], v[7]);
    o1.x = pk_bf16(v[8], v[9]); o1.y = pk_bf16(v[10], v[11]); o1.z = pk_bf16(v[12], v[13]); o1.w = pk_bf16(v[14], v[15]);
    u32x4* dy = (u32x4*)(ws + WS_Y + (size_t)row * DM * 2);
    dy[lane] = o0; dy[lane + 64] = o1;
    if (blockIdx.x < 4) {
        const int c = blockIdx.x * 256 + threadIdx.x, r = c & 7, kg = c >> 3;
        u32x4 m = *(const u32x4*)(mem0 + (size_t)r * DM + kg * 8);
        m.x = desent(m.x); m.y = desent(m.y); m.z = desent(m.z); m.w = desent(m.w);
        *(u32x4*)(ws + WS_MEM + (size_t)c * 16) = m;
    }
}

namespace g8 {
constexpr int BM = 256, BK = 64, HALF = 128, HTB = HALF * BK * 2, STAGE_BYTES = 8 * HTB;
__host__ __device__ __forceinline__ int lds_byte(int r, int c) { const int st = (r >> 4) * 2 + (c >> 5), rr = r & 15, cc = c & 31, ob = rr * 64 + cc * 2; return st * 1024 + (ob ^ (((ob >> 9) & 1) << 5)); }
__host__ __device__ __forceinline__ void stage_rc(int b, int& R, int& C) { const int st = b / 1024, sb = b % 1024, swz = sb ^ (((sb >> 9) & 1) << 5); R = (st >> 1) * 16 + swz / 64; C = (st & 1) * 32 + (swz % 64) / 2; }
__host__ __device__ __forceinline__ int bsw(int row) { return (row & 3) | (((row >> 3) & 1) << 2); }
struct Unit { const bf16_t* A; const bf16_t* B; int pm, pn; };

template <class Epi, class Sched>
__device__ __forceinline__ void gemm_phase(LAS unsigned char* lds, const int K, const int lda, const int ldb, const Sched& S, const Epi& E) {
    int tid = threadIdx.x; asm volatile("" : "+v"(tid));
    const int wid = __builtin_amdgcn_readfirstlane(tid >> 6), lane = tid & 63, wr = wid >> 2, wc = wid & 3, fr = lane & 15, fq = lane >> 4;
    const int nt = K / BK;
    unsigned voffA[2], voffB[2];
#pragma unroll
    for (int i = 0; i < 2; ++i) { int R, C; stage_rc(tid * 16 + i * 8192, R, C); voffA[i] = (unsigned)(R * lda + C) * 2u;
        const int b = tid * 16 + i * 8192, row = b >> 8, ch = (b & 255) >> 4, seg = (ch >> 1) ^ bsw(row);
        voffB[i] = (unsigned)(row * ldb + seg * 16 + (ch & 1) * 8) * 2u; }
    const size_t kstepA = (size_t)(BK * 2), kstepB = (size_t)BK * ldb * 2;
    const size_t hstepA = (size_t)HALF * lda * 2, hstepB = (size_t)HALF * 2;
    const unsigned ldsw = (unsigned)wid * 1024u;
    const int aoff = lds_byte(wr * 64 + fr, fq * 8);
    const int q4 = (lane & 15) >> 2, p4 = lane & 3, sB = q4 | ((fq & 1) << 2);
    const int boff0 = (8 * fq + q4) * 256 + (((wc * 2 + 0) ^ sB) * 32) + 8 * p4;
    const int boff1 = (8 * fq + q4) * 256 + (((wc * 2 + 1) ^ sB) * 32) + 8 * p4;
#define G8_SA(b, h) (((b) * 2 + (h)) * HTB)
#define G8_SB(b, h) ((4 + (b) * 2 + (h)) * HTB)
#define G8_STAGE(bufoff, gbase, voff) do { _Pragma("unroll") for (int _i = 0; _i < 2; ++_i) \
        __builtin_amdgcn_global_load_lds((const unsigned*)((const char*)(gbase) + (voff)[_i]), (LAS unsigned*)(lds + (bufoff) + ldsw + _i * 8192), 16, 0, 0); } while (0)
#define G8_LDA(dst, b, h) do { _Pragma("unroll") for (int m = 0; m < 4; ++m) _Pragma("unroll") for (int k = 0; k < 2; ++k) dst[m][k] = *(const LAS bf16x8*)(lds + G8_SA(b, h) + aoff + m * 2048 + k * 1024); } while (0)
#define G8_TR(off) __builtin_amdgcn_ds_read_tr16_b64_v4i16((LAS bf16x4*)(lds + (off)))
#define G8_LDB(dst, b, h) do { _Pragma("unroll") for (int k = 0; k < 2; ++k) { \
        bf16x4 a0 = G8_TR(G8_SB(b, h) + boff0 + k * 8192), a1 = G8_TR(G8_SB(b, h) + boff0 + k * 8192 + 1024); \
        bf16x4 c0 = G8_TR(G8_SB(b, h) + boff1 + k * 8192), c1 = G8_TR(G8_SB(b, h) + boff1 + k * 8192 + 1024); \
        dst[0][k] = __builtin_shufflevector(a0, a1, 0, 1, 2, 3, 4, 5, 6, 7); dst[1][k] = __builtin_shufflevector(c0, c1, 0, 1, 2, 3, 4, 5, 6, 7); } } while (0)
#define G8_MMA(ai, bj, At, Bt) do { __builtin_amdgcn_s_setprio(1); _Pragma("unroll") for (int m = 0; m < 4; ++m) _Pragma("unroll") for (int n = 0; n < 2; ++n) _Pragma("unroll") for (int k = 0; k < 2; ++k) \
        acc[ai][bj][m][n] = __builtin_amdgcn_mfma_f32_16x16x32_bf16(Bt[n][k], At[m][k], acc[ai][bj][m][n], 0, 0, 0); __builtin_amdgcn_s_setprio(0); } while (0)
#define G8_WAIT_V(n) asm volatile("s_waitcnt vmcnt(" #n ")" ::: "memory")
#define G8_WAIT_L(n) asm volatile("s_waitcnt lgkmcnt(" #n ")" ::: "memory")
#define G8_BAR __builtin_amdgcn_s_barrier()
#define G8_SCHED __builtin_amdgcn_sched_barrier(0)
    Unit cur, nxt; int ui = 0;
    if (!S.next(0, cur)) return;
    f32x4 acc[2][2][4][2];
#pragma unroll
    for (int a = 0; a < 2; ++a)
#pragma unroll
        for (int b = 0; b < 2; ++b)
#pragma unroll
            for (int m = 0; m < 4; ++m)
#pragma unroll
                for (int n = 0; n < 2; ++n) acc[a][b][m][n] = (f32x4){0.f, 0.f, 0.f, 0.f};
    bf16x8 At[4][2], B0[2][2], B1[2][2];
    const char* cA = (const char*)cur.A; const char* cB = (const char*)cur.B;
    G8_STAGE(G8_SB(0, 0), cB, voffB); G8_STAGE(G8_SA(0, 0), cA, voffA); G8_STAGE(G8_SB(0, 1), cB + hstepB, voffB); G8_STAGE(G8_SA(0, 1), cA + hstepA, voffA);
    if (wr == 1) G8_BAR;
    G8_WAIT_V(4); G8_BAR;
    G8_STAGE(G8_SB(1, 0), cB + kstepB, voffB); G8_STAGE(G8_SA(1, 0), cA + kstepA, voffA); G8_STAGE(G8_SB(1, 1), cB + hstepB + kstepB, voffB);
    G8_WAIT_V(6); G8_BAR;
    for (;;) {
        const bool has_next = S.next(ui + 1, nxt);
        const char* nA = has_next ? (const char*)nxt.A : cA; const char* nB = has_next ? (const char*)nxt.B : cB;
        for (int t = 0; t < nt; t += 2) {
            const bool last = (t == nt - 2);
            const char* a1 = cA + (size_t)(t + 1) * kstepA;
            const char* a2 = last ? nA : cA + (size_t)(t + 2) * kstepA; const char* b2 = last ? nB : cB + (size_t)(t + 2) * kstepB;
            const char* a3 = a2 + kstepA; const char* b3 = b2 + kstepB;
            G8_LDB(B0, 0, 0); G8_SCHED; G8_LDA(At, 0, 0); G8_STAGE(G8_SA(1, 1), a1 + hstepA, voffA);
            G8_WAIT_L(8); G8_BAR; G8_WAIT_L(0); G8_MMA(0, 0, At, B0); G8_BAR; G8_SCHED;
            G8_LDB(B1, 0, 1); G8_STAGE(G8_SB(0, 0), b2, voffB);
            G8_BAR; G8_WAIT_L(0); G8_MMA(0, 1, At, B1); G8_BAR;
            G8_LDA(At, 0, 1); G8_STAGE(G8_SA(0, 0), a2, voffA);
            G8_BAR; G8_WAIT_L(0); G8_MMA(1, 0, At, B0); G8_BAR; G8_SCHED;
            G8_STAGE(G8_SB(0, 1), b2 + hstepB, voffB);
            G8_WAIT_V(6); G8_BAR; G8_MMA(1, 1, At, B1); G8_BAR;
            G8_LDB(B0, 1, 0); G8_SCHED; G8_LDA(At, 1, 0); G8_STAGE(G8_SA(0, 1), a2 + hstepA, voffA);
            G8_WAIT_L(8); G8_BAR; G8_WAIT_L(0); G8_MMA(0, 0, At, B0); G8_BAR; G8_SCHED;
            G8_LDB(B1, 1, 1); G8_STAGE(G8_SB(1, 0), b3, voffB);
            G8_BAR; G8_WAIT_L(0); G8_MMA(0, 1, At, B1); G8_BAR;
            G8_LDA(At, 1, 1); G8_STAGE(G8_SA(1, 0), a3, voffA);
            G8_BAR; G8_WAIT_L(0); G8_MMA(1, 0, At, B0); G8_BAR; G8_SCHED;
            G8_STAGE(G8_SB(1, 1), b3 + hstepB, voffB);
            G8_WAIT_V(6); G8_BAR; G8_MMA(1, 1, At, B1); G8_BAR;
        }
        E(acc, cur, wr, wc, fr, fq);
        if (!has_next) break;
#pragma unroll
        for (int a = 0; a < 2; ++a)
#pragma unroll
            for (int b = 0; b < 2; ++b)
#pragma unroll
                for (int m = 0; m < 4; ++m)
#pragma unroll
                    for (int n = 0; n < 2; ++n) acc[a][b][m][n] = (f32x4){0.f, 0.f, 0.f, 0.f};
        cur = nxt; cA = nA; cB = nB; ++ui;
    }
    G8_WAIT_V(0);
    if (wr == 0) G8_BAR;
    G8_BAR;
#undef G8_SA
#undef G8_SB
#undef G8_STAGE
#undef G8_LDA
#undef G8_LDB
#undef G8_TR
#undef G8_MMA
#undef G8_WAIT_V
#undef G8_WAIT_L
#undef G8_BAR
#undef G8_SCHED
}
}

__device__ __forceinline__ float gelu_tanh(float x) {
    const float u = 0.7978845608028654f * (x + 0.044715f * x * x * x);
    const float th = 1.0f - 2.0f * __builtin_amdgcn_rcpf(__expf(2.0f * u) + 1.0f);
    return 0.5f * x * (1.0f + th);
}

struct SchedQKV {
    const bf16_t* A; const bf16_t* W0; long d1, d2; int c;
    __device__ __forceinline__ bool next(int i, g8::Unit& u) const {
        int L; if (i == 0) L = c; else if (i == 1 && c < 128) L = 256 + c; else return false;
        const int pm = L & 7, pn = L >> 3, mat = pn >> 4;
        u.A = A + (size_t)pm * 256 * DM; u.B = W0 + (mat == 1 ? d1 : 0L) + (mat == 2 ? d2 : 0L) + (pn & 15) * 256; u.pm = pm; u.pn = pn; return true;
    }
};
struct EpiQKV {
    char* ws;
    __device__ __forceinline__ void operator()(const f32x4 (&acc)[2][2][4][2], const g8::Unit& u, int wr, int wc, int fr, int fq) const {
        bf16_t* base = (bf16_t*)(ws + WS_Q + (size_t)(u.pn >> 4) * ((size_t)S_LEN * HD * 2));
        const int row0 = u.pm * 256 + wr * 64 + fr, col0 = (u.pn & 15) * 256 + wc * 32 + 4 * fq;
#pragma unroll
        for (int ai = 0; ai < 2; ++ai)
#pragma unroll
            for (int m = 0; m < 4; ++m) { bf16_t* rowp = base + (size_t)(row0 + ai * 128 + m * 16) * HD + col0;
#pragma unroll
                for (int bj = 0; bj < 2; ++bj)
#pragma unroll
                    for (int n = 0; n < 2; ++n) { const f32x4 v = acc[ai][bj][m][n]; u32x2 w; w.x = pk_bf16(v[0], v[1]); w.y = pk_bf16(v[2], v[3]); *(u32x2*)(rowp + bj * 128 + n * 16) = w; } }
    }
};
struct SchedMLP1 {
    const bf16_t* A; const bf16_t* W; int c;
    __device__ __forceinline__ bool next(int i, g8::Unit& u) const {
        if (i != 0 || c < 128 || c >= 240) return false;
        const int L = c - 128, pm = L & 7, pn = L >> 3;
        u.A = A + (size_t)pm * 256 * DM; u.B = W + pn * 256; u.pm = pm; u.pn = pn; return true;
    }
};
struct EpiMLP1 {
    char* ws; const bf16_t* b1;
    __device__ __forceinline__ void operator()(const f32x4 (&acc)[2][2][4][2], const g8::Unit& u, int wr, int wc, int fr, int fq) const {
        bf16_t* base = (bf16_t*)(ws + WS_H);
        const int row0 = u.pm * 256 + wr * 64 + fr, col0 = u.pn * 256 + wc * 32 + 4 * fq;
#pragma unroll
        for (int bj = 0; bj < 2; ++bj)
#pragma unroll
            for (int n = 0; n < 2; ++n) {
                const u32x2 bw = *(const u32x2*)(b1 + col0 + bj * 128 + n * 16);
                const float b0 = bf2f(bw.x & 0xffffu), b1f = bf2f(bw.x >> 16), b2f = bf2f(bw.y & 0xffffu), b3 = bf2f(bw.y >> 16);
#pragma unroll
                for (int ai = 0; ai < 2; ++ai)
#pragma unroll
                    for (int m = 0; m < 4; ++m) { const f32x4 v = acc[ai][bj][m][n];
                        const float g0 = gelu_tanh(rbf(rbf(v[0]) + b0)), g1 = gelu_tanh(rbf(rbf(v[1]) + b1f)), g2 = gelu_tanh(rbf(rbf(v[2]) + b2f)), g3 = gelu_tanh(rbf(rbf(v[3]) + b3));
                        u32x2 w; w.x = pk_bf16(g0, g1); w.y = pk_bf16(g2, g3);
                        *(u32x2*)(base + (size_t)(row0 + ai * 128 + m * 16) * FF + col0 + bj * 128 + n * 16) = w; }
                asm volatile("" ::: "memory");
            }
    }
};
__global__ void __launch_bounds__(512, 2) k_gemm1(const bf16_t* __restrict__ Wq, const bf16_t* __restrict__ Wk, const bf16_t* __restrict__ Wv,
                                                  const bf16_t* __restrict__ w1, const bf16_t* __restrict__ b1, char* ws) {
    extern __shared__ __attribute__((aligned(16))) unsigned char shm[];
    const int c = blockIdx.x;
    { SchedQKV S{(const bf16_t*)(ws + WS_XN), Wq, (long)(Wk - Wq), (long)(Wv - Wq), c}; EpiQKV E{ws};
      g8::gemm_phase<EpiQKV, SchedQKV>((LAS unsigned char*)shm, DM, DM, HD, S, E); }
    { SchedMLP1 S{(const bf16_t*)(ws + WS_Y), w1, c}; EpiMLP1 E{ws, b1};
      g8::gemm_phase<EpiMLP1, SchedMLP1>((LAS unsigned char*)shm, DM, DM, FF, S, E); }
}

template <int MODE>
__global__ void __launch_bounds__(256, 2) k_gemm128(const bf16_t* __restrict__ A, int K, const bf16_t* __restrict__ B, const bf16_t* __restrict__ aux,
                                                    const bf16_t* __restrict__ bout, bf16_t* __restrict__ out) {
    extern __shared__ __attribute__((aligned(16))) unsigned char shm_[];
    LAS unsigned char* lds = (LAS unsigned char*)shm_;
    const int tid = threadIdx.x, wid = __builtin_amdgcn_readfirstlane(tid >> 6), lane = tid & 63, wr = wid >> 1, wc = wid & 1, fr = lane & 15, fq = lane >> 4;
    const int b = blockIdx.x, pn = b & 7, pm = b >> 3;
    constexpr int ldb = DM;
    unsigned voffA[4], voffB[4];
#pragma unroll
    for (int i = 0; i < 4; ++i) { int R, C; g8::stage_rc(tid * 16 + i * 4096, R, C); voffA[i] = (unsigned)(R * K + C) * 2u;
        const int bb = tid * 16 + i * 4096, row = bb >> 8, ch = (bb & 255) >> 4, seg = (ch >> 1) ^ g8::bsw(row);
        voffB[i] = (unsigned)(row * ldb + seg * 16 + (ch & 1) * 8) * 2u; }
    const char* cA = (const char*)(A + (size_t)pm * 128 * K); const char* cB = (const char*)(B + pn * 128);
    const unsigned ldsw = (unsigned)wid * 1024u;
    const int aoff = g8::lds_byte(wr * 64 + fr, fq * 8);
    const int q4 = (lane & 15) >> 2, p4 = lane & 3, sB = q4 | ((fq & 1) << 2);
    int boff[4];
#pragma unroll
    for (int n = 0; n < 4; ++n) boff[n] = (8 * fq + q4) * 256 + (((wc * 4 + n) ^ sB) * 32) + 8 * p4;
    f32x4 acc[4][4];
#pragma unroll
    for (int m = 0; m < 4; ++m)
#pragma unroll
        for (int n = 0; n < 4; ++n) acc[m][n] = (f32x4){0.f, 0.f, 0.f, 0.f};
    const int nt = K / 64;
#define STG(buf, t) do { _Pragma("unroll") for (int _i = 0; _i < 4; ++_i) { \
        __builtin_amdgcn_global_load_lds((const unsigned*)(cA + (size_t)(t) * 128 + voffA[_i]), (LAS unsigned*)(lds + (buf) * 32768 + ldsw + _i * 4096), 16, 0, 0); \
        __builtin_amdgcn_global_load_lds((const unsigned*)(cB + (size_t)(t) * 64 * ldb * 2 + voffB[_i]), (LAS unsigned*)(lds + (buf) * 32768 + 16384 + ldsw + _i * 4096), 16, 0, 0); } } while (0)
    STG(0, 0);
    asm volatile("s_waitcnt vmcnt(0)" ::: "memory"); __builtin_amdgcn_s_barrier();
    for (int t = 0; t < nt; ++t) {
        const int cur = t & 1;
        if (t + 1 < nt) STG(cur ^ 1, t + 1);
        bf16x8 At[4][2], Bt[4][2];
#pragma unroll
        for (int k = 0; k < 2; ++k) {
#pragma unroll
            for (int m = 0; m < 4; ++m) At[m][k] = *(const LAS bf16x8*)(lds + cur * 32768 + aoff + m * 2048 + k * 1024);
#pragma unroll
            for (int n = 0; n < 4; ++n) {
                bf16x4 a0 = __builtin_amdgcn_ds_read_tr16_b64_v4i16((LAS bf16x4*)(lds + cur * 32768 + 16384 + boff[n] + k * 8192));
                bf16x4 a1 = __builtin_amdgcn_ds_read_tr16_b64_v4i16((LAS bf16x4*)(lds + cur * 32768 + 16384 + boff[n] + k * 8192 + 1024));
                Bt[n][k] = __builtin_shufflevector(a0, a1, 0, 1, 2, 3, 4, 5, 6, 7); }
        }
        __builtin_amdgcn_s_setprio(1);
#pragma unroll
        for (int k = 0; k < 2; ++k)
#pragma unroll
            for (int m = 0; m < 4; ++m)
#pragma unroll
                for (int n = 0; n < 4; ++n) acc[m][n] = __builtin_amdgcn_mfma_f32_16x16x32_bf16(Bt[n][k], At[m][k], acc[m][n], 0, 0, 0);
        __builtin_amdgcn_s_setprio(0);
        asm volatile("s_waitcnt vmcnt(0) lgkmcnt(0)" ::: "memory"); __builtin_amdgcn_s_barrier();
    }
#undef STG
    const int row0 = pm * 128 + wr * 64 + fr, col0 = pn * 128 + wc * 64 + 4 * fq;
#pragma unroll
    for (int m = 0; m < 4; ++m)
#pragma unroll
        for (int n = 0; n < 4; ++n) {
            const size_t off = (size_t)(row0 + m * 16) * DM + col0 + n * 16;
            const f32x4 v = acc[m][n]; float r[4];
            if (MODE == 0) {
                const u32x2 bw = *(const u32x2*)(aux + col0 + n * 16);
                r[0] = rbf(v[0]) + bf2f(bw.x & 0xffffu); r[1] = rbf(v[1]) + bf2f(bw.x >> 16); r[2] = rbf(v[2]) + bf2f(bw.y & 0xffffu); r[3] = rbf(v[3]) + bf2f(bw.y >> 16);
            } else {
                const u32x2 xw = *(const u32x2*)(aux + off), bw = *(const u32x2*)(bout + off);
                r[0] = rbf(bf2f(xw.x & 0xffffu) + rbf(v[0])) + bf2f(bw.x & 0xffffu); r[1] = rbf(bf2f(xw.x >> 16) + rbf(v[1])) + bf2f(bw.x >> 16);
                r[2] = rbf(bf2f(xw.y & 0xffffu) + rbf(v[2])) + bf2f(bw.y & 0xffffu); r[3] = rbf(bf2f(xw.y >> 16) + rbf(v[3])) + bf2f(bw.y >> 16);
            }
            u32x2 w; w.x = pk_bf16(r[0], r[1]); w.y = pk_bf16(r[2], r[3]);
            *(u32x2*)(out + off) = w;
        }
}

constexpr int CH_DELAY1 = 12, CH_DELAY2 = 8, CH_DELAY3 = 12;
constexpr unsigned SPIN_CAP = 200000u;
constexpr int KS_PITCH = 272;
constexpr int L_RED = 0;
constexpr int L_RED2 = 12288;
constexpr int L_QS = 13312;
constexpr int L_KS = 15488;
constexpr int L_VS = 41600;
constexpr int L_OS = 44672;
constexpr int L_END = 61056;
constexpr int CH_LDS = 96 * 1024;

__global__ void __launch_bounds__(512, 2) k_chain(const bf16_t* __restrict__ Wq, const bf16_t* __restrict__ Wk, const bf16_t* __restrict__ Wv,
                                                  const bf16_t* __restrict__ Wo, char* ws) {
    extern __shared__ __attribute__((aligned(16))) unsigned char shm_[];
    LAS unsigned char* lds = (LAS unsigned char*)shm_;
    const int b = blockIdx.x, tid = threadIdx.x, wave = __builtin_amdgcn_readfirstlane(tid >> 6), lane = tid & 63, fr = lane & 15, fq = lane >> 4;
    const int h = b >> 3, j = b & 7, g = b & 3, c = b >> 2;
    const __amdgpu_buffer_rsrc_t rs = __builtin_amdgcn_make_buffer_rsrc(ws + WS_SENT0, 0, (int)(WS_SENT1 - WS_SENT0), 0x00020000);
    constexpr int O_MEM = (int)(WS_MEM - WS_SENT0), O_QKVM = (int)(WS_QKVM - WS_SENT0), O_OM = (int)(WS_OM - WS_SENT0);
    const bf16_t* Kx = (const bf16_t*)(ws + WS_K); const bf16_t* Vx = (const bf16_t*)(ws + WS_V);
    bool dead = false;

    bf16x8 wq[3][4], wo[16];
#pragma unroll
    for (int t = 0; t < 3; ++t) {
        const bf16_t* W = (t == 0 ? Wq : (t == 1 ? Wk : Wv)) + h * DH + j * 16 + fr;
#pragma unroll
        for (int i = 0; i < 4; ++i)
#pragma unroll
            for (int jj = 0; jj < 8; ++jj) wq[t][i][jj] = (short)W[(size_t)(wave * 128 + i * 32 + fq * 8 + jj) * HD];
        asm volatile("" : "+v"(wq[t][0]), "+v"(wq[t][1]), "+v"(wq[t][2]), "+v"(wq[t][3]) :: "memory");
    }
#pragma unroll
    for (int i = 0; i < 16; ++i) {
#pragma unroll
        for (int jj = 0; jj < 8; ++jj) wo[i][jj] = (short)Wo[(size_t)(wave * 512 + i * 32 + fq * 8 + jj) * DM + c * 16 + fr];
        if ((i & 3) == 3) asm volatile("" : "+v"(wo[i - 3]), "+v"(wo[i - 2]), "+v"(wo[i - 1]), "+v"(wo[i]) :: "memory");
    }

    for (int i = tid; i < 2 * 8 * (KS_PITCH / 16); i += 512) { const int bf = i / (8 * (KS_PITCH / 16)), r = i % (8 * (KS_PITCH / 16));
        *(LAS u32x4*)(lds + L_KS + bf * 48 * KS_PITCH + 40 * KS_PITCH + r * 16) = (u32x4){0u, 0u, 0u, 0u}; }
    if (tid < 32) { const int bf = tid >> 4, r = tid & 15; *(LAS u32x4*)(lds + L_VS + bf * 1536 + 40 * 32 + r * 16) = (u32x4){0u, 0u, 0u, 0u}; }
    {
        const int r = tid >> 4, ch = tid & 15;
        *(LAS u32x4*)(lds + L_KS + (8 + r) * KS_PITCH + ch * 16) = *(const u32x4*)(Kx + (size_t)r * HD + h * DH + ch * 8);
        if (tid < 64) { const int r2 = tid >> 1, c2 = tid & 1; *(LAS u32x4*)(lds + L_VS + (8 + r2) * 32 + c2 * 16) = *(const u32x4*)(Vx + (size_t)r2 * HD + h * DH + j * 16 + c2 * 8); }
    }
    __syncthreads();

    for (int s = 0; s < NSEG; ++s) {
        const int kb = s & 1;
        {
            if (s > 0) __builtin_amdgcn_s_sleep(CH_DELAY1);
            u32x4 a[4];
            const int base = O_MEM + s * (int)MEM_B + wave * 2048 + fq * 128 + (lane & 7) * 16;
            for (unsigned spins = 0;;) {
                asm volatile("" ::: "memory");
#pragma unroll
                for (int i = 0; i < 4; ++i) a[i] = __builtin_amdgcn_raw_buffer_load_b128(rs, base + i * 512, 0, AUX_SC1);
                bool bad = false;
#pragma unroll
                for (int i = 0; i < 4; ++i) bad |= has_sent(a[i]);
                if (!__any(bad) || dead) break;
                if (++spins > SPIN_CAP) { dead = true; if (lane == 0) *(volatile unsigned*)(ws + WS_TMO) = 1u; break; }
            }
            f32x4 acc[3];
#pragma unroll
            for (int t = 0; t < 3; ++t) acc[t] = (f32x4){0.f, 0.f, 0.f, 0.f};
#pragma unroll
            for (int i = 0; i < 4; ++i)
#pragma unroll
                for (int t = 0; t < 3; ++t) acc[t] = __builtin_amdgcn_mfma_f32_16x16x32_bf16(wq[t][i], __builtin_bit_cast(bf16x8, a[i]), acc[t], 0, 0, 0);
            if (fr < 8) {
#pragma unroll
                for (int t = 0; t < 3; ++t) *(LAS f32x4*)(lds + L_RED + ((wave * 3 + t) * 128 + fr * 16 + fq * 4) * 4) = acc[t];
            }
        }
        __syncthreads();
        if (tid < 192) {
            const int t = tid >> 6, m = (tid & 63) >> 3, np = (tid & 7) * 2;
            f32x2 sum = (f32x2){0.f, 0.f};
#pragma unroll
            for (int w = 0; w < 8; ++w) sum += *(const LAS f32x2*)(lds + L_RED + ((w * 3 + t) * 128 + m * 16 + np) * 4);
            const unsigned pk = desent(pk_bf16(sum.x, sum.y));
            __builtin_amdgcn_raw_buffer_store_b32(pk, rs, O_QKVM + s * (int)QKVM_B + ((h * 3 + t) * 8 + m) * 256 + (j * 16 + np) * 2, 0, AUX_SC1);
            if (t == 2) *(LAS unsigned*)(lds + L_VS + kb * 1536 + m * 32 + np * 2) = pk;
        }
        if (s == NSEG - 1) break;
        __syncthreads();
        if (wave == 0) {
            __builtin_amdgcn_s_sleep(CH_DELAY2);
            u32x4 a[4];
            const int base = O_QKVM + s * (int)QKVM_B + h * 3 * 2048 + lane * 16;
            for (unsigned spins = 0;;) {
                asm volatile("" ::: "memory");
#pragma unroll
                for (int i = 0; i < 4; ++i) a[i] = __builtin_amdgcn_raw_buffer_load_b128(rs, base + i * 1024, 0, AUX_SC1);
                bool bad = false;
#pragma unroll
                for (int i = 0; i < 4; ++i) bad |= has_sent(a[i]);
                if (!__any(bad) || dead) break;
                if (++spins > SPIN_CAP) { dead = true; if (lane == 0) *(volatile unsigned*)(ws + WS_TMO) = 2u; break; }
            }
            {
                const int r0 = fq, ch = fr;
                *(LAS u32x4*)(lds + L_QS + r0 * KS_PITCH + ch * 16) = a[0];
                *(LAS u32x4*)(lds + L_QS + (4 + r0) * KS_PITCH + ch * 16) = a[1];
                *(LAS u32x4*)(lds + L_KS + kb * 48 * KS_PITCH + r0 * KS_PITCH + ch * 16) = a[2];
                *(LAS u32x4*)(lds + L_KS + kb * 48 * KS_PITCH + (4 + r0) * KS_PITCH + ch * 16) = a[3];
            }
            asm volatile("s_waitcnt lgkmcnt(0)" ::: "memory");
            f32x4 sc[3];
#pragma unroll
            for (int t = 0; t < 3; ++t) sc[t] = (f32x4){0.f, 0.f, 0.f, 0.f};
#pragma unroll
            for (int i = 0; i < 4; ++i) {
                const bf16x8 qf = *(const LAS bf16x8*)(lds + L_QS + (lane & 7) * KS_PITCH + i * 64 + fq * 16);
#pragma unroll
                for (int t = 0; t < 3; ++t) {
                    const bf16x8 kf = *(const LAS bf16x8*)(lds + L_KS + kb * 48 * KS_PITCH + (t * 16 + fr) * KS_PITCH + i * 64 + fq * 16);
                    sc[t] = __builtin_amdgcn_mfma_f32_16x16x32_bf16(kf, qf, sc[t], 0, 0, 0);
                }
            }
            const float scale = 0.08838834764831845f;
            float v[12]; float mx = -INFINITY;
#pragma unroll
            for (int t = 0; t < 3; ++t)
#pragma unroll
                for (int r = 0; r < 4; ++r) { float z = rbf(rbf(sc[t][r]) * scale); if (t == 2 && fq >= 2) z = -INFINITY; v[t * 4 + r] = z; mx = fmaxf(mx, z); }
            mx = fmaxf(mx, __shfl_xor(mx, 16)); mx = fmaxf(mx, __shfl_xor(mx, 32));
            float sum = 0.f;
#pragma unroll
            for (int i = 0; i < 12; ++i) { v[i] = __expf(v[i] - mx); sum += v[i]; }
            sum += __shfl_xor(sum, 16); sum += __shfl_xor(sum, 32);
            const float inv = 1.0f / sum;
            u32x4 p0, p1;
            p0.x = pk_bf16(v[0] * inv, v[1] * inv); p0.y = pk_bf16(v[2] * inv, v[3] * inv); p0.z = pk_bf16(v[4] * inv, v[5] * inv); p0.w = pk_bf16(v[6] * inv, v[7] * inv);
            p1.x = pk_bf16(v[8] * inv, v[9] * inv); p1.y = pk_bf16(v[10] * inv, v[11] * inv); p1.z = 0u; p1.w = 0u;
            const int voff = L_VS + kb * 1536 + (4 * fq + (fr >> 2)) * 32 + (lane & 3) * 8;
            const bf16x4 v0 = __builtin_amdgcn_ds_read_tr16_b64_v4i16((LAS bf16x4*)(lds + voff));
            const bf16x4 v1 = __builtin_amdgcn_ds_read_tr16_b64_v4i16((LAS bf16x4*)(lds + voff + 16 * 32));
            const bf16x4 v2 = __builtin_amdgcn_ds_read_tr16_b64_v4i16((LAS bf16x4*)(lds + voff + 32 * 32));
            const bf16x4 zz = (bf16x4){0, 0, 0, 0};
            f32x4 o = (f32x4){0.f, 0.f, 0.f, 0.f};
            o = __builtin_amdgcn_mfma_f32_16x16x32_bf16(__builtin_shufflevector(v0, v1, 0, 1, 2, 3, 4, 5, 6, 7), __builtin_bit_cast(bf16x8, p0), o, 0, 0, 0);
            o = __builtin_amdgcn_mfma_f32_16x16x32_bf16(__builtin_shufflevector(v2, zz, 0, 1, 2, 3, 4, 5, 6, 7), __builtin_bit_cast(bf16x8, p1), o, 0, 0, 0);
            if (fr < 8) {
                u32x2 w; w.x = desent(pk_bf16(o[0], o[1])); w.y = desent(pk_bf16(o[2], o[3]));
                const int off = O_OM + s * (int)OM_B + (fr >> 1) * 16384 + (h * 4 + (j >> 1)) * 128 + ((j & 1) * 2 + (fq >> 1)) * 32 + (fr & 1) * 16 + (fq & 1) * 8;
                __builtin_amdgcn_raw_buffer_store_b64(w, rs, off, 0, AUX_SC1);
            }
        } else {
            const int t2 = tid - 64; const int nb = kb ^ 1; const size_t row0 = (size_t)(s + 1) * SEG;
            for (int i = t2; i < 512 + 64; i += 448) {
                if (i < 512) { const int r = i >> 4, ch = i & 15;
                    *(LAS u32x4*)(lds + L_KS + nb * 48 * KS_PITCH + (8 + r) * KS_PITCH + ch * 16) = *(const u32x4*)(Kx + (row0 + r) * HD + h * DH + ch * 8); }
                else { const int r2 = (i - 512) >> 1, c2 = i & 1;
                    *(LAS u32x4*)(lds + L_VS + nb * 1536 + (8 + r2) * 32 + c2 * 16) = *(const u32x4*)(Vx + (row0 + r2) * HD + h * DH + j * 16 + c2 * 8); }
            }
        }
        {
            __builtin_amdgcn_s_sleep(CH_DELAY3);
            u32x4 a[2];
            const int base = O_OM + s * (int)OM_B + g * 16384 + wave * 2048 + lane * 16;
            for (unsigned spins = 0;;) {
                asm volatile("" ::: "memory");
#pragma unroll
                for (int i = 0; i < 2; ++i) a[i] = __builtin_amdgcn_raw_buffer_load_b128(rs, base + i * 1024, 0, AUX_SC1);
                const bool bad = has_sent(a[0]) || has_sent(a[1]);
                if (!__any(bad) || dead) break;
                if (++spins > SPIN_CAP) { dead = true; if (lane == 0) *(volatile unsigned*)(ws + WS_TMO) = 3u; break; }
            }
            *(LAS u32x4*)(lds + L_OS + wave * 2048 + lane * 16) = a[0];
            *(LAS u32x4*)(lds + L_OS + wave * 2048 + 1024 + lane * 16) = a[1];
            asm volatile("s_waitcnt lgkmcnt(0)" ::: "memory");
            f32x4 acc0 = (f32x4){0.f, 0.f, 0.f, 0.f}, acc1 = (f32x4){0.f, 0.f, 0.f, 0.f};
#pragma unroll
            for (int i = 0; i < 16; i += 2) {
                const bf16x8 f0 = *(const LAS bf16x8*)(lds + L_OS + wave * 2048 + i * 128 + fq * 32 + (lane & 1) * 16);
                const bf16x8 f1 = *(const LAS bf16x8*)(lds + L_OS + wave * 2048 + (i + 1) * 128 + fq * 32 + (lane & 1) * 16);
                acc0 = __builtin_amdgcn_mfma_f32_16x16x32_bf16(wo[i], f0, acc0, 0, 0, 0);
                acc1 = __builtin_amdgcn_mfma_f32_16x16x32_bf16(wo[i + 1], f1, acc1, 0, 0, 0);
            }
            acc0 += acc1;
            if (fr < 2) *(LAS f32x4*)(lds + L_RED2 + (wave * 32 + fr * 16 + fq * 4) * 4) = acc0;
        }
        __syncthreads();
        if (tid < 16) {
            const int m = tid >> 3, np = (tid & 7) * 2;
            f32x2 sum = (f32x2){0.f, 0.f};
#pragma unroll
            for (int w = 0; w < 8; ++w) sum += *(const LAS f32x2*)(lds + L_RED2 + (w * 32 + m * 16 + np) * 4);
            const unsigned pk = desent(pk_bf16(sum.x, sum.y));
            const int off = O_MEM + (s + 1) * (int)MEM_B + (c >> 1) * 512 + ((c & 1) * 2 + (np >> 3)) * 128 + (2 * g + m) * 16 + (np & 7) * 2;
            __builtin_amdgcn_raw_buffer_store_b32(pk, rs, off, 0, AUX_SC1);
        }
    }
}

constexpr int XV_PITCH = 272;
__global__ void __launch_bounds__(256) k_xattn(char* ws) {
    __shared__ __attribute__((aligned(16))) unsigned char shm_[4 * 48 * XV_PITCH];
    const int wave = threadIdx.x >> 6, lane = threadIdx.x & 63, fr = lane & 15, fq = lane >> 4;
    const int item = blockIdx.x * 4 + wave, s = item >> 5, h = item & 31;
    LAS unsigned char* vs = (LAS unsigned char*)shm_ + wave * 48 * XV_PITCH;
    const bf16_t* Q = (const bf16_t*)(ws + WS_Q) + (size_t)s * SEG * HD + h * DH;
    const bf16_t* K = (const bf16_t*)(ws + WS_K) + (size_t)s * SEG * HD + h * DH;
    const bf16_t* V = (const bf16_t*)(ws + WS_V) + (size_t)s * SEG * HD + h * DH;
    const bf16_t* KM = (const bf16_t*)(ws + WS_QKVM + (size_t)s * QKVM_B) + (h * 3 + 1) * 1024;
    const bf16_t* VM = KM + 1024;
#pragma unroll
    for (int i = 0; i < 12; ++i) {
        const int r = i * 4 + fq, ch = fr;
        u32x4 v = (u32x4){0u, 0u, 0u, 0u};
        if (r < 8) v = *(const u32x4*)(VM + r * DH + ch * 8); else if (r < 40) v = *(const u32x4*)(V + (size_t)(r - 8) * HD + ch * 8);
        *(LAS u32x4*)(vs + r * XV_PITCH + ch * 16) = v;
    }
    f32x4 sc[3][2];
#pragma unroll
    for (int t = 0; t < 3; ++t) { sc[t][0] = (f32x4){0.f, 0.f, 0.f, 0.f}; sc[t][1] = (f32x4){0.f, 0.f, 0.f, 0.f}; }
#pragma unroll
    for (int i = 0; i < 4; ++i) {
        const bf16x8 q0 = *(const bf16x8*)(Q + (size_t)fr * HD + i * 32 + fq * 8), q1 = *(const bf16x8*)(Q + (size_t)(16 + fr) * HD + i * 32 + fq * 8);
#pragma unroll
        for (int t = 0; t < 3; ++t) {
            const int key = t * 16 + fr;
            const bf16_t* kp = key < 8 ? KM + key * DH : K + (size_t)((key < 40 ? key : 39) - 8) * HD;
            const bf16x8 kf = *(const bf16x8*)(kp + i * 32 + fq * 8);
            sc[t][0] = __builtin_amdgcn_mfma_f32_16x16x32_bf16(kf, q0, sc[t][0], 0, 0, 0);
            sc[t][1] = __builtin_amdgcn_mfma_f32_16x16x32_bf16(kf, q1, sc[t][1], 0, 0, 0);
        }
    }
    const float scale = 0.08838834764831845f, NEG = rbf(-1000000000.0f);
    u32x4 p0[2], p1[2];
#pragma unroll
    for (int rt = 0; rt < 2; ++rt) {
        const int row = rt * 16 + fr;
        float v[12]; float mx = -INFINITY;
#pragma unroll
        for (int t = 0; t < 3; ++t)
#pragma unroll
            for (int r = 0; r < 4; ++r) { const int key = t * 16 + fq * 4 + r; float z = rbf(rbf(sc[t][rt][r]) * scale);
                if (key - 8 > row) z = NEG; if (key >= 40) z = -INFINITY; v[t * 4 + r] = z; mx = fmaxf(mx, z); }
        mx = fmaxf(mx, __shfl_xor(mx, 16)); mx = fmaxf(mx, __shfl_xor(mx, 32));
        float sum = 0.f;
#pragma unroll
        for (int i = 0; i < 12; ++i) { v[i] = __expf(v[i] - mx); sum += v[i]; }
        sum += __shfl_xor(sum, 16); sum += __shfl_xor(sum, 32);
        const float inv = 1.0f / sum;
        p0[rt].x = pk_bf16(v[0] * inv, v[1] * inv); p0[rt].y = pk_bf16(v[2] * inv, v[3] * inv); p0[rt].z = pk_bf16(v[4] * inv, v[5] * inv); p0[rt].w = pk_bf16(v[6] * inv, v[7] * inv);
        p1[rt].x = pk_bf16(v[8] * inv, v[9] * inv); p1[rt].y = pk_bf16(v[10] * inv, v[11] * inv); p1[rt].z = 0u; p1[rt].w = 0u;
    }
    asm volatile("s_waitcnt lgkmcnt(0)" ::: "memory");
    bf16_t* O = (bf16_t*)(ws + WS_O) + (size_t)s * SEG * HD + h * DH;
    const bf16x4 zz = (bf16x4){0, 0, 0, 0};
#pragma unroll
    for (int et = 0; et < 8; ++et) {
        const int voff = (4 * fq + (fr >> 2)) * XV_PITCH + et * 32 + (lane & 3) * 8;
        const bf16x4 v0 = __builtin_amdgcn_ds_read_tr16_b64_v4i16((LAS bf16x4*)(vs + voff));
        const bf16x4 v1 = __builtin_amdgcn_ds_read_tr16_b64_v4i16((LAS bf16x4*)(vs + voff + 16 * XV_PITCH));
        const bf16x4 v2 = __builtin_amdgcn_ds_read_tr16_b64_v4i16((LAS bf16x4*)(vs + voff + 32 * XV_PITCH));
        const bf16x8 va = __builtin_shufflevector(v0, v1, 0, 1, 2, 3, 4, 5, 6, 7), vb = __builtin_shufflevector(v2, zz, 0, 1, 2, 3, 4, 5, 6, 7);
#pragma unroll
        for (int rt = 0; rt < 2; ++rt) {
            f32x4 o = (f32x4){0.f, 0.f, 0.f, 0.f};
            o = __builtin_amdgcn_mfma_f32_16x16x32_bf16(va, __builtin_bit_cast(bf16x8, p0[rt]), o, 0, 0, 0);
            o = __builtin_amdgcn_mfma_f32_16x16x32_bf16(vb, __builtin_bit_cast(bf16x8, p1[rt]), o, 0, 0, 0);
            u32x2 w; w.x = pk_bf16(o[0], o[1]); w.y = pk_bf16(o[2], o[3]);
            *(u32x2*)(O + (size_t)(rt * 16 + fr) * HD + et * 16 + fq * 4) = w;
        }
    }
}

extern "C" void kernel_launch(void* const* d_in, const int* in_sizes, int n_in, void* d_out, int out_size, void* d_ws, size_t ws_size, hipStream_t stream) {
    const bf16_t* x = (const bf16_t*)d_in[0];
    const bf16_t* mem0 = (const bf16_t*)d_in[1];
    const bf16_t* Wq = (const bf16_t*)d_in[2];
    const bf16_t* Wk = (const bf16_t*)d_in[3];
    const bf16_t* Wv = (const bf16_t*)d_in[4];
    const bf16_t* Wo = (const bf16_t*)d_in[5];
    const bf16_t* w1 = (const bf16_t*)d_in[8];
    const bf16_t* b1 = (const bf16_t*)d_in[9];
    const bf16_t* w2 = (const bf16_t*)d_in[10];
    const bf16_t* b2 = (const bf16_t*)d_in[11];
    char* ws = (char*)d_ws;
    bf16_t* out = (bf16_t*)d_out;
    (void)hipFuncSetAttribute((const void*)k_gemm1, hipFuncAttributeMaxDynamicSharedMemorySize, g8::STAGE_BYTES);
    (void)hipFuncSetAttribute((const void*)k_gemm128<0>, hipFuncAttributeMaxDynamicSharedMemorySize, 65536);
    (void)hipFuncSetAttribute((const void*)k_gemm128<1>, hipFuncAttributeMaxDynamicSharedMemorySize, 65536);
    (void)hipFuncSetAttribute((const void*)k_chain, hipFuncAttributeMaxDynamicSharedMemorySize, CH_LDS);
    (void)hipMemsetAsync(ws + WS_SENT0, 0xFF, WS_SENT1 - WS_SENT0, stream);
    k_prep<<<S_LEN / 4, 256, 0, stream>>>(x, mem0, ws);
    k_gemm1<<<256, 512, g8::STAGE_BYTES, stream>>>(Wq, Wk, Wv, w1, b1, ws);
    k_gemm128<0><<<128, 256, 65536, stream>>>((const bf16_t*)(ws + WS_H), FF, w2, b2, nullptr, (bf16_t*)(ws + WS_B));
    k_chain<<<256, 512, CH_LDS, stream>>>(Wq, Wk, Wv, Wo, ws);
    k_xattn<<<NSEG * NH / 4, 256, 0, stream>>>(ws);
    k_gemm128<1><<<128, 256, 65536, stream>>>((const bf16_t*)(ws + WS_O), HD, Wo, x, (const bf16_t*)(ws + WS_B), out);
}
```

```cpp
#include <hip/hip_runtime.h>
#include <hip/hip_bf16.h>

#define LAS __attribute__((address_space(3)))
typedef unsigned short bf16_t;
typedef short bf16x8 __attribute__((ext_vector_type(8)));
typedef short bf16x4 __attribute__((ext_vector_type(4)));
typedef float f32x4 __attribute__((ext_vector_type(4)));
typedef float f32x2 __attribute__((ext_vector_type(2)));
typedef unsigned u32x4 __attribute__((ext_vector_type(4)));
typedef unsigned u32x2 __attribute__((ext_vector_type(2)));

constexpr int S_LEN = 2048, DM = 1024, NH = 32, DH = 128, HD = NH * DH, MM = 8, SEG = 32, NSEG = S_LEN / SEG, FF = 3584;
constexpr int AUX_SC1 = 16;
constexpr unsigned SENT = 0xFFFFFFFFu;

__device__ __forceinline__ float bf2f(unsigned b) { return __builtin_bit_cast(float, b << 16); }
typedef __bf16 bf16x2_t __attribute__((ext_vector_type(2)));
__device__ __forceinline__ unsigned pk_bf16(float lo, float hi) { const f32x2 v = {lo, hi}; return __builtin_bit_cast(unsigned, __builtin_convertvector(v, bf16x2_t)); }
__device__ __forceinline__ float rbf(float x) { return bf2f(pk_bf16(x, x) & 0xffffu); }
__device__ __forceinline__ unsigned desent(unsigned w) { return w == SENT ? 0x7FC07FC0u : w; }
__device__ __forceinline__ bool has_sent(u32x4 v) { return v.x == SENT || v.y == SENT || v.z == SENT || v.w == SENT; }

constexpr size_t WS_XN = 0;
constexpr size_t WS_Y = WS_XN + (size_t)S_LEN * DM * 2;
constexpr size_t WS_Q = WS_Y + (size_t)S_LEN * DM * 2;
constexpr size_t WS_K = WS_Q + (size_t)S_LEN * HD * 2;
constexpr size_t WS_V = WS_K + (size_t)S_LEN * HD * 2;
constexpr size_t WS_H = WS_V + (size_t)S_LEN * HD * 2;
constexpr size_t WS_O = WS_H + (size_t)S_LEN * FF * 2;
constexpr size_t WS_B = WS_O + (size_t)S_LEN * HD * 2;
constexpr size_t WS_SENT0 = WS_B + (size_t)S_LEN * DM * 2;
constexpr size_t MEM_B = (size_t)MM * DM * 2;
constexpr size_t WS_MEM = WS_SENT0;
constexpr size_t QKVM_B = (size_t)NH * 3 * MM * DH * 2;
constexpr size_t WS_QKVM = WS_MEM + (size_t)(NSEG + 1) * MEM_B;
constexpr size_t OM_B = (size_t)MM * HD * 2;
constexpr size_t WS_OM = WS_QKVM + (size_t)NSEG * QKVM_B;
constexpr size_t WS_SENT1 = WS_OM + (size_t)NSEG * OM_B;
constexpr size_t WS_CNT = WS_SENT1;
constexpr int CNT_H = 0;
constexpr int CNT_N = 64;
constexpr size_t WS_TMO = WS_CNT + (size_t)CNT_N * 256;
constexpr size_t WS_END = WS_TMO + 256;

__device__ __forceinline__ float wave_sum(float v) {
#pragma unroll
    for (int o = 32; o > 0; o >>= 1) v += __shfl_xor(v, o);
    return v;
}
__device__ __forceinline__ void ln_row(float (&v)[16]) {
    float s = 0.f;
#pragma unroll
    for (int i = 0; i < 16; ++i) s += v[i];
    const float mean = wave_sum(s) * (1.0f / DM);
    float q = 0.f;
#pragma unroll
    for (int i = 0; i < 16; ++i) { const float d = v[i] - mean; q += d * d; }
    const float var = rbf(wave_sum(q) * (1.0f / DM));
    const float mb = rbf(mean);
    const float den = rbf(sqrtf(rbf(var + 1e-5f)));
#pragma unroll
    for (int i = 0; i < 16; ++i) v[i] = rbf(rbf(v[i] - mb) / den);
}
__global__ void __launch_bounds__(256) k_prep(const bf16_t* __restrict__ x, const bf16_t* __restrict__ mem0, char* __restrict__ ws) {
    const int wave = threadIdx.x >> 6, lane = threadIdx.x & 63, row = blockIdx.x * 4 + wave;
    const u32x4* src = (const u32x4*)(x + (size_t)row * DM);
    u32x4 a = src[lane], b = src[lane + 64];
    float v[16];
    const unsigned w[8] = {a.x, a.y, a.z, a.w, b.x, b.y, b.z, b.w};
#pragma unroll
    for (int i = 0; i < 8; ++i) { v[2 * i] = bf2f(w[i] & 0xffffu); v[2 * i + 1] = bf2f(w[i] >> 16); }
    ln_row(v);
    u32x4 o0, o1;
    o0.x = pk_bf16(v[0], v[1]); o0.y = pk_bf16(v[2], v[3]); o0.z = pk_bf16(v[4], v[5]); o0.w = pk_bf16(v[6], v[7]);
    o1.x = pk_bf16(v[8], v[9]); o1.y = pk_bf16(v[10], v[11]); o1.z = pk_bf16(v[12], v[13]); o1.w = pk_bf16(v[14], v[15]);
    u32x4* dx = (u32x4*)(ws + WS_XN + (size_t)row * DM * 2);
    dx[lane] = o0; dx[lane + 64] = o1;
    ln_row(v);
    o0.x = pk_bf16(v[0], v[1]); o0.y = pk_bf16(v[2], v[3]); o0.z = pk_bf16(v[4], v[5]); o0.w = pk_bf16(v[6], v[7]);
    o1.x = pk_bf16(v[8], v[9]); o1.y = pk_bf16(v[10], v[11]); o1.z = pk_bf16(v[12], v[13]); o1.w = pk_bf16(v[14], v[15]);
    u32x4* dy = (u32x4*)(ws + WS_Y + (size_t)row * DM * 2);
    dy[lane] = o0; dy[lane + 64] = o1;
    if (blockIdx.x < 4) {
        const int c = blockIdx.x * 256 + threadIdx.x, r = c & 7, kg = c >> 3;
        u32x4 m = *(const u32x4*)(mem0 + (size_t)r * DM + kg * 8);
        m.x = desent(m.x); m.y = desent(m.y); m.z = desent(m.z); m.w = desent(m.w);
        *(u32x4*)(ws + WS_MEM + (size_t)c * 16) = m;
    }
}

namespace g8 {
constexpr int BM = 256, BK = 64, HALF = 128, HTB = HALF * BK * 2, STAGE_BYTES = 8 * HTB;
__host__ __device__ __forceinline__ int lds_byte(int r, int c) { const int st = (r >> 4) * 2 + (c >> 5), rr = r & 15, cc = c & 31, ob = rr * 64 + cc * 2; return st * 1024 + (ob ^ (((ob >> 9) & 1) << 5)); }
__host__ __device__ __forceinline__ void stage_rc(int b, int& R, int& C) { const int st = b / 1024, sb = b % 1024, swz = sb ^ (((sb >> 9) & 1) << 5); R = (st >> 1) * 16 + swz / 64; C = (st & 1) * 32 + (swz % 64) / 2; }
__host__ __device__ __forceinline__ int bsw(int row) { return (row & 3) | (((row >> 3) & 1) << 2); }
struct Unit { const bf16_t* A; const bf16_t* B; int pm, pn; };

template <class Epi, class Sched>
__device__ __forceinline__ void gemm_phase(LAS unsigned char* lds, const int K, const int lda, const int ldb, const Sched& S, const Epi& E) {
    int tid = threadIdx.x; asm volatile("" : "+v"(tid));
    const int wid = __builtin_amdgcn_readfirstlane(tid >> 6), lane = tid & 63, wr = wid >> 2, wc = wid & 3, fr = lane & 15, fq = lane >> 4;
    const int nt = K / BK;
    unsigned voffA[2], voffB[2];
#pragma unroll
    for (int i = 0; i < 2; ++i) { int R, C; stage_rc(tid * 16 + i * 8192, R, C); voffA[i] = (unsigned)(R * lda + C) * 2u;
        const int b = tid * 16 + i * 8192, row = b >> 8, ch = (b & 255) >> 4, seg = (ch >> 1) ^ bsw(row);
        voffB[i] = (unsigned)(row * ldb + seg * 16 + (ch & 1) * 8) * 2u; }
    const size_t kstepA = (size_t)(BK * 2), kstepB = (size_t)BK * ldb * 2;
    const size_t hstepA = (size_t)HALF * lda * 2, hstepB = (size_t)HALF * 2;
    const unsigned ldsw = (unsigned)wid * 1024u;
    const int aoff = lds_byte(wr * 64 + fr, fq * 8);
    const int q4 = (lane & 15) >> 2, p4 = lane & 3, sB = q4 | ((fq & 1) << 2);
    const int boff0 = (8 * fq + q4) * 256 + (((wc * 2 + 0) ^ sB) * 32) + 8 * p4;
    const int boff1 = (8 * fq + q4) * 256 + (((wc * 2 + 1) ^ sB) * 32) + 8 * p4;
#define G8_SA(b, h) (((b) * 2 + (h)) * HTB)
#define G8_SB(b, h) ((4 + (b) * 2 + (h)) * HTB)
#define G8_STAGE(bufoff, gbase, voff) do { _Pragma("unroll") for (int _i = 0; _i < 2; ++_i) \
        __builtin_amdgcn_global_load_lds((const unsigned*)((const char*)(gbase) + (voff)[_i]), (LAS unsigned*)(lds + (bufoff) + ldsw + _i * 8192), 16, 0, 0); } while (0)
#define G8_LDA(dst, b, h) do { _Pragma("unroll") for (int m = 0; m < 4; ++m) _Pragma("unroll") for (int k = 0; k < 2; ++k) dst[m][k] = *(const LAS bf16x8*)(lds + G8_SA(b, h) + aoff + m * 2048 + k * 1024); } while (0)
#define G8_TR(off) __builtin_amdgcn_ds_read_tr16_b64_v4i16((LAS bf16x4*)(lds + (off)))
#define G8_LDB(dst, b, h) do { _Pragma("unroll") for (int k = 0; k < 2; ++k) { \
        bf16x4 a0 = G8_TR(G8_SB(b, h) + boff0 + k * 8192), a1 = G8_TR(G8_SB(b, h) + boff0 + k * 8192 + 1024); \
        bf16x4 c0 = G8_TR(G8_SB(b, h) + boff1 + k * 8192), c1 = G8_TR(G8_SB(b, h) + boff1 + k * 8192 + 1024); \
        dst[0][k] = __builtin_shufflevector(a0, a1, 0, 1, 2, 3, 4, 5, 6, 7); dst[1][k] = __builtin_shufflevector(c0, c1, 0, 1, 2, 3, 4, 5, 6, 7); } } while (0)
#define G8_MMA(ai, bj, At, Bt) do { __builtin_amdgcn_s_setprio(1); _Pragma("unroll") for (int m = 0; m < 4; ++m) _Pragma("unroll") for (int n = 0; n < 2; ++n) _Pragma("unroll") for (int k = 0; k < 2; ++k) \
        acc[ai][bj][m][n] = __builtin_amdgcn_mfma_f32_16x16x32_bf16(Bt[n][k], At[m][k], acc[ai][bj][m][n], 0, 0, 0); __builtin_amdgcn_s_setprio(0); } while (0)
#define G8_WAIT_V(n) asm volatile("s_waitcnt vmcnt(" #n ")" ::: "memory")
#define G8_WAIT_L(n) asm volatile("s_waitcnt lgkmcnt(" #n ")" ::: "memory")
#define G8_BAR __builtin_amdgcn_s_barrier()
#define G8_SCHED __builtin_amdgcn_sched_barrier(0)
    Unit cur, nxt; int ui = 0;
    if (!S.next(0, cur)) return;
    f32x4 acc[2][2][4][2];
#pragma unroll
    for (int a = 0; a < 2; ++a)
#pragma unroll
        for (int b = 0; b < 2; ++b)
#pragma unroll
            for (int m = 0; m < 4; ++m)
#pragma unroll
                for (int n = 0; n < 2; ++n) acc[a][b][m][n] = (f32x4){0.f, 0.f, 0.f, 0.f};
    bf16x8 At[4][2], B0[2][2], B1[2][2];
    const char* cA = (const char*)cur.A; const char* cB = (const char*)cur.B;
    G8_STAGE(G8_SB(0, 0), cB, voffB); G8_STAGE(G8_SA(0, 0), cA, voffA); G8_STAGE(G8_SB(0, 1), cB + hstepB, voffB); G8_STAGE(G8_SA(0, 1), cA + hstepA, voffA);
    if (wr == 1) G8_BAR;
    G8_WAIT_V(4); G8_BAR;
    G8_STAGE(G8_SB(1, 0), cB + kstepB, voffB); G8_STAGE(G8_SA(1, 0), cA + kstepA, voffA); G8_STAGE(G8_SB(1, 1), cB + hstepB + kstepB, voffB);
    G8_WAIT_V(6); G8_BAR;
    for (;;) {
        const bool has_next = S.next(ui + 1, nxt);
        const char* nA = has_next ? (const char*)nxt.A : cA; const char* nB = has_next ? (const char*)nxt.B : cB;
        for (int t = 0; t < nt; t += 2) {
            const bool last = (t == nt - 2);
            const char* a1 = cA + (size_t)(t + 1) * kstepA;
            const char* a2 = last ? nA : cA + (size_t)(t + 2) * kstepA; const char* b2 = last ? nB : cB + (size_t)(t + 2) * kstepB;
            const char* a3 = a2 + kstepA; const char* b3 = b2 + kstepB;
            G8_LDB(B0, 0, 0); G8_SCHED; G8_LDA(At, 0, 0); G8_STAGE(G8_SA(1, 1), a1 + hstepA, voffA);
            G8_WAIT_L(8); G8_BAR; G8_WAIT_L(0); G8_MMA(0, 0, At, B0); G8_BAR; G8_SCHED;
            G8_LDB(B1, 0, 1); G8_STAGE(G8_SB(0, 0), b2, voffB);
            G8_BAR; G8_WAIT_L(0); G8_MMA(0, 1, At, B1); G8_BAR;
            G8_LDA(At, 0, 1); G8_STAGE(G8_SA(0, 0), a2, voffA);
            G8_BAR; G8_WAIT_L(0); G8_MMA(1, 0, At, B0); G8_BAR; G8_SCHED;
            G8_STAGE(G8_SB(0, 1), b2 + hstepB, voffB);
            G8_WAIT_V(6); G8_BAR; G8_MMA(1, 1, At, B1); G8_BAR;
            G8_LDB(B0, 1, 0); G8_SCHED; G8_LDA(At, 1, 0); G8_STAGE(G8_SA(0, 1), a2 + hstepA, voffA);
            G8_WAIT_L(8); G8_BAR; G8_WAIT_L(0); G8_MMA(0, 0, At, B0); G8_BAR; G8_SCHED;
            G8_LDB(B1, 1, 1); G8_STAGE(G8_SB(1, 0), b3, voffB);
            G8_BAR; G8_WAIT_L(0); G8_MMA(0, 1, At, B1); G8_BAR;
            G8_LDA(At, 1, 1); G8_STAGE(G8_SA(1, 0), a3, voffA);
            G8_BAR; G8_WAIT_L(0); G8_MMA(1, 0, At, B0); G8_BAR; G8_SCHED;
            G8_STAGE(G8_SB(1, 1), b3 + hstepB, voffB);
            G8_WAIT_V(6); G8_BAR; G8_MMA(1, 1, At, B1); G8_BAR;
        }
        E(acc, cur, wr, wc, fr, fq);
        if (!has_next) break;
#pragma unroll
        for (int a = 0; a < 2; ++a)
#pragma unroll
            for (int b = 0; b < 2; ++b)
#pragma unroll
                for (int m = 0; m < 4; ++m)
#pragma unroll
                    for (int n = 0; n < 2; ++n) acc[a][b][m][n] = (f32x4){0.f, 0.f, 0.f, 0.f};
        cur = nxt; cA = nA; cB = nB; ++ui;
    }
    G8_WAIT_V(0);
    if (wr == 0) G8_BAR;
    G8_BAR;
#undef G8_SA
#undef G8_SB
#undef G8_STAGE
#undef G8_LDA
#undef G8_LDB
#undef G8_TR
#undef G8_MMA
#undef G8_WAIT_V
#undef G8_WAIT_L
#undef G8_BAR
#undef G8_SCHED
}
}

__device__ __forceinline__ float gelu_tanh(float x) {
    const float u = 0.7978845608028654f * (x + 0.044715f * x * x * x);
    const float th = 1.0f - 2.0f * __builtin_amdgcn_rcpf(__expf(2.0f * u) + 1.0f);
    return 0.5f * x * (1.0f + th);
}

struct SchedQKV {
    const bf16_t* A; const bf16_t* W0; long d1, d2; int c;
    __device__ __forceinline__ bool next(int i, g8::Unit& u) const {
        int L; if (i == 0) L = c; else if (i == 1 && c < 128) L = 256 + c; else return false;
        const int pm = L & 7, pn = L >> 3, mat = pn >> 4;
        u.A = A + (size_t)pm * 256 * DM; u.B = W0 + (mat == 1 ? d1 : 0L) + (mat == 2 ? d2 : 0L) + (pn & 15) * 256; u.pm = pm; u.pn = pn; return true;
    }
};
struct EpiQKV {
    char* ws;
    __device__ __forceinline__ void operator()(const f32x4 (&acc)[2][2][4][2], const g8::Unit& u, int wr, int wc, int fr, int fq) const {
        bf16_t* base = (bf16_t*)(ws + WS_Q + (size_t)(u.pn >> 4) * ((size_t)S_LEN * HD * 2));
        const int row0 = u.pm * 256 + wr * 64 + fr, col0 = (u.pn & 15) * 256 + wc * 32 + 4 * fq;
#pragma unroll
        for (int ai = 0; ai < 2; ++ai)
#pragma unroll
            for (int m = 0; m < 4; ++m) { bf16_t* rowp = base + (size_t)(row0 + ai * 128 + m * 16) * HD + col0;
#pragma unroll
                for (int bj = 0; bj < 2; ++bj)
#pragma unroll
                    for (int n = 0; n < 2; ++n) { const f32x4 v = acc[ai][bj][m][n]; u32x2 w; w.x = pk_bf16(v[0], v[1]); w.y = pk_bf16(v[2], v[3]); *(u32x2*)(rowp + bj * 128 + n * 16) = w; } }
    }
};
struct SchedMLP1 {
    const bf16_t* A; const bf16_t* W; int c;
    __device__ __forceinline__ bool next(int i, g8::Unit& u) const {
        if (i != 0 || c < 128 || c >= 240) return false;
        const int L = c - 128, pm = L & 7, pn = L >> 3;
        u.A = A + (size_t)pm * 256 * DM; u.B = W + pn * 256; u.pm = pm; u.pn = pn; return true;
    }
};
struct EpiMLP1 {
    char* ws; const bf16_t* b1;
    __device__ __forceinline__ void operator()(const f32x4 (&acc)[2][2][4][2], const g8::Unit& u, int wr, int wc, int fr, int fq) const {
        bf16_t* base = (bf16_t*)(ws + WS_H);
        const int row0 = u.pm * 256 + wr * 64 + fr, col0 = u.pn * 256 + wc * 32 + 4 * fq;
#pragma unroll
        for (int bj = 0; bj < 2; ++bj)
#pragma unroll
            for (int n = 0; n < 2; ++n) {
                const u32x2 bw = *(const u32x2*)(b1 + col0 + bj * 128 + n * 16);
                const float b0 = bf2f(bw.x & 0xffffu), b1f = bf2f(bw.x >> 16), b2f = bf2f(bw.y & 0xffffu), b3 = bf2f(bw.y >> 16);
#pragma unroll
                for (int ai = 0; ai < 2; ++ai)
#pragma unroll
                    for (int m = 0; m < 4; ++m) { const f32x4 v = acc[ai][bj][m][n];
                        const float g0 = gelu_tanh(rbf(rbf(v[0]) + b0)), g1 = gelu_tanh(rbf(rbf(v[1]) + b1f)), g2 = gelu_tanh(rbf(rbf(v[2]) + b2f)), g3 = gelu_tanh(rbf(rbf(v[3]) + b3));
                        u32x2 w; w.x = pk_bf16(g0, g1); w.y = pk_bf16(g2, g3);
                        *(u32x2*)(base + (size_t)(row0 + ai * 128 + m * 16) * FF + col0 + bj * 128 + n * 16) = w; }
                asm volatile("" ::: "memory");
            }
    }
};
__global__ void __launch_bounds__(512, 2) k_gemm1(const bf16_t* __restrict__ Wq, const bf16_t* __restrict__ Wk, const bf16_t* __restrict__ Wv,
                                                  const bf16_t* __restrict__ w1, const bf16_t* __restrict__ b1, char* ws) {
    extern __shared__ __attribute__((aligned(16))) unsigned char shm[];
    const int c = blockIdx.x;
    { SchedQKV S{(const bf16_t*)(ws + WS_XN), Wq, (long)(Wk - Wq), (long)(Wv - Wq), c}; EpiQKV E{ws};
      g8::gemm_phase<EpiQKV, SchedQKV>((LAS unsigned char*)shm, DM, DM, HD, S, E); }
    { SchedMLP1 S{(const bf16_t*)(ws + WS_Y), w1, c}; EpiMLP1 E{ws, b1};
      g8::gemm_phase<EpiMLP1, SchedMLP1>((LAS unsigned char*)shm, DM, DM, FF, S, E); }
}

template <int MODE>
__global__ void __launch_bounds__(256, 2) k_gemm128(const bf16_t* __restrict__ A, int K, const bf16_t* __restrict__ B, const bf16_t* __restrict__ aux,
                                                    const bf16_t* __restrict__ bout, bf16_t* __restrict__ out) {
    extern __shared__ __attribute__((aligned(16))) unsigned char shm_[];
    LAS unsigned char* lds = (LAS unsigned char*)shm_;
    const int tid = threadIdx.x, wid = __builtin_amdgcn_readfirstlane(tid >> 6), lane = tid & 63, wr = wid >> 1, wc = wid & 1, fr = lane & 15, fq = lane >> 4;
    const int b = blockIdx.x, pn = b & 7, pm = b >> 3;
    constexpr int ldb = DM;
    unsigned voffA[4], voffB[4];
#pragma unroll
    for (int i = 0; i < 4; ++i) { int R, C; g8::stage_rc(tid * 16 + i * 4096, R, C); voffA[i] = (unsigned)(R * K + C) * 2u;
        const int bb = tid * 16 + i * 4096, row = bb >> 8, ch = (bb & 255) >> 4, seg = (ch >> 1) ^ g8::bsw(row);
        voffB[i] = (unsigned)(row * ldb + seg * 16 + (ch & 1) * 8) * 2u; }
    const char* cA = (const char*)(A + (size_t)pm * 128 * K); const char* cB = (const char*)(B + pn * 128);
    const unsigned ldsw = (unsigned)wid * 1024u;
    const int aoff = g8::lds_byte(wr * 64 + fr, fq * 8);
    const int q4 = (lane & 15) >> 2, p4 = lane & 3, sB = q4 | ((fq & 1) << 2);
    int boff[4];
#pragma unroll
    for (int n = 0; n < 4; ++n) boff[n] = (8 * fq + q4) * 256 + (((wc * 4 + n) ^ sB) * 32) + 8 * p4;
    f32x4 acc[4][4];
#pragma unroll
    for (int m = 0; m < 4; ++m)
#pragma unroll
        for (int n = 0; n < 4; ++n) acc[m][n] = (f32x4){0.f, 0.f, 0.f, 0.f};
    const int nt = K / 64;
#define STG(buf, t) do { _Pragma("unroll") for (int _i = 0; _i < 4; ++_i) { \
        __builtin_amdgcn_global_load_lds((const unsigned*)(cA + (size_t)(t) * 128 + voffA[_i]), (LAS unsigned*)(lds + (buf) * 32768 + ldsw + _i * 4096), 16, 0, 0); \
        __builtin_amdgcn_global_load_lds((const unsigned*)(cB + (size_t)(t) * 64 * ldb * 2 + voffB[_i]), (LAS unsigned*)(lds + (buf) * 32768 + 16384 + ldsw + _i * 4096), 16, 0, 0); } } while (0)
    STG(0, 0);
    asm volatile("s_waitcnt vmcnt(0)" ::: "memory"); __builtin_amdgcn_s_barrier();
    for (int t = 0; t < nt; ++t) {
        const int cur = t & 1;
        if (t + 1 < nt) STG(cur ^ 1, t + 1);
        bf16x8 At[4][2], Bt[4][2];
#pragma unroll
        for (int k = 0; k < 2; ++k) {
#pragma unroll
            for (int m = 0; m < 4; ++m) At[m][k] = *(const LAS bf16x8*)(lds + cur * 32768 + aoff + m * 2048 + k * 1024);
#pragma unroll
            for (int n = 0; n < 4; ++n) {
                bf16x4 a0 = __builtin_amdgcn_ds_read_tr16_b64_v4i16((LAS bf16x4*)(lds + cur * 32768 + 16384 + boff[n] + k * 8192));
                bf16x4 a1 = __builtin_amdgcn_ds_read_tr16_b64_v4i16((LAS bf16x4*)(lds + cur * 32768 + 16384 + boff[n] + k * 8192 + 1024));
                Bt[n][k] = __builtin_shufflevector(a0, a1, 0, 1, 2, 3, 4, 5, 6, 7); }
        }
        __builtin_amdgcn_s_setprio(1);
#pragma unroll
        for (int k = 0; k < 2; ++k)
#pragma unroll
            for (int m = 0; m < 4; ++m)
#pragma unroll
                for (int n = 0; n < 4; ++n) acc[m][n] = __builtin_amdgcn_mfma_f32_16x16x32_bf16(Bt[n][k], At[m][k], acc[m][n], 0, 0, 0);
        __builtin_amdgcn_s_setprio(0);
        asm volatile("s_waitcnt vmcnt(0) lgkmcnt(0)" ::: "memory"); __builtin_amdgcn_s_barrier();
    }
#undef STG
    const int row0 = pm * 128 + wr * 64 + fr, col0 = pn * 128 + wc * 64 + 4 * fq;
#pragma unroll
    for (int m = 0; m < 4; ++m)
#pragma unroll
        for (int n = 0; n < 4; ++n) {
            const size_t off = (size_t)(row0 + m * 16) * DM + col0 + n * 16;
            const f32x4 v = acc[m][n]; float r[4];
            if (MODE == 0) {
                const u32x2 bw = *(const u32x2*)(aux + col0 + n * 16);
                r[0] = rbf(v[0]) + bf2f(bw.x & 0xffffu); r[1] = rbf(v[1]) + bf2f(bw.x >> 16); r[2] = rbf(v[2]) + bf2f(bw.y & 0xffffu); r[3] = rbf(v[3]) + bf2f(bw.y >> 16);
            } else {
                const u32x2 xw = *(const u32x2*)(aux + off), bw = *(const u32x2*)(bout + off);
                r[0] = rbf(bf2f(xw.x & 0xffffu) + rbf(v[0])) + bf2f(bw.x & 0xffffu); r[1] = rbf(bf2f(xw.x >> 16) + rbf(v[1])) + bf2f(bw.x >> 16);
                r[2] = rbf(bf2f(xw.y & 0xffffu) + rbf(v[2])) + bf2f(bw.y & 0xffffu); r[3] = rbf(bf2f(xw.y >> 16) + rbf(v[3])) + bf2f(bw.y >> 16);
            }
            u32x2 w; w.x = pk_bf16(r[0], r[1]); w.y = pk_bf16(r[2], r[3]);
            *(u32x2*)(out + off) = w;
        }
}

constexpr unsigned SPIN_CAP = 200000u;
constexpr int KS_PITCH = 272;
constexpr int CH_LDS = 160 * 1024;

__device__ __forceinline__ float xmax16_32(float v) {
    auto a = __builtin_amdgcn_permlane16_swap(__builtin_bit_cast(unsigned, v), __builtin_bit_cast(unsigned, v), false, false);
    v = fmaxf(__builtin_bit_cast(float, (unsigned)a[0]), __builtin_bit_cast(float, (unsigned)a[1]));
    auto b = __builtin_amdgcn_permlane32_swap(__builtin_bit_cast(unsigned, v), __builtin_bit_cast(unsigned, v), false, false);
    return fmaxf(__builtin_bit_cast(float, (unsigned)b[0]), __builtin_bit_cast(float, (unsigned)b[1]));
}
__device__ __forceinline__ float xsum16_32(float v) {
    auto a = __builtin_amdgcn_permlane16_swap(__builtin_bit_cast(unsigned, v), __builtin_bit_cast(unsigned, v), false, false);
    v = __builtin_bit_cast(float, (unsigned)a[0]) + __builtin_bit_cast(float, (unsigned)a[1]);
    auto b = __builtin_amdgcn_permlane32_swap(__builtin_bit_cast(unsigned, v), __builtin_bit_cast(unsigned, v), false, false);
    return __builtin_bit_cast(float, (unsigned)b[0]) + __builtin_bit_cast(float, (unsigned)b[1]);
}

#define STAMP(i) do {} while (0)
#define CH_DELAY1 12
#define CH_DELAY2 8
#define CH_DELAY3 12

template <int CPH>
__device__ __forceinline__ void chain_body(LAS unsigned char* lds, const int b, const bf16_t* __restrict__ Wq, const bf16_t* __restrict__ Wk, const bf16_t* __restrict__ Wv,
                                           const bf16_t* __restrict__ Wo, char* ws) {
    constexpr int ES = 128 / CPH, NE = ES / 16, NT = 3 * NE, RG = CPH / 2, RPG = 8 / RG;
    constexpr int VP = ES * 2;
    constexpr int L_RED = 0;
    constexpr int L_RED2 = L_RED + 8 * NT * 512;
    constexpr int L_KS = L_RED2 + 8 * RPG * 64;
    constexpr int L_VS = L_KS + 2 * 32 * KS_PITCH;
    constexpr int L_OS = L_VS + 2 * 48 * VP;
    constexpr bool WOL = (CPH == 4);
    constexpr int L_WO = L_OS + 8 * 1024 * RPG;
    static_assert(L_WO + (WOL ? 65536 : 0) <= 156 * 1024, "LDS");
    const int tid = threadIdx.x, wave = __builtin_amdgcn_readfirstlane(tid >> 6), lane = tid & 63, fr = lane & 15, fq = lane >> 4;
    const int h = b / CPH, j = b % CPH, g = b % RG, c = b / RG;
    const __amdgpu_buffer_rsrc_t rs = __builtin_amdgcn_make_buffer_rsrc(ws + WS_SENT0, 0, (int)(WS_SENT1 - WS_SENT0), 0x00020000);
    constexpr int O_MEM = (int)(WS_MEM - WS_SENT0), O_QKVM = (int)(WS_QKVM - WS_SENT0), O_OM = (int)(WS_OM - WS_SENT0);
    const bf16_t* Kx = (const bf16_t*)(ws + WS_K); const bf16_t* Vx = (const bf16_t*)(ws + WS_V);
    bool dead = false;

    bf16x8 wq[NT][4], wo[16];
#pragma unroll
    for (int t = 0; t < NT; ++t) {
        const bf16_t* W = (t / NE == 0 ? Wq : (t / NE == 1 ? Wk : Wv)) + h * DH + j * ES + (t % NE) * 16 + fr;
#pragma unroll
        for (int i = 0; i < 4; ++i)
#pragma unroll
            for (int jj = 0; jj < 8; ++jj) wq[t][i][jj] = (short)W[(size_t)(wave * 128 + i * 32 + fq * 8 + jj) * HD];
        asm volatile("" : "+v"(wq[t][0]), "+v"(wq[t][1]), "+v"(wq[t][2]), "+v"(wq[t][3]) :: "memory");
    }
#pragma unroll
    for (int i = 0; i < 16; ++i) {
#pragma unroll
        for (int jj = 0; jj < 8; ++jj) wo[i][jj] = (short)Wo[(size_t)(wave * 512 + i * 32 + fq * 8 + jj) * DM + c * 16 + fr];
        if ((i & 3) == 3) asm volatile("" : "+v"(wo[i - 3]), "+v"(wo[i - 2]), "+v"(wo[i - 1]), "+v"(wo[i]) :: "memory");
        if (WOL && i >= 8) *(LAS bf16x8*)(lds + L_WO + (wave * 8 + i - 8) * 1024 + lane * 16) = wo[i];
    }

    if (tid < 2 * 8 * VP / 16) { const int bf = tid / (8 * VP / 16), r = tid % (8 * VP / 16); *(LAS u32x4*)(lds + L_VS + bf * 48 * VP + 40 * VP + r * 16) = (u32x4){0u, 0u, 0u, 0u}; }
    {
        const int r = tid >> 4, ch = tid & 15;
        *(LAS u32x4*)(lds + L_KS + r * KS_PITCH + ch * 16) = *(const u32x4*)(Kx + (size_t)r * HD + h * DH + ch * 8);
        if (tid < 32 * ES / 8) { const int r2 = tid / (ES / 8), c2 = tid % (ES / 8); *(LAS u32x4*)(lds + L_VS + (8 + r2) * VP + c2 * 16) = *(const u32x4*)(Vx + (size_t)r2 * HD + h * DH + j * ES + c2 * 8); }
    }
    __syncthreads();

    for (int s = 0; s < NSEG; ++s) {
        const int kb = s & 1;
        STAMP(0);
        {
            if (s > 0) __builtin_amdgcn_s_sleep(CH_DELAY1);
            u32x4 a[4];
            const int base = O_MEM + s * (int)MEM_B + wave * 2048 + fq * 128 + (lane & 7) * 16;
            for (unsigned spins = 0;;) {
                asm volatile("" ::: "memory");
#pragma unroll
                for (int i = 0; i < 4; ++i) a[i] = __builtin_amdgcn_raw_buffer_load_b128(rs, base + i * 512, 0, AUX_SC1);
                bool bad = false;
#pragma unroll
                for (int i = 0; i < 4; ++i) bad |= has_sent(a[i]);
                if (!__any(bad) || dead) break;
                if (++spins > SPIN_CAP) { dead = true; if (lane == 0) *(volatile unsigned*)(ws + WS_TMO) = 1u; break; }
            }
            STAMP(1);
            f32x4 acc[NT];
#pragma unroll
            for (int t = 0; t < NT; ++t) acc[t] = (f32x4){0.f, 0.f, 0.f, 0.f};
#pragma unroll
            for (int i = 0; i < 4; ++i)
#pragma unroll
                for (int t = 0; t < NT; ++t) acc[t] = __builtin_amdgcn_mfma_f32_16x16x32_bf16(wq[t][i], __builtin_bit_cast(bf16x8, a[i]), acc[t], 0, 0, 0);
            if (fr < 8) {
#pragma unroll
                for (int t = 0; t < NT; ++t) *(LAS f32x4*)(lds + L_RED + ((wave * NT + t) * 128 + fr * 16 + fq * 4) * 4) = acc[t];
            }
        }
        __syncthreads();
        if (tid < NT * 64) {
            const int t = tid >> 6, m = (tid & 63) >> 3, np = (tid & 7) * 2, mat = t / NE, col = (t % NE) * 16 + np;
            f32x2 sum = (f32x2){0.f, 0.f};
#pragma unroll
            for (int w = 0; w < 8; ++w) sum += *(const LAS f32x2*)(lds + L_RED + ((w * NT + t) * 128 + m * 16 + np) * 4);
            const unsigned pk = desent(pk_bf16(sum.x, sum.y));
            __builtin_amdgcn_raw_buffer_store_b32(pk, rs, O_QKVM + s * (int)QKVM_B + ((h * 3 + mat) * 8 + m) * 256 + (j * ES + col) * 2, 0, AUX_SC1);
            if (mat == 2) *(LAS unsigned*)(lds + L_VS + kb * 48 * VP + m * VP + col * 2) = pk;
        }
        STAMP(2);
        if (s == NSEG - 1) break;
        __syncthreads();
        if (wave == 0) {
            bf16x8 kx[3][4];
#pragma unroll
            for (int t = 0; t < 3; ++t) {
                int r = t * 16 + fr - 8; r = r < 0 ? 0 : (r > 31 ? 31 : r);
#pragma unroll
                for (int i = 0; i < 4; ++i) kx[t][i] = *(const LAS bf16x8*)(lds + L_KS + kb * 32 * KS_PITCH + r * KS_PITCH + i * 64 + fq * 16);
            }
            __builtin_amdgcn_s_sleep(CH_DELAY2);
            u32x4 qf[4], km[4];
            const int base = O_QKVM + s * (int)QKVM_B + h * 3 * 2048 + (lane & 7) * 256 + fq * 16;
            for (unsigned spins = 0;;) {
                asm volatile("" ::: "memory");
#pragma unroll
                for (int i = 0; i < 4; ++i) { qf[i] = __builtin_amdgcn_raw_buffer_load_b128(rs, base + i * 64, 0, AUX_SC1); km[i] = __builtin_amdgcn_raw_buffer_load_b128(rs, base + 2048 + i * 64, 0, AUX_SC1); }
                bool bad = false;
#pragma unroll
                for (int i = 0; i < 4; ++i) bad |= has_sent(qf[i]) || has_sent(km[i]);
                if (!__any(bad) || dead) break;
                if (++spins > SPIN_CAP) { dead = true; if (lane == 0) *(volatile unsigned*)(ws + WS_TMO) = 2u; break; }
            }
            STAMP(3);
            f32x4 sc[3];
#pragma unroll
            for (int t = 0; t < 3; ++t) sc[t] = (f32x4){0.f, 0.f, 0.f, 0.f};
#pragma unroll
            for (int i = 0; i < 4; ++i) {
                const bf16x8 k0 = fr < 8 ? __builtin_bit_cast(bf16x8, km[i]) : kx[0][i];
                sc[1] = __builtin_amdgcn_mfma_f32_16x16x32_bf16(kx[1][i], __builtin_bit_cast(bf16x8, qf[i]), sc[1], 0, 0, 0);
                sc[2] = __builtin_amdgcn_mfma_f32_16x16x32_bf16(kx[2][i], __builtin_bit_cast(bf16x8, qf[i]), sc[2], 0, 0, 0);
                sc[0] = __builtin_amdgcn_mfma_f32_16x16x32_bf16(k0, __builtin_bit_cast(bf16x8, qf[i]), sc[0], 0, 0, 0);
            }
            asm volatile("" ::: "memory");
            bf16x8 vf[NE][2];
            const bf16x4 zz = (bf16x4){0, 0, 0, 0};
#pragma unroll
            for (int et = 0; et < NE; ++et) {
                const int voff = L_VS + kb * 48 * VP + (4 * fq + (fr >> 2)) * VP + et * 32 + (lane & 3) * 8;
                const bf16x4 v0 = __builtin_amdgcn_ds_read_tr16_b64_v4i16((LAS bf16x4*)(lds + voff));
                const bf16x4 v1 = __builtin_amdgcn_ds_read_tr16_b64_v4i16((LAS bf16x4*)(lds + voff + 16 * VP));
                const bf16x4 v2 = __builtin_amdgcn_ds_read_tr16_b64_v4i16((LAS bf16x4*)(lds + voff + 32 * VP));
                vf[et][0] = __builtin_shufflevector(v0, v1, 0, 1, 2, 3, 4, 5, 6, 7); vf[et][1] = __builtin_shufflevector(v2, zz, 0, 1, 2, 3, 4, 5, 6, 7);
            }
            const float scale = 0.08838834764831845f;
            float v[12]; float mx = -INFINITY;
#pragma unroll
            for (int t = 0; t < 3; ++t)
#pragma unroll
                for (int r = 0; r < 4; ++r) { float z = rbf(rbf(sc[t][r]) * scale); if (t == 2 && fq >= 2) z = -INFINITY; v[t * 4 + r] = z; mx = fmaxf(mx, z); }
            mx = xmax16_32(mx);
            float sum = 0.f;
#pragma unroll
            for (int i = 0; i < 12; ++i) { v[i] = __expf(v[i] - mx); sum += v[i]; }
            sum = xsum16_32(sum);
            const float inv = 1.0f / sum;
            u32x4 p0, p1;
            p0.x = pk_bf16(v[0] * inv, v[1] * inv); p0.y = pk_bf16(v[2] * inv, v[3] * inv); p0.z = pk_bf16(v[4] * inv, v[5] * inv); p0.w = pk_bf16(v[6] * inv, v[7] * inv);
            p1.x = pk_bf16(v[8] * inv, v[9] * inv); p1.y = pk_bf16(v[10] * inv, v[11] * inv); p1.z = 0u; p1.w = 0u;
#pragma unroll
            for (int et = 0; et < NE; ++et) {
                f32x4 o = (f32x4){0.f, 0.f, 0.f, 0.f};
                o = __builtin_amdgcn_mfma_f32_16x16x32_bf16(vf[et][0], __builtin_bit_cast(bf16x8, p0), o, 0, 0, 0);
                o = __builtin_amdgcn_mfma_f32_16x16x32_bf16(vf[et][1], __builtin_bit_cast(bf16x8, p1), o, 0, 0, 0);
                if (fr < 8) {
                    u32x2 w; w.x = desent(pk_bf16(o[0], o[1])); w.y = desent(pk_bf16(o[2], o[3]));
                    const int k = h * DH + j * ES + et * 16 + 4 * fq;
                    const int off = O_OM + s * (int)OM_B + (fr / RPG) * (8192 * RPG) + (k >> 5) * (64 * RPG) + ((k >> 3) & 3) * (16 * RPG) + (fr % RPG) * 16 + (k & 7) * 2;
                    __builtin_amdgcn_raw_buffer_store_b64(w, rs, off, 0, AUX_SC1);
                }
            }
            STAMP(4);
        } else {
            const int t2 = tid - 64; const int nb = kb ^ 1; const size_t row0 = (size_t)(s + 1) * SEG;
            for (int i = t2; i < 512 + 32 * ES / 8; i += 448) {
                if (i < 512) { const int r = i >> 4, ch = i & 15;
                    *(LAS u32x4*)(lds + L_KS + nb * 32 * KS_PITCH + r * KS_PITCH + ch * 16) = *(const u32x4*)(Kx + (row0 + r) * HD + h * DH + ch * 8); }
                else { const int r2 = (i - 512) / (ES / 8), c2 = (i - 512) % (ES / 8);
                    *(LAS u32x4*)(lds + L_VS + nb * 48 * VP + (8 + r2) * VP + c2 * 16) = *(const u32x4*)(Vx + (row0 + r2) * HD + h * DH + j * ES + c2 * 8); }
            }
        }
        __syncthreads();
        {
            if (WOL) {
#pragma unroll
                for (int i = 8; i < 16; ++i) wo[i] = *(const LAS bf16x8*)(lds + L_WO + (wave * 8 + i - 8) * 1024 + lane * 16);
            }
            __builtin_amdgcn_s_sleep(CH_DELAY3);
            u32x4 a[RPG];
            const int base = O_OM + s * (int)OM_B + g * (8192 * RPG) + wave * (1024 * RPG) + lane * 16;
            for (unsigned spins = 0;;) {
                asm volatile("" ::: "memory");
#pragma unroll
                for (int i = 0; i < RPG; ++i) a[i] = __builtin_amdgcn_raw_buffer_load_b128(rs, base + i * 1024, 0, AUX_SC1);
                bool bad = false;
#pragma unroll
                for (int i = 0; i < RPG; ++i) bad |= has_sent(a[i]);
                if (!__any(bad) || dead) break;
                if (++spins > SPIN_CAP) { dead = true; if (lane == 0) *(volatile unsigned*)(ws + WS_TMO) = 3u; break; }
            }
            STAMP(5);
#pragma unroll
            for (int i = 0; i < RPG; ++i) *(LAS u32x4*)(lds + L_OS + wave * (1024 * RPG) + i * 1024 + lane * 16) = a[i];
            asm volatile("s_waitcnt lgkmcnt(0)" ::: "memory");
            f32x4 acc[4];
#pragma unroll
            for (int q = 0; q < 4; ++q) acc[q] = (f32x4){0.f, 0.f, 0.f, 0.f};
#pragma unroll
            for (int i = 0; i < 16; ++i) {
                const bf16x8 f = *(const LAS bf16x8*)(lds + L_OS + wave * (1024 * RPG) + i * (64 * RPG) + fq * (16 * RPG) + (lane % RPG) * 16);
                acc[i & 3] = __builtin_amdgcn_mfma_f32_16x16x32_bf16(wo[i], f, acc[i & 3], 0, 0, 0);
            }
            const f32x4 r = (acc[0] + acc[1]) + (acc[2] + acc[3]);
            if (fr < RPG) *(LAS f32x4*)(lds + L_RED2 + ((wave * RPG + fr) * 16 + fq * 4) * 4) = r;
        }
        __syncthreads();
        if (tid < RPG * 8) {
            const int m = tid >> 3, np = (tid & 7) * 2;
            f32x2 sum = (f32x2){0.f, 0.f};
#pragma unroll
            for (int w = 0; w < 8; ++w) sum += *(const LAS f32x2*)(lds + L_RED2 + ((w * RPG + m) * 16 + np) * 4);
            const unsigned pk = desent(pk_bf16(sum.x, sum.y));
            const int off = O_MEM + (s + 1) * (int)MEM_B + (c >> 1) * 512 + ((c & 1) * 2 + (np >> 3)) * 128 + (g * RPG + m) * 16 + (np & 7) * 2;
            __builtin_amdgcn_raw_buffer_store_b32(pk, rs, off, 0, AUX_SC1);
        }
        STAMP(6);
    }
}

#define CH_CPH 8
__global__ void __launch_bounds__(512, 2) k_chain(const bf16_t* __restrict__ Wq, const bf16_t* __restrict__ Wk, const bf16_t* __restrict__ Wv,
                                                  const bf16_t* __restrict__ Wo, char* ws) {
    extern __shared__ __attribute__((aligned(16))) unsigned char shm_[];
    chain_body<CH_CPH>((LAS unsigned char*)shm_, blockIdx.x, Wq, Wk, Wv, Wo, ws);
}

template <class Epi>
__device__ __forceinline__ void gemm128_w8(LAS unsigned char* lds, const bf16_t* A, const int lda, const bf16_t* B, const int ldb, const int K, const Epi& E) {
    int tid = threadIdx.x; asm volatile("" : "+v"(tid));
    const int wid = __builtin_amdgcn_readfirstlane(tid >> 6), lane = tid & 63, wr = wid >> 2, wc = wid & 3, fr = lane & 15, fq = lane >> 4;
    unsigned voffA[2], voffB[2];
#pragma unroll
    for (int i = 0; i < 2; ++i) { int R, C; g8::stage_rc(tid * 16 + i * 8192, R, C); voffA[i] = (unsigned)(R * lda + C) * 2u;
        const int bb = tid * 16 + i * 8192, row = bb >> 8, ch = (bb & 255) >> 4, seg = (ch >> 1) ^ g8::bsw(row);
        voffB[i] = (unsigned)(row * ldb + seg * 16 + (ch & 1) * 8) * 2u; }
    const char* cA = (const char*)A; const char* cB = (const char*)B;
    const unsigned ldsw = (unsigned)wid * 1024u;
    const int aoff = g8::lds_byte(wr * 64 + fr, fq * 8);
    const int q4 = (lane & 15) >> 2, p4 = lane & 3, sB = q4 | ((fq & 1) << 2);
    const int boff0 = (8 * fq + q4) * 256 + (((wc * 2 + 0) ^ sB) * 32) + 8 * p4;
    const int boff1 = (8 * fq + q4) * 256 + (((wc * 2 + 1) ^ sB) * 32) + 8 * p4;
    f32x4 acc[4][2];
#pragma unroll
    for (int m = 0; m < 4; ++m) { acc[m][0] = (f32x4){0.f, 0.f, 0.f, 0.f}; acc[m][1] = (f32x4){0.f, 0.f, 0.f, 0.f}; }
    const int nt = K / 64;
#define STG(buf, t) do { _Pragma("unroll") for (int _i = 0; _i < 2; ++_i) { \
        __builtin_amdgcn_global_load_lds((const unsigned*)(cA + (size_t)(t) * 128 + voffA[_i]), (LAS unsigned*)(lds + (buf) * 32768 + ldsw + _i * 8192), 16, 0, 0); \
        __builtin_amdgcn_global_load_lds((const unsigned*)(cB + (size_t)(t) * 64 * ldb * 2 + voffB[_i]), (LAS unsigned*)(lds + (buf) * 32768 + 16384 + ldsw + _i * 8192), 16, 0, 0); } } while (0)
    STG(0, 0); STG(1, 1);
    int cur = 0;
    for (int t = 0; t < nt; ++t) {
        if (t + 1 < nt) asm volatile("s_waitcnt vmcnt(4)" ::: "memory"); else asm volatile("s_waitcnt vmcnt(0)" ::: "memory");
        __builtin_amdgcn_s_barrier();
        asm volatile("" ::: "memory");
        int nx = cur + 2; if (nx >= 3) nx -= 3;
        if (t + 2 < nt) STG(nx, t + 2);
        bf16x8 At[4][2], Bt[2][2];
        LAS unsigned char* sa = lds + cur * 32768; LAS unsigned char* sb = sa + 16384;
#pragma unroll
        for (int k = 0; k < 2; ++k) {
            bf16x4 a0 = __builtin_amdgcn_ds_read_tr16_b64_v4i16((LAS bf16x4*)(sb + boff0 + k * 8192)), a1 = __builtin_amdgcn_ds_read_tr16_b64_v4i16((LAS bf16x4*)(sb + boff0 + k * 8192 + 1024));
            bf16x4 c0 = __builtin_amdgcn_ds_read_tr16_b64_v4i16((LAS bf16x4*)(sb + boff1 + k * 8192)), c1 = __builtin_amdgcn_ds_read_tr16_b64_v4i16((LAS bf16x4*)(sb + boff1 + k * 8192 + 1024));
            Bt[0][k] = __builtin_shufflevector(a0, a1, 0, 1, 2, 3, 4, 5, 6, 7); Bt[1][k] = __builtin_shufflevector(c0, c1, 0, 1, 2, 3, 4, 5, 6, 7);
#pragma unroll
            for (int m = 0; m < 4; ++m) At[m][k] = *(const LAS bf16x8*)(sa + aoff + m * 2048 + k * 1024);
        }
        __builtin_amdgcn_s_setprio(1);
#pragma unroll
        for (int k = 0; k < 2; ++k)
#pragma unroll
            for (int m = 0; m < 4; ++m)
#pragma unroll
                for (int n = 0; n < 2; ++n) acc[m][n] = __builtin_amdgcn_mfma_f32_16x16x32_bf16(Bt[n][k], At[m][k], acc[m][n], 0, 0, 0);
        __builtin_amdgcn_s_setprio(0);
        asm volatile("s_waitcnt lgkmcnt(0)" ::: "memory");
        cur = cur + 1 == 3 ? 0 : cur + 1;
    }
#undef STG
    __builtin_amdgcn_s_barrier();
    E(acc, wr, wc, fr, fq);
}

__device__ __forceinline__ void wait_counter(const char* ws, int idx, unsigned need) {
    if (threadIdx.x < 64) {
        const unsigned* p = (const unsigned*)(ws + WS_CNT + (size_t)idx * 256);
        for (unsigned spins = 0; (unsigned)__builtin_amdgcn_readfirstlane(__hip_atomic_load(p, __ATOMIC_RELAXED, __HIP_MEMORY_SCOPE_AGENT)) < need;) {
            __builtin_amdgcn_s_sleep(8);
            if (++spins > 400000u) break;
        }
        __builtin_amdgcn_fence(__ATOMIC_ACQUIRE, "agent");
        asm volatile("s_waitcnt vmcnt(0)" ::: "memory");
    }
    asm volatile("" ::: "memory"); __builtin_amdgcn_s_barrier(); asm volatile("" ::: "memory");
}
__device__ __forceinline__ void signal_counter(char* ws, int idx) {
    asm volatile("s_waitcnt vmcnt(0)" ::: "memory");
    if ((threadIdx.x & 63) == 0) __hip_atomic_fetch_add((unsigned*)(ws + WS_CNT + (size_t)idx * 256), 1u, __ATOMIC_RELAXED, __HIP_MEMORY_SCOPE_AGENT);
}

struct SchedOne {
    g8::Unit u; bool valid;
    __device__ __forceinline__ bool next(int i, g8::Unit& o) const { if (i != 0 || !valid) return false; o = u; return true; }
};
struct EpiMLP1W {
    char* ws; const bf16_t* b1;
    __device__ __forceinline__ void operator()(const f32x4 (&acc)[2][2][4][2], const g8::Unit& u, int wr, int wc, int fr, int fq) const {
        const __amdgpu_buffer_rsrc_t rh = __builtin_amdgcn_make_buffer_rsrc(ws + WS_H, 0, S_LEN * FF * 2, 0x00020000);
        const int row0 = u.pm * 256 + wr * 64 + fr, col0 = u.pn * 256 + wc * 32 + 4 * fq;
#pragma unroll
        for (int bj = 0; bj < 2; ++bj)
#pragma unroll
            for (int n = 0; n < 2; ++n) {
                const u32x2 bw = *(const u32x2*)(b1 + col0 + bj * 128 + n * 16);
                const float b0 = bf2f(bw.x & 0xffffu), b1f = bf2f(bw.x >> 16), b2f = bf2f(bw.y & 0xffffu), b3 = bf2f(bw.y >> 16);
#pragma unroll
                for (int ai = 0; ai < 2; ++ai)
#pragma unroll
                    for (int m = 0; m < 4; ++m) { const f32x4 v = acc[ai][bj][m][n];
                        const float g0 = gelu_tanh(rbf(rbf(v[0]) + b0)), g1 = gelu_tanh(rbf(rbf(v[1]) + b1f)), g2 = gelu_tanh(rbf(rbf(v[2]) + b2f)), g3 = gelu_tanh(rbf(rbf(v[3]) + b3));
                        u32x2 w; w.x = pk_bf16(g0, g1); w.y = pk_bf16(g2, g3);
                        __builtin_amdgcn_raw_buffer_store_b64(w, rh, ((row0 + ai * 128 + m * 16) * FF + col0 + bj * 128 + n * 16) * 2, 0, AUX_SC1); }
                asm volatile("" ::: "memory");
            }
        signal_counter(ws, CNT_H + u.pm);
    }
};
struct EpiMLP2 {
    bf16_t* out; const bf16_t* b2; int row0, col0;
    __device__ __forceinline__ void operator()(const f32x4 (&acc)[4][2], int wr, int wc, int fr, int fq) const {
#pragma unroll
        for (int n = 0; n < 2; ++n) {
            const int col = col0 + wc * 32 + n * 16 + 4 * fq;
            const u32x2 bw = *(const u32x2*)(b2 + col);
#pragma unroll
            for (int m = 0; m < 4; ++m) { const f32x4 v = acc[m][n];
                u32x2 w; w.x = pk_bf16(rbf(v[0]) + bf2f(bw.x & 0xffffu), rbf(v[1]) + bf2f(bw.x >> 16)); w.y = pk_bf16(rbf(v[2]) + bf2f(bw.y & 0xffffu), rbf(v[3]) + bf2f(bw.y >> 16));
                *(u32x2*)(out + (size_t)(row0 + wr * 64 + m * 16 + fr) * DM + col) = w; }
        }
    }
};

__global__ void __launch_bounds__(512, 2) k_mega(const bf16_t* __restrict__ Wq, const bf16_t* __restrict__ Wk, const bf16_t* __restrict__ Wv, const bf16_t* __restrict__ Wo,
                                                 const bf16_t* __restrict__ w1, const bf16_t* __restrict__ b1, const bf16_t* __restrict__ w2, const bf16_t* __restrict__ b2, char* ws) {
    extern __shared__ __attribute__((aligned(16))) unsigned char shm_[];
    LAS unsigned char* lds = (LAS unsigned char*)shm_;
    const int b = blockIdx.x;
    const int role = (b >> 3) & 1, r = (b >> 4) * 8 + (b & 7);
    if (role == 0) { chain_body<4>(lds, r, Wq, Wk, Wv, Wo, ws); return; }
    {
        SchedOne S; S.valid = true; S.u.pm = r & 7; S.u.pn = r >> 3; S.u.A = (const bf16_t*)(ws + WS_XN) + (size_t)S.u.pm * 256 * DM; S.u.B = Wq + S.u.pn * 256;
        EpiQKV E{ws}; g8::gemm_phase<EpiQKV, SchedOne>(lds, DM, DM, HD, S, E);
    }
    {
        SchedOne S; S.valid = r < 112; S.u.pm = r & 7; S.u.pn = r >> 3; S.u.A = (const bf16_t*)(ws + WS_Y) + (size_t)S.u.pm * 256 * DM; S.u.B = w1 + S.u.pn * 256;
        EpiMLP1W E{ws, b1}; g8::gemm_phase<EpiMLP1W, SchedOne>(lds, DM, DM, FF, S, E);
    }
    {
        const int pm = r >> 3, pn = r & 7;
        wait_counter(ws, CNT_H + (pm >> 1), 14 * 8);
        EpiMLP2 E{(bf16_t*)(ws + WS_B), b2, pm * 128, pn * 128};
        gemm128_w8<EpiMLP2>(lds, (const bf16_t*)(ws + WS_H) + (size_t)pm * 128 * FF, FF, w2 + pn * 128, DM, FF, E);
    }
}

struct SchedKV {
    const bf16_t* A; const bf16_t* Wk; long dv; int c;
    __device__ __forceinline__ bool next(int i, g8::Unit& u) const {
        if (i != 0) return false;
        const int pm = c & 7, pn = c >> 3;
        u.A = A + (size_t)pm * 256 * DM; u.B = Wk + (pn >= 16 ? dv : 0L) + (pn & 15) * 256; u.pm = pm; u.pn = 16 + pn; return true;
    }
};
__global__ void __launch_bounds__(512, 2) k_gemm_kv(const bf16_t* __restrict__ Wk, const bf16_t* __restrict__ Wv, char* ws) {
    extern __shared__ __attribute__((aligned(16))) unsigned char shm[];
    SchedKV S{(const bf16_t*)(ws + WS_XN), Wk, (long)(Wv - Wk), (int)blockIdx.x}; EpiQKV E{ws};
    g8::gemm_phase<EpiQKV, SchedKV>((LAS unsigned char*)shm, DM, DM, HD, S, E);
}

constexpr int XV_PITCH = 272;
__global__ void __launch_bounds__(256) k_xattn(char* ws) {
    __shared__ __attribute__((aligned(16))) unsigned char shm_[4 * 48 * XV_PITCH];
    const int wave = threadIdx.x >> 6, lane = threadIdx.x & 63, fr = lane & 15, fq = lane >> 4;
    const int item = blockIdx.x * 4 + wave, s = item >> 5, h = item & 31;
    LAS unsigned char* vs = (LAS unsigned char*)shm_ + wave * 48 * XV_PITCH;
    const bf16_t* Q = (const bf16_t*)(ws + WS_Q) + (size_t)s * SEG * HD + h * DH;
    const bf16_t* K = (const bf16_t*)(ws + WS_K) + (size_t)s * SEG * HD + h * DH;
    const bf16_t* V = (const bf16_t*)(ws + WS_V) + (size_t)s * SEG * HD + h * DH;
    const bf16_t* KM = (const bf16_t*)(ws + WS_QKVM + (size_t)s * QKVM_B) + (h * 3 + 1) * 1024;
    const bf16_t* VM = KM + 1024;
#pragma unroll
    for (int i = 0; i < 12; ++i) {
        const int r = i * 4 + fq, ch = fr;
        u32x4 v = (u32x4){0u, 0u, 0u, 0u};
        if (r < 8) v = *(const u32x4*)(VM + r * DH + ch * 8); else if (r < 40) v = *(const u32x4*)(V + (size_t)(r - 8) * HD + ch * 8);
        *(LAS u32x4*)(vs + r * XV_PITCH + ch * 16) = v;
    }
    f32x4 sc[3][2];
#pragma unroll
    for (int t = 0; t < 3; ++t) { sc[t][0] = (f32x4){0.f, 0.f, 0.f, 0.f}; sc[t][1] = (f32x4){0.f, 0.f, 0.f, 0.f}; }
#pragma unroll
    for (int i = 0; i < 4; ++i) {
        const bf16x8 q0 = *(const bf16x8*)(Q + (size_t)fr * HD + i * 32 + fq * 8), q1 = *(const bf16x8*)(Q + (size_t)(16 + fr) * HD + i * 32 + fq * 8);
#pragma unroll
        for (int t = 0; t < 3; ++t) {
            const int key = t * 16 + fr;
            const bf16_t* kp = key < 8 ? KM + key * DH : K + (size_t)((key < 40 ? key : 39) - 8) * HD;
            const bf16x8 kf = *(const bf16x8*)(kp + i * 32 + fq * 8);
            sc[t][0] = __builtin_amdgcn_mfma_f32_16x16x32_bf16(kf, q0, sc[t][0], 0, 0, 0);
            sc[t][1] = __builtin_amdgcn_mfma_f32_16x16x32_bf16(kf, q1, sc[t][1], 0, 0, 0);
        }
    }
    const float scale = 0.08838834764831845f, NEG = rbf(-1000000000.0f);
    u32x4 p0[2], p1[2];
#pragma unroll
    for (int rt = 0; rt < 2; ++rt) {
        const int row = rt * 16 + fr;
        float v[12]; float mx = -INFINITY;
#pragma unroll
        for (int t = 0; t < 3; ++t)
#pragma unroll
            for (int r = 0; r < 4; ++r) { const int key = t * 16 + fq * 4 + r; float z = rbf(rbf(sc[t][rt][r]) * scale);
                if (key - 8 > row) z = NEG; if (key >= 40) z = -INFINITY; v[t * 4 + r] = z; mx = fmaxf(mx, z); }
        mx = fmaxf(mx, __shfl_xor(mx, 16)); mx = fmaxf(mx, __shfl_xor(mx, 32));
        float sum = 0.f;
#pragma unroll
        for (int i = 0; i < 12; ++i) { v[i] = __expf(v[i] - mx); sum += v[i]; }
        sum += __shfl_xor(sum, 16); sum += __shfl_xor(sum, 32);
        const float inv = 1.0f / sum;
        p0[rt].x = pk_bf16(v[0] * inv, v[1] * inv); p0[rt].y = pk_bf16(v[2] * inv, v[3] * inv); p0[rt].z = pk_bf16(v[4] * inv, v[5] * inv); p0[rt].w = pk_bf16(v[6] * inv, v[7] * inv);
        p1[rt].x = pk_bf16(v[8] * inv, v[9] * inv); p1[rt].y = pk_bf16(v[10] * inv, v[11] * inv); p1[rt].z = 0u; p1[rt].w = 0u;
    }
    asm volatile("s_waitcnt lgkmcnt(0)" ::: "memory");
    bf16_t* O = (bf16_t*)(ws + WS_O) + (size_t)s * SEG * HD + h * DH;
    const bf16x4 zz = (bf16x4){0, 0, 0, 0};
#pragma unroll
    for (int et = 0; et < 8; ++et) {
        const int voff = (4 * fq + (fr >> 2)) * XV_PITCH + et * 32 + (lane & 3) * 8;
        const bf16x4 v0 = __builtin_amdgcn_ds_read_tr16_b64_v4i16((LAS bf16x4*)(vs + voff));
        const bf16x4 v1 = __builtin_amdgcn_ds_read_tr16_b64_v4i16((LAS bf16x4*)(vs + voff + 16 * XV_PITCH));
        const bf16x4 v2 = __builtin_amdgcn_ds_read_tr16_b64_v4i16((LAS bf16x4*)(vs + voff + 32 * XV_PITCH));
        const bf16x8 va = __builtin_shufflevector(v0, v1, 0, 1, 2, 3, 4, 5, 6, 7), vb = __builtin_shufflevector(v2, zz, 0, 1, 2, 3, 4, 5, 6, 7);
#pragma unroll
        for (int rt = 0; rt < 2; ++rt) {
            f32x4 o = (f32x4){0.f, 0.f, 0.f, 0.f};
            o = __builtin_amdgcn_mfma_f32_16x16x32_bf16(va, __builtin_bit_cast(bf16x8, p0[rt]), o, 0, 0, 0);
            o = __builtin_amdgcn_mfma_f32_16x16x32_bf16(vb, __builtin_bit_cast(bf16x8, p1[rt]), o, 0, 0, 0);
            u32x2 w; w.x = pk_bf16(o[0], o[1]); w.y = pk_bf16(o[2], o[3]);
            *(u32x2*)(O + (size_t)(rt * 16 + fr) * HD + et * 16 + fq * 4) = w;
        }
    }
}

extern "C" void kernel_launch(void* const* d_in, const int* in_sizes, int n_in, void* d_out, int out_size, void* d_ws, size_t ws_size, hipStream_t stream) {
    const bf16_t* x = (const bf16_t*)d_in[0];
    const bf16_t* mem0 = (const bf16_t*)d_in[1];
    const bf16_t* Wq = (const bf16_t*)d_in[2];
    const bf16_t* Wk = (const bf16_t*)d_in[3];
    const bf16_t* Wv = (const bf16_t*)d_in[4];
    const bf16_t* Wo = (const bf16_t*)d_in[5];
    const bf16_t* w1 = (const bf16_t*)d_in[8];
    const bf16_t* b1 = (const bf16_t*)d_in[9];
    const bf16_t* w2 = (const bf16_t*)d_in[10];
    const bf16_t* b2 = (const bf16_t*)d_in[11];
    char* ws = (char*)d_ws;
    bf16_t* out = (bf16_t*)d_out;
    (void)hipFuncSetAttribute((const void*)k_gemm_kv, hipFuncAttributeMaxDynamicSharedMemorySize, g8::STAGE_BYTES);
    (void)hipFuncSetAttribute((const void*)k_gemm128<1>, hipFuncAttributeMaxDynamicSharedMemorySize, 65536);
    (void)hipFuncSetAttribute((const void*)k_mega, hipFuncAttributeMaxDynamicSharedMemorySize, CH_LDS);
    (void)hipMemsetAsync(ws + WS_SENT0, 0xFF, WS_SENT1 - WS_SENT0, stream);
    (void)hipMemsetAsync(ws + WS_CNT, 0, (size_t)CNT_N * 256, stream);
    k_prep<<<S_LEN / 4, 256, 0, stream>>>(x, mem0, ws);
    k_gemm_kv<<<256, 512, g8::STAGE_BYTES, stream>>>(Wk, Wv, ws);
    k_mega<<<256, 512, CH_LDS, stream>>>(Wq, Wk, Wv, Wo, w1, b1, w2, b2, ws);
    k_xattn<<<NSEG * NH / 4, 256, 0, stream>>>(ws);
    k_gemm128<1><<<128, 256, 65536, stream>>>((const bf16_t*)(ws + WS_O), HD, Wo, x, (const bf16_t*)(ws + WS_B), out);
}
```

```cpp
#include <hip/hip_runtime.h>
#include <hip/hip_bf16.h>

#define LAS __attribute__((address_space(3)))
typedef unsigned short bf16_t;
typedef short bf16x8 __attribute__((ext_vector_type(8)));
typedef short bf16x4 __attribute__((ext_vector_type(4)));
typedef float f32x4 __attribute__((ext_vector_type(4)));
typedef float f32x2 __attribute__((ext_vector_type(2)));
typedef unsigned u32x4 __attribute__((ext_vector_type(4)));
typedef unsigned u32x2 __attribute__((ext_vector_type(2)));

constexpr int S_LEN = 2048, DM = 1024, NH = 32, DH = 128, HD = NH * DH, MM = 8, SEG = 32, NSEG = S_LEN / SEG, FF = 3584;
constexpr int AUX_SC1 = 16;
constexpr unsigned SENT = 0xFFFFFFFFu;

__device__ __forceinline__ float bf2f(unsigned b) { return __builtin_bit_cast(float, b << 16); }
typedef __bf16 bf16x2_t __attribute__((ext_vector_type(2)));
__device__ __forceinline__ unsigned pk_bf16(float lo, float hi) { const f32x2 v = {lo, hi}; return __builtin_bit_cast(unsigned, __builtin_convertvector(v, bf16x2_t)); }
__device__ __forceinline__ float rbf(float x) { return bf2f(pk_bf16(x, x) & 0xffffu); }
__device__ __forceinline__ unsigned desent(unsigned w) { return w == SENT ? 0x7FC07FC0u : w; }
__device__ __forceinline__ bool has_sent(u32x4 v) { return v.x == SENT || v.y == SENT || v.z == SENT || v.w == SENT; }

constexpr size_t WS_XN = 0;
constexpr size_t WS_Y = WS_XN + (size_t)S_LEN * DM * 2;
constexpr size_t WS_Q = WS_Y + (size_t)S_LEN * DM * 2;
constexpr size_t WS_K = WS_Q + (size_t)S_LEN * HD * 2;
constexpr size_t WS_V = WS_K + (size_t)S_LEN * HD * 2;
constexpr size_t WS_H = WS_V + (size_t)S_LEN * HD * 2;
constexpr size_t WS_O = WS_H + (size_t)S_LEN * FF * 2;
constexpr size_t WS_B = WS_O + (size_t)S_LEN * HD * 2;
constexpr size_t WS_SENT0 = WS_B + (size_t)S_LEN * DM * 2;
constexpr size_t MEM_B = (size_t)MM * DM * 2;
constexpr size_t WS_MEM = WS_SENT0;
constexpr size_t QKVM_B = (size_t)NH * 3 * MM * DH * 2;
constexpr size_t WS_QKVM = WS_MEM + (size_t)(NSEG + 1) * MEM_B;
constexpr size_t OM_B = (size_t)MM * HD * 2;
constexpr size_t WS_OM = WS_QKVM + (size_t)NSEG * QKVM_B;
constexpr size_t WS_SENT1 = WS_OM + (size_t)NSEG * OM_B;
constexpr size_t WS_CNT = WS_SENT1;
constexpr int CNT_H = 0;
constexpr int CNT_B = 8;
constexpr int CNT_O = 16;
constexpr int CNT_QKV = 24;
constexpr int CNT_STEP = 32;
constexpr int CNT_KV = 40;
constexpr int CNT_N = 40 + 256;
constexpr size_t WS_TMO = WS_CNT + (size_t)CNT_N * 256;
constexpr size_t WS_END = WS_TMO + 256;

__device__ __forceinline__ float wave_sum(float v) {
#pragma unroll
    for (int o = 32; o > 0; o >>= 1) v += __shfl_xor(v, o);
    return v;
}
__device__ __forceinline__ void ln_row(float (&v)[16]) {
    float s = 0.f;
#pragma unroll
    for (int i = 0; i < 16; ++i) s += v[i];
    const float mean = wave_sum(s) * (1.0f / DM);
    float q = 0.f;
#pragma unroll
    for (int i = 0; i < 16; ++i) { const float d = v[i] - mean; q += d * d; }
    const float var = rbf(wave_sum(q) * (1.0f / DM));
    const float mb = rbf(mean);
    const float den = rbf(sqrtf(rbf(var + 1e-5f)));
#pragma unroll
    for (int i = 0; i < 16; ++i) v[i] = rbf(rbf(v[i] - mb) / den);
}
__global__ void __launch_bounds__(256) k_prep(const bf16_t* __restrict__ x, const bf16_t* __restrict__ mem0, char* __restrict__ ws) {
    const int wave = threadIdx.x >> 6, lane = threadIdx.x & 63, row = blockIdx.x * 4 + wave;
    const u32x4* src = (const u32x4*)(x + (size_t)row * DM);
    u32x4 a = src[lane], b = src[lane + 64];
    float v[16];
    const unsigned w[8] = {a.x, a.y, a.z, a.w, b.x, b.y, b.z, b.w};
#pragma unroll
    for (int i = 0; i < 8; ++i) { v[2 * i] = bf2f(w[i] & 0xffffu); v[2 * i + 1] = bf2f(w[i] >> 16); }
    ln_row(v);
    u32x4 o0, o1;
    o0.x = pk_bf16(v[0], v[1]); o0.y = pk_bf16(v[2], v[3]); o0.z = pk_bf16(v[4], v[5]); o0.w = pk_bf16(v[6], v[7]);
    o1.x = pk_bf16(v[8], v[9]); o1.y = pk_bf16(v[10], v[11]); o1.z = pk_bf16(v[12], v[13]); o1.w = pk_bf16(v[14], v[15]);
    u32x4* dx = (u32x4*)(ws + WS_XN + (size_t)row * DM * 2);
    dx[lane] = o0; dx[lane + 64] = o1;
    ln_row(v);
    o0.x = pk_bf16(v[0], v[1]); o0.y = pk_bf16(v[2], v[3]); o0.z = pk_bf16(v[4], v[5]); o0.w = pk_bf16(v[6], v[7]);
    o1.x = pk_bf16(v[8], v[9]); o1.y = pk_bf16(v[10], v[11]); o1.z = pk_bf16(v[12], v[13]); o1.w = pk_bf16(v[14], v[15]);
    u32x4* dy = (u32x4*)(ws + WS_Y + (size_t)row * DM * 2);
    dy[lane] = o0; dy[lane + 64] = o1;
    {
        const size_t n16 = (WS_SENT1 - WS_SENT0 - MEM_B) / 16;
        u32x4* dst = (u32x4*)(ws + WS_SENT0 + MEM_B);
        const u32x4 f = (u32x4){SENT, SENT, SENT, SENT};
        for (size_t i = (size_t)blockIdx.x * 256 + threadIdx.x; i < n16; i += (size_t)gridDim.x * 256) dst[i] = f;
        if (blockIdx.x >= 8 && blockIdx.x < 8 + (CNT_N * 256 / 16 + 255) / 256) { const int i = (blockIdx.x - 8) * 256 + threadIdx.x; if (i < CNT_N * 256 / 16) ((u32x4*)(ws + WS_CNT))[i] = (u32x4){0u, 0u, 0u, 0u}; }
    }
    if (blockIdx.x < 4) {
        const int c = blockIdx.x * 256 + threadIdx.x, r = c & 7, kg = c >> 3;
        u32x4 m = *(const u32x4*)(mem0 + (size_t)r * DM + kg * 8);
        m.x = desent(m.x); m.y = desent(m.y); m.z = desent(m.z); m.w = desent(m.w);
        *(u32x4*)(ws + WS_MEM + (size_t)c * 16) = m;
    }
}

namespace g8 {
constexpr int BM = 256, BK = 64, HALF = 128, HTB = HALF * BK * 2, STAGE_BYTES = 8 * HTB;
__host__ __device__ __forceinline__ int lds_byte(int r, int c) { const int st = (r >> 4) * 2 + (c >> 5), rr = r & 15, cc = c & 31, ob = rr * 64 + cc * 2; return st * 1024 + (ob ^ (((ob >> 9) & 1) << 5)); }
__host__ __device__ __forceinline__ void stage_rc(int b, int& R, int& C) { const int st = b / 1024, sb = b % 1024, swz = sb ^ (((sb >> 9) & 1) << 5); R = (st >> 1) * 16 + swz / 64; C = (st & 1) * 32 + (swz % 64) / 2; }
__host__ __device__ __forceinline__ int bsw(int row) { return (row & 3) | (((row >> 3) & 1) << 2); }
struct Unit { const bf16_t* A; const bf16_t* B; int pm, pn; };

template <class Epi, class Sched>
__device__ __forceinline__ void gemm_phase(LAS unsigned char* lds, const int K, const int lda, const int ldb, const Sched& S, const Epi& E) {
    int tid = threadIdx.x; asm volatile("" : "+v"(tid));
    const int wid = __builtin_amdgcn_readfirstlane(tid >> 6), lane = tid & 63, wr = wid >> 2, wc = wid & 3, fr = lane & 15, fq = lane >> 4;
    const int nt = K / BK;
    unsigned voffA[2], voffB[2];
#pragma unroll
    for (int i = 0; i < 2; ++i) { int R, C; stage_rc(tid * 16 + i * 8192, R, C); voffA[i] = (unsigned)(R * lda + C) * 2u;
        const int b = tid * 16 + i * 8192, row = b >> 8, ch = (b & 255) >> 4, seg = (ch >> 1) ^ bsw(row);
        voffB[i] = (unsigned)(row * ldb + seg * 16 + (ch & 1) * 8) * 2u; }
    const size_t kstepA = (size_t)(BK * 2), kstepB = (size_t)BK * ldb * 2;
    const size_t hstepA = (size_t)HALF * lda * 2, hstepB = (size_t)HALF * 2;
    const unsigned ldsw = (unsigned)wid * 1024u;
    const int aoff = lds_byte(wr * 64 + fr, fq * 8);
    const int q4 = (lane & 15) >> 2, p4 = lane & 3, sB = q4 | ((fq & 1) << 2);
    const int boff0 = (8 * fq + q4) * 256 + (((wc * 2 + 0) ^ sB) * 32) + 8 * p4;
    const int boff1 = (8 * fq + q4) * 256 + (((wc * 2 + 1) ^ sB) * 32) + 8 * p4;
#define G8_SA(b, h) (((b) * 2 + (h)) * HTB)
#define G8_SB(b, h) ((4 + (b) * 2 + (h)) * HTB)
#define G8_STAGE(bufoff, gbase, voff) do { _Pragma("unroll") for (int _i = 0; _i < 2; ++_i) \
        __builtin_amdgcn_global_load_lds((const unsigned*)((const char*)(gbase) + (voff)[_i]), (LAS unsigned*)(lds + (bufoff) + ldsw + _i * 8192), 16, 0, 0); } while (0)
#define G8_LDA(dst, b, h) do { _Pragma("unroll") for (int m = 0; m < 4; ++m) _Pragma("unroll") for (int k = 0; k < 2; ++k) dst[m][k] = *(const LAS bf16x8*)(lds + G8_SA(b, h) + aoff + m * 2048 + k * 1024); } while (0)
#define G8_TR(off) __builtin_amdgcn_ds_read_tr16_b64_v4i16((LAS bf16x4*)(lds + (off)))
#define G8_LDB(dst, b, h) do { _Pragma("unroll") for (int k = 0; k < 2; ++k) { \
        bf16x4 a0 = G8_TR(G8_SB(b, h) + boff0 + k * 8192), a1 = G8_TR(G8_SB(b, h) + boff0 + k * 8192 + 1024); \
        bf16x4 c0 = G8_TR(G8_SB(b, h) + boff1 + k * 8192), c1 = G8_TR(G8_SB(b, h) + boff1 + k * 8192 + 1024); \
        dst[0][k] = __builtin_shufflevector(a0, a1, 0, 1, 2, 3, 4, 5, 6, 7); dst[1][k] = __builtin_shufflevector(c0, c1, 0, 1, 2, 3, 4, 5, 6, 7); } } while (0)
#define G8_MMA(ai, bj, At, Bt) do { __builtin_amdgcn_s_setprio(1); _Pragma("unroll") for (int m = 0; m < 4; ++m) _Pragma("unroll") for (int n = 0; n < 2; ++n) _Pragma("unroll") for (int k = 0; k < 2; ++k) \
        acc[ai][bj][m][n] = __builtin_amdgcn_mfma_f32_16x16x32_bf16(Bt[n][k], At[m][k], acc[ai][bj][m][n], 0, 0, 0); __builtin_amdgcn_s_setprio(0); } while (0)
#define G8_WAIT_V(n) asm volatile("s_waitcnt vmcnt(" #n ")" ::: "memory")
#define G8_WAIT_L(n) asm volatile("s_waitcnt lgkmcnt(" #n ")" ::: "memory")
#define G8_BAR __builtin_amdgcn_s_barrier()
#define G8_SCHED __builtin_amdgcn_sched_barrier(0)
    Unit cur, nxt; int ui = 0;
    if (!S.next(0, cur)) return;
    f32x4 acc[2][2][4][2];
#pragma unroll
    for (int a = 0; a < 2; ++a)
#pragma unroll
        for (int b = 0; b < 2; ++b)
#pragma unroll
            for (int m = 0; m < 4; ++m)
#pragma unroll
                for (int n = 0; n < 2; ++n) acc[a][b][m][n] = (f32x4){0.f, 0.f, 0.f, 0.f};
    bf16x8 At[4][2], B0[2][2], B1[2][2];
    const char* cA = (const char*)cur.A; const char* cB = (const char*)cur.B;
    G8_STAGE(G8_SB(0, 0), cB, voffB); G8_STAGE(G8_SA(0, 0), cA, voffA); G8_STAGE(G8_SB(0, 1), cB + hstepB, voffB); G8_STAGE(G8_SA(0, 1), cA + hstepA, voffA);
    if (wr == 1) G8_BAR;
    G8_WAIT_V(4); G8_BAR;
    G8_STAGE(G8_SB(1, 0), cB + kstepB, voffB); G8_STAGE(G8_SA(1, 0), cA + kstepA, voffA); G8_STAGE(G8_SB(1, 1), cB + hstepB + kstepB, voffB);
    G8_WAIT_V(6); G8_BAR;
    for (;;) {
        const bool has_next = S.next(ui + 1, nxt);
        const char* nA = has_next ? (const char*)nxt.A : cA; const char* nB = has_next ? (const char*)nxt.B : cB;
        for (int t = 0; t < nt; t += 2) {
            const bool last = (t == nt - 2);
            const char* a1 = cA + (size_t)(t + 1) * kstepA;
            const char* a2 = last ? nA : cA + (size_t)(t + 2) * kstepA; const char* b2 = last ? nB : cB + (size_t)(t + 2) * kstepB;
            const char* a3 = a2 + kstepA; const char* b3 = b2 + kstepB;
            G8_LDB(B0, 0, 0); G8_SCHED; G8_LDA(At, 0, 0); G8_STAGE(G8_SA(1, 1), a1 + hstepA, voffA);
            G8_WAIT_L(8); G8_BAR; G8_WAIT_L(0); G8_MMA(0, 0, At, B0); G8_BAR; G8_SCHED;
            G8_LDB(B1, 0, 1); G8_STAGE(G8_SB(0, 0), b2, voffB);
            G8_BAR; G8_WAIT_L(0); G8_MMA(0, 1, At, B1); G8_BAR;
            G8_LDA(At, 0, 1); G8_STAGE(G8_SA(0, 0), a2, voffA);
            G8_BAR; G8_WAIT_L(0); G8_MMA(1, 0, At, B0); G8_BAR; G8_SCHED;
            G8_STAGE(G8_SB(0, 1), b2 + hstepB, voffB);
            G8_WAIT_V(6); G8_BAR; G8_MMA(1, 1, At, B1); G8_BAR;
            G8_LDB(B0, 1, 0); G8_SCHED; G8_LDA(At, 1, 0); G8_STAGE(G8_SA(0, 1), a2 + hstepA, voffA);
            G8_WAIT_L(8); G8_BAR; G8_WAIT_L(0); G8_MMA(0, 0, At, B0); G8_BAR; G8_SCHED;
            G8_LDB(B1, 1, 1); G8_STAGE(G8_SB(1, 0), b3, voffB);
            G8_BAR; G8_WAIT_L(0); G8_MMA(0, 1, At, B1); G8_BAR;
            G8_LDA(At, 1, 1); G8_STAGE(G8_SA(1, 0), a3, voffA);
            G8_BAR; G8_WAIT_L(0); G8_MMA(1, 0, At, B0); G8_BAR; G8_SCHED;
            G8_STAGE(G8_SB(1, 1), b3 + hstepB, voffB);
            G8_WAIT_V(6); G8_BAR; G8_MMA(1, 1, At, B1); G8_BAR;
        }
        E(acc, cur, wr, wc, fr, fq);
        if (!has_next) break;
#pragma unroll
        for (int a = 0; a < 2; ++a)
#pragma unroll
            for (int b = 0; b < 2; ++b)
#pragma unroll
                for (int m = 0; m < 4; ++m)
#pragma unroll
                    for (int n = 0; n < 2; ++n) acc[a][b][m][n] = (f32x4){0.f, 0.f, 0.f, 0.f};
        cur = nxt; cA = nA; cB = nB; ++ui;
    }
    G8_WAIT_V(0);
    if (wr == 0) G8_BAR;
    G8_BAR;
#undef G8_SA
#undef G8_SB
#undef G8_STAGE
#undef G8_LDA
#undef G8_LDB
#undef G8_TR
#undef G8_MMA
#undef G8_WAIT_V
#undef G8_WAIT_L
#undef G8_BAR
#undef G8_SCHED
}
}

__device__ __forceinline__ float gelu_tanh(float x) {
    const float u = 0.7978845608028654f * (x + 0.044715f * x * x * x);
    const float th = 1.0f - 2.0f * __builtin_amdgcn_rcpf(__expf(2.0f * u) + 1.0f);
    return 0.5f * x * (1.0f + th);
}

__device__ __forceinline__ void wait_counter(const char* ws, int idx, unsigned need) {
    if (threadIdx.x < 64) {
        const unsigned* p = (const unsigned*)(ws + WS_CNT + (size_t)idx * 256);
        for (unsigned spins = 0; (unsigned)__builtin_amdgcn_readfirstlane(__hip_atomic_load(p, __ATOMIC_RELAXED, __HIP_MEMORY_SCOPE_AGENT)) < need;) {
            __builtin_amdgcn_s_sleep(8);
            if (++spins > 400000u) break;
        }
        __builtin_amdgcn_fence(__ATOMIC_ACQUIRE, "agent");
        asm volatile("s_waitcnt vmcnt(0)" ::: "memory");
    }
    asm volatile("" ::: "memory"); __builtin_amdgcn_s_barrier(); asm volatile("" ::: "memory");
}
__device__ __forceinline__ void signal_counter(char* ws, int idx) {
    asm volatile("s_waitcnt vmcnt(0)" ::: "memory");
    if ((threadIdx.x & 63) == 0) __hip_atomic_fetch_add((unsigned*)(ws + WS_CNT + (size_t)idx * 256), 1u, __ATOMIC_RELAXED, __HIP_MEMORY_SCOPE_AGENT);
}

__device__ __forceinline__ unsigned poll_ge(const char* ws, int idx, unsigned need) {
    const unsigned* p = (const unsigned*)(ws + WS_CNT + (size_t)idx * 256);
    unsigned v = 0;
    for (unsigned spins = 0; (v = (unsigned)__builtin_amdgcn_readfirstlane(__hip_atomic_load(p, __ATOMIC_RELAXED, __HIP_MEMORY_SCOPE_AGENT))) < need;) {
        __builtin_amdgcn_s_sleep(8);
        if (++spins > 400000u) break;
    }
    asm volatile("" ::: "memory");
    return v;
}

constexpr unsigned SPIN_CAP = 200000u;
constexpr int KS_PITCH = 272;
constexpr int CH_LDS = 160 * 1024;

__device__ __forceinline__ float xmax16_32(float v) {
    auto a = __builtin_amdgcn_permlane16_swap(__builtin_bit_cast(unsigned, v), __builtin_bit_cast(unsigned, v), false, false);
    v = fmaxf(__builtin_bit_cast(float, (unsigned)a[0]), __builtin_bit_cast(float, (unsigned)a[1]));
    auto b = __builtin_amdgcn_permlane32_swap(__builtin_bit_cast(unsigned, v), __builtin_bit_cast(unsigned, v), false, false);
    return fmaxf(__builtin_bit_cast(float, (unsigned)b[0]), __builtin_bit_cast(float, (unsigned)b[1]));
}
__device__ __forceinline__ float xsum16_32(float v) {
    auto a = __builtin_amdgcn_permlane16_swap(__builtin_bit_cast(unsigned, v), __builtin_bit_cast(unsigned, v), false, false);
    v = __builtin_bit_cast(float, (unsigned)a[0]) + __builtin_bit_cast(float, (unsigned)a[1]);
    auto b = __builtin_amdgcn_permlane32_swap(__builtin_bit_cast(unsigned, v), __builtin_bit_cast(unsigned, v), false, false);
    return __builtin_bit_cast(float, (unsigned)b[0]) + __builtin_bit_cast(float, (unsigned)b[1]);
}

#define STAMP(i) do {} while (0)
#define CH_DELAY1 12
#define CH_DELAY2 8
#define CH_DELAY3 12

template <int CPH>
__device__ __forceinline__ void chain_body(LAS unsigned char* lds, const int b, const bf16_t* __restrict__ Wq, const bf16_t* __restrict__ Wk, const bf16_t* __restrict__ Wv,
                                           const bf16_t* __restrict__ Wo, char* ws) {
    constexpr int ES = 128 / CPH, NE = ES / 16, NT = 3 * NE, RG = CPH / 2, RPG = 8 / RG;
    constexpr int VP = ES * 2;
    constexpr int L_RED = 0;
    constexpr int L_RED2 = L_RED + 8 * NT * 512;
    constexpr int L_KS = L_RED2 + 8 * RPG * 64;
    constexpr int L_VS = L_KS + 2 * 32 * KS_PITCH;
    constexpr int L_OS = L_VS + 2 * 48 * VP;
    constexpr bool WOL = (CPH == 4);
    constexpr int L_WO = L_OS + 8 * 1024 * RPG;
    static_assert(L_WO + (WOL ? 65536 : 0) <= 156 * 1024, "LDS");
    const int tid = threadIdx.x, wave = __builtin_amdgcn_readfirstlane(tid >> 6), lane = tid & 63, fr = lane & 15, fq = lane >> 4;
    const int h = b / CPH, j = b % CPH, g = b % RG, c = b / RG;
    const __amdgpu_buffer_rsrc_t rs = __builtin_amdgcn_make_buffer_rsrc(ws + WS_SENT0, 0, (int)(WS_SENT1 - WS_SENT0), 0x00020000);
    constexpr int O_MEM = (int)(WS_MEM - WS_SENT0), O_QKVM = (int)(WS_QKVM - WS_SENT0), O_OM = (int)(WS_OM - WS_SENT0);
    const __amdgpu_buffer_rsrc_t rkv = __builtin_amdgcn_make_buffer_rsrc(ws + WS_K, 0, (int)(WS_H - WS_K), 0x00020000);
    constexpr int O_V = (int)(WS_V - WS_K);
    bool dead = false;

    bf16x8 wq[NT][4], wo[16];
#pragma unroll
    for (int t = 0; t < NT; ++t) {
        const bf16_t* W = (t / NE == 0 ? Wq : (t / NE == 1 ? Wk : Wv)) + h * DH + j * ES + (t % NE) * 16 + fr;
#pragma unroll
        for (int i = 0; i < 4; ++i)
#pragma unroll
            for (int jj = 0; jj < 8; ++jj) wq[t][i][jj] = (short)W[(size_t)(wave * 128 + i * 32 + fq * 8 + jj) * HD];
        asm volatile("" : "+v"(wq[t][0]), "+v"(wq[t][1]), "+v"(wq[t][2]), "+v"(wq[t][3]) :: "memory");
    }
#pragma unroll
    for (int i = 0; i < 16; ++i) {
#pragma unroll
        for (int jj = 0; jj < 8; ++jj) wo[i][jj] = (short)Wo[(size_t)(wave * 512 + i * 32 + fq * 8 + jj) * DM + c * 16 + fr];
        if ((i & 3) == 3) asm volatile("" : "+v"(wo[i - 3]), "+v"(wo[i - 2]), "+v"(wo[i - 1]), "+v"(wo[i]) :: "memory");
        if (WOL && i >= 8) *(LAS bf16x8*)(lds + L_WO + (wave * 8 + i - 8) * 1024 + lane * 16) = wo[i];
    }

    if (tid < 2 * 8 * VP / 16) { const int bf = tid / (8 * VP / 16), r = tid % (8 * VP / 16); *(LAS u32x4*)(lds + L_VS + bf * 48 * VP + 40 * VP + r * 16) = (u32x4){0u, 0u, 0u, 0u}; }
    {
        poll_ge(ws, CNT_KV + (h >> 1), 32u); poll_ge(ws, CNT_KV + 16 + (h >> 1), 32u);
        const int r = tid >> 4, ch = tid & 15;
        *(LAS u32x4*)(lds + L_KS + r * KS_PITCH + ch * 16) = __builtin_amdgcn_raw_buffer_load_b128(rkv, (r * HD + h * DH + ch * 8) * 2, 0, AUX_SC1);
        if (tid < 32 * ES / 8) { const int r2 = tid / (ES / 8), c2 = tid % (ES / 8);
            *(LAS u32x4*)(lds + L_VS + (8 + r2) * VP + c2 * 16) = __builtin_amdgcn_raw_buffer_load_b128(rkv, O_V + (r2 * HD + h * DH + j * ES + c2 * 8) * 2, 0, AUX_SC1); }
    }
    __syncthreads();

    for (int s = 0; s < NSEG; ++s) {
        const int kb = s & 1;
        STAMP(0);
        {
            if (s > 0) __builtin_amdgcn_s_sleep(CH_DELAY1);
            u32x4 a[4];
            const int base = O_MEM + s * (int)MEM_B + wave * 2048 + fq * 128 + (lane & 7) * 16;
            for (unsigned spins = 0;;) {
                asm volatile("" ::: "memory");
#pragma unroll
                for (int i = 0; i < 4; ++i) a[i] = __builtin_amdgcn_raw_buffer_load_b128(rs, base + i * 512, 0, AUX_SC1);
                bool bad = false;
#pragma unroll
                for (int i = 0; i < 4; ++i) bad |= has_sent(a[i]);
                if (!__any(bad) || dead) break;
                if (++spins > SPIN_CAP) { dead = true; if (lane == 0) *(volatile unsigned*)(ws + WS_TMO) = 1u; break; }
            }
            STAMP(1);
            if (b == 0 && tid == 448 && (s & 7) == 0) __hip_atomic_store((unsigned*)(ws + WS_CNT + (size_t)CNT_STEP * 256), (unsigned)s, __ATOMIC_RELAXED, __HIP_MEMORY_SCOPE_AGENT);
            f32x4 acc[NT];
#pragma unroll
            for (int t = 0; t < NT; ++t) acc[t] = (f32x4){0.f, 0.f, 0.f, 0.f};
#pragma unroll
            for (int i = 0; i < 4; ++i)
#pragma unroll
                for (int t = 0; t < NT; ++t) acc[t] = __builtin_amdgcn_mfma_f32_16x16x32_bf16(wq[t][i], __builtin_bit_cast(bf16x8, a[i]), acc[t], 0, 0, 0);
            if (fr < 8) {
#pragma unroll
                for (int t = 0; t < NT; ++t) *(LAS f32x4*)(lds + L_RED + ((wave * NT + t) * 128 + fr * 16 + fq * 4) * 4) = acc[t];
            }
        }
        __syncthreads();
        if (tid < NT * 64) {
            const int t = tid >> 6, m = (tid & 63) >> 3, np = (tid & 7) * 2, mat = t / NE, col = (t % NE) * 16 + np;
            f32x2 sum = (f32x2){0.f, 0.f};
#pragma unroll
            for (int w = 0; w < 8; ++w) sum += *(const LAS f32x2*)(lds + L_RED + ((w * NT + t) * 128 + m * 16 + np) * 4);
            const unsigned pk = desent(pk_bf16(sum.x, sum.y));
            __builtin_amdgcn_raw_buffer_store_b32(pk, rs, O_QKVM + s * (int)QKVM_B + ((h * 3 + mat) * 8 + m) * 256 + (j * ES + col) * 2, 0, AUX_SC1);
            if (mat == 2) *(LAS unsigned*)(lds + L_VS + kb * 48 * VP + m * VP + col * 2) = pk;
        }
        STAMP(2);
        if (s == NSEG - 1) break;
        __syncthreads();
        if (wave == 0) {
            bf16x8 kx[3][4];
#pragma unroll
            for (int t = 0; t < 3; ++t) {
                int r = t * 16 + fr - 8; r = r < 0 ? 0 : (r > 31 ? 31 : r);
#pragma unroll
                for (int i = 0; i < 4; ++i) kx[t][i] = *(const LAS bf16x8*)(lds + L_KS + kb * 32 * KS_PITCH + r * KS_PITCH + i * 64 + fq * 16);
            }
            __builtin_amdgcn_s_sleep(CH_DELAY2);
            u32x4 qf[4], km[4];
            const int base = O_QKVM + s * (int)QKVM_B + h * 3 * 2048 + (lane & 7) * 256 + fq * 16;
            for (unsigned spins = 0;;) {
                asm volatile("" ::: "memory");
#pragma unroll
                for (int i = 0; i < 4; ++i) { qf[i] = __builtin_amdgcn_raw_buffer_load_b128(rs, base + i * 64, 0, AUX_SC1); km[i] = __builtin_amdgcn_raw_buffer_load_b128(rs, base + 2048 + i * 64, 0, AUX_SC1); }
                bool bad = false;
#pragma unroll
                for (int i = 0; i < 4; ++i) bad |= has_sent(qf[i]) || has_sent(km[i]);
                if (!__any(bad) || dead) break;
                if (++spins > SPIN_CAP) { dead = true; if (lane == 0) *(volatile unsigned*)(ws + WS_TMO) = 2u; break; }
            }
            STAMP(3);
            f32x4 sc[3];
#pragma unroll
            for (int t = 0; t < 3; ++t) sc[t] = (f32x4){0.f, 0.f, 0.f, 0.f};
#pragma unroll
            for (int i = 0; i < 4; ++i) {
                const bf16x8 k0 = fr < 8 ? __builtin_bit_cast(bf16x8, km[i]) : kx[0][i];
                sc[1] = __builtin_amdgcn_mfma_f32_16x16x32_bf16(kx[1][i], __builtin_bit_cast(bf16x8, qf[i]), sc[1], 0, 0, 0);
                sc[2] = __builtin_amdgcn_mfma_f32_16x16x32_bf16(kx[2][i], __builtin_bit_cast(bf16x8, qf[i]), sc[2], 0, 0, 0);
                sc[0] = __builtin_amdgcn_mfma_f32_16x16x32_bf16(k0, __builtin_bit_cast(bf16x8, qf[i]), sc[0], 0, 0, 0);
            }
            asm volatile("" ::: "memory");
            bf16x8 vf[NE][2];
            const bf16x4 zz = (bf16x4){0, 0, 0, 0};
#pragma unroll
            for (int et = 0; et < NE; ++et) {
                const int voff = L_VS + kb * 48 * VP + (4 * fq + (fr >> 2)) * VP + et * 32 + (lane & 3) * 8;
                const bf16x4 v0 = __builtin_amdgcn_ds_read_tr16_b64_v4i16((LAS bf16x4*)(lds + voff));
                const bf16x4 v1 = __builtin_amdgcn_ds_read_tr16_b64_v4i16((LAS bf16x4*)(lds + voff + 16 * VP));
                const bf16x4 v2 = __builtin_amdgcn_ds_read_tr16_b64_v4i16((LAS bf16x4*)(lds + voff + 32 * VP));
                vf[et][0] = __builtin_shufflevector(v0, v1, 0, 1, 2, 3, 4, 5, 6, 7); vf[et][1] = __builtin_shufflevector(v2, zz, 0, 1, 2, 3, 4, 5, 6, 7);
            }
            const float scale = 0.08838834764831845f;
            float v[12]; float mx = -INFINITY;
#pragma unroll
            for (int t = 0; t < 3; ++t)
#pragma unroll
                for (int r = 0; r < 4; ++r) { float z = rbf(rbf(sc[t][r]) * scale); if (t == 2 && fq >= 2) z = -INFINITY; v[t * 4 + r] = z; mx = fmaxf(mx, z); }
            mx = xmax16_32(mx);
            float sum = 0.f;
#pragma unroll
            for (int i = 0; i < 12; ++i) { v[i] = __expf(v[i] - mx); sum += v[i]; }
            sum = xsum16_32(sum);
            const float inv = 1.0f / sum;
            u32x4 p0, p1;
            p0.x = pk_bf16(v[0] * inv, v[1] * inv); p0.y = pk_bf16(v[2] * inv, v[3] * inv); p0.z = pk_bf16(v[4] * inv, v[5] * inv); p0.w = pk_bf16(v[6] * inv, v[7] * inv);
            p1.x = pk_bf16(v[8] * inv, v[9] * inv); p1.y = pk_bf16(v[10] * inv, v[11] * inv); p1.z = 0u; p1.w = 0u;
#pragma unroll
            for (int et = 0; et < NE; ++et) {
                f32x4 o = (f32x4){0.f, 0.f, 0.f, 0.f};
                o = __builtin_amdgcn_mfma_f32_16x16x32_bf16(vf[et][0], __builtin_bit_cast(bf16x8, p0), o, 0, 0, 0);
                o = __builtin_amdgcn_mfma_f32_16x16x32_bf16(vf[et][1], __builtin_bit_cast(bf16x8, p1), o, 0, 0, 0);
                if (fr < 8) {
                    u32x2 w; w.x = desent(pk_bf16(o[0], o[1])); w.y = desent(pk_bf16(o[2], o[3]));
                    const int k = h * DH + j * ES + et * 16 + 4 * fq;
                    const int off = O_OM + s * (int)OM_B + (fr / RPG) * (8192 * RPG) + (k >> 5) * (64 * RPG) + ((k >> 3) & 3) * (16 * RPG) + (fr % RPG) * 16 + (k & 7) * 2;
                    __builtin_amdgcn_raw_buffer_store_b64(w, rs, off, 0, AUX_SC1);
                }
            }
            STAMP(4);
        } else {
            const int t2 = tid - 64; const int nb = kb ^ 1; const int row0 = (s + 1) * SEG;
            if (((s + 1) & 7) == 0) { const int blk = (s + 1) >> 3; poll_ge(ws, CNT_KV + blk * 32 + (h >> 1), 8u); poll_ge(ws, CNT_KV + blk * 32 + 16 + (h >> 1), 8u); }
            for (int i = t2; i < 512 + 32 * ES / 8; i += 448) {
                if (i < 512) { const int r = i >> 4, ch = i & 15;
                    *(LAS u32x4*)(lds + L_KS + nb * 32 * KS_PITCH + r * KS_PITCH + ch * 16) = __builtin_amdgcn_raw_buffer_load_b128(rkv, ((row0 + r) * HD + h * DH + ch * 8) * 2, 0, AUX_SC1); }
                else { const int r2 = (i - 512) / (ES / 8), c2 = (i - 512) % (ES / 8);
                    *(LAS u32x4*)(lds + L_VS + nb * 48 * VP + (8 + r2) * VP + c2 * 16) = __builtin_amdgcn_raw_buffer_load_b128(rkv, O_V + ((row0 + r2) * HD + h * DH + j * ES + c2 * 8) * 2, 0, AUX_SC1); }
            }
        }
        __syncthreads();
        {
            if (WOL) {
#pragma unroll
                for (int i = 8; i < 16; ++i) wo[i] = *(const LAS bf16x8*)(lds + L_WO + (wave * 8 + i - 8) * 1024 + lane * 16);
            }
            __builtin_amdgcn_s_sleep(CH_DELAY3);
            u32x4 a[RPG];
            const int base = O_OM + s * (int)OM_B + g * (8192 * RPG) + wave * (1024 * RPG) + lane * 16;
            for (unsigned spins = 0;;) {
                asm volatile("" ::: "memory");
#pragma unroll
                for (int i = 0; i < RPG; ++i) a[i] = __builtin_amdgcn_raw_buffer_load_b128(rs, base + i * 1024, 0, AUX_SC1);
                bool bad = false;
#pragma unroll
                for (int i = 0; i < RPG; ++i) bad |= has_sent(a[i]);
                if (!__any(bad) || dead) break;
                if (++spins > SPIN_CAP) { dead = true; if (lane == 0) *(volatile unsigned*)(ws + WS_TMO) = 3u; break; }
            }
            STAMP(5);
#pragma unroll
            for (int i = 0; i < RPG; ++i) *(LAS u32x4*)(lds + L_OS + wave * (1024 * RPG) + i * 1024 + lane * 16) = a[i];
            asm volatile("s_waitcnt lgkmcnt(0)" ::: "memory");
            f32x4 acc[4];
#pragma unroll
            for (int q = 0; q < 4; ++q) acc[q] = (f32x4){0.f, 0.f, 0.f, 0.f};
#pragma unroll
            for (int i = 0; i < 16; ++i) {
                const bf16x8 f = *(const LAS bf16x8*)(lds + L_OS + wave * (1024 * RPG) + i * (64 * RPG) + fq * (16 * RPG) + (lane % RPG) * 16);
                acc[i & 3] = __builtin_amdgcn_mfma_f32_16x16x32_bf16(wo[i], f, acc[i & 3], 0, 0, 0);
            }
            const f32x4 r = (acc[0] + acc[1]) + (acc[2] + acc[3]);
            if (fr < RPG) *(LAS f32x4*)(lds + L_RED2 + ((wave * RPG + fr) * 16 + fq * 4) * 4) = r;
        }
        __syncthreads();
        if (tid < RPG * 8) {
            const int m = tid >> 3, np = (tid & 7) * 2;
            f32x2 sum = (f32x2){0.f, 0.f};
#pragma unroll
            for (int w = 0; w < 8; ++w) sum += *(const LAS f32x2*)(lds + L_RED2 + ((w * RPG + m) * 16 + np) * 4);
            const unsigned pk = desent(pk_bf16(sum.x, sum.y));
            const int off = O_MEM + (s + 1) * (int)MEM_B + (c >> 1) * 512 + ((c & 1) * 2 + (np >> 3)) * 128 + (g * RPG + m) * 16 + (np & 7) * 2;
            __builtin_amdgcn_raw_buffer_store_b32(pk, rs, off, 0, AUX_SC1);
        }
        STAMP(6);
    }
}

template <class Epi>
__device__ __forceinline__ void gemm128_w8(LAS unsigned char* lds, const bf16_t* A, const int lda, const bf16_t* B, const int ldb, const int K, const Epi& E) {
    int tid = threadIdx.x; asm volatile("" : "+v"(tid));
    const int wid = __builtin_amdgcn_readfirstlane(tid >> 6), lane = tid & 63, wr = wid >> 2, wc = wid & 3, fr = lane & 15, fq = lane >> 4;
    unsigned voffA[2], voffB[2];
#pragma unroll
    for (int i = 0; i < 2; ++i) { int R, C; g8::stage_rc(tid * 16 + i * 8192, R, C); voffA[i] = (unsigned)(R * lda + C) * 2u;
        const int bb = tid * 16 + i * 8192, row = bb >> 8, ch = (bb & 255) >> 4, seg = (ch >> 1) ^ g8::bsw(row);
        voffB[i] = (unsigned)(row * ldb + seg * 16 + (ch & 1) * 8) * 2u; }
    const char* cA = (const char*)A; const char* cB = (const char*)B;
    const unsigned ldsw = (unsigned)wid * 1024u;
    const int aoff = g8::lds_byte(wr * 64 + fr, fq * 8);
    const int q4 = (lane & 15) >> 2, p4 = lane & 3, sB = q4 | ((fq & 1) << 2);
    const int boff0 = (8 * fq + q4) * 256 + (((wc * 2 + 0) ^ sB) * 32) + 8 * p4;
    const int boff1 = (8 * fq + q4) * 256 + (((wc * 2 + 1) ^ sB) * 32) + 8 * p4;
    f32x4 acc[4][2];
#pragma unroll
    for (int m = 0; m < 4; ++m) { acc[m][0] = (f32x4){0.f, 0.f, 0.f, 0.f}; acc[m][1] = (f32x4){0.f, 0.f, 0.f, 0.f}; }
    const int nt = K / 64;
#define STG(buf, t) do { _Pragma("unroll") for (int _i = 0; _i < 2; ++_i) { \
        __builtin_amdgcn_global_load_lds((const unsigned*)(cA + (size_t)(t) * 128 + voffA[_i]), (LAS unsigned*)(lds + (buf) * 32768 + ldsw + _i * 8192), 16, 0, 0); \
        __builtin_amdgcn_global_load_lds((const unsigned*)(cB + (size_t)(t) * 64 * ldb * 2 + voffB[_i]), (LAS unsigned*)(lds + (buf) * 32768 + 16384 + ldsw + _i * 8192), 16, 0, 0); } } while (0)
    STG(0, 0); STG(1, 1);
    int cur = 0;
    for (int t = 0; t < nt; ++t) {
        if (t + 1 < nt) asm volatile("s_waitcnt vmcnt(4)" ::: "memory"); else asm volatile("s_waitcnt vmcnt(0)" ::: "memory");
        __builtin_amdgcn_s_barrier();
        asm volatile("" ::: "memory");
        int nx = cur + 2; if (nx >= 3) nx -= 3;
        if (t + 2 < nt) STG(nx, t + 2);
        bf16x8 At[4][2], Bt[2][2];
        LAS unsigned char* sa = lds + cur * 32768; LAS unsigned char* sb = sa + 16384;
#pragma unroll
        for (int k = 0; k < 2; ++k) {
            bf16x4 a0 = __builtin_amdgcn_ds_read_tr16_b64_v4i16((LAS bf16x4*)(sb + boff0 + k * 8192)), a1 = __builtin_amdgcn_ds_read_tr16_b64_v4i16((LAS bf16x4*)(sb + boff0 + k * 8192 + 1024));
            bf16x4 c0 = __builtin_amdgcn_ds_read_tr16_b64_v4i16((LAS bf16x4*)(sb + boff1 + k * 8192)), c1 = __builtin_amdgcn_ds_read_tr16_b64_v4i16((LAS bf16x4*)(sb + boff1 + k * 8192 + 1024));
            Bt[0][k] = __builtin_shufflevector(a0, a1, 0, 1, 2, 3, 4, 5, 6, 7); Bt[1][k] = __builtin_shufflevector(c0, c1, 0, 1, 2, 3, 4, 5, 6, 7);
#pragma unroll
            for (int m = 0; m < 4; ++m) At[m][k] = *(const LAS bf16x8*)(sa + aoff + m * 2048 + k * 1024);
        }
        __builtin_amdgcn_s_setprio(1);
#pragma unroll
        for (int k = 0; k < 2; ++k)
#pragma unroll
            for (int m = 0; m < 4; ++m)
#pragma unroll
                for (int n = 0; n < 2; ++n) acc[m][n] = __builtin_amdgcn_mfma_f32_16x16x32_bf16(Bt[n][k], At[m][k], acc[m][n], 0, 0, 0);
        __builtin_amdgcn_s_setprio(0);
        asm volatile("s_waitcnt lgkmcnt(0)" ::: "memory");
        cur = cur + 1 == 3 ? 0 : cur + 1;
    }
#undef STG
    __builtin_amdgcn_s_barrier();
    E(acc, wr, wc, fr, fq);
}

constexpr int XV_PITCH = 272;
template <bool INK>
__device__ __forceinline__ void xattn_item(LAS unsigned char* vs, char* ws, const int s, const int h) {
    const int lane = threadIdx.x & 63, fr = lane & 15, fq = lane >> 4;
    const bf16_t* Q = (const bf16_t*)(ws + WS_Q) + (size_t)s * SEG * HD + h * DH;
    const bf16_t* K = (const bf16_t*)(ws + WS_K) + (size_t)s * SEG * HD + h * DH;
    const bf16_t* V = (const bf16_t*)(ws + WS_V) + (size_t)s * SEG * HD + h * DH;
    const __amdgpu_buffer_rsrc_t rm = __builtin_amdgcn_make_buffer_rsrc(ws + WS_QKVM, 0, (int)(WS_OM - WS_QKVM), 0x00020000);
    const int kmo = s * (int)QKVM_B + (h * 3 + 1) * 2048, vmo = kmo + 2048;
    u32x4 km[4], vm[2];
    for (unsigned spins = 0;;) {
        asm volatile("" ::: "memory");
#pragma unroll
        for (int i = 0; i < 4; ++i) km[i] = __builtin_amdgcn_raw_buffer_load_b128(rm, kmo + (lane & 7) * 256 + i * 64 + fq * 16, 0, INK ? AUX_SC1 : 0);
#pragma unroll
        for (int i = 0; i < 2; ++i) vm[i] = __builtin_amdgcn_raw_buffer_load_b128(rm, vmo + (i * 4 + fq) * 256 + fr * 16, 0, INK ? AUX_SC1 : 0);
        if (!INK) break;
        bool bad = has_sent(vm[0]) || has_sent(vm[1]);
#pragma unroll
        for (int i = 0; i < 4; ++i) bad |= has_sent(km[i]);
        if (!__any(bad)) break;
        __builtin_amdgcn_s_sleep(16);
        if (++spins > SPIN_CAP) break;
    }
#pragma unroll
    for (int i = 0; i < 12; ++i) {
        const int r = i * 4 + fq;
        u32x4 v = (u32x4){0u, 0u, 0u, 0u};
        if (i < 2) v = vm[i]; else if (i < 10) v = *(const u32x4*)(V + (size_t)(r - 8) * HD + fr * 8);
        *(LAS u32x4*)(vs + r * XV_PITCH + fr * 16) = v;
    }
    f32x4 sc[3][2];
#pragma unroll
    for (int t = 0; t < 3; ++t) { sc[t][0] = (f32x4){0.f, 0.f, 0.f, 0.f}; sc[t][1] = (f32x4){0.f, 0.f, 0.f, 0.f}; }
#pragma unroll
    for (int i = 0; i < 4; ++i) {
        const bf16x8 q0 = *(const bf16x8*)(Q + (size_t)fr * HD + i * 32 + fq * 8), q1 = *(const bf16x8*)(Q + (size_t)(16 + fr) * HD + i * 32 + fq * 8);
#pragma unroll
        for (int t = 0; t < 3; ++t) {
            int r = t * 16 + fr - 8; r = r < 0 ? 0 : (r > 31 ? 31 : r);
            bf16x8 kf = *(const bf16x8*)(K + (size_t)r * HD + i * 32 + fq * 8);
            if (t == 0 && fr < 8) kf = __builtin_bit_cast(bf16x8, km[i]);
            sc[t][0] = __builtin_amdgcn_mfma_f32_16x16x32_bf16(kf, q0, sc[t][0], 0, 0, 0);
            sc[t][1] = __builtin_amdgcn_mfma_f32_16x16x32_bf16(kf, q1, sc[t][1], 0, 0, 0);
        }
    }
    const float scale = 0.08838834764831845f, NEG = rbf(-1000000000.0f);
    u32x4 p0[2], p1[2];
#pragma unroll
    for (int rt = 0; rt < 2; ++rt) {
        const int row = rt * 16 + fr;
        float v[12]; float mx = -INFINITY;
#pragma unroll
        for (int t = 0; t < 3; ++t)
#pragma unroll
            for (int r = 0; r < 4; ++r) { const int key = t * 16 + fq * 4 + r; float z = rbf(rbf(sc[t][rt][r]) * scale);
                if (key - 8 > row) z = NEG; if (key >= 40) z = -INFINITY; v[t * 4 + r] = z; mx = fmaxf(mx, z); }
        mx = xmax16_32(mx);
        float sum = 0.f;
#pragma unroll
        for (int i = 0; i < 12; ++i) { v[i] = __expf(v[i] - mx); sum += v[i]; }
        sum = xsum16_32(sum);
        const float inv = 1.0f / sum;
        p0[rt].x = pk_bf16(v[0] * inv, v[1] * inv); p0[rt].y = pk_bf16(v[2] * inv, v[3] * inv); p0[rt].z = pk_bf16(v[4] * inv, v[5] * inv); p0[rt].w = pk_bf16(v[6] * inv, v[7] * inv);
        p1[rt].x = pk_bf16(v[8] * inv, v[9] * inv); p1[rt].y = pk_bf16(v[10] * inv, v[11] * inv); p1[rt].z = 0u; p1[rt].w = 0u;
    }
    asm volatile("s_waitcnt lgkmcnt(0)" ::: "memory");
    const __amdgpu_buffer_rsrc_t ro = __builtin_amdgcn_make_buffer_rsrc(ws + WS_O, 0, S_LEN * HD * 2, 0x00020000);
    const int obase = (s * SEG * HD + h * DH) * 2;
    const bf16x4 zz = (bf16x4){0, 0, 0, 0};
#pragma unroll
    for (int et = 0; et < 8; ++et) {
        const int voff = (4 * fq + (fr >> 2)) * XV_PITCH + et * 32 + (lane & 3) * 8;
        const bf16x4 v0 = __builtin_amdgcn_ds_read_tr16_b64_v4i16((LAS bf16x4*)(vs + voff));
        const bf16x4 v1 = __builtin_amdgcn_ds_read_tr16_b64_v4i16((LAS bf16x4*)(vs + voff + 16 * XV_PITCH));
        const bf16x4 v2 = __builtin_amdgcn_ds_read_tr16_b64_v4i16((LAS bf16x4*)(vs + voff + 32 * XV_PITCH));
        const bf16x8 va = __builtin_shufflevector(v0, v1, 0, 1, 2, 3, 4, 5, 6, 7), vb = __builtin_shufflevector(v2, zz, 0, 1, 2, 3, 4, 5, 6, 7);
#pragma unroll
        for (int rt = 0; rt < 2; ++rt) {
            f32x4 o = (f32x4){0.f, 0.f, 0.f, 0.f};
            o = __builtin_amdgcn_mfma_f32_16x16x32_bf16(va, __builtin_bit_cast(bf16x8, p0[rt]), o, 0, 0, 0);
            o = __builtin_amdgcn_mfma_f32_16x16x32_bf16(vb, __builtin_bit_cast(bf16x8, p1[rt]), o, 0, 0, 0);
            u32x2 w; w.x = pk_bf16(o[0], o[1]); w.y = pk_bf16(o[2], o[3]);
            __builtin_amdgcn_raw_buffer_store_b64(w, ro, obase + ((rt * 16 + fr) * HD + et * 16 + fq * 4) * 2, 0, INK ? AUX_SC1 : 0);
        }
    }
    asm volatile("s_waitcnt lgkmcnt(0)" ::: "memory");
}

struct SchedOne {
    g8::Unit u; bool valid;
    __device__ __forceinline__ bool next(int i, g8::Unit& o) const { if (i != 0 || !valid) return false; o = u; return true; }
};
struct SchedKVQ {
    const bf16_t* A; const bf16_t* Wq; long dk, dv; int r;
    __device__ __forceinline__ bool next(int i, g8::Unit& u) const {
        const int L = r + 128 * i; if (L >= 352) return false;
        int pm, pn;
        if (L < 224) { pm = 1 + (L >> 5); pn = 16 + (L & 31); } else { pm = (L - 224) & 7; pn = (L - 224) >> 3; }
        const int mat = pn >> 4;
        u.A = A + (size_t)pm * 256 * DM; u.B = Wq + (mat == 1 ? dk : 0L) + (mat == 2 ? dv : 0L) + (pn & 15) * 256; u.pm = pm; u.pn = pn; return true;
    }
};
struct EpiQKV {
    char* ws;
    __device__ __forceinline__ void operator()(const f32x4 (&acc)[2][2][4][2], const g8::Unit& u, int wr, int wc, int fr, int fq) const {
        const __amdgpu_buffer_rsrc_t rq = __builtin_amdgcn_make_buffer_rsrc(ws + WS_Q, 0, (int)(WS_H - WS_Q), 0x00020000);
        const int base = (u.pn >> 4) * (S_LEN * HD * 2) + ((u.pm * 256 + wr * 64 + fr) * HD + (u.pn & 15) * 256 + wc * 32 + 4 * fq) * 2;
#pragma unroll
        for (int ai = 0; ai < 2; ++ai)
#pragma unroll
            for (int m = 0; m < 4; ++m)
#pragma unroll
                for (int bj = 0; bj < 2; ++bj)
#pragma unroll
                    for (int n = 0; n < 2; ++n) { const f32x4 v = acc[ai][bj][m][n]; u32x2 w; w.x = pk_bf16(v[0], v[1]); w.y = pk_bf16(v[2], v[3]);
                        __builtin_amdgcn_raw_buffer_store_b64(w, rq, base + ((ai * 128 + m * 16) * HD + bj * 128 + n * 16) * 2, 0, AUX_SC1); }
        asm volatile("s_waitcnt vmcnt(0)" ::: "memory");
        if ((threadIdx.x & 63) == 0) {
            if (u.pn >= 16) __hip_atomic_fetch_add((unsigned*)(ws + WS_CNT + (size_t)(CNT_KV + u.pm * 32 + u.pn - 16) * 256), 1u, __ATOMIC_RELAXED, __HIP_MEMORY_SCOPE_AGENT);
            __hip_atomic_fetch_add((unsigned*)(ws + WS_CNT + (size_t)(CNT_QKV + u.pm) * 256), 1u, __ATOMIC_RELAXED, __HIP_MEMORY_SCOPE_AGENT);
        }
    }
};
struct EpiKV0 {
    char* ws; int pm2, pn2;
    __device__ __forceinline__ void operator()(const f32x4 (&acc)[4][2], int wr, int wc, int fr, int fq) const {
        const __amdgpu_buffer_rsrc_t rq = __builtin_amdgcn_make_buffer_rsrc(ws + WS_K, 0, (int)(WS_H - WS_K), 0x00020000);
        const int base = (pn2 >> 5) * (S_LEN * HD * 2) + ((pm2 * 128 + wr * 64 + fr) * HD + (pn2 & 31) * 128 + wc * 32 + 4 * fq) * 2;
#pragma unroll
        for (int m = 0; m < 4; ++m)
#pragma unroll
            for (int n = 0; n < 2; ++n) { const f32x4 v = acc[m][n]; u32x2 w; w.x = pk_bf16(v[0], v[1]); w.y = pk_bf16(v[2], v[3]);
                __builtin_amdgcn_raw_buffer_store_b64(w, rq, base + (m * 16 * HD + n * 16) * 2, 0, AUX_SC1); }
        asm volatile("s_waitcnt vmcnt(0)" ::: "memory");
        if ((threadIdx.x & 63) == 0) {
            __hip_atomic_fetch_add((unsigned*)(ws + WS_CNT + (size_t)(CNT_KV + (pn2 >> 1)) * 256), 1u, __ATOMIC_RELAXED, __HIP_MEMORY_SCOPE_AGENT);
            __hip_atomic_fetch_add((unsigned*)(ws + WS_CNT + (size_t)CNT_QKV * 256), 1u, __ATOMIC_RELAXED, __HIP_MEMORY_SCOPE_AGENT);
        }
    }
};
struct EpiMLP1 {
    char* ws; const bf16_t* b1;
    __device__ __forceinline__ void operator()(const f32x4 (&acc)[2][2][4][2], const g8::Unit& u, int wr, int wc, int fr, int fq) const {
        const __amdgpu_buffer_rsrc_t rh = __builtin_amdgcn_make_buffer_rsrc(ws + WS_H, 0, S_LEN * FF * 2, 0x00020000);
        const int row0 = u.pm * 256 + wr * 64 + fr, col0 = u.pn * 256 + wc * 32 + 4 * fq;
#pragma unroll
        for (int bj = 0; bj < 2; ++bj)
#pragma unroll
            for (int n = 0; n < 2; ++n) {
                const u32x2 bw = *(const u32x2*)(b1 + col0 + bj * 128 + n * 16);
                const float b0 = bf2f(bw.x & 0xffffu), b1f = bf2f(bw.x >> 16), b2f = bf2f(bw.y & 0xffffu), b3 = bf2f(bw.y >> 16);
#pragma unroll
                for (int ai = 0; ai < 2; ++ai)
#pragma unroll
                    for (int m = 0; m < 4; ++m) { const f32x4 v = acc[ai][bj][m][n];
                        const float g0 = gelu_tanh(rbf(rbf(v[0]) + b0)), g1 = gelu_tanh(rbf(rbf(v[1]) + b1f)), g2 = gelu_tanh(rbf(rbf(v[2]) + b2f)), g3 = gelu_tanh(rbf(rbf(v[3]) + b3));
                        u32x2 w; w.x = pk_bf16(g0, g1); w.y = pk_bf16(g2, g3);
                        __builtin_amdgcn_raw_buffer_store_b64(w, rh, ((row0 + ai * 128 + m * 16) * FF + col0 + bj * 128 + n * 16) * 2, 0, AUX_SC1); }
                asm volatile("" ::: "memory");
            }
        signal_counter(ws, CNT_H + u.pm);
    }
};
struct EpiMLP2 {
    char* ws; const bf16_t* b2; int row0, col0;
    __device__ __forceinline__ void operator()(const f32x4 (&acc)[4][2], int wr, int wc, int fr, int fq) const {
        const __amdgpu_buffer_rsrc_t rb = __builtin_amdgcn_make_buffer_rsrc(ws + WS_B, 0, S_LEN * DM * 2, 0x00020000);
#pragma unroll
        for (int n = 0; n < 2; ++n) {
            const int col = col0 + wc * 32 + n * 16 + 4 * fq;
            const u32x2 bw = *(const u32x2*)(b2 + col);
#pragma unroll
            for (int m = 0; m < 4; ++m) { const f32x4 v = acc[m][n];
                u32x2 w; w.x = pk_bf16(rbf(v[0]) + bf2f(bw.x & 0xffffu), rbf(v[1]) + bf2f(bw.x >> 16)); w.y = pk_bf16(rbf(v[2]) + bf2f(bw.y & 0xffffu), rbf(v[3]) + bf2f(bw.y >> 16));
                __builtin_amdgcn_raw_buffer_store_b64(w, rb, ((row0 + wr * 64 + m * 16 + fr) * DM + col) * 2, 0, AUX_SC1); }
        }
        signal_counter(ws, CNT_B + (row0 >> 8));
    }
};
__device__ __forceinline__ u32x2 combine4(const f32x4 v, const u32x2 xw, const u32x2 bw) {
    u32x2 w;
    w.x = pk_bf16(rbf(bf2f(xw.x & 0xffffu) + rbf(v[0])) + bf2f(bw.x & 0xffffu), rbf(bf2f(xw.x >> 16) + rbf(v[1])) + bf2f(bw.x >> 16));
    w.y = pk_bf16(rbf(bf2f(xw.y & 0xffffu) + rbf(v[2])) + bf2f(bw.y & 0xffffu), rbf(bf2f(xw.y >> 16) + rbf(v[3])) + bf2f(bw.y >> 16));
    return w;
}
struct EpiOut {
    const bf16_t* x; const bf16_t* bout; bf16_t* out; int row0, col0;
    __device__ __forceinline__ void operator()(const f32x4 (&acc)[4][2], int wr, int wc, int fr, int fq) const {
#pragma unroll
        for (int m = 0; m < 4; ++m)
#pragma unroll
            for (int n = 0; n < 2; ++n) {
                const size_t off = (size_t)(row0 + wr * 64 + m * 16 + fr) * DM + col0 + wc * 32 + n * 16 + 4 * fq;
                *(u32x2*)(out + off) = combine4(acc[m][n], *(const u32x2*)(x + off), *(const u32x2*)(bout + off));
            }
    }
};

__global__ void __launch_bounds__(512, 2) k_mega(const bf16_t* __restrict__ x, const bf16_t* __restrict__ Wq, const bf16_t* __restrict__ Wk, const bf16_t* __restrict__ Wv,
                                                 const bf16_t* __restrict__ Wo, const bf16_t* __restrict__ w1, const bf16_t* __restrict__ b1, const bf16_t* __restrict__ w2,
                                                 const bf16_t* __restrict__ b2, char* ws, bf16_t* __restrict__ out) {
    extern __shared__ __attribute__((aligned(16))) unsigned char shm_[];
    LAS unsigned char* lds = (LAS unsigned char*)shm_;
    const int b = blockIdx.x;
    const int role = (b >> 3) & 1, r = (b >> 4) * 8 + (b & 7);
    if (role == 0) { chain_body<4>(lds, r, Wq, Wk, Wv, Wo, ws); return; }
    {
        const int pm2 = r & 1, pn2 = r >> 1;
        EpiKV0 E{ws, pm2, pn2};
        gemm128_w8<EpiKV0>(lds, (const bf16_t*)(ws + WS_XN) + (size_t)pm2 * 128 * DM, DM, (pn2 >= 32 ? Wv : Wk) + (pn2 & 31) * 128, HD, DM, E);
    }
    {
        SchedKVQ S{(const bf16_t*)(ws + WS_XN), Wq, (long)(Wk - Wq), (long)(Wv - Wq), r};
        EpiQKV E{ws}; g8::gemm_phase<EpiQKV, SchedKVQ>(lds, DM, DM, HD, S, E);
    }
    {
        const int L = r >= 96 ? r - 96 : 32 + r;
        SchedOne S; S.valid = (r >= 96) || (r < 80); S.u.pm = L & 7; S.u.pn = L >> 3; S.u.A = (const bf16_t*)(ws + WS_Y) + (size_t)S.u.pm * 256 * DM; S.u.B = w1 + S.u.pn * 256;
        EpiMLP1 E{ws, b1}; g8::gemm_phase<EpiMLP1, SchedOne>(lds, DM, DM, FF, S, E);
    }
    {
        const int pm = r >> 3, pn = r & 7;
        wait_counter(ws, CNT_H + (pm >> 1), 14 * 8);
        EpiMLP2 E{ws, b2, pm * 128, pn * 128};
        gemm128_w8<EpiMLP2>(lds, (const bf16_t*)(ws + WS_H) + (size_t)pm * 128 * FF, FF, w2 + pn * 128, DM, FF, E);
    }
    const int blk = r >> 4, t = r & 15;
    if (blk == 7) return;
    {
        if (threadIdx.x < 64) poll_ge(ws, CNT_STEP, (unsigned)(8 * blk + 8));
        wait_counter(ws, CNT_QKV + blk, (blk == 0 ? 128 * 8 : 32 * 8) + 16 * 8);
        const int wave = threadIdx.x >> 6;
#pragma unroll 1
        for (int i = 0; i < 2; ++i) { const int item = t * 16 + wave * 2 + i; xattn_item<true>(lds + wave * 48 * XV_PITCH, ws, blk * 8 + (item >> 5), item & 31); }
        signal_counter(ws, CNT_O + blk);
    }
    {
        if (threadIdx.x < 64) poll_ge(ws, CNT_B + blk, 16 * 8);
        wait_counter(ws, CNT_O + blk, 16 * 8);
        const int pm = blk * 2 + (t >> 3), pn = t & 7;
        EpiOut E{x, (const bf16_t*)(ws + WS_B), out, pm * 128, pn * 128};
        gemm128_w8<EpiOut>(lds, (const bf16_t*)(ws + WS_O) + (size_t)pm * 128 * HD, HD, Wo + pn * 128, DM, HD, E);
    }
}

__global__ void __launch_bounds__(256) k_tail_attn(char* ws) {
    __shared__ __attribute__((aligned(16))) unsigned char shm_[4 * 48 * XV_PITCH];
    const int wave = threadIdx.x >> 6, item = blockIdx.x * 4 + wave;
    xattn_item<false>((LAS unsigned char*)shm_ + wave * 48 * XV_PITCH, ws, 56 + (item >> 5), item & 31);
}
__global__ void __launch_bounds__(256) k_tail_gemm(const bf16_t* __restrict__ x, const bf16_t* __restrict__ Wo, const char* __restrict__ ws, bf16_t* __restrict__ out) {
    extern __shared__ __attribute__((aligned(16))) unsigned char shm_[];
    const int tid = threadIdx.x, wave = __builtin_amdgcn_readfirstlane(tid >> 6), lane = tid & 63, fr = lane & 15, fq = lane >> 4;
    LAS unsigned char* lds = (LAS unsigned char*)shm_ + wave * 24576;
    const int pn = blockIdx.x & 31, pm = blockIdx.x >> 5, row0 = 1792 + pm * 32, col0 = pn * 32;
    const char* cA = (const char*)((const bf16_t*)(ws + WS_O) + (size_t)row0 * HD + wave * 1024);
    const char* cB = (const char*)(Wo + (size_t)wave * 1024 * DM + col0);
    unsigned voffA[4], voffB[4];
#pragma unroll
    for (int i = 0; i < 4; ++i) { const int c = i * 64 + lane, row = c >> 3, ch = (c & 7) ^ (row & 7); voffA[i] = (unsigned)(row * HD + ch * 8) * 2u;
        const int k = c >> 2, cb = c & 3; voffB[i] = (unsigned)(k * DM + cb * 8) * 2u; }
#define STG(buf, t) do { _Pragma("unroll") for (int _i = 0; _i < 4; ++_i) { \
        __builtin_amdgcn_global_load_lds((const unsigned*)(cA + (size_t)(t) * 128 + voffA[_i]), (LAS unsigned*)(lds + (buf) * 8192 + _i * 1024), 16, 0, 0); \
        __builtin_amdgcn_global_load_lds((const unsigned*)(cB + (size_t)(t) * 64 * DM * 2 + voffB[_i]), (LAS unsigned*)(lds + (buf) * 8192 + 4096 + _i * 1024), 16, 0, 0); } } while (0)
    f32x4 acc[2][2];
#pragma unroll
    for (int m = 0; m < 2; ++m) { acc[m][0] = (f32x4){0.f, 0.f, 0.f, 0.f}; acc[m][1] = (f32x4){0.f, 0.f, 0.f, 0.f}; }
    STG(0, 0); STG(1, 1);
    int cur = 0;
    for (int t = 0; t < 16; ++t) {
        int nx = cur + 2; if (nx >= 3) nx -= 3;
        if (t + 2 < 16) { STG(nx, t + 2); asm volatile("s_waitcnt vmcnt(16)" ::: "memory"); }
        else if (t + 1 < 16) asm volatile("s_waitcnt vmcnt(8)" ::: "memory");
        else asm volatile("s_waitcnt vmcnt(0)" ::: "memory");
        LAS unsigned char* sa = lds + cur * 8192; LAS unsigned char* sb = sa + 4096;
        bf16x8 At[2][2], Bt[2][2];
#pragma unroll
        for (int k = 0; k < 2; ++k) {
#pragma unroll
            for (int m = 0; m < 2; ++m) { const int row = m * 16 + fr; At[m][k] = *(const LAS bf16x8*)(sa + row * 128 + (((k * 4 + fq) ^ (row & 7)) * 16)); }
#pragma unroll
            for (int n = 0; n < 2; ++n) {
                const int off = (k * 32 + 8 * fq + (fr >> 2)) * 64 + n * 32 + (lane & 3) * 8;
                const bf16x4 a0 = __builtin_amdgcn_ds_read_tr16_b64_v4i16((LAS bf16x4*)(sb + off)), a1 = __builtin_amdgcn_ds_read_tr16_b64_v4i16((LAS bf16x4*)(sb + off + 256));
                Bt[n][k] = __builtin_shufflevector(a0, a1, 0, 1, 2, 3, 4, 5, 6, 7); }
        }
#pragma unroll
        for (int k = 0; k < 2; ++k)
#pragma unroll
            for (int m = 0; m < 2; ++m)
#pragma unroll
                for (int n = 0; n < 2; ++n) acc[m][n] = __builtin_amdgcn_mfma_f32_16x16x32_bf16(Bt[n][k], At[m][k], acc[m][n], 0, 0, 0);
        asm volatile("s_waitcnt lgkmcnt(0)" ::: "memory");
        cur = cur + 1 == 3 ? 0 : cur + 1;
    }
#undef STG
    __syncthreads();
    LAS f32x4* red = (LAS f32x4*)shm_;
#pragma unroll
    for (int m = 0; m < 2; ++m)
#pragma unroll
        for (int n = 0; n < 2; ++n) red[((wave * 2 + m) * 2 + n) * 64 + lane] = acc[m][n];
    __syncthreads();
    {
        const int m = wave >> 1, n = wave & 1;
        const f32x4 v = (red[((0 * 2 + m) * 2 + n) * 64 + lane] + red[((1 * 2 + m) * 2 + n) * 64 + lane]) + (red[((2 * 2 + m) * 2 + n) * 64 + lane] + red[((3 * 2 + m) * 2 + n) * 64 + lane]);
        const size_t off = (size_t)(row0 + m * 16 + fr) * DM + col0 + n * 16 + 4 * fq;
        *(u32x2*)(out + off) = combine4(v, *(const u32x2*)(x + off), *(const u32x2*)((const bf16_t*)(ws + WS_B) + off));
    }
}

extern "C" void kernel_launch(void* const* d_in, const int* in_sizes, int n_in, void* d_out, int out_size, void* d_ws, size_t ws_size, hipStream_t stream) {
    const bf16_t* x = (const bf16_t*)d_in[0];
    const bf16_t* mem0 = (const bf16_t*)d_in[1];
    const bf16_t* Wq = (const bf16_t*)d_in[2];
    const bf16_t* Wk = (const bf16_t*)d_in[3];
    const bf16_t* Wv = (const bf16_t*)d_in[4];
    const bf16_t* Wo = (const bf16_t*)d_in[5];
    const bf16_t* w1 = (const bf16_t*)d_in[8];
    const bf16_t* b1 = (const bf16_t*)d_in[9];
    const bf16_t* w2 = (const bf16_t*)d_in[10];
    const bf16_t* b2 = (const bf16_t*)d_in[11];
    char* ws = (char*)d_ws;
    bf16_t* out = (bf16_t*)d_out;
    (void)hipFuncSetAttribute((const void*)k_mega, hipFuncAttributeMaxDynamicSharedMemorySize, CH_LDS);
    (void)hipFuncSetAttribute((const void*)k_tail_gemm, hipFuncAttributeMaxDynamicSharedMemorySize, 4 * 24576);
    k_prep<<<S_LEN / 4, 256, 0, stream>>>(x, mem0, ws);
    k_mega<<<256, 512, CH_LDS, stream>>>(x, Wq, Wk, Wv, Wo, w1, b1, w2, b2, ws, out);
    k_tail_attn<<<64, 256, 0, stream>>>(ws);
    k_tail_gemm<<<256, 256, 4 * 24576, stream>>>(x, Wo, ws, out);
}
```

```cpp
#include <hip/hip_runtime.h>
#include <hip/hip_bf16.h>

#define LAS __attribute__((address_space(3)))
typedef unsigned short bf16_t;
typedef short bf16x8 __attribute__((ext_vector_type(8)));
typedef short bf16x4 __attribute__((ext_vector_type(4)));
typedef float f32x4 __attribute__((ext_vector_type(4)));
typedef float f32x2 __attribute__((ext_vector_type(2)));
typedef unsigned u32x4 __attribute__((ext_vector_type(4)));
typedef unsigned u32x2 __attribute__((ext_vector_type(2)));

constexpr int S_LEN = 2048, DM = 1024, NH = 32, DH = 128, HD = NH * DH, MM = 8, SEG = 32, NSEG = S_LEN / SEG, FF = 3584;
constexpr int AUX_SC1 = 16;
constexpr unsigned SENT = 0xFFFFFFFFu;

__device__ __forceinline__ float bf2f(unsigned b) { return __builtin_bit_cast(float, b << 16); }
typedef __bf16 bf16x2_t __attribute__((ext_vector_type(2)));
__device__ __forceinline__ unsigned pk_bf16(float lo, float hi) { const f32x2 v = {lo, hi}; return __builtin_bit_cast(unsigned, __builtin_convertvector(v, bf16x2_t)); }
__device__ __forceinline__ float rbf(float x) { return bf2f(pk_bf16(x, x) & 0xffffu); }
__device__ __forceinline__ unsigned desent(unsigned w) { return w == SENT ? 0x7FC07FC0u : w; }
__device__ __forceinline__ unsigned umax2(unsigned a, unsigned b) { return a > b ? a : b; }
__device__ __forceinline__ unsigned umax4(u32x4 v) { return umax2(umax2(v.x, v.y), umax2(v.z, v.w)); }

constexpr size_t WS_XN = 0;
constexpr size_t WS_Y = WS_XN + (size_t)S_LEN * DM * 2;
constexpr size_t WS_Q = WS_Y + (size_t)S_LEN * DM * 2;
constexpr size_t WS_K = WS_Q + (size_t)S_LEN * HD * 2;
constexpr size_t WS_V = WS_K + (size_t)S_LEN * HD * 2;
constexpr size_t WS_H = WS_V + (size_t)S_LEN * HD * 2;
constexpr size_t WS_O = WS_H + (size_t)S_LEN * FF * 2;
constexpr size_t WS_B = WS_O + (size_t)S_LEN * HD * 2;
constexpr size_t WS_SENT0 = WS_B + (size_t)S_LEN * DM * 2;
constexpr size_t MEM_B = (size_t)MM * DM * 2;
constexpr size_t WS_MEM = WS_SENT0;
constexpr size_t QKVM_B = (size_t)NH * 3 * MM * DH * 2;
constexpr size_t WS_QKVM = WS_MEM + (size_t)(NSEG + 1) * MEM_B;
constexpr size_t OM_B = (size_t)MM * HD * 2;
constexpr size_t WS_OM = WS_QKVM + (size_t)NSEG * QKVM_B;
constexpr size_t WS_SENT1 = WS_OM + (size_t)NSEG * OM_B;
constexpr size_t WS_CNT = WS_SENT1;
constexpr int CNT_H = 0;
constexpr int CNT_B = 8;
constexpr int CNT_O = 16;
constexpr int CNT_QKV = 24;
constexpr int CNT_FILL = 33;
constexpr int CNT_STEP = 32;
constexpr int CNT_KV = 40;
constexpr int CNT_KV0B = 40 + 256;
constexpr int CNT_N = 40 + 256 + 32;
constexpr size_t WS_TMO = WS_CNT + (size_t)CNT_N * 256;
constexpr size_t WS_END = WS_TMO + 256;

__device__ __forceinline__ float wave_sum(float v) {
#pragma unroll
    for (int o = 32; o > 0; o >>= 1) v += __shfl_xor(v, o);
    return v;
}
__device__ __forceinline__ void ln_row(float (&v)[16]) {
    float s = 0.f;
#pragma unroll
    for (int i = 0; i < 16; ++i) s += v[i];
    const float mean = wave_sum(s) * (1.0f / DM);
    float q = 0.f;
#pragma unroll
    for (int i = 0; i < 16; ++i) { const float d = v[i] - mean; q += d * d; }
    const float var = rbf(wave_sum(q) * (1.0f / DM));
    const float mb = rbf(mean);
    const float den = rbf(sqrtf(rbf(var + 1e-5f)));
#pragma unroll
    for (int i = 0; i < 16; ++i) v[i] = rbf(rbf(v[i] - mb) / den);
}
__global__ void __launch_bounds__(256) k_prep(const bf16_t* __restrict__ x, const bf16_t* __restrict__ mem0, char* __restrict__ ws) {
    const int wave = threadIdx.x >> 6, lane = threadIdx.x & 63, row = blockIdx.x * 4 + wave;
    const u32x4* src = (const u32x4*)(x + (size_t)row * DM);
    u32x4 a = src[lane], b = src[lane + 64];
    float v[16];
    const unsigned w[8] = {a.x, a.y, a.z, a.w, b.x, b.y, b.z, b.w};
#pragma unroll
    for (int i = 0; i < 8; ++i) { v[2 * i] = bf2f(w[i] & 0xffffu); v[2 * i + 1] = bf2f(w[i] >> 16); }
    ln_row(v);
    u32x4 o0, o1;
    o0.x = pk_bf16(v[0], v[1]); o0.y = pk_bf16(v[2], v[3]); o0.z = pk_bf16(v[4], v[5]); o0.w = pk_bf16(v[6], v[7]);
    o1.x = pk_bf16(v[8], v[9]); o1.y = pk_bf16(v[10], v[11]); o1.z = pk_bf16(v[12], v[13]); o1.w = pk_bf16(v[14], v[15]);
    u32x4* dx = (u32x4*)(ws + WS_XN + (size_t)row * DM * 2);
    dx[lane] = o0; dx[lane + 64] = o1;
    ln_row(v);
    o0.x = pk_bf16(v[0], v[1]); o0.y = pk_bf16(v[2], v[3]); o0.z = pk_bf16(v[4], v[5]); o0.w = pk_bf16(v[6], v[7]);
    o1.x = pk_bf16(v[8], v[9]); o1.y = pk_bf16(v[10], v[11]); o1.z = pk_bf16(v[12], v[13]); o1.w = pk_bf16(v[14], v[15]);
    u32x4* dy = (u32x4*)(ws + WS_Y + (size_t)row * DM * 2);
    dy[lane] = o0; dy[lane + 64] = o1;
    {
        const size_t n16 = (WS_SENT1 - WS_SENT0 - MEM_B) / 16;
        u32x4* dst = (u32x4*)(ws + WS_SENT0 + MEM_B);
        const u32x4 f = (u32x4){SENT, SENT, SENT, SENT};
        for (size_t i = (size_t)blockIdx.x * 256 + threadIdx.x; i < n16; i += (size_t)gridDim.x * 256) dst[i] = f;
    }
    if (blockIdx.x >= 8 && blockIdx.x < 8 + (CNT_N * 256 / 16 + 255) / 256) {
        const int i = (blockIdx.x - 8) * 256 + threadIdx.x; if (i < CNT_N * 256 / 16) ((u32x4*)(ws + WS_CNT))[i] = (u32x4){0u, 0u, 0u, 0u}; }
    if (blockIdx.x < 4) {
        const int c = blockIdx.x * 256 + threadIdx.x, r = c & 7, kg = c >> 3;
        u32x4 m = *(const u32x4*)(mem0 + (size_t)r * DM + kg * 8);
        m.x = desent(m.x); m.y = desent(m.y); m.z = desent(m.z); m.w = desent(m.w);
        *(u32x4*)(ws + WS_MEM + (size_t)c * 16) = m;
    }
}

namespace g8 {
constexpr int BM = 256, BK = 64, HALF = 128, HTB = HALF * BK * 2, STAGE_BYTES = 8 * HTB;
__host__ __device__ __forceinline__ int lds_byte(int r, int c) { const int st = (r >> 4) * 2 + (c >> 5), rr = r & 15, cc = c & 31, ob = rr * 64 + cc * 2; return st * 1024 + (ob ^ (((ob >> 9) & 1) << 5)); }
__host__ __device__ __forceinline__ void stage_rc(int b, int& R, int& C) { const int st = b / 1024, sb = b % 1024, swz = sb ^ (((sb >> 9) & 1) << 5); R = (st >> 1) * 16 + swz / 64; C = (st & 1) * 32 + (swz % 64) / 2; }
__host__ __device__ __forceinline__ int bsw(int row) { return (row & 3) | (((row >> 3) & 1) << 2); }
struct Unit { const bf16_t* A; const bf16_t* B; int pm, pn; };

template <class Epi, class Sched>
__device__ __forceinline__ void gemm_phase(LAS unsigned char* lds, const int K, const int lda, const int ldb, const Sched& S, const Epi& E) {
    int tid = threadIdx.x; asm volatile("" : "+v"(tid));
    const int wid = __builtin_amdgcn_readfirstlane(tid >> 6), lane = tid & 63, wr = wid >> 2, wc = wid & 3, fr = lane & 15, fq = lane >> 4;
    const int nt = K / BK;
    unsigned voffA[2], voffB[2];
#pragma unroll
    for (int i = 0; i < 2; ++i) { int R, C; stage_rc(tid * 16 + i * 8192, R, C); voffA[i] = (unsigned)(R * lda + C) * 2u;
        const int b = tid * 16 + i * 8192, row = b >> 8, ch = (b & 255) >> 4, seg = (ch >> 1) ^ bsw(row);
        voffB[i] = (unsigned)(row * ldb + seg * 16 + (ch & 1) * 8) * 2u; }
    const size_t kstepA = (size_t)(BK * 2), kstepB = (size_t)BK * ldb * 2;
    const size_t hstepA = (size_t)HALF * lda * 2, hstepB = (size_t)HALF * 2;
    const unsigned ldsw = (unsigned)wid * 1024u;
    const int aoff = lds_byte(wr * 64 + fr, fq * 8);
    const int q4 = (lane & 15) >> 2, p4 = lane & 3, sB = q4 | ((fq & 1) << 2);
    const int boff0 = (8 * fq + q4) * 256 + (((wc * 2 + 0) ^ sB) * 32) + 8 * p4;
    const int boff1 = (8 * fq + q4) * 256 + (((wc * 2 + 1) ^ sB) * 32) + 8 * p4;
#define G8_SA(b, h) (((b) * 2 + (h)) * HTB)
#define G8_SB(b, h) ((4 + (b) * 2 + (h)) * HTB)
#define G8_STAGE(bufoff, gbase, voff) do { _Pragma("unroll") for (int _i = 0; _i < 2; ++_i) \
        __builtin_amdgcn_global_load_lds((const unsigned*)((const char*)(gbase) + (voff)[_i]), (LAS unsigned*)(lds + (bufoff) + ldsw + _i * 8192), 16, 0, 0); } while (0)
#define G8_LDA(dst, b, h) do { _Pragma("unroll") for (int m = 0; m < 4; ++m) _Pragma("unroll") for (int k = 0; k < 2; ++k) dst[m][k] = *(const LAS bf16x8*)(lds + G8_SA(b, h) + aoff + m * 2048 + k * 1024); } while (0)
#define G8_TR(off) __builtin_amdgcn_ds_read_tr16_b64_v4i16((LAS bf16x4*)(lds + (off)))
#define G8_LDB(dst, b, h) do { _Pragma("unroll") for (int k = 0; k < 2; ++k) { \
        bf16x4 a0 = G8_TR(G8_SB(b, h) + boff0 + k * 8192), a1 = G8_TR(G8_SB(b, h) + boff0 + k * 8192 + 1024); \
        bf16x4 c0 = G8_TR(G8_SB(b, h) + boff1 + k * 8192), c1 = G8_TR(G8_SB(b, h) + boff1 + k * 8192 + 1024); \
        dst[0][k] = __builtin_shufflevector(a0, a1, 0, 1, 2, 3, 4, 5, 6, 7); dst[1][k] = __builtin_shufflevector(c0, c1, 0, 1, 2, 3, 4, 5, 6, 7); } } while (0)
#define G8_MMA(ai, bj, At, Bt) do { __builtin_amdgcn_s_setprio(1); _Pragma("unroll") for (int m = 0; m < 4; ++m) _Pragma("unroll") for (int n = 0; n < 2; ++n) _Pragma("unroll") for (int k = 0; k < 2; ++k) \
        acc[ai][bj][m][n] = __builtin_amdgcn_mfma_f32_16x16x32_bf16(Bt[n][k], At[m][k], acc[ai][bj][m][n], 0, 0, 0); __builtin_amdgcn_s_setprio(0); } while (0)
#define G8_WAIT_V(n) asm volatile("s_waitcnt vmcnt(" #n ")" ::: "memory")
#define G8_WAIT_L(n) asm volatile("s_waitcnt lgkmcnt(" #n ")" ::: "memory")
#define G8_BAR __builtin_amdgcn_s_barrier()
#define G8_SCHED __builtin_amdgcn_sched_barrier(0)
    Unit cur, nxt; int ui = 0;
    if (!S.next(0, cur)) return;
    f32x4 acc[2][2][4][2];
#pragma unroll
    for (int a = 0; a < 2; ++a)
#pragma unroll
        for (int b = 0; b < 2; ++b)
#pragma unroll
            for (int m = 0; m < 4; ++m)
#pragma unroll
                for (int n = 0; n < 2; ++n) acc[a][b][m][n] = (f32x4){0.f, 0.f, 0.f, 0.f};
    bf16x8 At[4][2], B0[2][2], B1[2][2];
    const char* cA = (const char*)cur.A; const char* cB = (const char*)cur.B;
    G8_STAGE(G8_SB(0, 0), cB, voffB); G8_STAGE(G8_SA(0, 0), cA, voffA); G8_STAGE(G8_SB(0, 1), cB + hstepB, voffB); G8_STAGE(G8_SA(0, 1), cA + hstepA, voffA);
    if (wr == 1) G8_BAR;
    G8_WAIT_V(4); G8_BAR;
    G8_STAGE(G8_SB(1, 0), cB + kstepB, voffB); G8_STAGE(G8_SA(1, 0), cA + kstepA, voffA); G8_STAGE(G8_SB(1, 1), cB + hstepB + kstepB, voffB);
    G8_WAIT_V(6); G8_BAR;
    for (;;) {
        const bool has_next = S.next(ui + 1, nxt);
        const char* nA = has_next ? (const char*)nxt.A : cA; const char* nB = has_next ? (const char*)nxt.B : cB;
        for (int t = 0; t < nt; t += 2) {
            const bool last = (t == nt - 2);
            const char* a1 = cA + (size_t)(t + 1) * kstepA;
            const char* a2 = last ? nA : cA + (size_t)(t + 2) * kstepA; const char* b2 = last ? nB : cB + (size_t)(t + 2) * kstepB;
            const char* a3 = a2 + kstepA; const char* b3 = b2 + kstepB;
            G8_LDB(B0, 0, 0); G8_SCHED; G8_LDA(At, 0, 0); G8_STAGE(G8_SA(1, 1), a1 + hstepA, voffA);
            G8_WAIT_L(8); G8_BAR; G8_WAIT_L(0); G8_MMA(0, 0, At, B0); G8_BAR; G8_SCHED;
            G8_LDB(B1, 0, 1); G8_STAGE(G8_SB(0, 0), b2, voffB);
            G8_BAR; G8_WAIT_L(0); G8_MMA(0, 1, At, B1); G8_BAR;
            G8_LDA(At, 0, 1); G8_STAGE(G8_SA(0, 0), a2, voffA);
            G8_BAR; G8_WAIT_L(0); G8_MMA(1, 0, At, B0); G8_BAR; G8_SCHED;
            G8_STAGE(G8_SB(0, 1), b2 + hstepB, voffB);
            G8_WAIT_V(6); G8_BAR; G8_MMA(1, 1, At, B1); G8_BAR;
            G8_LDB(B0, 1, 0); G8_SCHED; G8_LDA(At, 1, 0); G8_STAGE(G8_SA(0, 1), a2 + hstepA, voffA);
            G8_WAIT_L(8); G8_BAR; G8_WAIT_L(0); G8_MMA(0, 0, At, B0); G8_BAR; G8_SCHED;
            G8_LDB(B1, 1, 1); G8_STAGE(G8_SB(1, 0), b3, voffB);
            G8_BAR; G8_WAIT_L(0); G8_MMA(0, 1, At, B1); G8_BAR;
            G8_LDA(At, 1, 1); G8_STAGE(G8_SA(1, 0), a3, voffA);
            G8_BAR; G8_WAIT_L(0); G8_MMA(1, 0, At, B0); G8_BAR; G8_SCHED;
            G8_STAGE(G8_SB(1, 1), b3 + hstepB, voffB);
            G8_WAIT_V(6); G8_BAR; G8_MMA(1, 1, At, B1); G8_BAR;
        }
        E(acc, cur, wr, wc, fr, fq);
        if (!has_next) break;
#pragma unroll
        for (int a = 0; a < 2; ++a)
#pragma unroll
            for (int b = 0; b < 2; ++b)
#pragma unroll
                for (int m = 0; m < 4; ++m)
#pragma unroll
                    for (int n = 0; n < 2; ++n) acc[a][b][m][n] = (f32x4){0.f, 0.f, 0.f, 0.f};
        cur = nxt; cA = nA; cB = nB; ++ui;
    }
    G8_WAIT_V(0);
    if (wr == 0) G8_BAR;
    G8_BAR;
#undef G8_SA
#undef G8_SB
#undef G8_STAGE
#undef G8_LDA
#undef G8_LDB
#undef G8_TR
#undef G8_MMA
#undef G8_WAIT_V
#undef G8_WAIT_L
#undef G8_BAR
#undef G8_SCHED
}
}

__device__ __forceinline__ float gelu_tanh(float x) {
    const float u = 0.7978845608028654f * (x + 0.044715f * x * x * x);
    const float th = 1.0f - 2.0f * __builtin_amdgcn_rcpf(__expf(2.0f * u) + 1.0f);
    return 0.5f * x * (1.0f + th);
}

__device__ __forceinline__ void wait_counter(const char* ws, int idx, unsigned need) {
    if (threadIdx.x < 64) {
        const unsigned* p = (const unsigned*)(ws + WS_CNT + (size_t)idx * 256);
        for (unsigned spins = 0; (unsigned)__builtin_amdgcn_readfirstlane(__hip_atomic_load(p, __ATOMIC_RELAXED, __HIP_MEMORY_SCOPE_AGENT)) < need;) {
            __builtin_amdgcn_s_sleep(8);
            if (++spins > 400000u) break;
        }
        __builtin_amdgcn_fence(__ATOMIC_ACQUIRE, "agent");
        asm volatile("s_waitcnt vmcnt(0)" ::: "memory");
    }
    asm volatile("" ::: "memory"); __builtin_amdgcn_s_barrier(); asm volatile("" ::: "memory");
}
__device__ __forceinline__ void signal_counter(char* ws, int idx) {
    asm volatile("s_waitcnt vmcnt(0)" ::: "memory");
    if ((threadIdx.x & 63) == 0) __hip_atomic_fetch_add((unsigned*)(ws + WS_CNT + (size_t)idx * 256), 1u, __ATOMIC_RELAXED, __HIP_MEMORY_SCOPE_AGENT);
}

__device__ __forceinline__ unsigned poll_ge(const char* ws, int idx, unsigned need) {
    const unsigned* p = (const unsigned*)(ws + WS_CNT + (size_t)idx * 256);
    unsigned v = 0;
    for (unsigned spins = 0; (v = (unsigned)__builtin_amdgcn_readfirstlane(__hip_atomic_load(p, __ATOMIC_RELAXED, __HIP_MEMORY_SCOPE_AGENT))) < need;) {
        __builtin_amdgcn_s_sleep(8);
        if (++spins > 400000u) break;
    }
    asm volatile("" ::: "memory");
    return v;
}

constexpr unsigned SPIN_CAP = 200000u;
constexpr int KS_PITCH = 272;
constexpr int CH_LDS = 160 * 1024;

__device__ __forceinline__ float xmax16_32(float v) {
    auto a = __builtin_amdgcn_permlane16_swap(__builtin_bit_cast(unsigned, v), __builtin_bit_cast(unsigned, v), false, false);
    v = fmaxf(__builtin_bit_cast(float, (unsigned)a[0]), __builtin_bit_cast(float, (unsigned)a[1]));
    auto b = __builtin_amdgcn_permlane32_swap(__builtin_bit_cast(unsigned, v), __builtin_bit_cast(unsigned, v), false, false);
    return fmaxf(__builtin_bit_cast(float, (unsigned)b[0]), __builtin_bit_cast(float, (unsigned)b[1]));
}
__device__ __forceinline__ float xsum16_32(float v) {
    auto a = __builtin_amdgcn_permlane16_swap(__builtin_bit_cast(unsigned, v), __builtin_bit_cast(unsigned, v), false, false);
    v = __builtin_bit_cast(float, (unsigned)a[0]) + __builtin_bit_cast(float, (unsigned)a[1]);
    auto b = __builtin_amdgcn_permlane32_swap(__builtin_bit_cast(unsigned, v), __builtin_bit_cast(unsigned, v), false, false);
    return __builtin_bit_cast(float, (unsigned)b[0]) + __builtin_bit_cast(float, (unsigned)b[1]);
}

__device__ __forceinline__ u32x4 quad_gather(unsigned v) {
    u32x4 r;
    r.x = (unsigned)__builtin_amdgcn_update_dpp(0, (int)v, 0x00, 0xf, 0xf, true); r.y = (unsigned)__builtin_amdgcn_update_dpp(0, (int)v, 0x55, 0xf, 0xf, true);
    r.z = (unsigned)__builtin_amdgcn_update_dpp(0, (int)v, 0xAA, 0xf, 0xf, true); r.w = (unsigned)__builtin_amdgcn_update_dpp(0, (int)v, 0xFF, 0xf, 0xf, true);
    return r;
}
#define STAMP(i) do {} while (0)
#define PCOUNT(sh) do {} while (0)
#define CH_DELAY1 14
#define CH_DELAY2 12
#define CH_DELAY3 12

template <int CPH>
__device__ __forceinline__ void chain_body(LAS unsigned char* lds, const int b, const bf16_t* __restrict__ Wq, const bf16_t* __restrict__ Wk, const bf16_t* __restrict__ Wv,
                                           const bf16_t* __restrict__ Wo, char* ws) {
    constexpr int ES = 128 / CPH, NE = ES / 16, NT = 3 * NE, RG = CPH / 2, RPG = 8 / RG;
    constexpr int VP = ES * 2;
    constexpr int L_RED = 0;
    constexpr int L_RED2 = L_RED + 8 * NT * 512;
    constexpr int L_KS = L_RED2 + 8 * RPG * 64;
    constexpr int L_VS = L_KS + 2 * 32 * KS_PITCH;
    constexpr int L_OS = L_VS + 2 * 48 * VP;
    constexpr bool WOL = (CPH == 4);
    constexpr int L_WO = L_OS + 8 * 1024 * RPG;
    static_assert(L_WO + (WOL ? 65536 : 0) <= 156 * 1024, "LDS");
    const int tid = threadIdx.x, wave = __builtin_amdgcn_readfirstlane(tid >> 6), lane = tid & 63, fr = lane & 15, fq = lane >> 4;
    const int h = b / CPH, j = b % CPH, g = b % RG, c = b / RG;
    const __amdgpu_buffer_rsrc_t rs = __builtin_amdgcn_make_buffer_rsrc(ws + WS_SENT0, 0, (int)(WS_SENT1 - WS_SENT0), 0x00020000);
    constexpr int O_MEM = (int)(WS_MEM - WS_SENT0), O_QKVM = (int)(WS_QKVM - WS_SENT0), O_OM = (int)(WS_OM - WS_SENT0);
    const __amdgpu_buffer_rsrc_t rkv = __builtin_amdgcn_make_buffer_rsrc(ws + WS_K, 0, (int)(WS_H - WS_K), 0x00020000);
    constexpr int O_V = (int)(WS_V - WS_K);
    bool dead = false;

    bf16x8 wq[NT][4], wo[16];
    {
        LAS unsigned char* stg = lds + wave * 8192;
        constexpr int CPR = ES / 8;
        static_assert(128 * CPR / 64 == 8, "8 chunks per lane and pass");
        u32x4 t[5][8];
        auto issue = [&](int ps) {
            if (ps < 3) { const bf16_t* W = (ps == 0 ? Wq : (ps == 1 ? Wk : Wv)) + (size_t)wave * 128 * HD + h * DH + j * ES;
#pragma unroll
                for (int i = 0; i < 8; ++i) { const int ci = i * 64 + lane; t[ps][i] = *(const u32x4*)(W + (size_t)(ci / CPR) * HD + (ci % CPR) * 8); } }
            else { const bf16_t* W = Wo + (size_t)(wave * 512 + (ps - 3) * 256) * DM + c * 16;
#pragma unroll
                for (int i = 0; i < 8; ++i) { const int ci = i * 64 + lane; t[ps][i] = *(const u32x4*)(W + (size_t)(ci >> 1) * DM + (ci & 1) * 8); } }
        };
        issue(0); issue(1); issue(2);
        asm volatile("" ::: "memory");
#pragma unroll
        for (int ps = 0; ps < 5; ++ps) {
#pragma unroll
            for (int i = 0; i < 8; ++i) *(LAS u32x4*)(stg + (i * 64 + lane) * 16) = t[ps][i];
            asm volatile("s_waitcnt lgkmcnt(0)" ::: "memory");
            if (ps + 3 < 5) issue(ps + 3);
            asm volatile("" ::: "memory");
            if (ps < 3) {
#pragma unroll
                for (int e = 0; e < NE; ++e)
#pragma unroll
                    for (int i = 0; i < 4; ++i) {
                        const int off = (i * 32 + 8 * fq + (fr >> 2)) * (ES * 2) + e * 32 + (lane & 3) * 8;
                        const bf16x4 a0 = __builtin_amdgcn_ds_read_tr16_b64_v4i16((LAS bf16x4*)(stg + off)), a1 = __builtin_amdgcn_ds_read_tr16_b64_v4i16((LAS bf16x4*)(stg + off + 4 * ES * 2));
                        wq[ps * NE + e][i] = __builtin_shufflevector(a0, a1, 0, 1, 2, 3, 4, 5, 6, 7);
                    }
            } else {
#pragma unroll
                for (int i = 0; i < 8; ++i) {
                    const int off = (i * 32 + 8 * fq + (fr >> 2)) * 32 + (lane & 3) * 8;
                    const bf16x4 a0 = __builtin_amdgcn_ds_read_tr16_b64_v4i16((LAS bf16x4*)(stg + off)), a1 = __builtin_amdgcn_ds_read_tr16_b64_v4i16((LAS bf16x4*)(stg + off + 128));
                    wo[(ps - 3) * 8 + i] = __builtin_shufflevector(a0, a1, 0, 1, 2, 3, 4, 5, 6, 7);
                }
            }
            asm volatile("s_waitcnt lgkmcnt(0)" ::: "memory");
        }
        if (WOL) {
#pragma unroll
            for (int i = 8; i < 16; ++i) *(LAS bf16x8*)(lds + L_WO + (wave * 8 + i - 8) * 1024 + lane * 16) = wo[i];
        }
    }
    __syncthreads();

    if (tid < 2 * 8 * VP / 16) { const int bf = tid / (8 * VP / 16), r = tid % (8 * VP / 16); *(LAS u32x4*)(lds + L_VS + bf * 48 * VP + 40 * VP + r * 16) = (u32x4){0u, 0u, 0u, 0u}; }
    {
        poll_ge(ws, CNT_KV + (h >> 1), 16u); poll_ge(ws, CNT_KV + 16 + (h >> 1), 16u);
        const int r = tid >> 4, ch = tid & 15;
        *(LAS u32x4*)(lds + L_KS + r * KS_PITCH + ch * 16) = __builtin_amdgcn_raw_buffer_load_b128(rkv, (r * HD + h * DH + ch * 8) * 2, 0, AUX_SC1);
        if (tid < 32 * ES / 8) { const int r2 = tid / (ES / 8), c2 = tid % (ES / 8);
            *(LAS u32x4*)(lds + L_VS + (8 + r2) * VP + c2 * 16) = __builtin_amdgcn_raw_buffer_load_b128(rkv, O_V + (r2 * HD + h * DH + j * ES + c2 * 8) * 2, 0, AUX_SC1); }
    }
    __syncthreads();

    for (int s = 0; s < NSEG; ++s) {
        const int kb = s & 1;
        unsigned long long pcnt = 0; (void)pcnt;
        STAMP(0);
        {
            if (s > 0) __builtin_amdgcn_s_sleep(CH_DELAY1);
            u32x4 a[4];
            const int base = O_MEM + s * (int)MEM_B + wave * 2048 + fq * 128 + (lane & 7) * 16;
            for (unsigned spins = 0;;) {
                asm volatile("" ::: "memory");
#pragma unroll
                for (int i = 0; i < 4; ++i) a[i] = __builtin_amdgcn_raw_buffer_load_b128(rs, base + i * 512, 0, AUX_SC1);
                unsigned mxs = 0;
#pragma unroll
                for (int i = 0; i < 4; ++i) mxs = umax2(mxs, umax4(a[i]));
                if (!__any(mxs == SENT) || dead) break;
                PCOUNT(0);
                if (++spins > SPIN_CAP) { dead = true; if (lane == 0) *(volatile unsigned*)(ws + WS_TMO) = 1u; break; }
            }
            STAMP(1);
            if (b == 0 && tid == 448 && (s & 7) == 0) __hip_atomic_store((unsigned*)(ws + WS_CNT + (size_t)CNT_STEP * 256), (unsigned)s, __ATOMIC_RELAXED, __HIP_MEMORY_SCOPE_AGENT);
            f32x4 acc[NT];
#pragma unroll
            for (int t = 0; t < NT; ++t) acc[t] = (f32x4){0.f, 0.f, 0.f, 0.f};
#pragma unroll
            for (int i = 0; i < 4; ++i)
#pragma unroll
                for (int t = 0; t < NT; ++t) acc[t] = __builtin_amdgcn_mfma_f32_16x16x32_bf16(wq[t][i], __builtin_bit_cast(bf16x8, a[i]), acc[t], 0, 0, 0);
            if (fr < 8) {
#pragma unroll
                for (int t = 0; t < NT; ++t) *(LAS f32x4*)(lds + L_RED + ((wave * NT + t) * 128 + fr * 16 + fq * 4) * 4) = acc[t];
            }
        }
        __syncthreads();
        if (tid < NT * 64) {
            const int t = tid >> 6, m = (tid & 63) >> 3, np = (tid & 7) * 2, mat = t / NE, col = (t % NE) * 16 + np;
            f32x2 sum = (f32x2){0.f, 0.f};
#pragma unroll
            for (int w = 0; w < 8; ++w) sum += *(const LAS f32x2*)(lds + L_RED + ((w * NT + t) * 128 + m * 16 + np) * 4);
            const unsigned pk = desent(pk_bf16(sum.x, sum.y));
            const u32x4 q4 = quad_gather(pk);
            if ((tid & 3) == 0) __builtin_amdgcn_raw_buffer_store_b128(q4, rs, O_QKVM + s * (int)QKVM_B + ((h * 3 + mat) * 8 + m) * 256 + (j * ES + col) * 2, 0, AUX_SC1);
            if (mat == 2) *(LAS unsigned*)(lds + L_VS + kb * 48 * VP + m * VP + col * 2) = pk;
        }
        STAMP(2);
        if (s == NSEG - 1) break;
        __syncthreads();
        if (wave == 0) {
            bf16x8 kx[3][4];
#pragma unroll
            for (int t = 0; t < 3; ++t) {
                int r = t * 16 + fr - 8; r = r < 0 ? 0 : (r > 31 ? 31 : r);
#pragma unroll
                for (int i = 0; i < 4; ++i) kx[t][i] = *(const LAS bf16x8*)(lds + L_KS + kb * 32 * KS_PITCH + r * KS_PITCH + i * 64 + fq * 16);
            }
            __builtin_amdgcn_s_sleep(CH_DELAY2);
            u32x4 qf[4], km[4];
            const int base = O_QKVM + s * (int)QKVM_B + h * 3 * 2048 + (lane & 7) * 256 + fq * 16;
            for (unsigned spins = 0;;) {
                asm volatile("" ::: "memory");
#pragma unroll
                for (int i = 0; i < 4; ++i) { qf[i] = __builtin_amdgcn_raw_buffer_load_b128(rs, base + i * 64, 0, AUX_SC1); km[i] = __builtin_amdgcn_raw_buffer_load_b128(rs, base + 2048 + i * 64, 0, AUX_SC1); }
                unsigned mxs = 0;
#pragma unroll
                for (int i = 0; i < 4; ++i) mxs = umax2(mxs, umax2(umax4(qf[i]), umax4(km[i])));
                if (!__any(mxs == SENT) || dead) break;
                PCOUNT(16);
                if (++spins > SPIN_CAP) { dead = true; if (lane == 0) *(volatile unsigned*)(ws + WS_TMO) = 2u; break; }
            }
            STAMP(3);
            f32x4 sc[3];
#pragma unroll
            for (int t = 0; t < 3; ++t) sc[t] = (f32x4){0.f, 0.f, 0.f, 0.f};
#pragma unroll
            for (int i = 0; i < 4; ++i) {
                const bf16x8 k0 = fr < 8 ? __builtin_bit_cast(bf16x8, km[i]) : kx[0][i];
                sc[1] = __builtin_amdgcn_mfma_f32_16x16x32_bf16(kx[1][i], __builtin_bit_cast(bf16x8, qf[i]), sc[1], 0, 0, 0);
                sc[2] = __builtin_amdgcn_mfma_f32_16x16x32_bf16(kx[2][i], __builtin_bit_cast(bf16x8, qf[i]), sc[2], 0, 0, 0);
                sc[0] = __builtin_amdgcn_mfma_f32_16x16x32_bf16(k0, __builtin_bit_cast(bf16x8, qf[i]), sc[0], 0, 0, 0);
            }
            asm volatile("" ::: "memory");
            bf16x8 vf[NE][2];
            const bf16x4 zz = (bf16x4){0, 0, 0, 0};
#pragma unroll
            for (int et = 0; et < NE; ++et) {
                const int voff = L_VS + kb * 48 * VP + (4 * fq + (fr >> 2)) * VP + et * 32 + (lane & 3) * 8;
                const bf16x4 v0 = __builtin_amdgcn_ds_read_tr16_b64_v4i16((LAS bf16x4*)(lds + voff));
                const bf16x4 v1 = __builtin_amdgcn_ds_read_tr16_b64_v4i16((LAS bf16x4*)(lds + voff + 16 * VP));
                const bf16x4 v2 = __builtin_amdgcn_ds_read_tr16_b64_v4i16((LAS bf16x4*)(lds + voff + 32 * VP));
                vf[et][0] = __builtin_shufflevector(v0, v1, 0, 1, 2, 3, 4, 5, 6, 7); vf[et][1] = __builtin_shufflevector(v2, zz, 0, 1, 2, 3, 4, 5, 6, 7);
            }
            const float scale = 0.08838834764831845f;
            const bool hi = fr >= 8;
            float v[6];
            v[0] = hi ? sc[1][2] : sc[0][0]; v[1] = hi ? sc[1][3] : sc[0][1]; v[2] = hi ? sc[2][0] : sc[0][2];
            v[3] = hi ? sc[2][1] : sc[0][3]; v[4] = hi ? sc[2][2] : sc[1][0]; v[5] = hi ? sc[2][3] : sc[1][1];
            float mx = -INFINITY;
#pragma unroll
            for (int i = 0; i < 6; ++i) { float z = rbf(rbf(v[i]) * scale); if (i >= 2 && hi && fq >= 2) z = -INFINITY; v[i] = z; mx = fmaxf(mx, z); }
            mx = fmaxf(mx, __builtin_bit_cast(float, __builtin_amdgcn_update_dpp(0, __builtin_bit_cast(int, mx), 0x128, 0xf, 0xf, true)));
            mx = xmax16_32(mx);
            float sum = 0.f;
#pragma unroll
            for (int i = 0; i < 6; ++i) { v[i] = __expf(v[i] - mx); sum += v[i]; }
            sum += __builtin_bit_cast(float, __builtin_amdgcn_update_dpp(0, __builtin_bit_cast(int, sum), 0x128, 0xf, 0xf, true));
            sum = xsum16_32(sum);
            const float inv = 1.0f / sum;
            const unsigned w0 = pk_bf16(v[0] * inv, v[1] * inv), w1 = pk_bf16(v[2] * inv, v[3] * inv), w2 = pk_bf16(v[4] * inv, v[5] * inv);
            u32x4 p0, p1;
            p0.x = w0; p0.y = w1; p0.z = w2; p0.w = (unsigned)__builtin_amdgcn_update_dpp(0, (int)w0, 0x128, 0xf, 0xf, true);
            p1.x = (unsigned)__builtin_amdgcn_update_dpp(0, (int)w1, 0x128, 0xf, 0xf, true); p1.y = (unsigned)__builtin_amdgcn_update_dpp(0, (int)w2, 0x128, 0xf, 0xf, true); p1.z = 0u; p1.w = 0u;
#pragma unroll
            for (int et = 0; et < NE; ++et) {
                f32x4 o = (f32x4){0.f, 0.f, 0.f, 0.f};
                o = __builtin_amdgcn_mfma_f32_16x16x32_bf16(vf[et][0], __builtin_bit_cast(bf16x8, p0), o, 0, 0, 0);
                o = __builtin_amdgcn_mfma_f32_16x16x32_bf16(vf[et][1], __builtin_bit_cast(bf16x8, p1), o, 0, 0, 0);
                {
                    const unsigned wx = desent(pk_bf16(o[0], o[1])), wy = desent(pk_bf16(o[2], o[3]));
                    auto sx = __builtin_amdgcn_permlane16_swap(wx, wx, false, false); auto sy = __builtin_amdgcn_permlane16_swap(wy, wy, false, false);
                    const u32x4 w = (u32x4){(unsigned)sx[0], (unsigned)sy[0], (unsigned)sx[1], (unsigned)sy[1]};
                    const int k = h * DH + j * ES + et * 16 + 4 * fq;
                    const int off = O_OM + s * (int)OM_B + (fr / RPG) * (8192 * RPG) + (k >> 5) * (64 * RPG) + ((k >> 3) & 3) * (16 * RPG) + (fr % RPG) * 16;
                    if (fr < 8 && (fq & 1) == 0) __builtin_amdgcn_raw_buffer_store_b128(w, rs, off, 0, AUX_SC1);
                }
            }
            STAMP(4);
        } else {
            const int t2 = tid - 64; const int nb = kb ^ 1; const int row0 = (s + 1) * SEG;
            if (((s + 1) & 7) == 0) { const int blk = (s + 1) >> 3; poll_ge(ws, CNT_KV + blk * 32 + (h >> 1), 8u); poll_ge(ws, CNT_KV + blk * 32 + 16 + (h >> 1), 8u); }
            if (s + 1 == 4) { poll_ge(ws, CNT_KV0B + (h >> 1), 16u); poll_ge(ws, CNT_KV0B + 16 + (h >> 1), 16u); }
            for (int i = t2; i < 512 + 32 * ES / 8; i += 448) {
                if (i < 512) { const int r = i >> 4, ch = i & 15;
                    *(LAS u32x4*)(lds + L_KS + nb * 32 * KS_PITCH + r * KS_PITCH + ch * 16) = __builtin_amdgcn_raw_buffer_load_b128(rkv, ((row0 + r) * HD + h * DH + ch * 8) * 2, 0, AUX_SC1); }
                else { const int r2 = (i - 512) / (ES / 8), c2 = (i - 512) % (ES / 8);
                    *(LAS u32x4*)(lds + L_VS + nb * 48 * VP + (8 + r2) * VP + c2 * 16) = __builtin_amdgcn_raw_buffer_load_b128(rkv, O_V + ((row0 + r2) * HD + h * DH + j * ES + c2 * 8) * 2, 0, AUX_SC1); }
            }
        }
        __syncthreads();
        {
            if (WOL) {
#pragma unroll
                for (int i = 8; i < 16; ++i) wo[i] = *(const LAS bf16x8*)(lds + L_WO + (wave * 8 + i - 8) * 1024 + lane * 16);
            }
            __builtin_amdgcn_s_sleep(CH_DELAY3);
            u32x4 a[RPG];
            const int base = O_OM + s * (int)OM_B + g * (8192 * RPG) + wave * (1024 * RPG) + lane * 16;
            for (unsigned spins = 0;;) {
                asm volatile("" ::: "memory");
#pragma unroll
                for (int i = 0; i < RPG; ++i) a[i] = __builtin_amdgcn_raw_buffer_load_b128(rs, base + i * 1024, 0, AUX_SC1);
                unsigned mxs = 0;
#pragma unroll
                for (int i = 0; i < RPG; ++i) mxs = umax2(mxs, umax4(a[i]));
                if (!__any(mxs == SENT) || dead) break;
                PCOUNT(32);
                if (++spins > SPIN_CAP) { dead = true; if (lane == 0) *(volatile unsigned*)(ws + WS_TMO) = 3u; break; }
            }
            STAMP(5);
#pragma unroll
            for (int i = 0; i < RPG; ++i) *(LAS u32x4*)(lds + L_OS + wave * (1024 * RPG) + i * 1024 + lane * 16) = a[i];
            asm volatile("s_waitcnt lgkmcnt(0)" ::: "memory");
            f32x4 acc[4];
#pragma unroll
            for (int q = 0; q < 4; ++q) acc[q] = (f32x4){0.f, 0.f, 0.f, 0.f};
#pragma unroll
            for (int i = 0; i < 16; ++i) {
                const bf16x8 f = *(const LAS bf16x8*)(lds + L_OS + wave * (1024 * RPG) + i * (64 * RPG) + fq * (16 * RPG) + (lane % RPG) * 16);
                acc[i & 3] = __builtin_amdgcn_mfma_f32_16x16x32_bf16(wo[i], f, acc[i & 3], 0, 0, 0);
            }
            const f32x4 r = (acc[0] + acc[1]) + (acc[2] + acc[3]);
            if (fr < RPG) *(LAS f32x4*)(lds + L_RED2 + ((wave * RPG + fr) * 16 + fq * 4) * 4) = r;
        }
        __syncthreads();
        if (tid < RPG * 8) {
            const int m = tid >> 3, np = (tid & 7) * 2;
            f32x2 sum = (f32x2){0.f, 0.f};
#pragma unroll
            for (int w = 0; w < 8; ++w) sum += *(const LAS f32x2*)(lds + L_RED2 + ((w * RPG + m) * 16 + np) * 4);
            const unsigned pk = desent(pk_bf16(sum.x, sum.y));
            const int off = O_MEM + (s + 1) * (int)MEM_B + (c >> 1) * 512 + ((c & 1) * 2 + (np >> 3)) * 128 + (g * RPG + m) * 16;
            const u32x4 q4 = quad_gather(pk);
            if ((tid & 3) == 0) __builtin_amdgcn_raw_buffer_store_b128(q4, rs, off, 0, AUX_SC1);
        }
        STAMP(6);
    }
}

template <int MT, class Epi>
__device__ __forceinline__ void gemm128_w8(LAS unsigned char* lds, const bf16_t* A, const int lda, const bf16_t* B, const int ldb, const int K, const Epi& E) {
    int tid = threadIdx.x; asm volatile("" : "+v"(tid));
    const int wid = __builtin_amdgcn_readfirstlane(tid >> 6), lane = tid & 63, wr = wid >> 2, wc = wid & 3, fr = lane & 15, fq = lane >> 4;
    constexpr int NA = MT / 2;
    unsigned voffA[NA], voffB[2];
#pragma unroll
    for (int i = 0; i < NA; ++i) { int R, C; g8::stage_rc(tid * 16 + i * 8192, R, C); voffA[i] = (unsigned)(R * lda + C) * 2u; }
#pragma unroll
    for (int i = 0; i < 2; ++i) { const int bb = tid * 16 + i * 8192, row = bb >> 8, ch = (bb & 255) >> 4, seg = (ch >> 1) ^ g8::bsw(row);
        voffB[i] = (unsigned)(row * ldb + seg * 16 + (ch & 1) * 8) * 2u; }
    const char* cA = (const char*)A; const char* cB = (const char*)B;
    const unsigned ldsw = (unsigned)wid * 1024u;
    const int aoff = g8::lds_byte(wr * 16 * MT + fr, fq * 8);
    const int q4 = (lane & 15) >> 2, p4 = lane & 3, sB = q4 | ((fq & 1) << 2);
    const int boff0 = (8 * fq + q4) * 256 + (((wc * 2 + 0) ^ sB) * 32) + 8 * p4;
    const int boff1 = (8 * fq + q4) * 256 + (((wc * 2 + 1) ^ sB) * 32) + 8 * p4;
    f32x4 acc[MT][2];
#pragma unroll
    for (int m = 0; m < MT; ++m) { acc[m][0] = (f32x4){0.f, 0.f, 0.f, 0.f}; acc[m][1] = (f32x4){0.f, 0.f, 0.f, 0.f}; }
    const int nt = K / 64;
#define STG(buf, t) do { _Pragma("unroll") for (int _i = 0; _i < NA; ++_i) \
        __builtin_amdgcn_global_load_lds((const unsigned*)(cA + (size_t)(t) * 128 + voffA[_i]), (LAS unsigned*)(lds + (buf) * 32768 + ldsw + _i * 8192), 16, 0, 0); \
        _Pragma("unroll") for (int _i = 0; _i < 2; ++_i) \
        __builtin_amdgcn_global_load_lds((const unsigned*)(cB + (size_t)(t) * 64 * ldb * 2 + voffB[_i]), (LAS unsigned*)(lds + (buf) * 32768 + 16384 + ldsw + _i * 8192), 16, 0, 0); } while (0)
    STG(0, 0); STG(1, 1); STG(2, 2);
    int cur = 0;
    for (int t = 0; t < nt; ++t) {
        if (MT == 4) { if (t + 2 < nt) asm volatile("s_waitcnt vmcnt(8)" ::: "memory"); else if (t + 1 < nt) asm volatile("s_waitcnt vmcnt(4)" ::: "memory"); else asm volatile("s_waitcnt vmcnt(0)" ::: "memory"); }
        else { if (t + 2 < nt) asm volatile("s_waitcnt vmcnt(6)" ::: "memory"); else if (t + 1 < nt) asm volatile("s_waitcnt vmcnt(3)" ::: "memory"); else asm volatile("s_waitcnt vmcnt(0)" ::: "memory"); }
        __builtin_amdgcn_s_barrier();
        asm volatile("" ::: "memory");
        if (t + 3 < nt) STG((cur + 3) & 3, t + 3);
        bf16x8 At[MT][2], Bt[2][2];
        LAS unsigned char* sa = lds + cur * 32768; LAS unsigned char* sb = sa + 16384;
#pragma unroll
        for (int k = 0; k < 2; ++k) {
            bf16x4 a0 = __builtin_amdgcn_ds_read_tr16_b64_v4i16((LAS bf16x4*)(sb + boff0 + k * 8192)), a1 = __builtin_amdgcn_ds_read_tr16_b64_v4i16((LAS bf16x4*)(sb + boff0 + k * 8192 + 1024));
            bf16x4 c0 = __builtin_amdgcn_ds_read_tr16_b64_v4i16((LAS bf16x4*)(sb + boff1 + k * 8192)), c1 = __builtin_amdgcn_ds_read_tr16_b64_v4i16((LAS bf16x4*)(sb + boff1 + k * 8192 + 1024));
            Bt[0][k] = __builtin_shufflevector(a0, a1, 0, 1, 2, 3, 4, 5, 6, 7); Bt[1][k] = __builtin_shufflevector(c0, c1, 0, 1, 2, 3, 4, 5, 6, 7);
#pragma unroll
            for (int m = 0; m < MT; ++m) At[m][k] = *(const LAS bf16x8*)(sa + aoff + m * 2048 + k * 1024);
        }
        __builtin_amdgcn_s_setprio(1);
#pragma unroll
        for (int k = 0; k < 2; ++k)
#pragma unroll
            for (int m = 0; m < MT; ++m)
#pragma unroll
                for (int n = 0; n < 2; ++n) acc[m][n] = __builtin_amdgcn_mfma_f32_16x16x32_bf16(Bt[n][k], At[m][k], acc[m][n], 0, 0, 0);
        __builtin_amdgcn_s_setprio(0);
        asm volatile("s_waitcnt lgkmcnt(0)" ::: "memory");
        cur = (cur + 1) & 3;
    }
#undef STG
    __builtin_amdgcn_s_barrier();
    E(acc, wr, wc, fr, fq);
}

constexpr int XV_PITCH = 272;
template <bool INK>
__device__ __forceinline__ void xattn_item(LAS unsigned char* vs, char* ws, const int s, const int h) {
    const int lane = threadIdx.x & 63, fr = lane & 15, fq = lane >> 4;
    const bf16_t* Q = (const bf16_t*)(ws + WS_Q) + (size_t)s * SEG * HD + h * DH;
    const bf16_t* K = (const bf16_t*)(ws + WS_K) + (size_t)s * SEG * HD + h * DH;
    const bf16_t* V = (const bf16_t*)(ws + WS_V) + (size_t)s * SEG * HD + h * DH;
    const __amdgpu_buffer_rsrc_t rm = __builtin_amdgcn_make_buffer_rsrc(ws + WS_QKVM, 0, (int)(WS_OM - WS_QKVM), 0x00020000);
    const int kmo = s * (int)QKVM_B + (h * 3 + 1) * 2048, vmo = kmo + 2048;
    u32x4 km[4], vm[2];
    for (unsigned spins = 0;;) {
        asm volatile("" ::: "memory");
#pragma unroll
        for (int i = 0; i < 4; ++i) km[i] = __builtin_amdgcn_raw_buffer_load_b128(rm, kmo + (lane & 7) * 256 + i * 64 + fq * 16, 0, INK ? AUX_SC1 : 0);
#pragma unroll
        for (int i = 0; i < 2; ++i) vm[i] = __builtin_amdgcn_raw_buffer_load_b128(rm, vmo + (i * 4 + fq) * 256 + fr * 16, 0, INK ? AUX_SC1 : 0);
        if (!INK) break;
        unsigned mxs = umax2(umax4(vm[0]), umax4(vm[1]));
#pragma unroll
        for (int i = 0; i < 4; ++i) mxs = umax2(mxs, umax4(km[i]));
        if (!__any(mxs == SENT)) break;
        __builtin_amdgcn_s_sleep(16);
        if (++spins > SPIN_CAP) break;
    }
#pragma unroll
    for (int i = 0; i < 12; ++i) {
        const int r = i * 4 + fq;
        u32x4 v = (u32x4){0u, 0u, 0u, 0u};
        if (i < 2) v = vm[i]; else if (i < 10) v = *(const u32x4*)(V + (size_t)(r - 8) * HD + fr * 8);
        *(LAS u32x4*)(vs + r * XV_PITCH + fr * 16) = v;
    }
    f32x4 sc[3][2];
#pragma unroll
    for (int t = 0; t < 3; ++t) { sc[t][0] = (f32x4){0.f, 0.f, 0.f, 0.f}; sc[t][1] = (f32x4){0.f, 0.f, 0.f, 0.f}; }
#pragma unroll
    for (int i = 0; i < 4; ++i) {
        const bf16x8 q0 = *(const bf16x8*)(Q + (size_t)fr * HD + i * 32 + fq * 8), q1 = *(const bf16x8*)(Q + (size_t)(16 + fr) * HD + i * 32 + fq * 8);
#pragma unroll
        for (int t = 0; t < 3; ++t) {
            int r = t * 16 + fr - 8; r = r < 0 ? 0 : (r > 31 ? 31 : r);
            bf16x8 kf = *(const bf16x8*)(K + (size_t)r * HD + i * 32 + fq * 8);
            if (t == 0 && fr < 8) kf = __builtin_bit_cast(bf16x8, km[i]);
            sc[t][0] = __builtin_amdgcn_mfma_f32_16x16x32_bf16(kf, q0, sc[t][0], 0, 0, 0);
            sc[t][1] = __builtin_amdgcn_mfma_f32_16x16x32_bf16(kf, q1, sc[t][1], 0, 0, 0);
        }
    }
    const float scale = 0.08838834764831845f, NEG = rbf(-1000000000.0f);
    u32x4 p0[2], p1[2];
#pragma unroll
    for (int rt = 0; rt < 2; ++rt) {
        const int row = rt * 16 + fr;
        float v[12]; float mx = -INFINITY;
#pragma unroll
        for (int t = 0; t < 3; ++t)
#pragma unroll
            for (int r = 0; r < 4; ++r) { const int key = t * 16 + fq * 4 + r; float z = rbf(rbf(sc[t][rt][r]) * scale);
                if (key - 8 > row) z = NEG; if (key >= 40) z = -INFINITY; v[t * 4 + r] = z; mx = fmaxf(mx, z); }
        mx = xmax16_32(mx);
        float sum = 0.f;
#pragma unroll
        for (int i = 0; i < 12; ++i) { v[i] = __expf(v[i] - mx); sum += v[i]; }
        sum = xsum16_32(sum);
        const float inv = 1.0f / sum;
        p0[rt].x = pk_bf16(v[0] * inv, v[1] * inv); p0[rt].y = pk_bf16(v[2] * inv, v[3] * inv); p0[rt].z = pk_bf16(v[4] * inv, v[5] * inv); p0[rt].w = pk_bf16(v[6] * inv, v[7] * inv);
        p1[rt].x = pk_bf16(v[8] * inv, v[9] * inv); p1[rt].y = pk_bf16(v[10] * inv, v[11] * inv); p1[rt].z = 0u; p1[rt].w = 0u;
    }
    asm volatile("s_waitcnt lgkmcnt(0)" ::: "memory");
    const __amdgpu_buffer_rsrc_t ro = __builtin_amdgcn_make_buffer_rsrc(ws + WS_O, 0, S_LEN * HD * 2, 0x00020000);
    const int obase = (s * SEG * HD + h * DH) * 2;
    const bf16x4 zz = (bf16x4){0, 0, 0, 0};
    u32x2 ow[8][2];
#pragma unroll
    for (int et = 0; et < 8; ++et) {
        const int voff = (4 * fq + (fr >> 2)) * XV_PITCH + et * 32 + (lane & 3) * 8;
        const bf16x4 v0 = __builtin_amdgcn_ds_read_tr16_b64_v4i16((LAS bf16x4*)(vs + voff));
        const bf16x4 v1 = __builtin_amdgcn_ds_read_tr16_b64_v4i16((LAS bf16x4*)(vs + voff + 16 * XV_PITCH));
        const bf16x4 v2 = __builtin_amdgcn_ds_read_tr16_b64_v4i16((LAS bf16x4*)(vs + voff + 32 * XV_PITCH));
        const bf16x8 va = __builtin_shufflevector(v0, v1, 0, 1, 2, 3, 4, 5, 6, 7), vb = __builtin_shufflevector(v2, zz, 0, 1, 2, 3, 4, 5, 6, 7);
#pragma unroll
        for (int rt = 0; rt < 2; ++rt) {
            f32x4 o = (f32x4){0.f, 0.f, 0.f, 0.f};
            o = __builtin_amdgcn_mfma_f32_16x16x32_bf16(va, __builtin_bit_cast(bf16x8, p0[rt]), o, 0, 0, 0);
            o = __builtin_amdgcn_mfma_f32_16x16x32_bf16(vb, __builtin_bit_cast(bf16x8, p1[rt]), o, 0, 0, 0);
            ow[et][rt].x = pk_bf16(o[0], o[1]); ow[et][rt].y = pk_bf16(o[2], o[3]);
        }
    }
    asm volatile("s_waitcnt lgkmcnt(0)" ::: "memory");
#pragma unroll
    for (int et = 0; et < 8; ++et)
#pragma unroll
        for (int rt = 0; rt < 2; ++rt) *(LAS u32x2*)(vs + (rt * 16 + fr) * XV_PITCH + et * 32 + fq * 8) = ow[et][rt];
    asm volatile("s_waitcnt lgkmcnt(0)" ::: "memory");
#pragma unroll
    for (int i = 0; i < 8; ++i) {
        const int r = i * 4 + fq;
        const u32x4 w = *(const LAS u32x4*)(vs + r * XV_PITCH + fr * 16);
        __builtin_amdgcn_raw_buffer_store_b128(w, ro, obase + (r * HD + fr * 8) * 2, 0, INK ? AUX_SC1 : 0);
    }
    asm volatile("s_waitcnt lgkmcnt(0)" ::: "memory");
}

struct SchedOne {
    g8::Unit u; bool valid;
    __device__ __forceinline__ bool next(int i, g8::Unit& o) const { if (i != 0 || !valid) return false; o = u; return true; }
};
struct SchedKVQ {
    const bf16_t* A; const bf16_t* Wq; long dk, dv; int r;
    __device__ __forceinline__ bool next(int i, g8::Unit& u) const {
        const int L = r + 128 * i; if (L >= 352) return false;
        int pm, pn;
        if (L < 224) { pm = 1 + (L >> 5); pn = 16 + (L & 31); } else { pm = (L - 224) & 7; pn = (L - 224) >> 3; }
        const int mat = pn >> 4;
        u.A = A + (size_t)pm * 256 * DM; u.B = Wq + (mat == 1 ? dk : 0L) + (mat == 2 ? dv : 0L) + (pn & 15) * 256; u.pm = pm; u.pn = pn; return true;
    }
};
struct EpiQKV {
    char* ws;
    __device__ __forceinline__ void operator()(const f32x4 (&acc)[2][2][4][2], const g8::Unit& u, int wr, int wc, int fr, int fq) const {
        const __amdgpu_buffer_rsrc_t rq = __builtin_amdgcn_make_buffer_rsrc(ws + WS_Q, 0, (int)(WS_H - WS_Q), 0x00020000);
        const int base = (u.pn >> 4) * (S_LEN * HD * 2) + ((u.pm * 256 + wr * 64 + fr) * HD + (u.pn & 15) * 256 + wc * 32 + 4 * fq) * 2;
#pragma unroll
        for (int ai = 0; ai < 2; ++ai)
#pragma unroll
            for (int m = 0; m < 4; ++m)
#pragma unroll
                for (int bj = 0; bj < 2; ++bj)
#pragma unroll
                    for (int n = 0; n < 2; ++n) { const f32x4 v = acc[ai][bj][m][n]; u32x2 w; w.x = pk_bf16(v[0], v[1]); w.y = pk_bf16(v[2], v[3]);
                        __builtin_amdgcn_raw_buffer_store_b64(w, rq, base + ((ai * 128 + m * 16) * HD + bj * 128 + n * 16) * 2, 0, AUX_SC1); }
        asm volatile("s_waitcnt vmcnt(0)" ::: "memory");
        if ((threadIdx.x & 63) == 0) {
            if (u.pn >= 16) __hip_atomic_fetch_add((unsigned*)(ws + WS_CNT + (size_t)(CNT_KV + u.pm * 32 + u.pn - 16) * 256), 1u, __ATOMIC_RELAXED, __HIP_MEMORY_SCOPE_AGENT);
            __hip_atomic_fetch_add((unsigned*)(ws + WS_CNT + (size_t)(CNT_QKV + u.pm) * 256), 1u, __ATOMIC_RELAXED, __HIP_MEMORY_SCOPE_AGENT);
        }
    }
};
struct EpiKV0 {
    char* ws; int pm2, pn2;
    __device__ __forceinline__ void operator()(const f32x4 (&acc)[4][2], int wr, int wc, int fr, int fq) const {
        const __amdgpu_buffer_rsrc_t rq = __builtin_amdgcn_make_buffer_rsrc(ws + WS_K, 0, (int)(WS_H - WS_K), 0x00020000);
        const int base = (pn2 >> 5) * (S_LEN * HD * 2) + ((pm2 * 128 + wr * 64 + fr) * HD + (pn2 & 31) * 128 + wc * 32 + 4 * fq) * 2;
#pragma unroll
        for (int m = 0; m < 4; ++m)
#pragma unroll
            for (int n = 0; n < 2; ++n) { const f32x4 v = acc[m][n]; u32x2 w; w.x = pk_bf16(v[0], v[1]); w.y = pk_bf16(v[2], v[3]);
                __builtin_amdgcn_raw_buffer_store_b64(w, rq, base + (m * 16 * HD + n * 16) * 2, 0, AUX_SC1); }
        asm volatile("s_waitcnt vmcnt(0)" ::: "memory");
        if ((threadIdx.x & 63) == 0) {
            __hip_atomic_fetch_add((unsigned*)(ws + WS_CNT + (size_t)((pm2 == 0 ? CNT_KV : CNT_KV0B) + (pn2 >> 1)) * 256), 1u, __ATOMIC_RELAXED, __HIP_MEMORY_SCOPE_AGENT);
            __hip_atomic_fetch_add((unsigned*)(ws + WS_CNT + (size_t)CNT_QKV * 256), 1u, __ATOMIC_RELAXED, __HIP_MEMORY_SCOPE_AGENT);
        }
    }
};
struct EpiMLP1 {
    char* ws; const bf16_t* b1;
    __device__ __forceinline__ void operator()(const f32x4 (&acc)[2][2][4][2], const g8::Unit& u, int wr, int wc, int fr, int fq) const {
        const __amdgpu_buffer_rsrc_t rh = __builtin_amdgcn_make_buffer_rsrc(ws + WS_H, 0, S_LEN * FF * 2, 0x00020000);
        const int row0 = u.pm * 256 + wr * 64 + fr, col0 = u.pn * 256 + wc * 32 + 4 * fq;
#pragma unroll
        for (int bj = 0; bj < 2; ++bj)
#pragma unroll
            for (int n = 0; n < 2; ++n) {
                const u32x2 bw = *(const u32x2*)(b1 + col0 + bj * 128 + n * 16);
                const float b0 = bf2f(bw.x & 0xffffu), b1f = bf2f(bw.x >> 16), b2f = bf2f(bw.y & 0xffffu), b3 = bf2f(bw.y >> 16);
#pragma unroll
                for (int ai = 0; ai < 2; ++ai)
#pragma unroll
                    for (int m = 0; m < 4; ++m) { const f32x4 v = acc[ai][bj][m][n];
                        const float g0 = gelu_tanh(rbf(rbf(v[0]) + b0)), g1 = gelu_tanh(rbf(rbf(v[1]) + b1f)), g2 = gelu_tanh(rbf(rbf(v[2]) + b2f)), g3 = gelu_tanh(rbf(rbf(v[3]) + b3));
                        u32x2 w; w.x = pk_bf16(g0, g1); w.y = pk_bf16(g2, g3);
                        __builtin_amdgcn_raw_buffer_store_b64(w, rh, ((row0 + ai * 128 + m * 16) * FF + col0 + bj * 128 + n * 16) * 2, 0, AUX_SC1); }
                asm volatile("" ::: "memory");
            }
        signal_counter(ws, CNT_H + u.pm);
    }
};
struct EpiMLP2 {
    char* ws; const bf16_t* b2; int row0, col0;
    __device__ __forceinline__ void operator()(const f32x4 (&acc)[4][2], int wr, int wc, int fr, int fq) const {
        const __amdgpu_buffer_rsrc_t rb = __builtin_amdgcn_make_buffer_rsrc(ws + WS_B, 0, S_LEN * DM * 2, 0x00020000);
#pragma unroll
        for (int n = 0; n < 2; ++n) {
            const int col = col0 + wc * 32 + n * 16 + 4 * fq;
            const u32x2 bw = *(const u32x2*)(b2 + col);
#pragma unroll
            for (int m = 0; m < 4; ++m) { const f32x4 v = acc[m][n];
                u32x2 w; w.x = pk_bf16(rbf(v[0]) + bf2f(bw.x & 0xffffu), rbf(v[1]) + bf2f(bw.x >> 16)); w.y = pk_bf16(rbf(v[2]) + bf2f(bw.y & 0xffffu), rbf(v[3]) + bf2f(bw.y >> 16));
                __builtin_amdgcn_raw_buffer_store_b64(w, rb, ((row0 + wr * 64 + m * 16 + fr) * DM + col) * 2, 0, AUX_SC1); }
        }
        signal_counter(ws, CNT_B + (row0 >> 8));
    }
};
__device__ __forceinline__ u32x2 combine4(const f32x4 v, const u32x2 xw, const u32x2 bw) {
    u32x2 w;
    w.x = pk_bf16(rbf(bf2f(xw.x & 0xffffu) + rbf(v[0])) + bf2f(bw.x & 0xffffu), rbf(bf2f(xw.x >> 16) + rbf(v[1])) + bf2f(bw.x >> 16));
    w.y = pk_bf16(rbf(bf2f(xw.y & 0xffffu) + rbf(v[2])) + bf2f(bw.y & 0xffffu), rbf(bf2f(xw.y >> 16) + rbf(v[3])) + bf2f(bw.y >> 16));
    return w;
}
struct EpiOut {
    const bf16_t* x; const bf16_t* bout; bf16_t* out; int row0, col0;
    __device__ __forceinline__ void operator()(const f32x4 (&acc)[4][2], int wr, int wc, int fr, int fq) const {
#pragma unroll
        for (int m = 0; m < 4; ++m)
#pragma unroll
            for (int n = 0; n < 2; ++n) {
                const size_t off = (size_t)(row0 + wr * 64 + m * 16 + fr) * DM + col0 + wc * 32 + n * 16 + 4 * fq;
                *(u32x2*)(out + off) = combine4(acc[m][n], *(const u32x2*)(x + off), *(const u32x2*)(bout + off));
            }
    }
};

constexpr int TAIL_BLK0 = 6;
__global__ void __launch_bounds__(512, 2) k_mega(const bf16_t* __restrict__ x, const bf16_t* __restrict__ Wq, const bf16_t* __restrict__ Wk, const bf16_t* __restrict__ Wv,
                                                 const bf16_t* __restrict__ Wo, const bf16_t* __restrict__ w1, const bf16_t* __restrict__ b1, const bf16_t* __restrict__ w2,
                                                 const bf16_t* __restrict__ b2, char* ws, bf16_t* __restrict__ out) {
    extern __shared__ __attribute__((aligned(16))) unsigned char shm_[];
    LAS unsigned char* lds = (LAS unsigned char*)shm_;
    const int b = blockIdx.x;
    const int role = (b >> 3) & 1, r = (b >> 4) * 8 + (b & 7);
    if (role == 0) { chain_body<4>(lds, r, Wq, Wk, Wv, Wo, ws); return; }
#define WSTAMP(i) do {} while (0)
    WSTAMP(0);
    {
        const int pm2 = r & 1, pn2 = r >> 1;
        EpiKV0 E{ws, pm2, pn2};
        gemm128_w8<4, EpiKV0>(lds, (const bf16_t*)(ws + WS_XN) + (size_t)pm2 * 128 * DM, DM, (pn2 >= 32 ? Wv : Wk) + (pn2 & 31) * 128, HD, DM, E);
    }
    WSTAMP(1);
    {
        SchedKVQ S{(const bf16_t*)(ws + WS_XN), Wq, (long)(Wk - Wq), (long)(Wv - Wq), r};
        EpiQKV E{ws}; g8::gemm_phase<EpiQKV, SchedKVQ>(lds, DM, DM, HD, S, E);
    }
    WSTAMP(2);
    {
        const int L = r >= 96 ? r - 96 : 32 + r;
        SchedOne S; S.valid = (r >= 96) || (r < 80); S.u.pm = L & 7; S.u.pn = L >> 3; S.u.A = (const bf16_t*)(ws + WS_Y) + (size_t)S.u.pm * 256 * DM; S.u.B = w1 + S.u.pn * 256;
        EpiMLP1 E{ws, b1}; g8::gemm_phase<EpiMLP1, SchedOne>(lds, DM, DM, FF, S, E);
    }
    WSTAMP(3);
    {
        const int pm = r >> 3, pn = r & 7;
        wait_counter(ws, CNT_H + (pm >> 1), 14 * 8);
        EpiMLP2 E{ws, b2, pm * 128, pn * 128};
        gemm128_w8<4, EpiMLP2>(lds, (const bf16_t*)(ws + WS_H) + (size_t)pm * 128 * FF, FF, w2 + pn * 128, DM, FF, E);
    }
    WSTAMP(4);
    const int blk = r >> 4, t = r & 15;
    if (blk >= TAIL_BLK0) return;
    {
        if (threadIdx.x < 64) poll_ge(ws, CNT_STEP, (unsigned)(8 * blk + 8));
        wait_counter(ws, CNT_QKV + blk, (blk == 0 ? 128 * 8 : 32 * 8) + 16 * 8);
        WSTAMP(5);
        const int wave = threadIdx.x >> 6;
#pragma unroll 1
        for (int i = 0; i < 2; ++i) { const int item = t * 16 + wave * 2 + i; xattn_item<true>(lds + wave * 48 * XV_PITCH, ws, blk * 8 + (item >> 5), item & 31); }
        signal_counter(ws, CNT_O + blk);
    }
    {
        if (threadIdx.x < 64) poll_ge(ws, CNT_B + blk, 16 * 8);
        wait_counter(ws, CNT_O + blk, 16 * 8);
        WSTAMP(6);
        const int pm = blk * 2 + (t >> 3), pn = t & 7;
        EpiOut E{x, (const bf16_t*)(ws + WS_B), out, pm * 128, pn * 128};
        gemm128_w8<4, EpiOut>(lds, (const bf16_t*)(ws + WS_O) + (size_t)pm * 128 * HD, HD, Wo + pn * 128, DM, HD, E);
    }
    WSTAMP(7);
}

__global__ void __launch_bounds__(256) k_tail_attn(char* ws) {
    __shared__ __attribute__((aligned(16))) unsigned char shm_[4 * 48 * XV_PITCH];
    const int wave = threadIdx.x >> 6, item = blockIdx.x * 4 + wave;
    xattn_item<false>((LAS unsigned char*)shm_ + wave * 48 * XV_PITCH, ws, TAIL_BLK0 * 8 + (item >> 5), item & 31);
}
__global__ void __launch_bounds__(256) k_tail_gemm(const bf16_t* __restrict__ x, const bf16_t* __restrict__ Wo, const char* __restrict__ ws, bf16_t* __restrict__ out) {
    extern __shared__ __attribute__((aligned(16))) unsigned char shm_[];
    const int tid = threadIdx.x, wave = __builtin_amdgcn_readfirstlane(tid >> 6), lane = tid & 63, fr = lane & 15, fq = lane >> 4;
    LAS unsigned char* lds = (LAS unsigned char*)shm_ + wave * 24576;
    const int pn = blockIdx.x & 31, pm = blockIdx.x >> 5, row0 = TAIL_BLK0 * 256 + pm * 32, col0 = pn * 32;
    const char* cA = (const char*)((const bf16_t*)(ws + WS_O) + (size_t)row0 * HD + wave * 1024);
    const char* cB = (const char*)(Wo + (size_t)wave * 1024 * DM + col0);
    unsigned voffA[4], voffB[4];
#pragma unroll
    for (int i = 0; i < 4; ++i) { const int c = i * 64 + lane, row = c >> 3, ch = (c & 7) ^ (row & 7); voffA[i] = (unsigned)(row * HD + ch * 8) * 2u;
        const int k = c >> 2, cb = c & 3; voffB[i] = (unsigned)(k * DM + cb * 8) * 2u; }
#define STG(buf, t) do { _Pragma("unroll") for (int _i = 0; _i < 4; ++_i) { \
        __builtin_amdgcn_global_load_lds((const unsigned*)(cA + (size_t)(t) * 128 + voffA[_i]), (LAS unsigned*)(lds + (buf) * 8192 + _i * 1024), 16, 0, 0); \
        __builtin_amdgcn_global_load_lds((const unsigned*)(cB + (size_t)(t) * 64 * DM * 2 + voffB[_i]), (LAS unsigned*)(lds + (buf) * 8192 + 4096 + _i * 1024), 16, 0, 0); } } while (0)
    f32x4 acc[2][2];
#pragma unroll
    for (int m = 0; m < 2; ++m) { acc[m][0] = (f32x4){0.f, 0.f, 0.f, 0.f}; acc[m][1] = (f32x4){0.f, 0.f, 0.f, 0.f}; }
    STG(0, 0); STG(1, 1);
    int cur = 0;
    for (int t = 0; t < 16; ++t) {
        int nx = cur + 2; if (nx >= 3) nx -= 3;
        if (t + 2 < 16) { STG(nx, t + 2); asm volatile("s_waitcnt vmcnt(16)" ::: "memory"); }
        else if (t + 1 < 16) asm volatile("s_waitcnt vmcnt(8)" ::: "memory");
        else asm volatile("s_waitcnt vmcnt(0)" ::: "memory");
        LAS unsigned char* sa = lds + cur * 8192; LAS unsigned char* sb = sa + 4096;
        bf16x8 At[2][2], Bt[2][2];
#pragma unroll
        for (int k = 0; k < 2; ++k) {
#pragma unroll
            for (int m = 0; m < 2; ++m) { const int row = m * 16 + fr; At[m][k] = *(const LAS bf16x8*)(sa + row * 128 + (((k * 4 + fq) ^ (row & 7)) * 16)); }
#pragma unroll
            for (int n = 0; n < 2; ++n) {
                const int off = (k * 32 + 8 * fq + (fr >> 2)) * 64 + n * 32 + (lane & 3) * 8;
                const bf16x4 a0 = __builtin_amdgcn_ds_read_tr16_b64_v4i16((LAS bf16x4*)(sb + off)), a1 = __builtin_amdgcn_ds_read_tr16_b64_v4i16((LAS bf16x4*)(sb + off + 256));
                Bt[n][k] = __builtin_shufflevector(a0, a1, 0, 1, 2, 3, 4, 5, 6, 7); }
        }
#pragma unroll
        for (int k = 0; k < 2; ++k)
#pragma unroll
            for (int m = 0; m < 2; ++m)
#pragma unroll
                for (int n = 0; n < 2; ++n) acc[m][n] = __builtin_amdgcn_mfma_f32_16x16x32_bf16(Bt[n][k], At[m][k], acc[m][n], 0, 0, 0);
        asm volatile("s_waitcnt lgkmcnt(0)" ::: "memory");
        cur = cur + 1 == 3 ? 0 : cur + 1;
    }
#undef STG
    __syncthreads();
    LAS f32x4* red = (LAS f32x4*)shm_;
#pragma unroll
    for (int m = 0; m < 2; ++m)
#pragma unroll
        for (int n = 0; n < 2; ++n) red[((wave * 2 + m) * 2 + n) * 64 + lane] = acc[m][n];
    __syncthreads();
    {
        const int m = wave >> 1, n = wave & 1;
        const f32x4 v = (red[((0 * 2 + m) * 2 + n) * 64 + lane] + red[((1 * 2 + m) * 2 + n) * 64 + lane]) + (red[((2 * 2 + m) * 2 + n) * 64 + lane] + red[((3 * 2 + m) * 2 + n) * 64 + lane]);
        const size_t off = (size_t)(row0 + m * 16 + fr) * DM + col0 + n * 16 + 4 * fq;
        *(u32x2*)(out + off) = combine4(v, *(const u32x2*)(x + off), *(const u32x2*)((const bf16_t*)(ws + WS_B) + off));
    }
}

extern "C" void kernel_launch(void* const* d_in, const int* in_sizes, int n_in, void* d_out, int out_size, void* d_ws, size_t ws_size, hipStream_t stream) {
    const bf16_t* x = (const bf16_t*)d_in[0];
    const bf16_t* mem0 = (const bf16_t*)d_in[1];
    const bf16_t* Wq = (const bf16_t*)d_in[2];
    const bf16_t* Wk = (const bf16_t*)d_in[3];
    const bf16_t* Wv = (const bf16_t*)d_in[4];
    const bf16_t* Wo = (const bf16_t*)d_in[5];
    const bf16_t* w1 = (const bf16_t*)d_in[8];
    const bf16_t* b1 = (const bf16_t*)d_in[9];
    const bf16_t* w2 = (const bf16_t*)d_in[10];
    const bf16_t* b2 = (const bf16_t*)d_in[11];
    char* ws = (char*)d_ws;
    bf16_t* out = (bf16_t*)d_out;
    (void)hipFuncSetAttribute((const void*)k_mega, hipFuncAttributeMaxDynamicSharedMemorySize, CH_LDS);
    (void)hipFuncSetAttribute((const void*)k_tail_gemm, hipFuncAttributeMaxDynamicSharedMemorySize, 4 * 24576);
    k_prep<<<S_LEN / 4, 256, 0, stream>>>(x, mem0, ws);
    k_mega<<<256, 512, CH_LDS, stream>>>(x, Wq, Wk, Wv, Wo, w1, b1, w2, b2, ws, out);
    k_tail_attn<<<64 * (8 - TAIL_BLK0), 256, 0, stream>>>(ws);
    k_tail_gemm<<<256 * (8 - TAIL_BLK0), 256, 4 * 24576, stream>>>(x, Wo, ws, out);
}
```

```cpp
#include <hip/hip_runtime.h>
#include <hip/hip_bf16.h>

#define LAS __attribute__((address_space(3)))
typedef unsigned short bf16_t;
typedef short bf16x8 __attribute__((ext_vector_type(8)));
typedef short bf16x4 __attribute__((ext_vector_type(4)));
typedef float f32x4 __attribute__((ext_vector_type(4)));
typedef float f32x2 __attribute__((ext_vector_type(2)));
typedef unsigned u32x4 __attribute__((ext_vector_type(4)));
typedef unsigned u32x2 __attribute__((ext_vector_type(2)));

constexpr int S_LEN = 2048, DM = 1024, NH = 32, DH = 128, HD = NH * DH, MM = 8, SEG = 32, NSEG = S_LEN / SEG, FF = 3584;
constexpr int AUX_SC1 = 16;
constexpr unsigned SENT = 0xFFFFFFFFu;

__device__ __forceinline__ float bf2f(unsigned b) { return __builtin_bit_cast(float, b << 16); }
typedef __bf16 bf16x2_t __attribute__((ext_vector_type(2)));
__device__ __forceinline__ unsigned pk_bf16(float lo, float hi) { const f32x2 v = {lo, hi}; return __builtin_bit_cast(unsigned, __builtin_convertvector(v, bf16x2_t)); }
__device__ __forceinline__ float rbf(float x) { return bf2f(pk_bf16(x, x) & 0xffffu); }
__device__ __forceinline__ unsigned desent(unsigned w) { return w == SENT ? 0x7FC07FC0u : w; }
__device__ __forceinline__ unsigned umax2(unsigned a, unsigned b) { return a > b ? a : b; }
__device__ __forceinline__ unsigned umax4(u32x4 v) { return umax2(umax2(v.x, v.y), umax2(v.z, v.w)); }

constexpr size_t WS_XN = 0;
constexpr size_t WS_Y = WS_XN + (size_t)S_LEN * DM * 2;
constexpr size_t WS_Q = WS_Y + (size_t)S_LEN * DM * 2;
constexpr size_t WS_K = WS_Q + (size_t)S_LEN * HD * 2;
constexpr size_t WS_V = WS_K + (size_t)S_LEN * HD * 2;
constexpr size_t WS_H = WS_V + (size_t)S_LEN * HD * 2;
constexpr size_t WS_O = WS_H + (size_t)S_LEN * FF * 2;
constexpr size_t WS_B = WS_O + (size_t)S_LEN * HD * 2;
constexpr size_t WS_SENT0 = WS_B + (size_t)S_LEN * DM * 2;
constexpr size_t MEM_B = (size_t)MM * DM * 2;
constexpr size_t WS_MEM = WS_SENT0;
constexpr size_t QKVM_B = (size_t)NH * 3 * MM * DH * 2;
constexpr size_t WS_QKVM = WS_MEM + (size_t)(NSEG + 1) * MEM_B;
constexpr size_t OM_B = (size_t)MM * HD * 2;
constexpr size_t WS_OM = WS_QKVM + (size_t)NSEG * QKVM_B;
constexpr size_t WS_SENT1 = WS_OM + (size_t)NSEG * OM_B;
constexpr size_t WS_CNT = WS_SENT1;
constexpr int CNT_H = 0;
constexpr int CNT_B = 8;
constexpr int CNT_O = 16;
constexpr int CNT_QKV = 24;
constexpr int CNT_FILL = 33;
constexpr int CNT_STEP = 32;
constexpr int CNT_KV = 40;
constexpr int CNT_KV0B = 40 + 256;
constexpr int CNT_N = 40 + 256 + 32;
constexpr size_t WS_TMO = WS_CNT + (size_t)CNT_N * 256;
constexpr size_t WS_END = WS_TMO + 256;

__device__ __forceinline__ float wave_sum(float v) {
#pragma unroll
    for (int o = 32; o > 0; o >>= 1) v += __shfl_xor(v, o);
    return v;
}
__device__ __forceinline__ void ln_row(float (&v)[16]) {
    float s = 0.f;
#pragma unroll
    for (int i = 0; i < 16; ++i) s += v[i];
    const float mean = wave_sum(s) * (1.0f / DM);
    float q = 0.f;
#pragma unroll
    for (int i = 0; i < 16; ++i) { const float d = v[i] - mean; q += d * d; }
    const float var = rbf(wave_sum(q) * (1.0f / DM));
    const float mb = rbf(mean);
    const float den = rbf(sqrtf(rbf(var + 1e-5f)));
#pragma unroll
    for (int i = 0; i < 16; ++i) v[i] = rbf(rbf(v[i] - mb) / den);
}
__global__ void __launch_bounds__(256) k_prep(const bf16_t* __restrict__ x, const bf16_t* __restrict__ mem0, char* __restrict__ ws) {
    const int wave = threadIdx.x >> 6, lane = threadIdx.x & 63, row = blockIdx.x * 4 + wave;
    const u32x4* src = (const u32x4*)(x + (size_t)row * DM);
    u32x4 a = src[lane], b = src[lane + 64];
    float v[16];
    const unsigned w[8] = {a.x, a.y, a.z, a.w, b.x, b.y, b.z, b.w};
#pragma unroll
    for (int i = 0; i < 8; ++i) { v[2 * i] = bf2f(w[i] & 0xffffu); v[2 * i + 1] = bf2f(w[i] >> 16); }
    ln_row(v);
    u32x4 o0, o1;
    o0.x = pk_bf16(v[0], v[1]); o0.y = pk_bf16(v[2], v[3]); o0.z = pk_bf16(v[4], v[5]); o0.w = pk_bf16(v[6], v[7]);
    o1.x = pk_bf16(v[8], v[9]); o1.y = pk_bf16(v[10], v[11]); o1.z = pk_bf16(v[12], v[13]); o1.w = pk_bf16(v[14], v[15]);
    u32x4* dx = (u32x4*)(ws + WS_XN + (size_t)row * DM * 2);
    dx[lane] = o0; dx[lane + 64] = o1;
    ln_row(v);
    o0.x = pk_bf16(v[0], v[1]); o0.y = pk_bf16(v[2], v[3]); o0.z = pk_bf16(v[4], v[5]); o0.w = pk_bf16(v[6], v[7]);
    o1.x = pk_bf16(v[8], v[9]); o1.y = pk_bf16(v[10], v[11]); o1.z = pk_bf16(v[12], v[13]); o1.w = pk_bf16(v[14], v[15]);
    u32x4* dy = (u32x4*)(ws + WS_Y + (size_t)row * DM * 2);
    dy[lane] = o0; dy[lane + 64] = o1;
    {
        const size_t n16 = (WS_SENT1 - WS_SENT0 - MEM_B) / 16;
        u32x4* dst = (u32x4*)(ws + WS_SENT0 + MEM_B);
        const u32x4 f = (u32x4){SENT, SENT, SENT, SENT};
        for (size_t i = (size_t)blockIdx.x * 256 + threadIdx.x; i < n16; i += (size_t)gridDim.x * 256) dst[i] = f;
    }
    if (blockIdx.x >= 8 && blockIdx.x < 8 + (CNT_N * 256 / 16 + 255) / 256) {
        const int i = (blockIdx.x - 8) * 256 + threadIdx.x; if (i < CNT_N * 256 / 16) ((u32x4*)(ws + WS_CNT))[i] = (u32x4){0u, 0u, 0u, 0u}; }
    if (blockIdx.x < 4) {
        const int c = blockIdx.x * 256 + threadIdx.x, r = c & 7, kg = c >> 3;
        u32x4 m = *(const u32x4*)(mem0 + (size_t)r * DM + kg * 8);
        m.x = desent(m.x); m.y = desent(m.y); m.z = desent(m.z); m.w = desent(m.w);
        *(u32x4*)(ws + WS_MEM + (size_t)c * 16) = m;
    }
}

namespace g8 {
constexpr int BM = 256, BK = 64, HALF = 128, HTB = HALF * BK * 2, STAGE_BYTES = 8 * HTB;
__host__ __device__ __forceinline__ int lds_byte(int r, int c) { const int st = (r >> 4) * 2 + (c >> 5), rr = r & 15, cc = c & 31, ob = rr * 64 + cc * 2; return st * 1024 + (ob ^ (((ob >> 9) & 1) << 5)); }
__host__ __device__ __forceinline__ void stage_rc(int b, int& R, int& C) { const int st = b / 1024, sb = b % 1024, swz = sb ^ (((sb >> 9) & 1) << 5); R = (st >> 1) * 16 + swz / 64; C = (st & 1) * 32 + (swz % 64) / 2; }
__host__ __device__ __forceinline__ int bsw(int row) { return (row & 3) | (((row >> 3) & 1) << 2); }
struct Unit { const bf16_t* A; const bf16_t* B; int pm, pn; };

template <class Epi, class Sched>
__device__ __forceinline__ void gemm_phase(LAS unsigned char* lds, const int K, const int lda, const int ldb, const Sched& S, const Epi& E) {
    int tid = threadIdx.x; asm volatile("" : "+v"(tid));
    const int wid = __builtin_amdgcn_readfirstlane(tid >> 6), lane = tid & 63, wr = wid >> 2, wc = wid & 3, fr = lane & 15, fq = lane >> 4;
    const int nt = K / BK;
    unsigned voffA[2], voffB[2];
#pragma unroll
    for (int i = 0; i < 2; ++i) { int R, C; stage_rc(tid * 16 + i * 8192, R, C); voffA[i] = (unsigned)(R * lda + C) * 2u;
        const int b = tid * 16 + i * 8192, row = b >> 8, ch = (b & 255) >> 4, seg = (ch >> 1) ^ bsw(row);
        voffB[i] = (unsigned)(row * ldb + seg * 16 + (ch & 1) * 8) * 2u; }
    const size_t kstepA = (size_t)(BK * 2), kstepB = (size_t)BK * ldb * 2;
    const size_t hstepA = (size_t)HALF * lda * 2, hstepB = (size_t)HALF * 2;
    const unsigned ldsw = (unsigned)wid * 1024u;
    const int aoff = lds_byte(wr * 64 + fr, fq * 8);
    const int q4 = (lane & 15) >> 2, p4 = lane & 3, sB = q4 | ((fq & 1) << 2);
    const int boff0 = (8 * fq + q4) * 256 + (((wc * 2 + 0) ^ sB) * 32) + 8 * p4;
    const int boff1 = (8 * fq + q4) * 256 + (((wc * 2 + 1) ^ sB) * 32) + 8 * p4;
#define G8_SA(b, h) (((b) * 2 + (h)) * HTB)
#define G8_SB(b, h) ((4 + (b) * 2 + (h)) * HTB)
#define G8_STAGE(bufoff, gbase, voff) do { _Pragma("unroll") for (int _i = 0; _i < 2; ++_i) \
        __builtin_amdgcn_global_load_lds((const unsigned*)((const char*)(gbase) + (voff)[_i]), (LAS unsigned*)(lds + (bufoff) + ldsw + _i * 8192), 16, 0, 0); } while (0)
#define G8_LDA(dst, b, h) do { _Pragma("unroll") for (int m = 0; m < 4; ++m) _Pragma("unroll") for (int k = 0; k < 2; ++k) dst[m][k] = *(const LAS bf16x8*)(lds + G8_SA(b, h) + aoff + m * 2048 + k * 1024); } while (0)
#define G8_TR(off) __builtin_amdgcn_ds_read_tr16_b64_v4i16((LAS bf16x4*)(lds + (off)))
#define G8_LDB(dst, b, h) do { _Pragma("unroll") for (int k = 0; k < 2; ++k) { \
        bf16x4 a0 = G8_TR(G8_SB(b, h) + boff0 + k * 8192), a1 = G8_TR(G8_SB(b, h) + boff0 + k * 8192 + 1024); \
        bf16x4 c0 = G8_TR(G8_SB(b, h) + boff1 + k * 8192), c1 = G8_TR(G8_SB(b, h) + boff1 + k * 8192 + 1024); \
        dst[0][k] = __builtin_shufflevector(a0, a1, 0, 1, 2, 3, 4, 5, 6, 7); dst[1][k] = __builtin_shufflevector(c0, c1, 0, 1, 2, 3, 4, 5, 6, 7); } } while (0)
#define G8_MMA(ai, bj, At, Bt) do { __builtin_amdgcn_s_setprio(1); _Pragma("unroll") for (int m = 0; m < 4; ++m) _Pragma("unroll") for (int n = 0; n < 2; ++n) _Pragma("unroll") for (int k = 0; k < 2; ++k) \
        acc[ai][bj][m][n] = __builtin_amdgcn_mfma_f32_16x16x32_bf16(Bt[n][k], At[m][k], acc[ai][bj][m][n], 0, 0, 0); __builtin_amdgcn_s_setprio(0); } while (0)
#define G8_WAIT_V(n) asm volatile("s_waitcnt vmcnt(" #n ")" ::: "memory")
#define G8_WAIT_L(n) asm volatile("s_waitcnt lgkmcnt(" #n ")" ::: "memory")
#define G8_BAR __builtin_amdgcn_s_barrier()
#define G8_SCHED __builtin_amdgcn_sched_barrier(0)
    Unit cur, nxt; int ui = 0;
    if (!S.next(0, cur)) return;
    f32x4 acc[2][2][4][2];
#pragma unroll
    for (int a = 0; a < 2; ++a)
#pragma unroll
        for (int b = 0; b < 2; ++b)
#pragma unroll
            for (int m = 0; m < 4; ++m)
#pragma unroll
                for (int n = 0; n < 2; ++n) acc[a][b][m][n] = (f32x4){0.f, 0.f, 0.f, 0.f};
    bf16x8 At[4][2], B0[2][2], B1[2][2];
    const char* cA = (const char*)cur.A; const char* cB = (const char*)cur.B;
    G8_STAGE(G8_SB(0, 0), cB, voffB); G8_STAGE(G8_SA(0, 0), cA, voffA); G8_STAGE(G8_SB(0, 1), cB + hstepB, voffB); G8_STAGE(G8_SA(0, 1), cA + hstepA, voffA);
    if (wr == 1) G8_BAR;
    G8_WAIT_V(4); G8_BAR;
    G8_STAGE(G8_SB(1, 0), cB + kstepB, voffB); G8_STAGE(G8_SA(1, 0), cA + kstepA, voffA); G8_STAGE(G8_SB(1, 1), cB + hstepB + kstepB, voffB);
    G8_WAIT_V(6); G8_BAR;
    for (;;) {
        const bool has_next = S.next(ui + 1, nxt);
        const char* nA = has_next ? (const char*)nxt.A : cA; const char* nB = has_next ? (const char*)nxt.B : cB;
        for (int t = 0; t < nt; t += 2) {
            const bool last = (t == nt - 2);
            const char* a1 = cA + (size_t)(t + 1) * kstepA;
            const char* a2 = last ? nA : cA + (size_t)(t + 2) * kstepA; const char* b2 = last ? nB : cB + (size_t)(t + 2) * kstepB;
            const char* a3 = a2 + kstepA; const char* b3 = b2 + kstepB;
            G8_LDB(B0, 0, 0); G8_SCHED; G8_LDA(At, 0, 0); G8_STAGE(G8_SA(1, 1), a1 + hstepA, voffA);
            G8_WAIT_L(8); G8_BAR; G8_WAIT_L(0); G8_MMA(0, 0, At, B0); G8_BAR; G8_SCHED;
            G8_LDB(B1, 0, 1); G8_STAGE(G8_SB(0, 0), b2, voffB);
            G8_BAR; G8_WAIT_L(0); G8_MMA(0, 1, At, B1); G8_BAR;
            G8_LDA(At, 0, 1); G8_STAGE(G8_SA(0, 0), a2, voffA);
            G8_BAR; G8_WAIT_L(0); G8_MMA(1, 0, At, B0); G8_BAR; G8_SCHED;
            G8_STAGE(G8_SB(0, 1), b2 + hstepB, voffB);
            G8_WAIT_V(6); G8_BAR; G8_MMA(1, 1, At, B1); G8_BAR;
            G8_LDB(B0, 1, 0); G8_SCHED; G8_LDA(At, 1, 0); G8_STAGE(G8_SA(0, 1), a2 + hstepA, voffA);
            G8_WAIT_L(8); G8_BAR; G8_WAIT_L(0); G8_MMA(0, 0, At, B0); G8_BAR; G8_SCHED;
            G8_LDB(B1, 1, 1); G8_STAGE(G8_SB(1, 0), b3, voffB);
            G8_BAR; G8_WAIT_L(0); G8_MMA(0, 1, At, B1); G8_BAR;
            G8_LDA(At, 1, 1); G8_STAGE(G8_SA(1, 0), a3, voffA);
            G8_BAR; G8_WAIT_L(0); G8_MMA(1, 0, At, B0); G8_BAR; G8_SCHED;
            G8_STAGE(G8_SB(1, 1), b3 + hstepB, voffB);
            G8_WAIT_V(6); G8_BAR; G8_MMA(1, 1, At, B1); G8_BAR;
        }
        E(acc, cur, wr, wc, fr, fq);
        if (!has_next) break;
#pragma unroll
        for (int a = 0; a < 2; ++a)
#pragma unroll
            for (int b = 0; b < 2; ++b)
#pragma unroll
                for (int m = 0; m < 4; ++m)
#pragma unroll
                    for (int n = 0; n < 2; ++n) acc[a][b][m][n] = (f32x4){0.f, 0.f, 0.f, 0.f};
        cur = nxt; cA = nA; cB = nB; ++ui;
    }
    G8_WAIT_V(0);
    if (wr == 0) G8_BAR;
    G8_BAR;
#undef G8_SA
#undef G8_SB
#undef G8_STAGE
#undef G8_LDA
#undef G8_LDB
#undef G8_TR
#undef G8_MMA
#undef G8_WAIT_V
#undef G8_WAIT_L
#undef G8_BAR
#undef G8_SCHED
}
}

__device__ __forceinline__ float gelu_tanh(float x) {
    const float u = 0.7978845608028654f * (x + 0.044715f * x * x * x);
    const float th = 1.0f - 2.0f * __builtin_amdgcn_rcpf(__expf(2.0f * u) + 1.0f);
    return 0.5f * x * (1.0f + th);
}

__device__ __forceinline__ void wait_counter(const char* ws, int idx, unsigned need) {
    if (threadIdx.x < 64) {
        const unsigned* p = (const unsigned*)(ws + WS_CNT + (size_t)idx * 256);
        for (unsigned spins = 0; (unsigned)__builtin_amdgcn_readfirstlane(__hip_atomic_load(p, __ATOMIC_RELAXED, __HIP_MEMORY_SCOPE_AGENT)) < need;) {
            __builtin_amdgcn_s_sleep(8);
            if (++spins > 400000u) break;
        }
        __builtin_amdgcn_fence(__ATOMIC_ACQUIRE, "agent");
        asm volatile("s_waitcnt vmcnt(0)" ::: "memory");
    }
    asm volatile("" ::: "memory"); __builtin_amdgcn_s_barrier(); asm volatile("" ::: "memory");
}
__device__ __forceinline__ void signal_counter(char* ws, int idx) {
    asm volatile("s_waitcnt vmcnt(0)" ::: "memory");
    if ((threadIdx.x & 63) == 0) __hip_atomic_fetch_add((unsigned*)(ws + WS_CNT + (size_t)idx * 256), 1u, __ATOMIC_RELAXED, __HIP_MEMORY_SCOPE_AGENT);
}

__device__ __forceinline__ unsigned poll_ge(const char* ws, int idx, unsigned need) {
    const unsigned* p = (const unsigned*)(ws + WS_CNT + (size_t)idx * 256);
    unsigned v = 0;
    for (unsigned spins = 0; (v = (unsigned)__builtin_amdgcn_readfirstlane(__hip_atomic_load(p, __ATOMIC_RELAXED, __HIP_MEMORY_SCOPE_AGENT))) < need;) {
        __builtin_amdgcn_s_sleep(8);
        if (++spins > 400000u) break;
    }
    asm volatile("" ::: "memory");
    return v;
}

constexpr unsigned SPIN_CAP = 200000u;
constexpr int KS_PITCH = 272;
constexpr int CH_LDS = 160 * 1024;

__device__ __forceinline__ float xmax16_32(float v) {
    auto a = __builtin_amdgcn_permlane16_swap(__builtin_bit_cast(unsigned, v), __builtin_bit_cast(unsigned, v), false, false);
    v = fmaxf(__builtin_bit_cast(float, (unsigned)a[0]), __builtin_bit_cast(float, (unsigned)a[1]));
    auto b = __builtin_amdgcn_permlane32_swap(__builtin_bit_cast(unsigned, v), __builtin_bit_cast(unsigned, v), false, false);
    return fmaxf(__builtin_bit_cast(float, (unsigned)b[0]), __builtin_bit_cast(float, (unsigned)b[1]));
}
__device__ __forceinline__ float xsum16_32(float v) {
    auto a = __builtin_amdgcn_permlane16_swap(__builtin_bit_cast(unsigned, v), __builtin_bit_cast(unsigned, v), false, false);
    v = __builtin_bit_cast(float, (unsigned)a[0]) + __builtin_bit_cast(float, (unsigned)a[1]);
    auto b = __builtin_amdgcn_permlane32_swap(__builtin_bit_cast(unsigned, v), __builtin_bit_cast(unsigned, v), false, false);
    return __builtin_bit_cast(float, (unsigned)b[0]) + __builtin_bit_cast(float, (unsigned)b[1]);
}

__device__ __forceinline__ u32x4 quad_gather(unsigned v) {
    u32x4 r;
    r.x = (unsigned)__builtin_amdgcn_update_dpp(0, (int)v, 0x00, 0xf, 0xf, true); r.y = (unsigned)__builtin_amdgcn_update_dpp(0, (int)v, 0x55, 0xf, 0xf, true);
    r.z = (unsigned)__builtin_amdgcn_update_dpp(0, (int)v, 0xAA, 0xf, 0xf, true); r.w = (unsigned)__builtin_amdgcn_update_dpp(0, (int)v, 0xFF, 0xf, 0xf, true);
    return r;
}
#define STAMP(i) do {} while (0)
#define PCOUNT(sh) do {} while (0)
#define CH_DELAY1 14
#define CH_DELAY2 12
#define CH_DELAY3 12

template <int CPH>
__device__ __forceinline__ void chain_body(LAS unsigned char* lds, const int b, const bf16_t* __restrict__ Wq, const bf16_t* __restrict__ Wk, const bf16_t* __restrict__ Wv,
                                           const bf16_t* __restrict__ Wo, char* ws) {
    constexpr int ES = 128 / CPH, NE = ES / 16, NT = 3 * NE, RG = CPH / 2, RPG = 8 / RG;
    constexpr int VP = ES * 2;
    constexpr int L_RED = 0;
    constexpr int L_RED2 = L_RED + 8 * NT * 512;
    constexpr int L_KS = L_RED2 + 8 * RPG * 64;
    constexpr int L_VS = L_KS + 2 * 32 * KS_PITCH;
    constexpr int L_OS = L_VS + 2 * 48 * VP;
    constexpr bool WOL = (CPH == 4);
    constexpr int L_WO = L_OS + 8 * 1024 * RPG;
    static_assert(L_WO + (WOL ? 65536 : 0) <= 156 * 1024, "LDS");
    const int tid = threadIdx.x, wave = __builtin_amdgcn_readfirstlane(tid >> 6), lane = tid & 63, fr = lane & 15, fq = lane >> 4;
    const int h = b / CPH, j = b % CPH, g = b % RG, c = b / RG;
    const __amdgpu_buffer_rsrc_t rs = __builtin_amdgcn_make_buffer_rsrc(ws + WS_SENT0, 0, (int)(WS_SENT1 - WS_SENT0), 0x00020000);
    constexpr int O_MEM = (int)(WS_MEM - WS_SENT0), O_QKVM = (int)(WS_QKVM - WS_SENT0), O_OM = (int)(WS_OM - WS_SENT0);
    const __amdgpu_buffer_rsrc_t rkv = __builtin_amdgcn_make_buffer_rsrc(ws + WS_K, 0, (int)(WS_H - WS_K), 0x00020000);
    constexpr int O_V = (int)(WS_V - WS_K);
    bool dead = false;

    bf16x8 wq[NT][4], wo[16];
    {
        LAS unsigned char* stg = lds + wave * 8192;
        constexpr int CPR = ES / 8;
        static_assert(128 * CPR / 64 == 8, "8 chunks per lane and pass");
        u32x4 t[5][8];
        auto issue = [&](int ps) {
            if (ps < 3) { const bf16_t* W = (ps == 0 ? Wq : (ps == 1 ? Wk : Wv)) + (size_t)wave * 128 * HD + h * DH + j * ES;
#pragma unroll
                for (int i = 0; i < 8; ++i) { const int ci = i * 64 + lane; t[ps][i] = *(const u32x4*)(W + (size_t)(ci / CPR) * HD + (ci % CPR) * 8); } }
            else { const bf16_t* W = Wo + (size_t)(wave * 512 + (ps - 3) * 256) * DM + c * 16;
#pragma unroll
                for (int i = 0; i < 8; ++i) { const int ci = i * 64 + lane; t[ps][i] = *(const u32x4*)(W + (size_t)(ci >> 1) * DM + (ci & 1) * 8); } }
        };
        issue(0); issue(1); issue(2);
        asm volatile("" ::: "memory");
#pragma unroll
        for (int ps = 0; ps < 5; ++ps) {
#pragma unroll
            for (int i = 0; i < 8; ++i) *(LAS u32x4*)(stg + (i * 64 + lane) * 16) = t[ps][i];
            asm volatile("s_waitcnt lgkmcnt(0)" ::: "memory");
            if (ps + 3 < 5) issue(ps + 3);
            asm volatile("" ::: "memory");
            if (ps < 3) {
#pragma unroll
                for (int e = 0; e < NE; ++e)
#pragma unroll
                    for (int i = 0; i < 4; ++i) {
                        const int off = (i * 32 + 8 * fq + (fr >> 2)) * (ES * 2) + e * 32 + (lane & 3) * 8;
                        const bf16x4 a0 = __builtin_amdgcn_ds_read_tr16_b64_v4i16((LAS bf16x4*)(stg + off)), a1 = __builtin_amdgcn_ds_read_tr16_b64_v4i16((LAS bf16x4*)(stg + off + 4 * ES * 2));
                        wq[ps * NE + e][i] = __builtin_shufflevector(a0, a1, 0, 1, 2, 3, 4, 5, 6, 7);
                    }
            } else {
#pragma unroll
                for (int i = 0; i < 8; ++i) {
                    const int off = (i * 32 + 8 * fq + (fr >> 2)) * 32 + (lane & 3) * 8;
                    const bf16x4 a0 = __builtin_amdgcn_ds_read_tr16_b64_v4i16((LAS bf16x4*)(stg + off)), a1 = __builtin_amdgcn_ds_read_tr16_b64_v4i16((LAS bf16x4*)(stg + off + 128));
                    wo[(ps - 3) * 8 + i] = __builtin_shufflevector(a0, a1, 0, 1, 2, 3, 4, 5, 6, 7);
                }
            }
            asm volatile("s_waitcnt lgkmcnt(0)" ::: "memory");
        }
        if (WOL) {
#pragma unroll
            for (int i = 8; i < 16; ++i) *(LAS bf16x8*)(lds + L_WO + (wave * 8 + i - 8) * 1024 + lane * 16) = wo[i];
        }
    }
    __syncthreads();

    if (tid < 2 * 8 * VP / 16) { const int bf = tid / (8 * VP / 16), r = tid % (8 * VP / 16); *(LAS u32x4*)(lds + L_VS + bf * 48 * VP + 40 * VP + r * 16) = (u32x4){0u, 0u, 0u, 0u}; }
    {
        poll_ge(ws, CNT_KV + (h >> 1), 16u); poll_ge(ws, CNT_KV + 16 + (h >> 1), 16u);
        const int r = tid >> 4, ch = tid & 15;
        *(LAS u32x4*)(lds + L_KS + r * KS_PITCH + ch * 16) = __builtin_amdgcn_raw_buffer_load_b128(rkv, (r * HD + h * DH + ch * 8) * 2, 0, AUX_SC1);
        if (tid < 32 * ES / 8) { const int r2 = tid / (ES / 8), c2 = tid % (ES / 8);
            *(LAS u32x4*)(lds + L_VS + (8 + r2) * VP + c2 * 16) = __builtin_amdgcn_raw_buffer_load_b128(rkv, O_V + (r2 * HD + h * DH + j * ES + c2 * 8) * 2, 0, AUX_SC1); }
    }
    __syncthreads();

    for (int s = 0; s < NSEG; ++s) {
        const int kb = s & 1;
        unsigned long long pcnt = 0; (void)pcnt;
        STAMP(0);
        {
            if (s > 0) __builtin_amdgcn_s_sleep(CH_DELAY1);
            u32x4 a[4];
            const int base = O_MEM + s * (int)MEM_B + wave * 2048 + fq * 128 + (lane & 7) * 16;
            for (unsigned spins = 0;;) {
                asm volatile("" ::: "memory");
#pragma unroll
                for (int i = 0; i < 4; ++i) a[i] = __builtin_amdgcn_raw_buffer_load_b128(rs, base + i * 512, 0, AUX_SC1);
                unsigned mxs = 0;
#pragma unroll
                for (int i = 0; i < 4; ++i) mxs = umax2(mxs, umax4(a[i]));
                if (!__any(mxs == SENT) || dead) break;
                PCOUNT(0);
                if (++spins > SPIN_CAP) { dead = true; if (lane == 0) *(volatile unsigned*)(ws + WS_TMO) = 1u; break; }
            }
            STAMP(1);
            if (b == 0 && tid == 448 && (s & 7) == 0) __hip_atomic_store((unsigned*)(ws + WS_CNT + (size_t)CNT_STEP * 256), (unsigned)s, __ATOMIC_RELAXED, __HIP_MEMORY_SCOPE_AGENT);
            f32x4 acc[NT];
#pragma unroll
            for (int t = 0; t < NT; ++t) acc[t] = (f32x4){0.f, 0.f, 0.f, 0.f};
#pragma unroll
            for (int i = 0; i < 4; ++i)
#pragma unroll
                for (int t = 0; t < NT; ++t) acc[t] = __builtin_amdgcn_mfma_f32_16x16x32_bf16(wq[t][i], __builtin_bit_cast(bf16x8, a[i]), acc[t], 0, 0, 0);
            if (fr < 8) {
#pragma unroll
                for (int t = 0; t < NT; ++t) *(LAS f32x4*)(lds + L_RED + ((wave * NT + t) * 128 + fr * 16 + fq * 4) * 4) = acc[t];
            }
        }
        __syncthreads();
        if (tid < NT * 64) {
            const int t = tid >> 6, m = (tid & 63) >> 3, np = (tid & 7) * 2, mat = t / NE, col = (t % NE) * 16 + np;
            f32x2 sum = (f32x2){0.f, 0.f};
#pragma unroll
            for (int w = 0; w < 8; ++w) sum += *(const LAS f32x2*)(lds + L_RED + ((w * NT + t) * 128 + m * 16 + np) * 4);
            const unsigned pk = desent(pk_bf16(sum.x, sum.y));
            const u32x4 q4 = quad_gather(pk);
            if ((tid & 3) == 0) __builtin_amdgcn_raw_buffer_store_b128(q4, rs, O_QKVM + s * (int)QKVM_B + ((h * 3 + mat) * 8 + m) * 256 + (j * ES + col) * 2, 0, AUX_SC1);
            if (mat == 2) *(LAS unsigned*)(lds + L_VS + kb * 48 * VP + m * VP + col * 2) = pk;
        }
        STAMP(2);
        if (s == NSEG - 1) break;
        __syncthreads();
        if (wave == 0) {
            bf16x8 kx[3][4];
#pragma unroll
            for (int t = 0; t < 3; ++t) {
                int r = t * 16 + fr - 8; r = r < 0 ? 0 : (r > 31 ? 31 : r);
#pragma unroll
                for (int i = 0; i < 4; ++i) kx[t][i] = *(const LAS bf16x8*)(lds + L_KS + kb * 32 * KS_PITCH + r * KS_PITCH + i * 64 + fq * 16);
            }
            __builtin_amdgcn_s_sleep(CH_DELAY2);
            u32x4 qf[4], km[4];
            const int base = O_QKVM + s * (int)QKVM_B + h * 3 * 2048 + (lane & 7) * 256 + fq * 16;
            for (unsigned spins = 0;;) {
                asm volatile("" ::: "memory");
#pragma unroll
                for (int i = 0; i < 4; ++i) { qf[i] = __builtin_amdgcn_raw_buffer_load_b128(rs, base + i * 64, 0, AUX_SC1); km[i] = __builtin_amdgcn_raw_buffer_load_b128(rs, base + 2048 + i * 64, 0, AUX_SC1); }
                unsigned mxs = 0;
#pragma unroll
                for (int i = 0; i < 4; ++i) mxs = umax2(mxs, umax2(umax4(qf[i]), umax4(km[i])));
                if (!__any(mxs == SENT) || dead) break;
                PCOUNT(16);
                if (++spins > SPIN_CAP) { dead = true; if (lane == 0) *(volatile unsigned*)(ws + WS_TMO) = 2u; break; }
            }
            STAMP(3);
            f32x4 sc[3];
#pragma unroll
            for (int t = 0; t < 3; ++t) sc[t] = (f32x4){0.f, 0.f, 0.f, 0.f};
#pragma unroll
            for (int i = 0; i < 4; ++i) {
                const bf16x8 k0 = fr < 8 ? __builtin_bit_cast(bf16x8, km[i]) : kx[0][i];
                sc[1] = __builtin_amdgcn_mfma_f32_16x16x32_bf16(kx[1][i], __builtin_bit_cast(bf16x8, qf[i]), sc[1], 0, 0, 0);
                sc[2] = __builtin_amdgcn_mfma_f32_16x16x32_bf16(kx[2][i], __builtin_bit_cast(bf16x8, qf[i]), sc[2], 0, 0, 0);
                sc[0] = __builtin_amdgcn_mfma_f32_16x16x32_bf16(k0, __builtin_bit_cast(bf16x8, qf[i]), sc[0], 0, 0, 0);
            }
            asm volatile("" ::: "memory");
            bf16x8 vf[NE][2];
            const bf16x4 zz = (bf16x4){0, 0, 0, 0};
#pragma unroll
            for (int et = 0; et < NE; ++et) {
                const int voff = L_VS + kb * 48 * VP + (4 * fq + (fr >> 2)) * VP + et * 32 + (lane & 3) * 8;
                const bf16x4 v0 = __builtin_amdgcn_ds_read_tr16_b64_v4i16((LAS bf16x4*)(lds + voff));
                const bf16x4 v1 = __builtin_amdgcn_ds_read_tr16_b64_v4i16((LAS bf16x4*)(lds + voff + 16 * VP));
                const bf16x4 v2 = __builtin_amdgcn_ds_read_tr16_b64_v4i16((LAS bf16x4*)(lds + voff + 32 * VP));
                vf[et][0] = __builtin_shufflevector(v0, v1, 0, 1, 2, 3, 4, 5, 6, 7); vf[et][1] = __builtin_shufflevector(v2, zz, 0, 1, 2, 3, 4, 5, 6, 7);
            }
            const float scale = 0.08838834764831845f;
            const bool hi = fr >= 8;
            float v[6];
            v[0] = hi ? sc[1][2] : sc[0][0]; v[1] = hi ? sc[1][3] : sc[0][1]; v[2] = hi ? sc[2][0] : sc[0][2];
            v[3] = hi ? sc[2][1] : sc[0][3]; v[4] = hi ? sc[2][2] : sc[1][0]; v[5] = hi ? sc[2][3] : sc[1][1];
            float mx = -INFINITY;
#pragma unroll
            for (int i = 0; i < 6; ++i) { float z = rbf(rbf(v[i]) * scale); if (i >= 2 && hi && fq >= 2) z = -INFINITY; v[i] = z; mx = fmaxf(mx, z); }
            mx = fmaxf(mx, __builtin_bit_cast(float, __builtin_amdgcn_update_dpp(0, __builtin_bit_cast(int, mx), 0x128, 0xf, 0xf, true)));
            mx = xmax16_32(mx);
            float sum = 0.f;
#pragma unroll
            for (int i = 0; i < 6; ++i) { v[i] = __expf(v[i] - mx); sum += v[i]; }
            sum += __builtin_bit_cast(float, __builtin_amdgcn_update_dpp(0, __builtin_bit_cast(int, sum), 0x128, 0xf, 0xf, true));
            sum = xsum16_32(sum);
            const float inv = 1.0f / sum;
            const unsigned w0 = pk_bf16(v[0] * inv, v[1] * inv), w1 = pk_bf16(v[2] * inv, v[3] * inv), w2 = pk_bf16(v[4] * inv, v[5] * inv);
            u32x4 p0, p1;
            p0.x = w0; p0.y = w1; p0.z = w2; p0.w = (unsigned)__builtin_amdgcn_update_dpp(0, (int)w0, 0x128, 0xf, 0xf, true);
            p1.x = (unsigned)__builtin_amdgcn_update_dpp(0, (int)w1, 0x128, 0xf, 0xf, true); p1.y = (unsigned)__builtin_amdgcn_update_dpp(0, (int)w2, 0x128, 0xf, 0xf, true); p1.z = 0u; p1.w = 0u;
#pragma unroll
            for (int et = 0; et < NE; ++et) {
                f32x4 o = (f32x4){0.f, 0.f, 0.f, 0.f};
                o = __builtin_amdgcn_mfma_f32_16x16x32_bf16(vf[et][0], __builtin_bit_cast(bf16x8, p0), o, 0, 0, 0);
                o = __builtin_amdgcn_mfma_f32_16x16x32_bf16(vf[et][1], __builtin_bit_cast(bf16x8, p1), o, 0, 0, 0);
                {
                    const unsigned wx = desent(pk_bf16(o[0], o[1])), wy = desent(pk_bf16(o[2], o[3]));
                    auto sx = __builtin_amdgcn_permlane16_swap(wx, wx, false, false); auto sy = __builtin_amdgcn_permlane16_swap(wy, wy, false, false);
                    const u32x4 w = (u32x4){(unsigned)sx[0], (unsigned)sy[0], (unsigned)sx[1], (unsigned)sy[1]};
                    const int k = h * DH + j * ES + et * 16 + 4 * fq;
                    const int off = O_OM + s * (int)OM_B + (fr / RPG) * (8192 * RPG) + (k >> 5) * (64 * RPG) + ((k >> 3) & 3) * (16 * RPG) + (fr % RPG) * 16;
                    if (fr < 8 && (fq & 1) == 0) __builtin_amdgcn_raw_buffer_store_b128(w, rs, off, 0, AUX_SC1);
                }
            }
            STAMP(4);
        } else {
            const int t2 = tid - 64; const int nb = kb ^ 1; const int row0 = (s + 1) * SEG;
            if (((s + 1) & 7) == 0) { const int blk = (s + 1) >> 3; poll_ge(ws, CNT_KV + blk * 32 + (h >> 1), 8u); poll_ge(ws, CNT_KV + blk * 32 + 16 + (h >> 1), 8u); }
            if (s + 1 == 4) { poll_ge(ws, CNT_KV0B + (h >> 1), 16u); poll_ge(ws, CNT_KV0B + 16 + (h >> 1), 16u); }
            for (int i = t2; i < 512 + 32 * ES / 8; i += 448) {
                if (i < 512) { const int r = i >> 4, ch = i & 15;
                    *(LAS u32x4*)(lds + L_KS + nb * 32 * KS_PITCH + r * KS_PITCH + ch * 16) = __builtin_amdgcn_raw_buffer_load_b128(rkv, ((row0 + r) * HD + h * DH + ch * 8) * 2, 0, AUX_SC1); }
                else { const int r2 = (i - 512) / (ES / 8), c2 = (i - 512) % (ES / 8);
                    *(LAS u32x4*)(lds + L_VS + nb * 48 * VP + (8 + r2) * VP + c2 * 16) = __builtin_amdgcn_raw_buffer_load_b128(rkv, O_V + ((row0 + r2) * HD + h * DH + j * ES + c2 * 8) * 2, 0, AUX_SC1); }
            }
        }
        __syncthreads();
        {
            if (WOL) {
#pragma unroll
                for (int i = 8; i < 16; ++i) wo[i] = *(const LAS bf16x8*)(lds + L_WO + (wave * 8 + i - 8) * 1024 + lane * 16);
            }
            __builtin_amdgcn_s_sleep(CH_DELAY3);
            u32x4 a[RPG];
            constexpr int KPG = 16 / RPG;
            const int base = O_OM + s * (int)OM_B + g * (8192 * RPG) + wave * (1024 * RPG) + (fr / RPG) * (64 * RPG) + fq * (16 * RPG) + (fr % RPG) * 16;
            for (unsigned spins = 0;;) {
                asm volatile("" ::: "memory");
#pragma unroll
                for (int i = 0; i < RPG; ++i) a[i] = __builtin_amdgcn_raw_buffer_load_b128(rs, base + i * 1024, 0, AUX_SC1);
                unsigned mxs = 0;
#pragma unroll
                for (int i = 0; i < RPG; ++i) mxs = umax2(mxs, umax4(a[i]));
                if (!__any(mxs == SENT) || dead) break;
                PCOUNT(32);
                if (++spins > SPIN_CAP) { dead = true; if (lane == 0) *(volatile unsigned*)(ws + WS_TMO) = 3u; break; }
            }
            STAMP(5);
            f32x4 acc[KPG];
#pragma unroll
            for (int q = 0; q < KPG; ++q) acc[q] = (f32x4){0.f, 0.f, 0.f, 0.f};
#pragma unroll
            for (int gq = 0; gq < RPG; ++gq)
#pragma unroll
                for (int q = 0; q < KPG; ++q) acc[q] = __builtin_amdgcn_mfma_f32_16x16x32_bf16(wo[gq * KPG + q], __builtin_bit_cast(bf16x8, a[gq]), acc[q], 0, 0, 0);
            f32x4 r = acc[0];
#pragma unroll
            for (int q = 1; q < KPG; ++q) if (fr / RPG == q) r = acc[q];
#pragma unroll
            for (int e = 0; e < 4; ++e) {
                if (RPG <= 2) r[e] += __builtin_bit_cast(float, __builtin_amdgcn_update_dpp(0, __builtin_bit_cast(int, r[e]), 0x122, 0xf, 0xf, true));
                r[e] += __builtin_bit_cast(float, __builtin_amdgcn_update_dpp(0, __builtin_bit_cast(int, r[e]), 0x124, 0xf, 0xf, true));
                r[e] += __builtin_bit_cast(float, __builtin_amdgcn_update_dpp(0, __builtin_bit_cast(int, r[e]), 0x128, 0xf, 0xf, true));
            }
            if (fr < RPG) *(LAS f32x4*)(lds + L_RED2 + ((wave * RPG + fr) * 16 + fq * 4) * 4) = r;
        }
        __syncthreads();
        if (tid < RPG * 8) {
            const int m = tid >> 3, np = (tid & 7) * 2;
            f32x2 sum = (f32x2){0.f, 0.f};
#pragma unroll
            for (int w = 0; w < 8; ++w) sum += *(const LAS f32x2*)(lds + L_RED2 + ((w * RPG + m) * 16 + np) * 4);
            const unsigned pk = desent(pk_bf16(sum.x, sum.y));
            const int off = O_MEM + (s + 1) * (int)MEM_B + (c >> 1) * 512 + ((c & 1) * 2 + (np >> 3)) * 128 + (g * RPG + m) * 16;
            const u32x4 q4 = quad_gather(pk);
            if ((tid & 3) == 0) __builtin_amdgcn_raw_buffer_store_b128(q4, rs, off, 0, AUX_SC1);
        }
        STAMP(6);
    }
}

template <int MT, class Epi>
__device__ __forceinline__ void gemm128_w8(LAS unsigned char* lds, const bf16_t* A, const int lda, const bf16_t* B, const int ldb, const int K, const Epi& E) {
    int tid = threadIdx.x; asm volatile("" : "+v"(tid));
    const int wid = __builtin_amdgcn_readfirstlane(tid >> 6), lane = tid & 63, wr = wid >> 2, wc = wid & 3, fr = lane & 15, fq = lane >> 4;
    constexpr int NA = MT / 2;
    unsigned voffA[NA], voffB[2];
#pragma unroll
    for (int i = 0; i < NA; ++i) { int R, C; g8::stage_rc(tid * 16 + i * 8192, R, C); voffA[i] = (unsigned)(R * lda + C) * 2u; }
#pragma unroll
    for (int i = 0; i < 2; ++i) { const int bb = tid * 16 + i * 8192, row = bb >> 8, ch = (bb & 255) >> 4, seg = (ch >> 1) ^ g8::bsw(row);
        voffB[i] = (unsigned)(row * ldb + seg * 16 + (ch & 1) * 8) * 2u; }
    const char* cA = (const char*)A; const char* cB = (const char*)B;
    const unsigned ldsw = (unsigned)wid * 1024u;
    const int aoff = g8::lds_byte(wr * 16 * MT + fr, fq * 8);
    const int q4 = (lane & 15) >> 2, p4 = lane & 3, sB = q4 | ((fq & 1) << 2);
    const int boff0 = (8 * fq + q4) * 256 + (((wc * 2 + 0) ^ sB) * 32) + 8 * p4;
    const int boff1 = (8 * fq + q4) * 256 + (((wc * 2 + 1) ^ sB) * 32) + 8 * p4;
    f32x4 acc[MT][2];
#pragma unroll
    for (int m = 0; m < MT; ++m) { acc[m][0] = (f32x4){0.f, 0.f, 0.f, 0.f}; acc[m][1] = (f32x4){0.f, 0.f, 0.f, 0.f}; }
    const int nt = K / 64;
#define STG(buf, t) do { _Pragma("unroll") for (int _i = 0; _i < NA; ++_i) \
        __builtin_amdgcn_global_load_lds((const unsigned*)(cA + (size_t)(t) * 128 + voffA[_i]), (LAS unsigned*)(lds + (buf) * 32768 + ldsw + _i * 8192), 16, 0, 0); \
        _Pragma("unroll") for (int _i = 0; _i < 2; ++_i) \
        __builtin_amdgcn_global_load_lds((const unsigned*)(cB + (size_t)(t) * 64 * ldb * 2 + voffB[_i]), (LAS unsigned*)(lds + (buf) * 32768 + 16384 + ldsw + _i * 8192), 16, 0, 0); } while (0)
    STG(0, 0); STG(1, 1); STG(2, 2);
    int cur = 0;
    for (int t = 0; t < nt; ++t) {
        if (MT == 4) { if (t + 2 < nt) asm volatile("s_waitcnt vmcnt(8)" ::: "memory"); else if (t + 1 < nt) asm volatile("s_waitcnt vmcnt(4)" ::: "memory"); else asm volatile("s_waitcnt vmcnt(0)" ::: "memory"); }
        else { if (t + 2 < nt) asm volatile("s_waitcnt vmcnt(6)" ::: "memory"); else if (t + 1 < nt) asm volatile("s_waitcnt vmcnt(3)" ::: "memory"); else asm volatile("s_waitcnt vmcnt(0)" ::: "memory"); }
        __builtin_amdgcn_s_barrier();
        asm volatile("" ::: "memory");
        if (t + 3 < nt) STG((cur + 3) & 3, t + 3);
        bf16x8 At[MT][2], Bt[2][2];
        LAS unsigned char* sa = lds + cur * 32768; LAS unsigned char* sb = sa + 16384;
#pragma unroll
        for (int k = 0; k < 2; ++k) {
            bf16x4 a0 = __builtin_amdgcn_ds_read_tr16_b64_v4i16((LAS bf16x4*)(sb + boff0 + k * 8192)), a1 = __builtin_amdgcn_ds_read_tr16_b64_v4i16((LAS bf16x4*)(sb + boff0 + k * 8192 + 1024));
            bf16x4 c0 = __builtin_amdgcn_ds_read_tr16_b64_v4i16((LAS bf16x4*)(sb + boff1 + k * 8192)), c1 = __builtin_amdgcn_ds_read_tr16_b64_v4i16((LAS bf16x4*)(sb + boff1 + k * 8192 + 1024));
            Bt[0][k] = __builtin_shufflevector(a0, a1, 0, 1, 2, 3, 4, 5, 6, 7); Bt[1][k] = __builtin_shufflevector(c0, c1, 0, 1, 2, 3, 4, 5, 6, 7);
#pragma unroll
            for (int m = 0; m < MT; ++m) At[m][k] = *(const LAS bf16x8*)(sa + aoff + m * 2048 + k * 1024);
        }
        __builtin_amdgcn_s_setprio(1);
#pragma unroll
        for (int k = 0; k < 2; ++k)
#pragma unroll
            for (int m = 0; m < MT; ++m)
#pragma unroll
                for (int n = 0; n < 2; ++n) acc[m][n] = __builtin_amdgcn_mfma_f32_16x16x32_bf16(Bt[n][k], At[m][k], acc[m][n], 0, 0, 0);
        __builtin_amdgcn_s_setprio(0);
        asm volatile("s_waitcnt lgkmcnt(0)" ::: "memory");
        cur = (cur + 1) & 3;
    }
#undef STG
    __builtin_amdgcn_s_barrier();
    E(acc, wr, wc, fr, fq);
}

constexpr int XV_PITCH = 272;
template <bool INK>
__device__ __forceinline__ void xattn_item(LAS unsigned char* vs, char* ws, const int s, const int h) {
    const int lane = threadIdx.x & 63, fr = lane & 15, fq = lane >> 4;
    const bf16_t* Q = (const bf16_t*)(ws + WS_Q) + (size_t)s * SEG * HD + h * DH;
    const bf16_t* K = (const bf16_t*)(ws + WS_K) + (size_t)s * SEG * HD + h * DH;
    const bf16_t* V = (const bf16_t*)(ws + WS_V) + (size_t)s * SEG * HD + h * DH;
    const __amdgpu_buffer_rsrc_t rm = __builtin_amdgcn_make_buffer_rsrc(ws + WS_QKVM, 0, (int)(WS_OM - WS_QKVM), 0x00020000);
    const int kmo = s * (int)QKVM_B + (h * 3 + 1) * 2048, vmo = kmo + 2048;
    u32x4 km[4], vm[2];
    for (unsigned spins = 0;;) {
        asm volatile("" ::: "memory");
#pragma unroll
        for (int i = 0; i < 4; ++i) km[i] = __builtin_amdgcn_raw_buffer_load_b128(rm, kmo + (lane & 7) * 256 + i * 64 + fq * 16, 0, INK ? AUX_SC1 : 0);
#pragma unroll
        for (int i = 0; i < 2; ++i) vm[i] = __builtin_amdgcn_raw_buffer_load_b128(rm, vmo + (i * 4 + fq) * 256 + fr * 16, 0, INK ? AUX_SC1 : 0);
        if (!INK) break;
        unsigned mxs = umax2(umax4(vm[0]), umax4(vm[1]));
#pragma unroll
        for (int i = 0; i < 4; ++i) mxs = umax2(mxs, umax4(km[i]));
        if (!__any(mxs == SENT)) break;
        __builtin_amdgcn_s_sleep(16);
        if (++spins > SPIN_CAP) break;
    }
#pragma unroll
    for (int i = 0; i < 12; ++i) {
        const int r = i * 4 + fq;
        u32x4 v = (u32x4){0u, 0u, 0u, 0u};
        if (i < 2) v = vm[i]; else if (i < 10) v = *(const u32x4*)(V + (size_t)(r - 8) * HD + fr * 8);
        *(LAS u32x4*)(vs + r * XV_PITCH + fr * 16) = v;
    }
    f32x4 sc[3][2];
#pragma unroll
    for (int t = 0; t < 3; ++t) { sc[t][0] = (f32x4){0.f, 0.f, 0.f, 0.f}; sc[t][1] = (f32x4){0.f, 0.f, 0.f, 0.f}; }
#pragma unroll
    for (int i = 0; i < 4; ++i) {
        const bf16x8 q0 = *(const bf16x8*)(Q + (size_t)fr * HD + i * 32 + fq * 8), q1 = *(const bf16x8*)(Q + (size_t)(16 + fr) * HD + i * 32 + fq * 8);
#pragma unroll
        for (int t = 0; t < 3; ++t) {
            int r = t * 16 + fr - 8; r = r < 0 ? 0 : (r > 31 ? 31 : r);
            bf16x8 kf = *(const bf16x8*)(K + (size_t)r * HD + i * 32 + fq * 8);
            if (t == 0 && fr < 8) kf = __builtin_bit_cast(bf16x8, km[i]);
            sc[t][0] = __builtin_amdgcn_mfma_f32_16x16x32_bf16(kf, q0, sc[t][0], 0, 0, 0);
            sc[t][1] = __builtin_amdgcn_mfma_f32_16x16x32_bf16(kf, q1, sc[t][1], 0, 0, 0);
        }
    }
    const float scale = 0.08838834764831845f, NEG = rbf(-1000000000.0f);
    u32x4 p0[2], p1[2];
#pragma unroll
    for (int rt = 0; rt < 2; ++rt) {
        const int row = rt * 16 + fr;
        float v[12]; float mx = -INFINITY;
#pragma unroll
        for (int t = 0; t < 3; ++t)
#pragma unroll
            for (int r = 0; r < 4; ++r) { const int key = t * 16 + fq * 4 + r; float z = rbf(rbf(sc[t][rt][r]) * scale);
                if (key - 8 > row) z = NEG; if (key >= 40) z = -INFINITY; v[t * 4 + r] = z; mx = fmaxf(mx, z); }
        mx = xmax16_32(mx);
        float sum = 0.f;
#pragma unroll
        for (int i = 0; i < 12; ++i) { v[i] = __expf(v[i] - mx); sum += v[i]; }
        sum = xsum16_32(sum);
        const float inv = 1.0f / sum;
        p0[rt].x = pk_bf16(v[0] * inv, v[1] * inv); p0[rt].y = pk_bf16(v[2] * inv, v[3] * inv); p0[rt].z = pk_bf16(v[4] * inv, v[5] * inv); p0[rt].w = pk_bf16(v[6] * inv, v[7] * inv);
        p1[rt].x = pk_bf16(v[8] * inv, v[9] * inv); p1[rt].y = pk_bf16(v[10] * inv, v[11] * inv); p1[rt].z = 0u; p1[rt].w = 0u;
    }
    asm volatile("s_waitcnt lgkmcnt(0)" ::: "memory");
    const __amdgpu_buffer_rsrc_t ro = __builtin_amdgcn_make_buffer_rsrc(ws + WS_O, 0, S_LEN * HD * 2, 0x00020000);
    const int obase = (s * SEG * HD + h * DH) * 2;
    const bf16x4 zz = (bf16x4){0, 0, 0, 0};
    u32x2 ow[8][2];
#pragma unroll
    for (int et = 0; et < 8; ++et) {
        const int voff = (4 * fq + (fr >> 2)) * XV_PITCH + et * 32 + (lane & 3) * 8;
        const bf16x4 v0 = __builtin_amdgcn_ds_read_tr16_b64_v4i16((LAS bf16x4*)(vs + voff));
        const bf16x4 v1 = __builtin_amdgcn_ds_read_tr16_b64_v4i16((LAS bf16x4*)(vs + voff + 16 * XV_PITCH));
        const bf16x4 v2 = __builtin_amdgcn_ds_read_tr16_b64_v4i16((LAS bf16x4*)(vs + voff + 32 * XV_PITCH));
        const bf16x8 va = __builtin_shufflevector(v0, v1, 0, 1, 2, 3, 4, 5, 6, 7), vb = __builtin_shufflevector(v2, zz, 0, 1, 2, 3, 4, 5, 6, 7);
#pragma unroll
        for (int rt = 0; rt < 2; ++rt) {
            f32x4 o = (f32x4){0.f, 0.f, 0.f, 0.f};
            o = __builtin_amdgcn_mfma_f32_16x16x32_bf16(va, __builtin_bit_cast(bf16x8, p0[rt]), o, 0, 0, 0);
            o = __builtin_amdgcn_mfma_f32_16x16x32_bf16(vb, __builtin_bit_cast(bf16x8, p1[rt]), o, 0, 0, 0);
            ow[et][rt].x = pk_bf16(o[0], o[1]); ow[et][rt].y = pk_bf16(o[2], o[3]);
        }
    }
    asm volatile("s_waitcnt lgkmcnt(0)" ::: "memory");
#pragma unroll
    for (int et = 0; et < 8; ++et)
#pragma unroll
        for (int rt = 0; rt < 2; ++rt) *(LAS u32x2*)(vs + (rt * 16 + fr) * XV_PITCH + et * 32 + fq * 8) = ow[et][rt];
    asm volatile("s_waitcnt lgkmcnt(0)" ::: "memory");
#pragma unroll
    for (int i = 0; i < 8; ++i) {
        const int r = i * 4 + fq;
        const u32x4 w = *(const LAS u32x4*)(vs + r * XV_PITCH + fr * 16);
        __builtin_amdgcn_raw_buffer_store_b128(w, ro, obase + (r * HD + fr * 8) * 2, 0, INK ? AUX_SC1 : 0);
    }
    asm volatile("s_waitcnt lgkmcnt(0)" ::: "memory");
}

struct SchedOne {
    g8::Unit u; bool valid;
    __device__ __forceinline__ bool next(int i, g8::Unit& o) const { if (i != 0 || !valid) return false; o = u; return true; }
};
struct SchedKVQ {
    const bf16_t* A; const bf16_t* Wq; long dk, dv; int r;
    __device__ __forceinline__ bool next(int i, g8::Unit& u) const {
        const int L = r + 128 * i; if (L >= 352) return false;
        int pm, pn;
        if (L < 224) { pm = 1 + (L >> 5); pn = 16 + (L & 31); } else { pm = (L - 224) & 7; pn = (L - 224) >> 3; }
        const int mat = pn >> 4;
        u.A = A + (size_t)pm * 256 * DM; u.B = Wq + (mat == 1 ? dk : 0L) + (mat == 2 ? dv : 0L) + (pn & 15) * 256; u.pm = pm; u.pn = pn; return true;
    }
};
struct EpiQKV {
    char* ws;
    __device__ __forceinline__ void operator()(const f32x4 (&acc)[2][2][4][2], const g8::Unit& u, int wr, int wc, int fr, int fq) const {
        const __amdgpu_buffer_rsrc_t rq = __builtin_amdgcn_make_buffer_rsrc(ws + WS_Q, 0, (int)(WS_H - WS_Q), 0x00020000);
        const int base = (u.pn >> 4) * (S_LEN * HD * 2) + ((u.pm * 256 + wr * 64 + fr) * HD + (u.pn & 15) * 256 + wc * 32 + 4 * fq) * 2;
#pragma unroll
        for (int ai = 0; ai < 2; ++ai)
#pragma unroll
            for (int m = 0; m < 4; ++m)
#pragma unroll
                for (int bj = 0; bj < 2; ++bj)
#pragma unroll
                    for (int n = 0; n < 2; ++n) { const f32x4 v = acc[ai][bj][m][n]; u32x2 w; w.x = pk_bf16(v[0], v[1]); w.y = pk_bf16(v[2], v[3]);
                        __builtin_amdgcn_raw_buffer_store_b64(w, rq, base + ((ai * 128 + m * 16) * HD + bj * 128 + n * 16) * 2, 0, AUX_SC1); }
        asm volatile("s_waitcnt vmcnt(0)" ::: "memory");
        if ((threadIdx.x & 63) == 0) {
            if (u.pn >= 16) __hip_atomic_fetch_add((unsigned*)(ws + WS_CNT + (size_t)(CNT_KV + u.pm * 32 + u.pn - 16) * 256), 1u, __ATOMIC_RELAXED, __HIP_MEMORY_SCOPE_AGENT);
            __hip_atomic_fetch_add((unsigned*)(ws + WS_CNT + (size_t)(CNT_QKV + u.pm) * 256), 1u, __ATOMIC_RELAXED, __HIP_MEMORY_SCOPE_AGENT);
        }
    }
};
struct EpiKV0 {
    char* ws; int pm2, pn2;
    __device__ __forceinline__ void operator()(const f32x4 (&acc)[4][2], int wr, int wc, int fr, int fq) const {
        const __amdgpu_buffer_rsrc_t rq = __builtin_amdgcn_make_buffer_rsrc(ws + WS_K, 0, (int)(WS_H - WS_K), 0x00020000);
        const int base = (pn2 >> 5) * (S_LEN * HD * 2) + ((pm2 * 128 + wr * 64 + fr) * HD + (pn2 & 31) * 128 + wc * 32 + 4 * fq) * 2;
#pragma unroll
        for (int m = 0; m < 4; ++m)
#pragma unroll
            for (int n = 0; n < 2; ++n) { const f32x4 v = acc[m][n]; u32x2 w; w.x = pk_bf16(v[0], v[1]); w.y = pk_bf16(v[2], v[3]);
                __builtin_amdgcn_raw_buffer_store_b64(w, rq, base + (m * 16 * HD + n * 16) * 2, 0, AUX_SC1); }
        asm volatile("s_waitcnt vmcnt(0)" ::: "memory");
        if ((threadIdx.x & 63) == 0) {
            __hip_atomic_fetch_add((unsigned*)(ws + WS_CNT + (size_t)((pm2 == 0 ? CNT_KV : CNT_KV0B) + (pn2 >> 1)) * 256), 1u, __ATOMIC_RELAXED, __HIP_MEMORY_SCOPE_AGENT);
            __hip_atomic_fetch_add((unsigned*)(ws + WS_CNT + (size_t)CNT_QKV * 256), 1u, __ATOMIC_RELAXED, __HIP_MEMORY_SCOPE_AGENT);
        }
    }
};
struct EpiMLP1 {
    char* ws; const bf16_t* b1;
    __device__ __forceinline__ void operator()(const f32x4 (&acc)[2][2][4][2], const g8::Unit& u, int wr, int wc, int fr, int fq) const {
        const __amdgpu_buffer_rsrc_t rh = __builtin_amdgcn_make_buffer_rsrc(ws + WS_H, 0, S_LEN * FF * 2, 0x00020000);
        const int row0 = u.pm * 256 + wr * 64 + fr, col0 = u.pn * 256 + wc * 32 + 4 * fq;
#pragma unroll
        for (int bj = 0; bj < 2; ++bj)
#pragma unroll
            for (int n = 0; n < 2; ++n) {
                const u32x2 bw = *(const u32x2*)(b1 + col0 + bj * 128 + n * 16);
                const float b0 = bf2f(bw.x & 0xffffu), b1f = bf2f(bw.x >> 16), b2f = bf2f(bw.y & 0xffffu), b3 = bf2f(bw.y >> 16);
#pragma unroll
                for (int ai = 0; ai < 2; ++ai)
#pragma unroll
                    for (int m = 0; m < 4; ++m) { const f32x4 v = acc[ai][bj][m][n];
                        const float g0 = gelu_tanh(rbf(rbf(v[0]) + b0)), g1 = gelu_tanh(rbf(rbf(v[1]) + b1f)), g2 = gelu_tanh(rbf(rbf(v[2]) + b2f)), g3 = gelu_tanh(rbf(rbf(v[3]) + b3));
                        u32x2 w; w.x = pk_bf16(g0, g1); w.y = pk_bf16(g2, g3);
                        __builtin_amdgcn_raw_buffer_store_b64(w, rh, ((row0 + ai * 128 + m * 16) * FF + col0 + bj * 128 + n * 16) * 2, 0, AUX_SC1); }
                asm volatile("" ::: "memory");
            }
        signal_counter(ws, CNT_H + u.pm);
    }
};
struct EpiMLP2 {
    char* ws; const bf16_t* b2; int row0, col0;
    __device__ __forceinline__ void operator()(const f32x4 (&acc)[4][2], int wr, int wc, int fr, int fq) const {
        const __amdgpu_buffer_rsrc_t rb = __builtin_amdgcn_make_buffer_rsrc(ws + WS_B, 0, S_LEN * DM * 2, 0x00020000);
#pragma unroll
        for (int n = 0; n < 2; ++n) {
            const int col = col0 + wc * 32 + n * 16 + 4 * fq;
            const u32x2 bw = *(const u32x2*)(b2 + col);
#pragma unroll
            for (int m = 0; m < 4; ++m) { const f32x4 v = acc[m][n];
                u32x2 w; w.x = pk_bf16(rbf(v[0]) + bf2f(bw.x & 0xffffu), rbf(v[1]) + bf2f(bw.x >> 16)); w.y = pk_bf16(rbf(v[2]) + bf2f(bw.y & 0xffffu), rbf(v[3]) + bf2f(bw.y >> 16));
                __builtin_amdgcn_raw_buffer_store_b64(w, rb, ((row0 + wr * 64 + m * 16 + fr) * DM + col) * 2, 0, AUX_SC1); }
        }
        signal_counter(ws, CNT_B + (row0 >> 8));
    }
};
__device__ __forceinline__ u32x2 combine4(const f32x4 v, const u32x2 xw, const u32x2 bw) {
    u32x2 w;
    w.x = pk_bf16(rbf(bf2f(xw.x & 0xffffu) + rbf(v[0])) + bf2f(bw.x & 0xffffu), rbf(bf2f(xw.x >> 16) + rbf(v[1])) + bf2f(bw.x >> 16));
    w.y = pk_bf16(rbf(bf2f(xw.y & 0xffffu) + rbf(v[2])) + bf2f(bw.y & 0xffffu), rbf(bf2f(xw.y >> 16) + rbf(v[3])) + bf2f(bw.y >> 16));
    return w;
}
struct EpiOut {
    const bf16_t* x; const bf16_t* bout; bf16_t* out; int row0, col0;
    __device__ __forceinline__ void operator()(const f32x4 (&acc)[4][2], int wr, int wc, int fr, int fq) const {
#pragma unroll
        for (int m = 0; m < 4; ++m)
#pragma unroll
            for (int n = 0; n < 2; ++n) {
                const size_t off = (size_t)(row0 + wr * 64 + m * 16 + fr) * DM + col0 + wc * 32 + n * 16 + 4 * fq;
                *(u32x2*)(out + off) = combine4(acc[m][n], *(const u32x2*)(x + off), *(const u32x2*)(bout + off));
            }
    }
};

constexpr int TAIL_BLK0 = 6;
__global__ void __launch_bounds__(512, 2) k_mega(const bf16_t* __restrict__ x, const bf16_t* __restrict__ Wq, const bf16_t* __restrict__ Wk, const bf16_t* __restrict__ Wv,
                                                 const bf16_t* __restrict__ Wo, const bf16_t* __restrict__ w1, const bf16_t* __restrict__ b1, const bf16_t* __restrict__ w2,
                                                 const bf16_t* __restrict__ b2, char* ws, bf16_t* __restrict__ out) {
    extern __shared__ __attribute__((aligned(16))) unsigned char shm_[];
    LAS unsigned char* lds = (LAS unsigned char*)shm_;
    const int b = blockIdx.x;
    const int role = (b >> 3) & 1, r = (b >> 4) * 8 + (b & 7);
    if (role == 0) { chain_body<4>(lds, r, Wq, Wk, Wv, Wo, ws); return; }
#define WSTAMP(i) do {} while (0)
    WSTAMP(0);
    {
        const int pm2 = r & 1, pn2 = r >> 1;
        EpiKV0 E{ws, pm2, pn2};
        gemm128_w8<4, EpiKV0>(lds, (const bf16_t*)(ws + WS_XN) + (size_t)pm2 * 128 * DM, DM, (pn2 >= 32 ? Wv : Wk) + (pn2 & 31) * 128, HD, DM, E);
    }
    WSTAMP(1);
    {
        SchedKVQ S{(const bf16_t*)(ws + WS_XN), Wq, (long)(Wk - Wq), (long)(Wv - Wq), r};
        EpiQKV E{ws}; g8::gemm_phase<EpiQKV, SchedKVQ>(lds, DM, DM, HD, S, E);
    }
    WSTAMP(2);
    {
        const int L = r >= 96 ? r - 96 : 32 + r;
        SchedOne S; S.valid = (r >= 96) || (r < 80); S.u.pm = L & 7; S.u.pn = L >> 3; S.u.A = (const bf16_t*)(ws + WS_Y) + (size_t)S.u.pm * 256 * DM; S.u.B = w1 + S.u.pn * 256;
        EpiMLP1 E{ws, b1}; g8::gemm_phase<EpiMLP1, SchedOne>(lds, DM, DM, FF, S, E);
    }
    WSTAMP(3);
    {
        const int pm = r >> 3, pn = r & 7;
        wait_counter(ws, CNT_H + (pm >> 1), 14 * 8);
        EpiMLP2 E{ws, b2, pm * 128, pn * 128};
        gemm128_w8<4, EpiMLP2>(lds, (const bf16_t*)(ws + WS_H) + (size_t)pm * 128 * FF, FF, w2 + pn * 128, DM, FF, E);
    }
    WSTAMP(4);
    const int blk = r >> 4, t = r & 15;
    if (blk >= TAIL_BLK0) return;
    {
        if (threadIdx.x < 64) poll_ge(ws, CNT_STEP, (unsigned)(8 * blk + 8));
        wait_counter(ws, CNT_QKV + blk, (blk == 0 ? 128 * 8 : 32 * 8) + 16 * 8);
        WSTAMP(5);
        const int wave = threadIdx.x >> 6;
#pragma unroll 1
        for (int i = 0; i < 2; ++i) { const int item = t * 16 + wave * 2 + i; xattn_item<true>(lds + wave * 48 * XV_PITCH, ws, blk * 8 + (item >> 5), item & 31); }
        signal_counter(ws, CNT_O + blk);
    }
    {
        if (threadIdx.x < 64) poll_ge(ws, CNT_B + blk, 16 * 8);
        wait_counter(ws, CNT_O + blk, 16 * 8);
        WSTAMP(6);
        const int pm = blk * 2 + (t >> 3), pn = t & 7;
        EpiOut E{x, (const bf16_t*)(ws + WS_B), out, pm * 128, pn * 128};
        gemm128_w8<4, EpiOut>(lds, (const bf16_t*)(ws + WS_O) + (size_t)pm * 128 * HD, HD, Wo + pn * 128, DM, HD, E);
    }
    WSTAMP(7);
}

__global__ void __launch_bounds__(256) k_tail_attn(char* ws) {
    __shared__ __attribute__((aligned(16))) unsigned char shm_[4 * 48 * XV_PITCH];
    const int wave = threadIdx.x >> 6, item = blockIdx.x * 4 + wave;
    xattn_item<false>((LAS unsigned char*)shm_ + wave * 48 * XV_PITCH, ws, TAIL_BLK0 * 8 + (item >> 5), item & 31);
}
__global__ void __launch_bounds__(256) k_tail_gemm(const bf16_t* __restrict__ x, const bf16_t* __restrict__ Wo, const char* __restrict__ ws, bf16_t* __restrict__ out) {
    extern __shared__ __attribute__((aligned(16))) unsigned char shm_[];
    const int tid = threadIdx.x, wave = __builtin_amdgcn_readfirstlane(tid >> 6), lane = tid & 63, fr = lane & 15, fq = lane >> 4;
    LAS unsigned char* lds = (LAS unsigned char*)shm_ + wave * 24576;
    const int pn = blockIdx.x & 31, pm = blockIdx.x >> 5, row0 = TAIL_BLK0 * 256 + pm * 32, col0 = pn * 32;
    const char* cA = (const char*)((const bf16_t*)(ws + WS_O) + (size_t)row0 * HD + wave * 1024);
    const char* cB = (const char*)(Wo + (size_t)wave * 1024 * DM + col0);
    unsigned voffA[4], voffB[4];
#pragma unroll
    for (int i = 0; i < 4; ++i) { const int c = i * 64 + lane, row = c >> 3, ch = (c & 7) ^ (row & 7); voffA[i] = (unsigned)(row * HD + ch * 8) * 2u;
        const int k = c >> 2, cb = c & 3; voffB[i] = (unsigned)(k * DM + cb * 8) * 2u; }
#define STG(buf, t) do { _Pragma("unroll") for (int _i = 0; _i < 4; ++_i) { \
        __builtin_amdgcn_global_load_lds((const unsigned*)(cA + (size_t)(t) * 128 + voffA[_i]), (LAS unsigned*)(lds + (buf) * 8192 + _i * 1024), 16, 0, 0); \
        __builtin_amdgcn_global_load_lds((const unsigned*)(cB + (size_t)(t) * 64 * DM * 2 + voffB[_i]), (LAS unsigned*)(lds + (buf) * 8192 + 4096 + _i * 1024), 16, 0, 0); } } while (0)
    f32x4 acc[2][2];
#pragma unroll
    for (int m = 0; m < 2; ++m) { acc[m][0] = (f32x4){0.f, 0.f, 0.f, 0.f}; acc[m][1] = (f32x4){0.f, 0.f, 0.f, 0.f}; }
    STG(0, 0); STG(1, 1);
    int cur = 0;
    for (int t = 0; t < 16; ++t) {
        int nx = cur + 2; if (nx >= 3) nx -= 3;
        if (t + 2 < 16) { STG(nx, t + 2); asm volatile("s_waitcnt vmcnt(16)" ::: "memory"); }
        else if (t + 1 < 16) asm volatile("s_waitcnt vmcnt(8)" ::: "memory");
        else asm volatile("s_waitcnt vmcnt(0)" ::: "memory");
        LAS unsigned char* sa = lds + cur * 8192; LAS unsigned char* sb = sa + 4096;
        bf16x8 At[2][2], Bt[2][2];
#pragma unroll
        for (int k = 0; k < 2; ++k) {
#pragma unroll
            for (int m = 0; m < 2; ++m) { const int row = m * 16 + fr; At[m][k] = *(const LAS bf16x8*)(sa + row * 128 + (((k * 4 + fq) ^ (row & 7)) * 16)); }
#pragma unroll
            for (int n = 0; n < 2; ++n) {
                const int off = (k * 32 + 8 * fq + (fr >> 2)) * 64 + n * 32 + (lane & 3) * 8;
                const bf16x4 a0 = __builtin_amdgcn_ds_read_tr16_b64_v4i16((LAS bf16x4*)(sb + off)), a1 = __builtin_amdgcn_ds_read_tr16_b64_v4i16((LAS bf16x4*)(sb + off + 256));
                Bt[n][k] = __builtin_shufflevector(a0, a1, 0, 1, 2, 3, 4, 5, 6, 7); }
        }
#pragma unroll
        for (int k = 0; k < 2; ++k)
#pragma unroll
            for (int m = 0; m < 2; ++m)
#pragma unroll
                for (int n = 0; n < 2; ++n) acc[m][n] = __builtin_amdgcn_mfma_f32_16x16x32_bf16(Bt[n][k], At[m][k], acc[m][n], 0, 0, 0);
        asm volatile("s_waitcnt lgkmcnt(0)" ::: "memory");
        cur = cur + 1 == 3 ? 0 : cur + 1;
    }
#undef STG
    __syncthreads();
    LAS f32x4* red = (LAS f32x4*)shm_;
#pragma unroll
    for (int m = 0; m < 2; ++m)
#pragma unroll
        for (int n = 0; n < 2; ++n) red[((wave * 2 + m) * 2 + n) * 64 + lane] = acc[m][n];
    __syncthreads();
    {
        const int m = wave >> 1, n = wave & 1;
        const f32x4 v = (red[((0 * 2 + m) * 2 + n) * 64 + lane] + red[((1 * 2 + m) * 2 + n) * 64 + lane]) + (red[((2 * 2 + m) * 2 + n) * 64 + lane] + red[((3 * 2 + m) * 2 + n) * 64 + lane]);
        const size_t off = (size_t)(row0 + m * 16 + fr) * DM + col0 + n * 16 + 4 * fq;
        *(u32x2*)(out + off) = combine4(v, *(const u32x2*)(x + off), *(const u32x2*)((const bf16_t*)(ws + WS_B) + off));
    }
}

extern "C" void kernel_launch(void* const* d_in, const int* in_sizes, int n_in, void* d_out, int out_size, void* d_ws, size_t ws_size, hipStream_t stream) {
    const bf16_t* x = (const bf16_t*)d_in[0];
    const bf16_t* mem0 = (const bf16_t*)d_in[1];
    const bf16_t* Wq = (const bf16_t*)d_in[2];
    const bf16_t* Wk = (const bf16_t*)d_in[3];
    const bf16_t* Wv = (const bf16_t*)d_in[4];
    const bf16_t* Wo = (const bf16_t*)d_in[5];
    const bf16_t* w1 = (const bf16_t*)d_in[8];
    const bf16_t* b1 = (const bf16_t*)d_in[9];
    const bf16_t* w2 = (const bf16_t*)d_in[10];
    const bf16_t* b2 = (const bf16_t*)d_in[11];
    char* ws = (char*)d_ws;
    bf16_t* out = (bf16_t*)d_out;
    (void)hipFuncSetAttribute((const void*)k_mega, hipFuncAttributeMaxDynamicSharedMemorySize, CH_LDS);
    (void)hipFuncSetAttribute((const void*)k_tail_gemm, hipFuncAttributeMaxDynamicSharedMemorySize, 4 * 24576);
    k_prep<<<S_LEN / 4, 256, 0, stream>>>(x, mem0, ws);
    k_mega<<<256, 512, CH_LDS, stream>>>(x, Wq, Wk, Wv, Wo, w1, b1, w2, b2, ws, out);
    k_tail_attn<<<64 * (8 - TAIL_BLK0), 256, 0, stream>>>(ws);
    k_tail_gemm<<<256 * (8 - TAIL_BLK0), 256, 4 * 24576, stream>>>(x, Wo, ws, out);
}
```

```cpp
#include <hip/hip_runtime.h>
#include <hip/hip_bf16.h>

#define LAS __attribute__((address_space(3)))
typedef unsigned short bf16_t;
typedef short bf16x8 __attribute__((ext_vector_type(8)));
typedef short bf16x4 __attribute__((ext_vector_type(4)));
typedef float f32x4 __attribute__((ext_vector_type(4)));
typedef float f32x2 __attribute__((ext_vector_type(2)));
typedef unsigned u32x4 __attribute__((ext_vector_type(4)));
typedef unsigned u32x2 __attribute__((ext_vector_type(2)));

constexpr int S_LEN = 2048, DM = 1024, NH = 32, DH = 128, HD = NH * DH, MM = 8, SEG = 32, NSEG = S_LEN / SEG, FF = 3584;
constexpr int AUX_SC1 = 16;
constexpr unsigned SENT = 0xFFFFFFFFu;

__device__ __forceinline__ float bf2f(unsigned b) { return __builtin_bit_cast(float, b << 16); }
typedef __bf16 bf16x2_t __attribute__((ext_vector_type(2)));
__device__ __forceinline__ unsigned pk_bf16(float lo, float hi) { const f32x2 v = {lo, hi}; return __builtin_bit_cast(unsigned, __builtin_convertvector(v, bf16x2_t)); }
__device__ __forceinline__ float rbf(float x) { return bf2f(pk_bf16(x, x) & 0xffffu); }
__device__ __forceinline__ unsigned desent(unsigned w) { return w == SENT ? 0x7FC07FC0u : w; }
__device__ __forceinline__ unsigned umax2(unsigned a, unsigned b) { return a > b ? a : b; }
__device__ __forceinline__ unsigned umax4(u32x4 v) { return umax2(umax2(v.x, v.y), umax2(v.z, v.w)); }

constexpr size_t WS_XN = 0;
constexpr size_t WS_Y = WS_XN + (size_t)S_LEN * DM * 2;
constexpr size_t WS_Q = WS_Y + (size_t)S_LEN * DM * 2;
constexpr size_t WS_K = WS_Q + (size_t)S_LEN * HD * 2;
constexpr size_t WS_V = WS_K + (size_t)S_LEN * HD * 2;
constexpr size_t WS_H = WS_V + (size_t)S_LEN * HD * 2;
constexpr size_t WS_O = WS_H + (size_t)S_LEN * FF * 2;
constexpr size_t WS_B = WS_O + (size_t)S_LEN * HD * 2;
constexpr size_t WS_SENT0 = WS_B + (size_t)S_LEN * DM * 2;
constexpr size_t MEM_B = (size_t)MM * DM * 2;
constexpr size_t WS_MEM = WS_SENT0;
constexpr size_t QKVM_B = (size_t)NH * 3 * MM * DH * 2;
constexpr size_t WS_QKVM = WS_MEM + (size_t)(NSEG + 1) * MEM_B;
constexpr size_t OM_B = (size_t)MM * HD * 2;
constexpr size_t WS_OM = WS_QKVM + (size_t)NSEG * QKVM_B;
constexpr size_t WS_SENT1 = WS_OM + (size_t)NSEG * OM_B;
constexpr size_t WS_CNT = WS_SENT1;
constexpr int CNT_H = 0;
constexpr int CNT_B = 8;
constexpr int CNT_O = 16;
constexpr int CNT_QKV = 24;
constexpr int CNT_FILL = 33;
constexpr int CNT_STEP = 32;
constexpr int CNT_KV = 40;
constexpr int CNT_KV0B = 40 + 256;
constexpr int CNT_N = 40 + 256 + 32;
constexpr size_t WS_TMO = WS_CNT + (size_t)CNT_N * 256;
constexpr size_t WS_END = WS_TMO + 256;

__device__ __forceinline__ float wave_sum(float v) {
#pragma unroll
    for (int o = 32; o > 0; o >>= 1) v += __shfl_xor(v, o);
    return v;
}
__device__ __forceinline__ void ln_row(float (&v)[16]) {
    float s = 0.f;
#pragma unroll
    for (int i = 0; i < 16; ++i) s += v[i];
    const float mean = wave_sum(s) * (1.0f / DM);
    float q = 0.f;
#pragma unroll
    for (int i = 0; i < 16; ++i) { const float d = v[i] - mean; q += d * d; }
    const float var = rbf(wave_sum(q) * (1.0f / DM));
    const float mb = rbf(mean);
    const float den = rbf(sqrtf(rbf(var + 1e-5f)));
#pragma unroll
    for (int i = 0; i < 16; ++i) v[i] = rbf(rbf(v[i] - mb) / den);
}
__global__ void __launch_bounds__(256) k_prep(const bf16_t* __restrict__ x, const bf16_t* __restrict__ mem0, char* __restrict__ ws) {
    const int wave = threadIdx.x >> 6, lane = threadIdx.x & 63, row = blockIdx.x * 4 + wave;
    const u32x4* src = (const u32x4*)(x + (size_t)row * DM);
    u32x4 a = src[lane], b = src[lane + 64];
    float v[16];
    const unsigned w[8] = {a.x, a.y, a.z, a.w, b.x, b.y, b.z, b.w};
#pragma unroll
    for (int i = 0; i < 8; ++i) { v[2 * i] = bf2f(w[i] & 0xffffu); v[2 * i + 1] = bf2f(w[i] >> 16); }
    ln_row(v);
    u32x4 o0, o1;
    o0.x = pk_bf16(v[0], v[1]); o0.y = pk_bf16(v[2], v[3]); o0.z = pk_bf16(v[4], v[5]); o0.w = pk_bf16(v[6], v[7]);
    o1.x = pk_bf16(v[8], v[9]); o1.y = pk_bf16(v[10], v[11]); o1.z = pk_bf16(v[12], v[13]); o1.w = pk_bf16(v[14], v[15]);
    u32x4* dx = (u32x4*)(ws + WS_XN + (size_t)row * DM * 2);
    dx[lane] = o0; dx[lane + 64] = o1;
    ln_row(v);
    o0.x = pk_bf16(v[0], v[1]); o0.y = pk_bf16(v[2], v[3]); o0.z = pk_bf16(v[4], v[5]); o0.w = pk_bf16(v[6], v[7]);
    o1.x = pk_bf16(v[8], v[9]); o1.y = pk_bf16(v[10], v[11]); o1.z = pk_bf16(v[12], v[13]); o1.w = pk_bf16(v[14], v[15]);
    u32x4* dy = (u32x4*)(ws + WS_Y + (size_t)row * DM * 2);
    dy[lane] = o0; dy[lane + 64] = o1;
    {
        const size_t n16 = (WS_SENT1 - WS_SENT0 - MEM_B) / 16;
        u32x4* dst = (u32x4*)(ws + WS_SENT0 + MEM_B);
        const u32x4 f = (u32x4){SENT, SENT, SENT, SENT};
        for (size_t i = (size_t)blockIdx.x * 256 + threadIdx.x; i < n16; i += (size_t)gridDim.x * 256) dst[i] = f;
    }
    if (blockIdx.x >= 8 && blockIdx.x < 8 + (CNT_N * 256 / 16 + 255) / 256) {
        const int i = (blockIdx.x - 8) * 256 + threadIdx.x; if (i < CNT_N * 256 / 16) ((u32x4*)(ws + WS_CNT))[i] = (u32x4){0u, 0u, 0u, 0u}; }
    if (blockIdx.x < 4) {
        const int c = blockIdx.x * 256 + threadIdx.x, r = c & 7, kg = c >> 3;
        u32x4 m = *(const u32x4*)(mem0 + (size_t)r * DM + kg * 8);
        m.x = desent(m.x); m.y = desent(m.y); m.z = desent(m.z); m.w = desent(m.w);
        *(u32x4*)(ws + WS_MEM + (size_t)c * 16) = m;
    }
}

namespace g8 {
constexpr int BM = 256, BK = 64, HALF = 128, HTB = HALF * BK * 2, STAGE_BYTES = 8 * HTB;
__host__ __device__ __forceinline__ int lds_byte(int r, int c) { const int st = (r >> 4) * 2 + (c >> 5), rr = r & 15, cc = c & 31, ob = rr * 64 + cc * 2; return st * 1024 + (ob ^ (((ob >> 9) & 1) << 5)); }
__host__ __device__ __forceinline__ void stage_rc(int b, int& R, int& C) { const int st = b / 1024, sb = b % 1024, swz = sb ^ (((sb >> 9) & 1) << 5); R = (st >> 1) * 16 + swz / 64; C = (st & 1) * 32 + (swz % 64) / 2; }
__host__ __device__ __forceinline__ int bsw(int row) { return (row & 3) | (((row >> 3) & 1) << 2); }
struct Unit { const bf16_t* A; const bf16_t* B; int pm, pn; };

template <class Epi, class Sched>
__device__ __forceinline__ void gemm_phase(LAS unsigned char* lds, const int K, const int lda, const int ldb, const Sched& S, const Epi& E) {
    int tid = threadIdx.x; asm volatile("" : "+v"(tid));
    const int wid = __builtin_amdgcn_readfirstlane(tid >> 6), lane = tid & 63, wr = wid >> 2, wc = wid & 3, fr = lane & 15, fq = lane >> 4;
    const int nt = K / BK;
    unsigned voffA[2], voffB[2];
#pragma unroll
    for (int i = 0; i < 2; ++i) { int R, C; stage_rc(tid * 16 + i * 8192, R, C); voffA[i] = (unsigned)(R * lda + C) * 2u;
        const int b = tid * 16 + i * 8192, row = b >> 8, ch = (b & 255) >> 4, seg = (ch >> 1) ^ bsw(row);
        voffB[i] = (unsigned)(row * ldb + seg * 16 + (ch & 1) * 8) * 2u; }
    const size_t kstepA = (size_t)(BK * 2), kstepB = (size_t)BK * ldb * 2;
    const size_t hstepA = (size_t)HALF * lda * 2, hstepB = (size_t)HALF * 2;
    const unsigned ldsw = (unsigned)wid * 1024u;
    const int aoff = lds_byte(wr * 64 + fr, fq * 8);
    const int q4 = (lane & 15) >> 2, p4 = lane & 3, sB = q4 | ((fq & 1) << 2);
    const int boff0 = (8 * fq + q4) * 256 + (((wc * 2 + 0) ^ sB) * 32) + 8 * p4;
    const int boff1 = (8 * fq + q4) * 256 + (((wc * 2 + 1) ^ sB) * 32) + 8 * p4;
#define G8_SA(b, h) (((b) * 2 + (h)) * HTB)
#define G8_SB(b, h) ((4 + (b) * 2 + (h)) * HTB)
#define G8_STAGE(bufoff, gbase, voff) do { _Pragma("unroll") for (int _i = 0; _i < 2; ++_i) \
        __builtin_amdgcn_global_load_lds((const unsigned*)((const char*)(gbase) + (voff)[_i]), (LAS unsigned*)(lds + (bufoff) + ldsw + _i * 8192), 16, 0, 0); } while (0)
#define G8_LDA(dst, b, h) do { _Pragma("unroll") for (int m = 0; m < 4; ++m) _Pragma("unroll") for (int k = 0; k < 2; ++k) dst[m][k] = *(const LAS bf16x8*)(lds + G8_SA(b, h) + aoff + m * 2048 + k * 1024); } while (0)
#define G8_TR(off) __builtin_amdgcn_ds_read_tr16_b64_v4i16((LAS bf16x4*)(lds + (off)))
#define G8_LDB(dst, b, h) do { _Pragma("unroll") for (int k = 0; k < 2; ++k) { \
        bf16x4 a0 = G8_TR(G8_SB(b, h) + boff0 + k * 8192), a1 = G8_TR(G8_SB(b, h) + boff0 + k * 8192 + 1024); \
        bf16x4 c0 = G8_TR(G8_SB(b, h) + boff1 + k * 8192), c1 = G8_TR(G8_SB(b, h) + boff1 + k * 8192 + 1024); \
        dst[0][k] = __builtin_shufflevector(a0, a1, 0, 1, 2, 3, 4, 5, 6, 7); dst[1][k] = __builtin_shufflevector(c0, c1, 0, 1, 2, 3, 4, 5, 6, 7); } } while (0)
#define G8_MMA(ai, bj, At, Bt) do { __builtin_amdgcn_s_setprio(1); _Pragma("unroll") for (int m = 0; m < 4; ++m) _Pragma("unroll") for (int n = 0; n < 2; ++n) _Pragma("unroll") for (int k = 0; k < 2; ++k) \
        acc[ai][bj][m][n] = __builtin_amdgcn_mfma_f32_16x16x32_bf16(Bt[n][k], At[m][k], acc[ai][bj][m][n], 0, 0, 0); __builtin_amdgcn_s_setprio(0); } while (0)
#define G8_WAIT_V(n) asm volatile("s_waitcnt vmcnt(" #n ")" ::: "memory")
#define G8_WAIT_L(n) asm volatile("s_waitcnt lgkmcnt(" #n ")" ::: "memory")
#define G8_BAR __builtin_amdgcn_s_barrier()
#define G8_SCHED __builtin_amdgcn_sched_barrier(0)
    Unit cur, nxt; int ui = 0;
    if (!S.next(0, cur)) return;
    f32x4 acc[2][2][4][2];
#pragma unroll
    for (int a = 0; a < 2; ++a)
#pragma unroll
        for (int b = 0; b < 2; ++b)
#pragma unroll
            for (int m = 0; m < 4; ++m)
#pragma unroll
                for (int n = 0; n < 2; ++n) acc[a][b][m][n] = (f32x4){0.f, 0.f, 0.f, 0.f};
    bf16x8 At[4][2], B0[2][2], B1[2][2];
    const char* cA = (const char*)cur.A; const char* cB = (const char*)cur.B;
    G8_STAGE(G8_SB(0, 0), cB, voffB); G8_STAGE(G8_SA(0, 0), cA, voffA); G8_STAGE(G8_SB(0, 1), cB + hstepB, voffB); G8_STAGE(G8_SA(0, 1), cA + hstepA, voffA);
    if (wr == 1) G8_BAR;
    G8_WAIT_V(4); G8_BAR;
    G8_STAGE(G8_SB(1, 0), cB + kstepB, voffB); G8_STAGE(G8_SA(1, 0), cA + kstepA, voffA); G8_STAGE(G8_SB(1, 1), cB + hstepB + kstepB, voffB);
    G8_WAIT_V(6); G8_BAR;
    for (;;) {
        const bool has_next = S.next(ui + 1, nxt);
        const char* nA = has_next ? (const char*)nxt.A : cA; const char* nB = has_next ? (const char*)nxt.B : cB;
        for (int t = 0; t < nt; t += 2) {
            const bool last = (t == nt - 2);
            const char* a1 = cA + (size_t)(t + 1) * kstepA;
            const char* a2 = last ? nA : cA + (size_t)(t + 2) * kstepA; const char* b2 = last ? nB : cB + (size_t)(t + 2) * kstepB;
            const char* a3 = a2 + kstepA; const char* b3 = b2 + kstepB;
            G8_LDB(B0, 0, 0); G8_SCHED; G8_LDA(At, 0, 0); G8_STAGE(G8_SA(1, 1), a1 + hstepA, voffA);
            G8_WAIT_L(8); G8_BAR; G8_WAIT_L(0); G8_MMA(0, 0, At, B0); G8_BAR; G8_SCHED;
            G8_LDB(B1, 0, 1); G8_STAGE(G8_SB(0, 0), b2, voffB);
            G8_BAR; G8_WAIT_L(0); G8_MMA(0, 1, At, B1); G8_BAR;
            G8_LDA(At, 0, 1); G8_STAGE(G8_SA(0, 0), a2, voffA);
            G8_BAR; G8_WAIT_L(0); G8_MMA(1, 0, At, B0); G8_BAR; G8_SCHED;
            G8_STAGE(G8_SB(0, 1), b2 + hstepB, voffB);
            G8_WAIT_V(6); G8_BAR; G8_MMA(1, 1, At, B1); G8_BAR;
            G8_LDB(B0, 1, 0); G8_SCHED; G8_LDA(At, 1, 0); G8_STAGE(G8_SA(0, 1), a2 + hstepA, voffA);
            G8_WAIT_L(8); G8_BAR; G8_WAIT_L(0); G8_MMA(0, 0, At, B0); G8_BAR; G8_SCHED;
            G8_LDB(B1, 1, 1); G8_STAGE(G8_SB(1, 0), b3, voffB);
            G8_BAR; G8_WAIT_L(0); G8_MMA(0, 1, At, B1); G8_BAR;
            G8_LDA(At, 1, 1); G8_STAGE(G8_SA(1, 0), a3, voffA);
            G8_BAR; G8_WAIT_L(0); G8_MMA(1, 0, At, B0); G8_BAR; G8_SCHED;
            G8_STAGE(G8_SB(1, 1), b3 + hstepB, voffB);
            G8_WAIT_V(6); G8_BAR; G8_MMA(1, 1, At, B1); G8_BAR;
        }
        E(acc, cur, wr, wc, fr, fq);
        if (!has_next) break;
#pragma unroll
        for (int a = 0; a < 2; ++a)
#pragma unroll
            for (int b = 0; b < 2; ++b)
#pragma unroll
                for (int m = 0; m < 4; ++m)
#pragma unroll
                    for (int n = 0; n < 2; ++n) acc[a][b][m][n] = (f32x4){0.f, 0.f, 0.f, 0.f};
        cur = nxt; cA = nA; cB = nB; ++ui;
    }
    G8_WAIT_V(0);
    if (wr == 0) G8_BAR;
    G8_BAR;
#undef G8_SA
#undef G8_SB
#undef G8_STAGE
#undef G8_LDA
#undef G8_LDB
#undef G8_TR
#undef G8_MMA
#undef G8_WAIT_V
#undef G8_WAIT_L
#undef G8_BAR
#undef G8_SCHED
}
}

__device__ __forceinline__ float gelu_tanh(float x) {
    const float u = 0.7978845608028654f * (x + 0.044715f * x * x * x);
    const float th = 1.0f - 2.0f * __builtin_amdgcn_rcpf(__expf(2.0f * u) + 1.0f);
    return 0.5f * x * (1.0f + th);
}

__device__ __forceinline__ void wait_counter(const char* ws, int idx, unsigned need) {
    if (threadIdx.x < 64) {
        const unsigned* p = (const unsigned*)(ws + WS_CNT + (size_t)idx * 256);
        for (unsigned spins = 0; (unsigned)__builtin_amdgcn_readfirstlane(__hip_atomic_load(p, __ATOMIC_RELAXED, __HIP_MEMORY_SCOPE_AGENT)) < need;) {
            __builtin_amdgcn_s_sleep(8);
            if (++spins > 400000u) break;
        }
        __builtin_amdgcn_fence(__ATOMIC_ACQUIRE, "agent");
        asm volatile("s_waitcnt vmcnt(0)" ::: "memory");
    }
    asm volatile("" ::: "memory"); __builtin_amdgcn_s_barrier(); asm volatile("" ::: "memory");
}
__device__ __forceinline__ void signal_counter(char* ws, int idx) {
    asm volatile("s_waitcnt vmcnt(0)" ::: "memory");
    if ((threadIdx.x & 63) == 0) __hip_atomic_fetch_add((unsigned*)(ws + WS_CNT + (size_t)idx * 256), 1u, __ATOMIC_RELAXED, __HIP_MEMORY_SCOPE_AGENT);
}

__device__ __forceinline__ unsigned poll_ge(const char* ws, int idx, unsigned need) {
    const unsigned* p = (const unsigned*)(ws + WS_CNT + (size_t)idx * 256);
    unsigned v = 0;
    for (unsigned spins = 0; (v = (unsigned)__builtin_amdgcn_readfirstlane(__hip_atomic_load(p, __ATOMIC_RELAXED, __HIP_MEMORY_SCOPE_AGENT))) < need;) {
        __builtin_amdgcn_s_sleep(8);
        if (++spins > 400000u) break;
    }
    asm volatile("" ::: "memory");
    return v;
}

constexpr unsigned SPIN_CAP = 200000u;
constexpr int KS_PITCH = 272;
constexpr int CH_LDS = 160 * 1024;

__device__ __forceinline__ float xmax16_32(float v) {
    auto a = __builtin_amdgcn_permlane16_swap(__builtin_bit_cast(unsigned, v), __builtin_bit_cast(unsigned, v), false, false);
    v = fmaxf(__builtin_bit_cast(float, (unsigned)a[0]), __builtin_bit_cast(float, (unsigned)a[1]));
    auto b = __builtin_amdgcn_permlane32_swap(__builtin_bit_cast(unsigned, v), __builtin_bit_cast(unsigned, v), false, false);
    return fmaxf(__builtin_bit_cast(float, (unsigned)b[0]), __builtin_bit_cast(float, (unsigned)b[1]));
}
__device__ __forceinline__ float xsum16_32(float v) {
    auto a = __builtin_amdgcn_permlane16_swap(__builtin_bit_cast(unsigned, v), __builtin_bit_cast(unsigned, v), false, false);
    v = __builtin_bit_cast(float, (unsigned)a[0]) + __builtin_bit_cast(float, (unsigned)a[1]);
    auto b = __builtin_amdgcn_permlane32_swap(__builtin_bit_cast(unsigned, v), __builtin_bit_cast(unsigned, v), false, false);
    return __builtin_bit_cast(float, (unsigned)b[0]) + __builtin_bit_cast(float, (unsigned)b[1]);
}

__device__ __forceinline__ u32x4 quad_gather(unsigned v) {
    u32x4 r;
    r.x = (unsigned)__builtin_amdgcn_update_dpp(0, (int)v, 0x00, 0xf, 0xf, true); r.y = (unsigned)__builtin_amdgcn_update_dpp(0, (int)v, 0x55, 0xf, 0xf, true);
    r.z = (unsigned)__builtin_amdgcn_update_dpp(0, (int)v, 0xAA, 0xf, 0xf, true); r.w = (unsigned)__builtin_amdgcn_update_dpp(0, (int)v, 0xFF, 0xf, 0xf, true);
    return r;
}
#define STAMP(i) do {} while (0)
#define PCOUNT(sh) do {} while (0)
#define CH_DELAY1 14
#define CH_DELAY2 12
#define CH_DELAY3 12

template <int CPH>
__device__ __forceinline__ void chain_body(LAS unsigned char* lds, const int b, const bf16_t* __restrict__ Wq, const bf16_t* __restrict__ Wk, const bf16_t* __restrict__ Wv,
                                           const bf16_t* __restrict__ Wo, char* ws) {
    constexpr int ES = 128 / CPH, NE = ES / 16, NT = 3 * NE, RG = CPH / 2, RPG = 8 / RG;
    constexpr int VP = ES * 2;
    constexpr int L_RED = 0;
    constexpr int L_RED2 = L_RED + 8 * NT * 512;
    constexpr int L_KS = L_RED2 + 8 * RPG * 64;
    constexpr int L_VS = L_KS + 2 * 32 * KS_PITCH;
    constexpr int L_OS = L_VS + 2 * 48 * VP;
    constexpr bool WOL = (CPH == 4);
    constexpr int L_WO = L_OS + 8 * 1024 * RPG;
    static_assert(L_WO + (WOL ? 65536 : 0) <= 156 * 1024, "LDS");
    const int tid = threadIdx.x, wave = __builtin_amdgcn_readfirstlane(tid >> 6), lane = tid & 63, fr = lane & 15, fq = lane >> 4;
    const int xc = b & 7, yc = b >> 3;
    const int h = xc + 8 * (yc / CPH), j = yc % CPH, g = yc % RG, c = xc * 8 + yc / RG;
    const __amdgpu_buffer_rsrc_t rs = __builtin_amdgcn_make_buffer_rsrc(ws + WS_SENT0, 0, (int)(WS_SENT1 - WS_SENT0), 0x00020000);
    constexpr int O_MEM = (int)(WS_MEM - WS_SENT0), O_QKVM = (int)(WS_QKVM - WS_SENT0), O_OM = (int)(WS_OM - WS_SENT0);
    const __amdgpu_buffer_rsrc_t rkv = __builtin_amdgcn_make_buffer_rsrc(ws + WS_K, 0, (int)(WS_H - WS_K), 0x00020000);
    constexpr int O_V = (int)(WS_V - WS_K);
    bool dead = false;

    bf16x8 wq[NT][4], wo[16];
    {
        LAS unsigned char* stg = lds + wave * 8192;
        constexpr int CPR = ES / 8;
        static_assert(128 * CPR / 64 == 8, "8 chunks per lane and pass");
        u32x4 t[5][8];
        auto issue = [&](int ps) {
            if (ps < 3) { const bf16_t* W = (ps == 0 ? Wq : (ps == 1 ? Wk : Wv)) + (size_t)wave * 128 * HD + h * DH + j * ES;
#pragma unroll
                for (int i = 0; i < 8; ++i) { const int ci = i * 64 + lane; t[ps][i] = *(const u32x4*)(W + (size_t)(ci / CPR) * HD + (ci % CPR) * 8); } }
            else { const bf16_t* W = Wo + (size_t)(wave * 512 + (ps - 3) * 256) * DM + c * 16;
#pragma unroll
                for (int i = 0; i < 8; ++i) { const int ci = i * 64 + lane; t[ps][i] = *(const u32x4*)(W + (size_t)(ci >> 1) * DM + (ci & 1) * 8); } }
        };
        issue(0); issue(1); issue(2);
        asm volatile("" ::: "memory");
#pragma unroll
        for (int ps = 0; ps < 5; ++ps) {
#pragma unroll
            for (int i = 0; i < 8; ++i) *(LAS u32x4*)(stg + (i * 64 + lane) * 16) = t[ps][i];
            asm volatile("s_waitcnt lgkmcnt(0)" ::: "memory");
            if (ps + 3 < 5) issue(ps + 3);
            asm volatile("" ::: "memory");
            if (ps < 3) {
#pragma unroll
                for (int e = 0; e < NE; ++e)
#pragma unroll
                    for (int i = 0; i < 4; ++i) {
                        const int off = (i * 32 + 8 * fq + (fr >> 2)) * (ES * 2) + e * 32 + (lane & 3) * 8;
                        const bf16x4 a0 = __builtin_amdgcn_ds_read_tr16_b64_v4i16((LAS bf16x4*)(stg + off)), a1 = __builtin_amdgcn_ds_read_tr16_b64_v4i16((LAS bf16x4*)(stg + off + 4 * ES * 2));
                        wq[ps * NE + e][i] = __builtin_shufflevector(a0, a1, 0, 1, 2, 3, 4, 5, 6, 7);
                    }
            } else {
#pragma unroll
                for (int i = 0; i < 8; ++i) {
                    const int off = (i * 32 + 8 * fq + (fr >> 2)) * 32 + (lane & 3) * 8;
                    const bf16x4 a0 = __builtin_amdgcn_ds_read_tr16_b64_v4i16((LAS bf16x4*)(stg + off)), a1 = __builtin_amdgcn_ds_read_tr16_b64_v4i16((LAS bf16x4*)(stg + off + 128));
                    wo[(ps - 3) * 8 + i] = __builtin_shufflevector(a0, a1, 0, 1, 2, 3, 4, 5, 6, 7);
                }
            }
            asm volatile("s_waitcnt lgkmcnt(0)" ::: "memory");
        }
        if (WOL) {
#pragma unroll
            for (int i = 8; i < 16; ++i) *(LAS bf16x8*)(lds + L_WO + (wave * 8 + i - 8) * 1024 + lane * 16) = wo[i];
        }
    }
    __syncthreads();

    if (tid < 2 * 8 * VP / 16) { const int bf = tid / (8 * VP / 16), r = tid % (8 * VP / 16); *(LAS u32x4*)(lds + L_VS + bf * 48 * VP + 40 * VP + r * 16) = (u32x4){0u, 0u, 0u, 0u}; }
    {
        poll_ge(ws, CNT_KV + (h >> 1), 16u); poll_ge(ws, CNT_KV + 16 + (h >> 1), 16u);
        const int r = tid >> 4, ch = tid & 15;
        *(LAS u32x4*)(lds + L_KS + r * KS_PITCH + ch * 16) = __builtin_amdgcn_raw_buffer_load_b128(rkv, (r * HD + h * DH + ch * 8) * 2, 0, AUX_SC1);
        if (tid < 32 * ES / 8) { const int r2 = tid / (ES / 8), c2 = tid % (ES / 8);
            *(LAS u32x4*)(lds + L_VS + (8 + r2) * VP + c2 * 16) = __builtin_amdgcn_raw_buffer_load_b128(rkv, O_V + (r2 * HD + h * DH + j * ES + c2 * 8) * 2, 0, AUX_SC1); }
    }
    __syncthreads();

    for (int s = 0; s < NSEG; ++s) {
        const int kb = s & 1;
        unsigned long long pcnt = 0; (void)pcnt;
        STAMP(0);
        {
            if (s > 0) __builtin_amdgcn_s_sleep(CH_DELAY1);
            u32x4 a[4];
            const int base = O_MEM + s * (int)MEM_B + wave * 2048 + fq * 128 + (lane & 7) * 16;
            for (unsigned spins = 0;;) {
                asm volatile("" ::: "memory");
#pragma unroll
                for (int i = 0; i < 4; ++i) a[i] = __builtin_amdgcn_raw_buffer_load_b128(rs, base + i * 512, 0, AUX_SC1);
                unsigned mxs = 0;
#pragma unroll
                for (int i = 0; i < 4; ++i) mxs = umax2(mxs, umax4(a[i]));
                if (!__any(mxs == SENT) || dead) break;
                PCOUNT(0);
                if (++spins > SPIN_CAP) { dead = true; if (lane == 0) *(volatile unsigned*)(ws + WS_TMO) = 1u; break; }
            }
            STAMP(1);
            if (b == 0 && tid == 448 && (s & 7) == 0) __hip_atomic_store((unsigned*)(ws + WS_CNT + (size_t)CNT_STEP * 256), (unsigned)s, __ATOMIC_RELAXED, __HIP_MEMORY_SCOPE_AGENT);
            f32x4 acc[NT];
#pragma unroll
            for (int t = 0; t < NT; ++t) acc[t] = (f32x4){0.f, 0.f, 0.f, 0.f};
#pragma unroll
            for (int i = 0; i < 4; ++i)
#pragma unroll
                for (int t = 0; t < NT; ++t) acc[t] = __builtin_amdgcn_mfma_f32_16x16x32_bf16(wq[t][i], __builtin_bit_cast(bf16x8, a[i]), acc[t], 0, 0, 0);
            if (fr < 8) {
#pragma unroll
                for (int t = 0; t < NT; ++t) *(LAS f32x4*)(lds + L_RED + ((wave * NT + t) * 128 + fr * 16 + fq * 4) * 4) = acc[t];
            }
        }
        __syncthreads();
        if (tid < NT * 64) {
            const int t = tid >> 6, m = (tid & 63) >> 3, np = (tid & 7) * 2, mat = t / NE, col = (t % NE) * 16 + np;
            f32x2 sum = (f32x2){0.f, 0.f};
#pragma unroll
            for (int w = 0; w < 8; ++w) sum += *(const LAS f32x2*)(lds + L_RED + ((w * NT + t) * 128 + m * 16 + np) * 4);
            const unsigned pk = desent(pk_bf16(sum.x, sum.y));
            const u32x4 q4 = quad_gather(pk);
            if ((tid & 3) == 0) __builtin_amdgcn_raw_buffer_store_b128(q4, rs, O_QKVM + s * (int)QKVM_B + ((h * 3 + mat) * 8 + m) * 256 + (j * ES + col) * 2, 0, AUX_SC1);
            if (mat == 2) *(LAS unsigned*)(lds + L_VS + kb * 48 * VP + m * VP + col * 2) = pk;
        }
        STAMP(2);
        if (s == NSEG - 1) break;
        __syncthreads();
        if (wave == 0) {
            bf16x8 kx[3][4];
#pragma unroll
            for (int t = 0; t < 3; ++t) {
                int r = t * 16 + fr - 8; r = r < 0 ? 0 : (r > 31 ? 31 : r);
#pragma unroll
                for (int i = 0; i < 4; ++i) kx[t][i] = *(const LAS bf16x8*)(lds + L_KS + kb * 32 * KS_PITCH + r * KS_PITCH + i * 64 + fq * 16);
            }
            __builtin_amdgcn_s_sleep(CH_DELAY2);
            u32x4 qf[4], km[4];
            const int base = O_QKVM + s * (int)QKVM_B + h * 3 * 2048 + (lane & 7) * 256 + fq * 16;
            for (unsigned spins = 0;;) {
                asm volatile("" ::: "memory");
#pragma unroll
                for (int i = 0; i < 4; ++i) { qf[i] = __builtin_amdgcn_raw_buffer_load_b128(rs, base + i * 64, 0, AUX_SC1); km[i] = __builtin_amdgcn_raw_buffer_load_b128(rs, base + 2048 + i * 64, 0, AUX_SC1); }
                unsigned mxs = 0;
#pragma unroll
                for (int i = 0; i < 4; ++i) mxs = umax2(mxs, umax2(umax4(qf[i]), umax4(km[i])));
                if (!__any(mxs == SENT) || dead) break;
                PCOUNT(16);
                if (++spins > SPIN_CAP) { dead = true; if (lane == 0) *(volatile unsigned*)(ws + WS_TMO) = 2u; break; }
            }
            STAMP(3);
            f32x4 sc[3];
#pragma unroll
            for (int t = 0; t < 3; ++t) sc[t] = (f32x4){0.f, 0.f, 0.f, 0.f};
#pragma unroll
            for (int i = 0; i < 4; ++i) {
                const bf16x8 k0 = fr < 8 ? __builtin_bit_cast(bf16x8, km[i]) : kx[0][i];
                sc[1] = __builtin_amdgcn_mfma_f32_16x16x32_bf16(kx[1][i], __builtin_bit_cast(bf16x8, qf[i]), sc[1], 0, 0, 0);
                sc[2] = __builtin_amdgcn_mfma_f32_16x16x32_bf16(kx[2][i], __builtin_bit_cast(bf16x8, qf[i]), sc[2], 0, 0, 0);
                sc[0] = __builtin_amdgcn_mfma_f32_16x16x32_bf16(k0, __builtin_bit_cast(bf16x8, qf[i]), sc[0], 0, 0, 0);
            }
            asm volatile("" ::: "memory");
            bf16x8 vf[NE][2];
            const bf16x4 zz = (bf16x4){0, 0, 0, 0};
#pragma unroll
            for (int et = 0; et < NE; ++et) {
                const int voff = L_VS + kb * 48 * VP + (4 * fq + (fr >> 2)) * VP + et * 32 + (lane & 3) * 8;
                const bf16x4 v0 = __builtin_amdgcn_ds_read_tr16_b64_v4i16((LAS bf16x4*)(lds + voff));
                const bf16x4 v1 = __builtin_amdgcn_ds_read_tr16_b64_v4i16((LAS bf16x4*)(lds + voff + 16 * VP));
                const bf16x4 v2 = __builtin_amdgcn_ds_read_tr16_b64_v4i16((LAS bf16x4*)(lds + voff + 32 * VP));
                vf[et][0] = __builtin_shufflevector(v0, v1, 0, 1, 2, 3, 4, 5, 6, 7); vf[et][1] = __builtin_shufflevector(v2, zz, 0, 1, 2, 3, 4, 5, 6, 7);
            }
            const float scale = 0.08838834764831845f;
            const bool hi = fr >= 8;
            float v[6];
            v[0] = hi ? sc[1][2] : sc[0][0]; v[1] = hi ? sc[1][3] : sc[0][1]; v[2] = hi ? sc[2][0] : sc[0][2];
            v[3] = hi ? sc[2][1] : sc[0][3]; v[4] = hi ? sc[2][2] : sc[1][0]; v[5] = hi ? sc[2][3] : sc[1][1];
            float mx = -INFINITY;
#pragma unroll
            for (int i = 0; i < 6; ++i) { float z = rbf(rbf(v[i]) * scale); if (i >= 2 && hi && fq >= 2) z = -INFINITY; v[i] = z; mx = fmaxf(mx, z); }
            mx = fmaxf(mx, __builtin_bit_cast(float, __builtin_amdgcn_update_dpp(0, __builtin_bit_cast(int, mx), 0x128, 0xf, 0xf, true)));
            mx = xmax16_32(mx);
            float sum = 0.f;
#pragma unroll
            for (int i = 0; i < 6; ++i) { v[i] = __expf(v[i] - mx); sum += v[i]; }
            sum += __builtin_bit_cast(float, __builtin_amdgcn_update_dpp(0, __builtin_bit_cast(int, sum), 0x128, 0xf, 0xf, true));
            sum = xsum16_32(sum);
            const float inv = 1.0f / sum;
            const unsigned w0 = pk_bf16(v[0] * inv, v[1] * inv), w1 = pk_bf16(v[2] * inv, v[3] * inv), w2 = pk_bf16(v[4] * inv, v[5] * inv);
            u32x4 p0, p1;
            p0.x = w0; p0.y = w1; p0.z = w2; p0.w = (unsigned)__builtin_amdgcn_update_dpp(0, (int)w0, 0x128, 0xf, 0xf, true);
            p1.x = (unsigned)__builtin_amdgcn_update_dpp(0, (int)w1, 0x128, 0xf, 0xf, true); p1.y = (unsigned)__builtin_amdgcn_update_dpp(0, (int)w2, 0x128, 0xf, 0xf, true); p1.z = 0u; p1.w = 0u;
#pragma unroll
            for (int et = 0; et < NE; ++et) {
                f32x4 o = (f32x4){0.f, 0.f, 0.f, 0.f};
                o = __builtin_amdgcn_mfma_f32_16x16x32_bf16(vf[et][0], __builtin_bit_cast(bf16x8, p0), o, 0, 0, 0);
                o = __builtin_amdgcn_mfma_f32_16x16x32_bf16(vf[et][1], __builtin_bit_cast(bf16x8, p1), o, 0, 0, 0);
                {
                    const unsigned wx = desent(pk_bf16(o[0], o[1])), wy = desent(pk_bf16(o[2], o[3]));
                    auto sx = __builtin_amdgcn_permlane16_swap(wx, wx, false, false); auto sy = __builtin_amdgcn_permlane16_swap(wy, wy, false, false);
                    const u32x4 w = (u32x4){(unsigned)sx[0], (unsigned)sy[0], (unsigned)sx[1], (unsigned)sy[1]};
                    const int k = h * DH + j * ES + et * 16 + 4 * fq;
                    const int off = O_OM + s * (int)OM_B + (fr / RPG) * (8192 * RPG) + (k >> 5) * (64 * RPG) + ((k >> 3) & 3) * (16 * RPG) + (fr % RPG) * 16;
                    if (fr < 8 && (fq & 1) == 0) __builtin_amdgcn_raw_buffer_store_b128(w, rs, off, 0, AUX_SC1);
                }
            }
            STAMP(4);
        } else {
            const int t2 = tid - 64; const int nb = kb ^ 1; const int row0 = (s + 1) * SEG;
            if (((s + 1) & 7) == 0) { const int blk = (s + 1) >> 3; poll_ge(ws, CNT_KV + blk * 32 + (h >> 1), 8u); poll_ge(ws, CNT_KV + blk * 32 + 16 + (h >> 1), 8u); }
            if (s + 1 == 4) { poll_ge(ws, CNT_KV0B + (h >> 1), 16u); poll_ge(ws, CNT_KV0B + 16 + (h >> 1), 16u); }
            for (int i = t2; i < 512 + 32 * ES / 8; i += 448) {
                if (i < 512) { const int r = i >> 4, ch = i & 15;
                    *(LAS u32x4*)(lds + L_KS + nb * 32 * KS_PITCH + r * KS_PITCH + ch * 16) = __builtin_amdgcn_raw_buffer_load_b128(rkv, ((row0 + r) * HD + h * DH + ch * 8) * 2, 0, AUX_SC1); }
                else { const int r2 = (i - 512) / (ES / 8), c2 = (i - 512) % (ES / 8);
                    *(LAS u32x4*)(lds + L_VS + nb * 48 * VP + (8 + r2) * VP + c2 * 16) = __builtin_amdgcn_raw_buffer_load_b128(rkv, O_V + ((row0 + r2) * HD + h * DH + j * ES + c2 * 8) * 2, 0, AUX_SC1); }
            }
        }
        __syncthreads();
        {
            if (WOL) {
#pragma unroll
                for (int i = 8; i < 16; ++i) wo[i] = *(const LAS bf16x8*)(lds + L_WO + (wave * 8 + i - 8) * 1024 + lane * 16);
            }
            __builtin_amdgcn_s_sleep(CH_DELAY3);
            u32x4 a[RPG];
            constexpr int KPG = 16 / RPG;
            const int base = O_OM + s * (int)OM_B + g * (8192 * RPG) + wave * (1024 * RPG) + (fr / RPG) * (64 * RPG) + fq * (16 * RPG) + (fr % RPG) * 16;
            for (unsigned spins = 0;;) {
                asm volatile("" ::: "memory");
#pragma unroll
                for (int i = 0; i < RPG; ++i) a[i] = __builtin_amdgcn_raw_buffer_load_b128(rs, base + i * 1024, 0, AUX_SC1);
                unsigned mxs = 0;
#pragma unroll
                for (int i = 0; i < RPG; ++i) mxs = umax2(mxs, umax4(a[i]));
                if (!__any(mxs == SENT) || dead) break;
                PCOUNT(32);
                if (++spins > SPIN_CAP) { dead = true; if (lane == 0) *(volatile unsigned*)(ws + WS_TMO) = 3u; break; }
            }
            STAMP(5);
            f32x4 acc[KPG];
#pragma unroll
            for (int q = 0; q < KPG; ++q) acc[q] = (f32x4){0.f, 0.f, 0.f, 0.f};
#pragma unroll
            for (int gq = 0; gq < RPG; ++gq)
#pragma unroll
                for (int q = 0; q < KPG; ++q) acc[q] = __builtin_amdgcn_mfma_f32_16x16x32_bf16(wo[gq * KPG + q], __builtin_bit_cast(bf16x8, a[gq]), acc[q], 0, 0, 0);
            f32x4 r = acc[0];
#pragma unroll
            for (int q = 1; q < KPG; ++q) if (fr / RPG == q) r = acc[q];
#pragma unroll
            for (int e = 0; e < 4; ++e) {
                if (RPG <= 2) r[e] += __builtin_bit_cast(float, __builtin_amdgcn_update_dpp(0, __builtin_bit_cast(int, r[e]), 0x122, 0xf, 0xf, true));
                r[e] += __builtin_bit_cast(float, __builtin_amdgcn_update_dpp(0, __builtin_bit_cast(int, r[e]), 0x124, 0xf, 0xf, true));
                r[e] += __builtin_bit_cast(float, __builtin_amdgcn_update_dpp(0, __builtin_bit_cast(int, r[e]), 0x128, 0xf, 0xf, true));
            }
            if (fr < RPG) *(LAS f32x4*)(lds + L_RED2 + ((wave * RPG + fr) * 16 + fq * 4) * 4) = r;
        }
        __syncthreads();
        if (tid < RPG * 8) {
            const int m = tid >> 3, np = (tid & 7) * 2;
            f32x2 sum = (f32x2){0.f, 0.f};
#pragma unroll
            for (int w = 0; w < 8; ++w) sum += *(const LAS f32x2*)(lds + L_RED2 + ((w * RPG + m) * 16 + np) * 4);
            const unsigned pk = desent(pk_bf16(sum.x, sum.y));
            const int off = O_MEM + (s + 1) * (int)MEM_B + (c >> 1) * 512 + ((c & 1) * 2 + (np >> 3)) * 128 + (g * RPG + m) * 16;
            const u32x4 q4 = quad_gather(pk);
            if ((tid & 3) == 0) __builtin_amdgcn_raw_buffer_store_b128(q4, rs, off, 0, AUX_SC1);
        }
        STAMP(6);
    }
}

template <int MT, class Epi>
__device__ __forceinline__ void gemm128_w8(LAS unsigned char* lds, const bf16_t* A, const int lda, const bf16_t* B, const int ldb, const int K, const Epi& E) {
    int tid = threadIdx.x; asm volatile("" : "+v"(tid));
    const int wid = __builtin_amdgcn_readfirstlane(tid >> 6), lane = tid & 63, wr = wid >> 2, wc = wid & 3, fr = lane & 15, fq = lane >> 4;
    constexpr int NA = MT / 2;
    unsigned voffA[NA], voffB[2];
#pragma unroll
    for (int i = 0; i < NA; ++i) { int R, C; g8::stage_rc(tid * 16 + i * 8192, R, C); voffA[i] = (unsigned)(R * lda + C) * 2u; }
#pragma unroll
    for (int i = 0; i < 2; ++i) { const int bb = tid * 16 + i * 8192, row = bb >> 8, ch = (bb & 255) >> 4, seg = (ch >> 1) ^ g8::bsw(row);
        voffB[i] = (unsigned)(row * ldb + seg * 16 + (ch & 1) * 8) * 2u; }
    const char* cA = (const char*)A; const char* cB = (const char*)B;
    const unsigned ldsw = (unsigned)wid * 1024u;
    const int aoff = g8::lds_byte(wr * 16 * MT + fr, fq * 8);
    const int q4 = (lane & 15) >> 2, p4 = lane & 3, sB = q4 | ((fq & 1) << 2);
    const int boff0 = (8 * fq + q4) * 256 + (((wc * 2 + 0) ^ sB) * 32) + 8 * p4;
    const int boff1 = (8 * fq + q4) * 256 + (((wc * 2 + 1) ^ sB) * 32) + 8 * p4;
    f32x4 acc[MT][2];
#pragma unroll
    for (int m = 0; m < MT; ++m) { acc[m][0] = (f32x4){0.f, 0.f, 0.f, 0.f}; acc[m][1] = (f32x4){0.f, 0.f, 0.f, 0.f}; }
    const int nt = K / 64;
#define STG(buf, t) do { _Pragma("unroll") for (int _i = 0; _i < NA; ++_i) \
        __builtin_amdgcn_global_load_lds((const unsigned*)(cA + (size_t)(t) * 128 + voffA[_i]), (LAS unsigned*)(lds + (buf) * 32768 + ldsw + _i * 8192), 16, 0, 0); \
        _Pragma("unroll") for (int _i = 0; _i < 2; ++_i) \
        __builtin_amdgcn_global_load_lds((const unsigned*)(cB + (size_t)(t) * 64 * ldb * 2 + voffB[_i]), (LAS unsigned*)(lds + (buf) * 32768 + 16384 + ldsw + _i * 8192), 16, 0, 0); } while (0)
    STG(0, 0); STG(1, 1); STG(2, 2);
    int cur = 0;
    for (int t = 0; t < nt; ++t) {
        if (MT == 4) { if (t + 2 < nt) asm volatile("s_waitcnt vmcnt(8)" ::: "memory"); else if (t + 1 < nt) asm volatile("s_waitcnt vmcnt(4)" ::: "memory"); else asm volatile("s_waitcnt vmcnt(0)" ::: "memory"); }
        else { if (t + 2 < nt) asm volatile("s_waitcnt vmcnt(6)" ::: "memory"); else if (t + 1 < nt) asm volatile("s_waitcnt vmcnt(3)" ::: "memory"); else asm volatile("s_waitcnt vmcnt(0)" ::: "memory"); }
        __builtin_amdgcn_s_barrier();
        asm volatile("" ::: "memory");
        if (t + 3 < nt) STG((cur + 3) & 3, t + 3);
        bf16x8 At[MT][2], Bt[2][2];
        LAS unsigned char* sa = lds + cur * 32768; LAS unsigned char* sb = sa + 16384;
#pragma unroll
        for (int k = 0; k < 2; ++k) {
            bf16x4 a0 = __builtin_amdgcn_ds_read_tr16_b64_v4i16((LAS bf16x4*)(sb + boff0 + k * 8192)), a1 = __builtin_amdgcn_ds_read_tr16_b64_v4i16((LAS bf16x4*)(sb + boff0 + k * 8192 + 1024));
            bf16x4 c0 = __builtin_amdgcn_ds_read_tr16_b64_v4i16((LAS bf16x4*)(sb + boff1 + k * 8192)), c1 = __builtin_amdgcn_ds_read_tr16_b64_v4i16((LAS bf16x4*)(sb + boff1 + k * 8192 + 1024));
            Bt[0][k] = __builtin_shufflevector(a0, a1, 0, 1, 2, 3, 4, 5, 6, 7); Bt[1][k] = __builtin_shufflevector(c0, c1, 0, 1, 2, 3, 4, 5, 6, 7);
#pragma unroll
            for (int m = 0; m < MT; ++m) At[m][k] = *(const LAS bf16x8*)(sa + aoff + m * 2048 + k * 1024);
        }
        __builtin_amdgcn_s_setprio(1);
#pragma unroll
        for (int k = 0; k < 2; ++k)
#pragma unroll
            for (int m = 0; m < MT; ++m)
#pragma unroll
                for (int n = 0; n < 2; ++n) acc[m][n] = __builtin_amdgcn_mfma_f32_16x16x32_bf16(Bt[n][k], At[m][k], acc[m][n], 0, 0, 0);
        __builtin_amdgcn_s_setprio(0);
        asm volatile("s_waitcnt lgkmcnt(0)" ::: "memory");
        cur = (cur + 1) & 3;
    }
#undef STG
    __builtin_amdgcn_s_barrier();
    E(acc, wr, wc, fr, fq);
}

constexpr int XV_PITCH = 272;
template <bool INK>
__device__ __forceinline__ void xattn_item(LAS unsigned char* vs, char* ws, const int s, const int h) {
    const int lane = threadIdx.x & 63, fr = lane & 15, fq = lane >> 4;
    const bf16_t* Q = (const bf16_t*)(ws + WS_Q) + (size_t)s * SEG * HD + h * DH;
    const bf16_t* K = (const bf16_t*)(ws + WS_K) + (size_t)s * SEG * HD + h * DH;
    const bf16_t* V = (const bf16_t*)(ws + WS_V) + (size_t)s * SEG * HD + h * DH;
    const __amdgpu_buffer_rsrc_t rm = __builtin_amdgcn_make_buffer_rsrc(ws + WS_QKVM, 0, (int)(WS_OM - WS_QKVM), 0x00020000);
    const int kmo = s * (int)QKVM_B + (h * 3 + 1) * 2048, vmo = kmo + 2048;
    u32x4 km[4], vm[2];
    for (unsigned spins = 0;;) {
        asm volatile("" ::: "memory");
#pragma unroll
        for (int i = 0; i < 4; ++i) km[i] = __builtin_amdgcn_raw_buffer_load_b128(rm, kmo + (lane & 7) * 256 + i * 64 + fq * 16, 0, INK ? AUX_SC1 : 0);
#pragma unroll
        for (int i = 0; i < 2; ++i) vm[i] = __builtin_amdgcn_raw_buffer_load_b128(rm, vmo + (i * 4 + fq) * 256 + fr * 16, 0, INK ? AUX_SC1 : 0);
        if (!INK) break;
        unsigned mxs = umax2(umax4(vm[0]), umax4(vm[1]));
#pragma unroll
        for (int i = 0; i < 4; ++i) mxs = umax2(mxs, umax4(km[i]));
        if (!__any(mxs == SENT)) break;
        __builtin_amdgcn_s_sleep(16);
        if (++spins > SPIN_CAP) break;
    }
#pragma unroll
    for (int i = 0; i < 12; ++i) {
        const int r = i * 4 + fq;
        u32x4 v = (u32x4){0u, 0u, 0u, 0u};
        if (i < 2) v = vm[i]; else if (i < 10) v = *(const u32x4*)(V + (size_t)(r - 8) * HD + fr * 8);
        *(LAS u32x4*)(vs + r * XV_PITCH + fr * 16) = v;
    }
    f32x4 sc[3][2];
#pragma unroll
    for (int t = 0; t < 3; ++t) { sc[t][0] = (f32x4){0.f, 0.f, 0.f, 0.f}; sc[t][1] = (f32x4){0.f, 0.f, 0.f, 0.f}; }
#pragma unroll
    for (int i = 0; i < 4; ++i) {
        const bf16x8 q0 = *(const bf16x8*)(Q + (size_t)fr * HD + i * 32 + fq * 8), q1 = *(const bf16x8*)(Q + (size_t)(16 + fr) * HD + i * 32 + fq * 8);
#pragma unroll
        for (int t = 0; t < 3; ++t) {
            int r = t * 16 + fr - 8; r = r < 0 ? 0 : (r > 31 ? 31 : r);
            bf16x8 kf = *(const bf16x8*)(K + (size_t)r * HD + i * 32 + fq * 8);
            if (t == 0 && fr < 8) kf = __builtin_bit_cast(bf16x8, km[i]);
            sc[t][0] = __builtin_amdgcn_mfma_f32_16x16x32_bf16(kf, q0, sc[t][0], 0, 0, 0);
            sc[t][1] = __builtin_amdgcn_mfma_f32_16x16x32_bf16(kf, q1, sc[t][1], 0, 0, 0);
        }
    }
    const float scale = 0.08838834764831845f, NEG = rbf(-1000000000.0f);
    u32x4 p0[2], p1[2];
#pragma unroll
    for (int rt = 0; rt < 2; ++rt) {
        const int row = rt * 16 + fr;
        float v[12]; float mx = -INFINITY;
#pragma unroll
        for (int t = 0; t < 3; ++t)
#pragma unroll
            for (int r = 0; r < 4; ++r) { const int key = t * 16 + fq * 4 + r; float z = rbf(rbf(sc[t][rt][r]) * scale);
                if (key - 8 > row) z = NEG; if (key >= 40) z = -INFINITY; v[t * 4 + r] = z; mx = fmaxf(mx, z); }
        mx = xmax16_32(mx);
        float sum = 0.f;
#pragma unroll
        for (int i = 0; i < 12; ++i) { v[i] = __expf(v[i] - mx); sum += v[i]; }
        sum = xsum16_32(sum);
        const float inv = 1.0f / sum;
        p0[rt].x = pk_bf16(v[0] * inv, v[1] * inv); p0[rt].y = pk_bf16(v[2] * inv, v[3] * inv); p0[rt].z = pk_bf16(v[4] * inv, v[5] * inv); p0[rt].w = pk_bf16(v[6] * inv, v[7] * inv);
        p1[rt].x = pk_bf16(v[8] * inv, v[9] * inv); p1[rt].y = pk_bf16(v[10] * inv, v[11] * inv); p1[rt].z = 0u; p1[rt].w = 0u;
    }
    asm volatile("s_waitcnt lgkmcnt(0)" ::: "memory");
    const __amdgpu_buffer_rsrc_t ro = __builtin_amdgcn_make_buffer_rsrc(ws + WS_O, 0, S_LEN * HD * 2, 0x00020000);
    const int obase = (s * SEG * HD + h * DH) * 2;
    const bf16x4 zz = (bf16x4){0, 0, 0, 0};
    u32x2 ow[8][2];
#pragma unroll
    for (int et = 0; et < 8; ++et) {
        const int voff = (4 * fq + (fr >> 2)) * XV_PITCH + et * 32 + (lane & 3) * 8;
        const bf16x4 v0 = __builtin_amdgcn_ds_read_tr16_b64_v4i16((LAS bf16x4*)(vs + voff));
        const bf16x4 v1 = __builtin_amdgcn_ds_read_tr16_b64_v4i16((LAS bf16x4*)(vs + voff + 16 * XV_PITCH));
        const bf16x4 v2 = __builtin_amdgcn_ds_read_tr16_b64_v4i16((LAS bf16x4*)(vs + voff + 32 * XV_PITCH));
        const bf16x8 va = __builtin_shufflevector(v0, v1, 0, 1, 2, 3, 4, 5, 6, 7), vb = __builtin_shufflevector(v2, zz, 0, 1, 2, 3, 4, 5, 6, 7);
#pragma unroll
        for (int rt = 0; rt < 2; ++rt) {
            f32x4 o = (f32x4){0.f, 0.f, 0.f, 0.f};
            o = __builtin_amdgcn_mfma_f32_16x16x32_bf16(va, __builtin_bit_cast(bf16x8, p0[rt]), o, 0, 0, 0);
            o = __builtin_amdgcn_mfma_f32_16x16x32_bf16(vb, __builtin_bit_cast(bf16x8, p1[rt]), o, 0, 0, 0);
            ow[et][rt].x = pk_bf16(o[0], o[1]); ow[et][rt].y = pk_bf16(o[2], o[3]);
        }
    }
    asm volatile("s_waitcnt lgkmcnt(0)" ::: "memory");
#pragma unroll
    for (int et = 0; et < 8; ++et)
#pragma unroll
        for (int rt = 0; rt < 2; ++rt) *(LAS u32x2*)(vs + (rt * 16 + fr) * XV_PITCH + et * 32 + fq * 8) = ow[et][rt];
    asm volatile("s_waitcnt lgkmcnt(0)" ::: "memory");
#pragma unroll
    for (int i = 0; i < 8; ++i) {
        const int r = i * 4 + fq;
        const u32x4 w = *(const LAS u32x4*)(vs + r * XV_PITCH + fr * 16);
        __builtin_amdgcn_raw_buffer_store_b128(w, ro, obase + (r * HD + fr * 8) * 2, 0, INK ? AUX_SC1 : 0);
    }
    asm volatile("s_waitcnt lgkmcnt(0)" ::: "memory");
}

struct SchedOne {
    g8::Unit u; bool valid;
    __device__ __forceinline__ bool next(int i, g8::Unit& o) const { if (i != 0 || !valid) return false; o = u; return true; }
};
struct SchedKVQ {
    const bf16_t* A; const bf16_t* Wq; long dk, dv; int r;
    __device__ __forceinline__ bool next(int i, g8::Unit& u) const {
        const int L = r + 128 * i; if (L >= 352) return false;
        int pm, pn;
        if (L < 224) { pm = 1 + (L >> 5); pn = 16 + (L & 31); } else { pm = (L - 224) & 7; pn = (L - 224) >> 3; }
        const int mat = pn >> 4;
        u.A = A + (size_t)pm * 256 * DM; u.B = Wq + (mat == 1 ? dk : 0L) + (mat == 2 ? dv : 0L) + (pn & 15) * 256; u.pm = pm; u.pn = pn; return true;
    }
};
struct EpiQKV {
    char* ws;
    __device__ __forceinline__ void operator()(const f32x4 (&acc)[2][2][4][2], const g8::Unit& u, int wr, int wc, int fr, int fq) const {
        const __amdgpu_buffer_rsrc_t rq = __builtin_amdgcn_make_buffer_rsrc(ws + WS_Q, 0, (int)(WS_H - WS_Q), 0x00020000);
        const int base = (u.pn >> 4) * (S_LEN * HD * 2) + ((u.pm * 256 + wr * 64 + fr) * HD + (u.pn & 15) * 256 + wc * 32 + 4 * fq) * 2;
#pragma unroll
        for (int ai = 0; ai < 2; ++ai)
#pragma unroll
            for (int m = 0; m < 4; ++m)
#pragma unroll
                for (int bj = 0; bj < 2; ++bj)
#pragma unroll
                    for (int n = 0; n < 2; ++n) { const f32x4 v = acc[ai][bj][m][n]; u32x2 w; w.x = pk_bf16(v[0], v[1]); w.y = pk_bf16(v[2], v[3]);
                        __builtin_amdgcn_raw_buffer_store_b64(w, rq, base + ((ai * 128 + m * 16) * HD + bj * 128 + n * 16) * 2, 0, AUX_SC1); }
        asm volatile("s_waitcnt vmcnt(0)" ::: "memory");
        if ((threadIdx.x & 63) == 0) {
            if (u.pn >= 16) __hip_atomic_fetch_add((unsigned*)(ws + WS_CNT + (size_t)(CNT_KV + u.pm * 32 + u.pn - 16) * 256), 1u, __ATOMIC_RELAXED, __HIP_MEMORY_SCOPE_AGENT);
            __hip_atomic_fetch_add((unsigned*)(ws + WS_CNT + (size_t)(CNT_QKV + u.pm) * 256), 1u, __ATOMIC_RELAXED, __HIP_MEMORY_SCOPE_AGENT);
        }
    }
};
struct EpiKV0 {
    char* ws; int pm2, pn2;
    __device__ __forceinline__ void operator()(const f32x4 (&acc)[4][2], int wr, int wc, int fr, int fq) const {
        const __amdgpu_buffer_rsrc_t rq = __builtin_amdgcn_make_buffer_rsrc(ws + WS_K, 0, (int)(WS_H - WS_K), 0x00020000);
        const int base = (pn2 >> 5) * (S_LEN * HD * 2) + ((pm2 * 128 + wr * 64 + fr) * HD + (pn2 & 31) * 128 + wc * 32 + 4 * fq) * 2;
#pragma unroll
        for (int m = 0; m < 4; ++m)
#pragma unroll
            for (int n = 0; n < 2; ++n) { const f32x4 v = acc[m][n]; u32x2 w; w.x = pk_bf16(v[0], v[1]); w.y = pk_bf16(v[2], v[3]);
                __builtin_amdgcn_raw_buffer_store_b64(w, rq, base + (m * 16 * HD + n * 16) * 2, 0, AUX_SC1); }
        asm volatile("s_waitcnt vmcnt(0)" ::: "memory");
        if ((threadIdx.x & 63) == 0) {
            __hip_atomic_fetch_add((unsigned*)(ws + WS_CNT + (size_t)((pm2 == 0 ? CNT_KV : CNT_KV0B) + (pn2 >> 1)) * 256), 1u, __ATOMIC_RELAXED, __HIP_MEMORY_SCOPE_AGENT);
            __hip_atomic_fetch_add((unsigned*)(ws + WS_CNT + (size_t)CNT_QKV * 256), 1u, __ATOMIC_RELAXED, __HIP_MEMORY_SCOPE_AGENT);
        }
    }
};
struct EpiMLP1 {
    char* ws; const bf16_t* b1;
    __device__ __forceinline__ void operator()(const f32x4 (&acc)[2][2][4][2], const g8::Unit& u, int wr, int wc, int fr, int fq) const {
        const __amdgpu_buffer_rsrc_t rh = __builtin_amdgcn_make_buffer_rsrc(ws + WS_H, 0, S_LEN * FF * 2, 0x00020000);
        const int row0 = u.pm * 256 + wr * 64 + fr, col0 = u.pn * 256 + wc * 32 + 4 * fq;
#pragma unroll
        for (int bj = 0; bj < 2; ++bj)
#pragma unroll
            for (int n = 0; n < 2; ++n) {
                const u32x2 bw = *(const u32x2*)(b1 + col0 + bj * 128 + n * 16);
                const float b0 = bf2f(bw.x & 0xffffu), b1f = bf2f(bw.x >> 16), b2f = bf2f(bw.y & 0xffffu), b3 = bf2f(bw.y >> 16);
#pragma unroll
                for (int ai = 0; ai < 2; ++ai)
#pragma unroll
                    for (int m = 0; m < 4; ++m) { const f32x4 v = acc[ai][bj][m][n];
                        const float g0 = gelu_tanh(rbf(rbf(v[0]) + b0)), g1 = gelu_tanh(rbf(rbf(v[1]) + b1f)), g2 = gelu_tanh(rbf(rbf(v[2]) + b2f)), g3 = gelu_tanh(rbf(rbf(v[3]) + b3));
                        u32x2 w; w.x = pk_bf16(g0, g1); w.y = pk_bf16(g2, g3);
                        __builtin_amdgcn_raw_buffer_store_b64(w, rh, ((row0 + ai * 128 + m * 16) * FF + col0 + bj * 128 + n * 16) * 2, 0, AUX_SC1); }
                asm volatile("" ::: "memory");
            }
        signal_counter(ws, CNT_H + u.pm);
    }
};
struct EpiMLP2 {
    char* ws; const bf16_t* b2; int row0, col0;
    __device__ __forceinline__ void operator()(const f32x4 (&acc)[4][2], int wr, int wc, int fr, int fq) const {
        const __amdgpu_buffer_rsrc_t rb = __builtin_amdgcn_make_buffer_rsrc(ws + WS_B, 0, S_LEN * DM * 2, 0x00020000);
#pragma unroll
        for (int n = 0; n < 2; ++n) {
            const int col = col0 + wc * 32 + n * 16 + 4 * fq;
            const u32x2 bw = *(const u32x2*)(b2 + col);
#pragma unroll
            for (int m = 0; m < 4; ++m) { const f32x4 v = acc[m][n];
                u32x2 w; w.x = pk_bf16(rbf(v[0]) + bf2f(bw.x & 0xffffu), rbf(v[1]) + bf2f(bw.x >> 16)); w.y = pk_bf16(rbf(v[2]) + bf2f(bw.y & 0xffffu), rbf(v[3]) + bf2f(bw.y >> 16));
                __builtin_amdgcn_raw_buffer_store_b64(w, rb, ((row0 + wr * 64 + m * 16 + fr) * DM + col) * 2, 0, AUX_SC1); }
        }
        signal_counter(ws, CNT_B + (row0 >> 8));
    }
};
__device__ __forceinline__ u32x2 combine4(const f32x4 v, const u32x2 xw, const u32x2 bw) {
    u32x2 w;
    w.x = pk_bf16(rbf(bf2f(xw.x & 0xffffu) + rbf(v[0])) + bf2f(bw.x & 0xffffu), rbf(bf2f(xw.x >> 16) + rbf(v[1])) + bf2f(bw.x >> 16));
    w.y = pk_bf16(rbf(bf2f(xw.y & 0xffffu) + rbf(v[2])) + bf2f(bw.y & 0xffffu), rbf(bf2f(xw.y >> 16) + rbf(v[3])) + bf2f(bw.y >> 16));
    return w;
}
struct EpiOut {
    const bf16_t* x; const bf16_t* bout; bf16_t* out; int row0, col0;
    __device__ __forceinline__ void operator()(const f32x4 (&acc)[4][2], int wr, int wc, int fr, int fq) const {
#pragma unroll
        for (int m = 0; m < 4; ++m)
#pragma unroll
            for (int n = 0; n < 2; ++n) {
                const size_t off = (size_t)(row0 + wr * 64 + m * 16 + fr) * DM + col0 + wc * 32 + n * 16 + 4 * fq;
                *(u32x2*)(out + off) = combine4(acc[m][n], *(const u32x2*)(x + off), *(const u32x2*)(bout + off));
            }
    }
};

constexpr int TAIL_BLK0 = 6;
__global__ void __launch_bounds__(512, 2) k_mega(const bf16_t* __restrict__ x, const bf16_t* __restrict__ Wq, const bf16_t* __restrict__ Wk, const bf16_t* __restrict__ Wv,
                                                 const bf16_t* __restrict__ Wo, const bf16_t* __restrict__ w1, const bf16_t* __restrict__ b1, const bf16_t* __restrict__ w2,
                                                 const bf16_t* __restrict__ b2, char* ws, bf16_t* __restrict__ out) {
    extern __shared__ __attribute__((aligned(16))) unsigned char shm_[];
    LAS unsigned char* lds = (LAS unsigned char*)shm_;
    const int b = blockIdx.x;
    const int role = (b >> 3) & 1, r = (b >> 4) * 8 + (b & 7);
    if (role == 0) { chain_body<4>(lds, r, Wq, Wk, Wv, Wo, ws); return; }
#define WSTAMP(i) do {} while (0)
    WSTAMP(0);
    {
        const int pm2 = r & 1, pn2 = r >> 1;
        EpiKV0 E{ws, pm2, pn2};
        gemm128_w8<4, EpiKV0>(lds, (const bf16_t*)(ws + WS_XN) + (size_t)pm2 * 128 * DM, DM, (pn2 >= 32 ? Wv : Wk) + (pn2 & 31) * 128, HD, DM, E);
    }
    WSTAMP(1);
    {
        SchedKVQ S{(const bf16_t*)(ws + WS_XN), Wq, (long)(Wk - Wq), (long)(Wv - Wq), r};
        EpiQKV E{ws}; g8::gemm_phase<EpiQKV, SchedKVQ>(lds, DM, DM, HD, S, E);
    }
    WSTAMP(2);
    {
        const int L = r >= 96 ? r - 96 : 32 + r;
        SchedOne S; S.valid = (r >= 96) || (r < 80); S.u.pm = L & 7; S.u.pn = L >> 3; S.u.A = (const bf16_t*)(ws + WS_Y) + (size_t)S.u.pm * 256 * DM; S.u.B = w1 + S.u.pn * 256;
        EpiMLP1 E{ws, b1}; g8::gemm_phase<EpiMLP1, SchedOne>(lds, DM, DM, FF, S, E);
    }
    WSTAMP(3);
    {
        const int pm = r >> 3, pn = r & 7;
        wait_counter(ws, CNT_H + (pm >> 1), 14 * 8);
        EpiMLP2 E{ws, b2, pm * 128, pn * 128};
        gemm128_w8<4, EpiMLP2>(lds, (const bf16_t*)(ws + WS_H) + (size_t)pm * 128 * FF, FF, w2 + pn * 128, DM, FF, E);
    }
    WSTAMP(4);
    const int blk = r >> 4, t = r & 15;
    if (blk >= TAIL_BLK0) return;
    {
        if (threadIdx.x < 64) poll_ge(ws, CNT_STEP, (unsigned)(8 * blk + 8));
        wait_counter(ws, CNT_QKV + blk, (blk == 0 ? 128 * 8 : 32 * 8) + 16 * 8);
        WSTAMP(5);
        const int wave = threadIdx.x >> 6;
#pragma unroll 1
        for (int i = 0; i < 2; ++i) { const int item = t * 16 + wave * 2 + i; xattn_item<true>(lds + wave * 48 * XV_PITCH, ws, blk * 8 + (item >> 5), item & 31); }
        signal_counter(ws, CNT_O + blk);
    }
    {
        if (threadIdx.x < 64) poll_ge(ws, CNT_B + blk, 16 * 8);
        wait_counter(ws, CNT_O + blk, 16 * 8);
        WSTAMP(6);
        const int pm = blk * 2 + (t >> 3), pn = t & 7;
        EpiOut E{x, (const bf16_t*)(ws + WS_B), out, pm * 128, pn * 128};
        gemm128_w8<4, EpiOut>(lds, (const bf16_t*)(ws + WS_O) + (size_t)pm * 128 * HD, HD, Wo + pn * 128, DM, HD, E);
    }
    WSTAMP(7);
}

__global__ void __launch_bounds__(256) k_tail_attn(char* ws) {
    __shared__ __attribute__((aligned(16))) unsigned char shm_[4 * 48 * XV_PITCH];
    const int wave = threadIdx.x >> 6, item = blockIdx.x * 4 + wave;
    xattn_item<false>((LAS unsigned char*)shm_ + wave * 48 * XV_PITCH, ws, TAIL_BLK0 * 8 + (item >> 5), item & 31);
}
__global__ void __launch_bounds__(256) k_tail_gemm(const bf16_t* __restrict__ x, const bf16_t* __restrict__ Wo, const char* __restrict__ ws, bf16_t* __restrict__ out) {
    extern __shared__ __attribute__((aligned(16))) unsigned char shm_[];
    const int tid = threadIdx.x, wave = __builtin_amdgcn_readfirstlane(tid >> 6), lane = tid & 63, fr = lane & 15, fq = lane >> 4;
    LAS unsigned char* lds = (LAS unsigned char*)shm_ + wave * 16384;
    const int pn = blockIdx.x & 31, pm = blockIdx.x >> 5, row0 = TAIL_BLK0 * 256 + pm * 32, col0 = pn * 32;
    const char* cA = (const char*)((const bf16_t*)(ws + WS_O) + (size_t)row0 * HD + wave * 1024);
    const char* cB = (const char*)(Wo + (size_t)wave * 1024 * DM + col0);
    unsigned voffA[4], voffB[4];
#pragma unroll
    for (int i = 0; i < 4; ++i) { const int c = i * 64 + lane, row = c >> 3, ch = (c & 7) ^ (row & 7); voffA[i] = (unsigned)(row * HD + ch * 8) * 2u;
        const int k = c >> 2, cb = c & 3; voffB[i] = (unsigned)(k * DM + cb * 8) * 2u; }
#define STG(buf, t) do { _Pragma("unroll") for (int _i = 0; _i < 4; ++_i) { \
        __builtin_amdgcn_global_load_lds((const unsigned*)(cA + (size_t)(t) * 128 + voffA[_i]), (LAS unsigned*)(lds + (buf) * 8192 + _i * 1024), 16, 0, 0); \
        __builtin_amdgcn_global_load_lds((const unsigned*)(cB + (size_t)(t) * 64 * DM * 2 + voffB[_i]), (LAS unsigned*)(lds + (buf) * 8192 + 4096 + _i * 1024), 16, 0, 0); } } while (0)
    f32x4 acc[2][2];
#pragma unroll
    for (int m = 0; m < 2; ++m) { acc[m][0] = (f32x4){0.f, 0.f, 0.f, 0.f}; acc[m][1] = (f32x4){0.f, 0.f, 0.f, 0.f}; }
    STG(0, 0);
    int cur = 0;
    for (int t = 0; t < 16; ++t) {
        if (t + 1 < 16) { STG(cur ^ 1, t + 1); asm volatile("s_waitcnt vmcnt(8)" ::: "memory"); }
        else asm volatile("s_waitcnt vmcnt(0)" ::: "memory");
        LAS unsigned char* sa = lds + cur * 8192; LAS unsigned char* sb = sa + 4096;
        bf16x8 At[2][2], Bt[2][2];
#pragma unroll
        for (int k = 0; k < 2; ++k) {
#pragma unroll
            for (int m = 0; m < 2; ++m) { const int row = m * 16 + fr; At[m][k] = *(const LAS bf16x8*)(sa + row * 128 + (((k * 4 + fq) ^ (row & 7)) * 16)); }
#pragma unroll
            for (int n = 0; n < 2; ++n) {
                const int off = (k * 32 + 8 * fq + (fr >> 2)) * 64 + n * 32 + (lane & 3) * 8;
                const bf16x4 a0 = __builtin_amdgcn_ds_read_tr16_b64_v4i16((LAS bf16x4*)(sb + off)), a1 = __builtin_amdgcn_ds_read_tr16_b64_v4i16((LAS bf16x4*)(sb + off + 256));
                Bt[n][k] = __builtin_shufflevector(a0, a1, 0, 1, 2, 3, 4, 5, 6, 7); }
        }
#pragma unroll
        for (int k = 0; k < 2; ++k)
#pragma unroll
            for (int m = 0; m < 2; ++m)
#pragma unroll
                for (int n = 0; n < 2; ++n) acc[m][n] = __builtin_amdgcn_mfma_f32_16x16x32_bf16(Bt[n][k], At[m][k], acc[m][n], 0, 0, 0);
        asm volatile("s_waitcnt lgkmcnt(0)" ::: "memory");
        cur ^= 1;
    }
#undef STG
    __syncthreads();
    LAS f32x4* red = (LAS f32x4*)shm_;
#pragma unroll
    for (int m = 0; m < 2; ++m)
#pragma unroll
        for (int n = 0; n < 2; ++n) red[((wave * 2 + m) * 2 + n) * 64 + lane] = acc[m][n];
    __syncthreads();
    {
        const int m = wave >> 1, n = wave & 1;
        const f32x4 v = (red[((0 * 2 + m) * 2 + n) * 64 + lane] + red[((1 * 2 + m) * 2 + n) * 64 + lane]) + (red[((2 * 2 + m) * 2 + n) * 64 + lane] + red[((3 * 2 + m) * 2 + n) * 64 + lane]);
        const size_t off = (size_t)(row0 + m * 16 + fr) * DM + col0 + n * 16 + 4 * fq;
        *(u32x2*)(out + off) = combine4(v, *(const u32x2*)(x + off), *(const u32x2*)((const bf16_t*)(ws + WS_B) + off));
    }
}

extern "C" void kernel_launch(void* const* d_in, const int* in_sizes, int n_in, void* d_out, int out_size, void* d_ws, size_t ws_size, hipStream_t stream) {
    const bf16_t* x = (const bf16_t*)d_in[0];
    const bf16_t* mem0 = (const bf16_t*)d_in[1];
    const bf16_t* Wq = (const bf16_t*)d_in[2];
    const bf16_t* Wk = (const bf16_t*)d_in[3];
    const bf16_t* Wv = (const bf16_t*)d_in[4];
    const bf16_t* Wo = (const bf16_t*)d_in[5];
    const bf16_t* w1 = (const bf16_t*)d_in[8];
    const bf16_t* b1 = (const bf16_t*)d_in[9];
    const bf16_t* w2 = (const bf16_t*)d_in[10];
    const bf16_t* b2 = (const bf16_t*)d_in[11];
    char* ws = (char*)d_ws;
    bf16_t* out = (bf16_t*)d_out;
    (void)hipFuncSetAttribute((const void*)k_mega, hipFuncAttributeMaxDynamicSharedMemorySize, CH_LDS);
    (void)hipFuncSetAttribute((const void*)k_tail_gemm, hipFuncAttributeMaxDynamicSharedMemorySize, 4 * 16384);
    k_prep<<<S_LEN / 4, 256, 0, stream>>>(x, mem0, ws);
    k_mega<<<256, 512, CH_LDS, stream>>>(x, Wq, Wk, Wv, Wo, w1, b1, w2, b2, ws, out);
    k_tail_attn<<<64 * (8 - TAIL_BLK0), 256, 0, stream>>>(ws);
    k_tail_gemm<<<256 * (8 - TAIL_BLK0), 256, 4 * 16384, stream>>>(x, Wo, ws, out);
}
```

```cpp
#include <hip/hip_runtime.h>
#include <hip/hip_bf16.h>

#define LAS __attribute__((address_space(3)))
typedef unsigned short bf16_t;
typedef short bf16x8 __attribute__((ext_vector_type(8)));
typedef short bf16x4 __attribute__((ext_vector_type(4)));
typedef float f32x4 __attribute__((ext_vector_type(4)));
typedef float f32x2 __attribute__((ext_vector_type(2)));
typedef unsigned u32x4 __attribute__((ext_vector_type(4)));
typedef unsigned u32x2 __attribute__((ext_vector_type(2)));

constexpr int S_LEN = 2048, DM = 1024, NH = 32, DH = 128, HD = NH * DH, MM = 8, SEG = 32, NSEG = S_LEN / SEG, FF = 3584;
constexpr int AUX_SC1 = 16;
constexpr unsigned SENT = 0xFFFFFFFFu;

__device__ __forceinline__ float bf2f(unsigned b) { return __builtin_bit_cast(float, b << 16); }
typedef __bf16 bf16x2_t __attribute__((ext_vector_type(2)));
__device__ __forceinline__ unsigned pk_bf16(float lo, float hi) { const f32x2 v = {lo, hi}; return __builtin_bit_cast(unsigned, __builtin_convertvector(v, bf16x2_t)); }
__device__ __forceinline__ float rbf(float x) { return bf2f(pk_bf16(x, x) & 0xffffu); }
__device__ __forceinline__ unsigned desent(unsigned w) { return w == SENT ? 0x7FC07FC0u : w; }
__device__ __forceinline__ unsigned umax2(unsigned a, unsigned b) { return a > b ? a : b; }
__device__ __forceinline__ unsigned umax4(u32x4 v) { return umax2(umax2(v.x, v.y), umax2(v.z, v.w)); }

constexpr size_t WS_XN = 0;
constexpr size_t WS_Y = WS_XN + (size_t)S_LEN * DM * 2;
constexpr size_t WS_Q = WS_Y + (size_t)S_LEN * DM * 2;
constexpr size_t WS_K = WS_Q + (size_t)S_LEN * HD * 2;
constexpr size_t WS_V = WS_K + (size_t)S_LEN * HD * 2;
constexpr size_t WS_H = WS_V + (size_t)S_LEN * HD * 2;
constexpr size_t WS_O = WS_H + (size_t)S_LEN * FF * 2;
constexpr size_t WS_B = WS_O + (size_t)S_LEN * HD * 2;
constexpr size_t WS_SENT0 = WS_B + (size_t)S_LEN * DM * 2;
constexpr size_t MEM_B = (size_t)MM * DM * 2;
constexpr size_t WS_MEM = WS_SENT0;
constexpr size_t QKVM_B = (size_t)NH * 3 * MM * DH * 2;
constexpr size_t WS_QKVM = WS_MEM + (size_t)(NSEG + 1) * MEM_B;
constexpr size_t OM_B = (size_t)MM * HD * 2;
constexpr size_t WS_OM = WS_QKVM + (size_t)NSEG * QKVM_B;
constexpr size_t WS_SENT1 = WS_OM + (size_t)NSEG * OM_B;
constexpr size_t WS_CNT = WS_SENT1;
constexpr int CNT_H = 0;
constexpr int CNT_B = 8;
constexpr int CNT_O = 16;
constexpr int CNT_QKV = 24;
constexpr int CNT_FILL = 33;
constexpr int CNT_STEP = 32;
constexpr int CNT_KV = 40;
constexpr int CNT_KV0B = 40 + 256;
constexpr int CNT_N = 40 + 256 + 32;
constexpr size_t WS_TMO = WS_CNT + (size_t)CNT_N * 256;
constexpr size_t WS_END = WS_TMO + 256;

__device__ __forceinline__ float wave_sum(float v) {
#pragma unroll
    for (int o = 32; o > 0; o >>= 1) v += __shfl_xor(v, o);
    return v;
}
__device__ __forceinline__ void ln_row(float (&v)[16]) {
    float s = 0.f;
#pragma unroll
    for (int i = 0; i < 16; ++i) s += v[i];
    const float mean = wave_sum(s) * (1.0f / DM);
    float q = 0.f;
#pragma unroll
    for (int i = 0; i < 16; ++i) { const float d = v[i] - mean; q += d * d; }
    const float var = rbf(wave_sum(q) * (1.0f / DM));
    const float mb = rbf(mean);
    const float den = rbf(sqrtf(rbf(var + 1e-5f)));
#pragma unroll
    for (int i = 0; i < 16; ++i) v[i] = rbf(rbf(v[i] - mb) / den);
}
__global__ void __launch_bounds__(256) k_prep(const bf16_t* __restrict__ x, const bf16_t* __restrict__ mem0, char* __restrict__ ws) {
    const int wave = threadIdx.x >> 6, lane = threadIdx.x & 63, row = blockIdx.x * 4 + wave;
    const u32x4* src = (const u32x4*)(x + (size_t)row * DM);
    u32x4 a = src[lane], b = src[lane + 64];
    float v[16];
    const unsigned w[8] = {a.x, a.y, a.z, a.w, b.x, b.y, b.z, b.w};
#pragma unroll
    for (int i = 0; i < 8; ++i) { v[2 * i] = bf2f(w[i] & 0xffffu); v[2 * i + 1] = bf2f(w[i] >> 16); }
    ln_row(v);
    u32x4 o0, o1;
    o0.x = pk_bf16(v[0], v[1]); o0.y = pk_bf16(v[2], v[3]); o0.z = pk_bf16(v[4], v[5]); o0.w = pk_bf16(v[6], v[7]);
    o1.x = pk_bf16(v[8], v[9]); o1.y = pk_bf16(v[10], v[11]); o1.z = pk_bf16(v[12], v[13]); o1.w = pk_bf16(v[14], v[15]);
    u32x4* dx = (u32x4*)(ws + WS_XN + (size_t)row * DM * 2);
    dx[lane] = o0; dx[lane + 64] = o1;
    ln_row(v);
    o0.x = pk_bf16(v[0], v[1]); o0.y = pk_bf16(v[2], v[3]); o0.z = pk_bf16(v[4], v[5]); o0.w = pk_bf16(v[6], v[7]);
    o1.x = pk_bf16(v[8], v[9]); o1.y = pk_bf16(v[10], v[11]); o1.z = pk_bf16(v[12], v[13]); o1.w = pk_bf16(v[14], v[15]);
    u32x4* dy = (u32x4*)(ws + WS_Y + (size_t)row * DM * 2);
    dy[lane] = o0; dy[lane + 64] = o1;
    {
        const size_t n16 = (WS_SENT1 - WS_SENT0 - MEM_B) / 16;
        u32x4* dst = (u32x4*)(ws + WS_SENT0 + MEM_B);
        const u32x4 f = (u32x4){SENT, SENT, SENT, SENT};
        for (size_t i = (size_t)blockIdx.x * 256 + threadIdx.x; i < n16; i += (size_t)gridDim.x * 256) dst[i] = f;
    }
    if (blockIdx.x >= 8 && blockIdx.x < 8 + (CNT_N * 256 / 16 + 255) / 256) {
        const int i = (blockIdx.x - 8) * 256 + threadIdx.x; if (i < CNT_N * 256 / 16) ((u32x4*)(ws + WS_CNT))[i] = (u32x4){0u, 0u, 0u, 0u}; }
    if (blockIdx.x < 4) {
        const int c = blockIdx.x * 256 + threadIdx.x, r = c & 7, kg = c >> 3;
        u32x4 m = *(const u32x4*)(mem0 + (size_t)r * DM + kg * 8);
        m.x = desent(m.x); m.y = desent(m.y); m.z = desent(m.z); m.w = desent(m.w);
        *(u32x4*)(ws + WS_MEM + (size_t)c * 16) = m;
    }
}

namespace g8 {
constexpr int BM = 256, BK = 64, HALF = 128, HTB = HALF * BK * 2, STAGE_BYTES = 8 * HTB;
__host__ __device__ __forceinline__ int lds_byte(int r, int c) { const int st = (r >> 4) * 2 + (c >> 5), rr = r & 15, cc = c & 31, ob = rr * 64 + cc * 2; return st * 1024 + (ob ^ (((ob >> 9) & 1) << 5)); }
__host__ __device__ __forceinline__ void stage_rc(int b, int& R, int& C) { const int st = b / 1024, sb = b % 1024, swz = sb ^ (((sb >> 9) & 1) << 5); R = (st >> 1) * 16 + swz / 64; C = (st & 1) * 32 + (swz % 64) / 2; }
__host__ __device__ __forceinline__ int bsw(int row) { return (row & 3) | (((row >> 3) & 1) << 2); }
struct Unit { const bf16_t* A; const bf16_t* B; int pm, pn; };

template <class Epi, class Sched>
__device__ __forceinline__ void gemm_phase(LAS unsigned char* lds, const int K, const int lda, const int ldb, const Sched& S, const Epi& E) {
    int tid = threadIdx.x; asm volatile("" : "+v"(tid));
    const int wid = __builtin_amdgcn_readfirstlane(tid >> 6), lane = tid & 63, wr = wid >> 2, wc = wid & 3, fr = lane & 15, fq = lane >> 4;
    const int nt = K / BK;
    unsigned voffA[2], voffB[2];
#pragma unroll
    for (int i = 0; i < 2; ++i) { int R, C; stage_rc(tid * 16 + i * 8192, R, C); voffA[i] = (unsigned)(R * lda + C) * 2u;
        const int b = tid * 16 + i * 8192, row = b >> 8, ch = (b & 255) >> 4, seg = (ch >> 1) ^ bsw(row);
        voffB[i] = (unsigned)(row * ldb + seg * 16 + (ch & 1) * 8) * 2u; }
    const size_t kstepA = (size_t)(BK * 2), kstepB = (size_t)BK * ldb * 2;
    const size_t hstepA = (size_t)HALF * lda * 2, hstepB = (size_t)HALF * 2;
    const unsigned ldsw = (unsigned)wid * 1024u;
    const int aoff = lds_byte(wr * 64 + fr, fq * 8);
    const int q4 = (lane & 15) >> 2, p4 = lane & 3, sB = q4 | ((fq & 1) << 2);
    const int boff0 = (8 * fq + q4) * 256 + (((wc * 2 + 0) ^ sB) * 32) + 8 * p4;
    const int boff1 = (8 * fq + q4) * 256 + (((wc * 2 + 1) ^ sB) * 32) + 8 * p4;
#define G8_SA(b, h) (((b) * 2 + (h)) * HTB)
#define G8_SB(b, h) ((4 + (b) * 2 + (h)) * HTB)
#define G8_STAGE(bufoff, gbase, voff) do { _Pragma("unroll") for (int _i = 0; _i < 2; ++_i) \
        __builtin_amdgcn_global_load_lds((const unsigned*)((const char*)(gbase) + (voff)[_i]), (LAS unsigned*)(lds + (bufoff) + ldsw + _i * 8192), 16, 0, 0); } while (0)
#define G8_LDA(dst, b, h) do { _Pragma("unroll") for (int m = 0; m < 4; ++m) _Pragma("unroll") for (int k = 0; k < 2; ++k) dst[m][k] = *(const LAS bf16x8*)(lds + G8_SA(b, h) + aoff + m * 2048 + k * 1024); } while (0)
#define G8_TR(off) __builtin_amdgcn_ds_read_tr16_b64_v4i16((LAS bf16x4*)(lds + (off)))
#define G8_LDB(dst, b, h) do { _Pragma("unroll") for (int k = 0; k < 2; ++k) { \
        bf16x4 a0 = G8_TR(G8_SB(b, h) + boff0 + k * 8192), a1 = G8_TR(G8_SB(b, h) + boff0 + k * 8192 + 1024); \
        bf16x4 c0 = G8_TR(G8_SB(b, h) + boff1 + k * 8192), c1 = G8_TR(G8_SB(b, h) + boff1 + k * 8192 + 1024); \
        dst[0][k] = __builtin_shufflevector(a0, a1, 0, 1, 2, 3, 4, 5, 6, 7); dst[1][k] = __builtin_shufflevector(c0, c1, 0, 1, 2, 3, 4, 5, 6, 7); } } while (0)
#define G8_MMA(ai, bj, At, Bt) do { __builtin_amdgcn_s_setprio(1); _Pragma("unroll") for (int m = 0; m < 4; ++m) _Pragma("unroll") for (int n = 0; n < 2; ++n) _Pragma("unroll") for (int k = 0; k < 2; ++k) \
        acc[ai][bj][m][n] = __builtin_amdgcn_mfma_f32_16x16x32_bf16(Bt[n][k], At[m][k], acc[ai][bj][m][n], 0, 0, 0); __builtin_amdgcn_s_setprio(0); } while (0)
#define G8_WAIT_V(n) asm volatile("s_waitcnt vmcnt(" #n ")" ::: "memory")
#define G8_WAIT_L(n) asm volatile("s_waitcnt lgkmcnt(" #n ")" ::: "memory")
#define G8_BAR __builtin_amdgcn_s_barrier()
#define G8_SCHED __builtin_amdgcn_sched_barrier(0)
    Unit cur, nxt; int ui = 0;
    if (!S.next(0, cur)) return;
    f32x4 acc[2][2][4][2];
#pragma unroll
    for (int a = 0; a < 2; ++a)
#pragma unroll
        for (int b = 0; b < 2; ++b)
#pragma unroll
            for (int m = 0; m < 4; ++m)
#pragma unroll
                for (int n = 0; n < 2; ++n) acc[a][b][m][n] = (f32x4){0.f, 0.f, 0.f, 0.f};
    bf16x8 At[4][2], B0[2][2], B1[2][2];
    const char* cA = (const char*)cur.A; const char* cB = (const char*)cur.B;
    G8_STAGE(G8_SB(0, 0), cB, voffB); G8_STAGE(G8_SA(0, 0), cA, voffA); G8_STAGE(G8_SB(0, 1), cB + hstepB, voffB); G8_STAGE(G8_SA(0, 1), cA + hstepA, voffA);
    if (wr == 1) G8_BAR;
    G8_WAIT_V(4); G8_BAR;
    G8_STAGE(G8_SB(1, 0), cB + kstepB, voffB); G8_STAGE(G8_SA(1, 0), cA + kstepA, voffA); G8_STAGE(G8_SB(1, 1), cB + hstepB + kstepB, voffB);
    G8_WAIT_V(6); G8_BAR;
    for (;;) {
        const bool has_next = S.next(ui + 1, nxt);
        const char* nA = has_next ? (const char*)nxt.A : cA; const char* nB = has_next ? (const char*)nxt.B : cB;
        for (int t = 0; t < nt; t += 2) {
            const bool last = (t == nt - 2);
            const char* a1 = cA + (size_t)(t + 1) * kstepA;
            const char* a2 = last ? nA : cA + (size_t)(t + 2) * kstepA; const char* b2 = last ? nB : cB + (size_t)(t + 2) * kstepB;
            const char* a3 = a2 + kstepA; const char* b3 = b2 + kstepB;
            G8_LDB(B0, 0, 0); G8_SCHED; G8_LDA(At, 0, 0); G8_STAGE(G8_SA(1, 1), a1 + hstepA, voffA);
            G8_WAIT_L(8); G8_BAR; G8_WAIT_L(0); G8_MMA(0, 0, At, B0); G8_BAR; G8_SCHED;
            G8_LDB(B1, 0, 1); G8_STAGE(G8_SB(0, 0), b2, voffB);
            G8_BAR; G8_WAIT_L(0); G8_MMA(0, 1, At, B1); G8_BAR;
            G8_LDA(At, 0, 1); G8_STAGE(G8_SA(0, 0), a2, voffA);
            G8_BAR; G8_WAIT_L(0); G8_MMA(1, 0, At, B0); G8_BAR; G8_SCHED;
            G8_STAGE(G8_SB(0, 1), b2 + hstepB, voffB);
            G8_WAIT_V(6); G8_BAR; G8_MMA(1, 1, At, B1); G8_BAR;
            G8_LDB(B0, 1, 0); G8_SCHED; G8_LDA(At, 1, 0); G8_STAGE(G8_SA(0, 1), a2 + hstepA, voffA);
            G8_WAIT_L(8); G8_BAR; G8_WAIT_L(0); G8_MMA(0, 0, At, B0); G8_BAR; G8_SCHED;
            G8_LDB(B1, 1, 1); G8_STAGE(G8_SB(1, 0), b3, voffB);
            G8_BAR; G8_WAIT_L(0); G8_MMA(0, 1, At, B1); G8_BAR;
            G8_LDA(At, 1, 1); G8_STAGE(G8_SA(1, 0), a3, voffA);
            G8_BAR; G8_WAIT_L(0); G8_MMA(1, 0, At, B0); G8_BAR; G8_SCHED;
            G8_STAGE(G8_SB(1, 1), b3 + hstepB, voffB);
            G8_WAIT_V(6); G8_BAR; G8_MMA(1, 1, At, B1); G8_BAR;
        }
        E(acc, cur, wr, wc, fr, fq);
        if (!has_next) break;
#pragma unroll
        for (int a = 0; a < 2; ++a)
#pragma unroll
            for (int b = 0; b < 2; ++b)
#pragma unroll
                for (int m = 0; m < 4; ++m)
#pragma unroll
                    for (int n = 0; n < 2; ++n) acc[a][b][m][n] = (f32x4){0.f, 0.f, 0.f, 0.f};
        cur = nxt; cA = nA; cB = nB; ++ui;
    }
    G8_WAIT_V(0);
    if (wr == 0) G8_BAR;
    G8_BAR;
#undef G8_SA
#undef G8_SB
#undef G8_STAGE
#undef G8_LDA
#undef G8_LDB
#undef G8_TR
#undef G8_MMA
#undef G8_WAIT_V
#undef G8_WAIT_L
#undef G8_BAR
#undef G8_SCHED
}
}

__device__ __forceinline__ float gelu_tanh(float x) {
    const float u = 0.7978845608028654f * (x + 0.044715f * x * x * x);
    const float th = 1.0f - 2.0f * __builtin_amdgcn_rcpf(__expf(2.0f * u) + 1.0f);
    return 0.5f * x * (1.0f + th);
}

__device__ __forceinline__ void wait_counter(const char* ws, int idx, unsigned need) {
    if (threadIdx.x < 64) {
        const unsigned* p = (const unsigned*)(ws + WS_CNT + (size_t)idx * 256);
        for (unsigned spins = 0; (unsigned)__builtin_amdgcn_readfirstlane(__hip_atomic_load(p, __ATOMIC_RELAXED, __HIP_MEMORY_SCOPE_AGENT)) < need;) {
            __builtin_amdgcn_s_sleep(8);
            if (++spins > 400000u) break;
        }
        __builtin_amdgcn_fence(__ATOMIC_ACQUIRE, "agent");
        asm volatile("s_waitcnt vmcnt(0)" ::: "memory");
    }
    asm volatile("" ::: "memory"); __builtin_amdgcn_s_barrier(); asm volatile("" ::: "memory");
}
__device__ __forceinline__ void signal_counter(char* ws, int idx) {
    asm volatile("s_waitcnt vmcnt(0)" ::: "memory");
    if ((threadIdx.x & 63) == 0) __hip_atomic_fetch_add((unsigned*)(ws + WS_CNT + (size_t)idx * 256), 1u, __ATOMIC_RELAXED, __HIP_MEMORY_SCOPE_AGENT);
}

__device__ __forceinline__ unsigned poll_ge(const char* ws, int idx, unsigned need) {
    const unsigned* p = (const unsigned*)(ws + WS_CNT + (size_t)idx * 256);
    unsigned v = 0;
    for (unsigned spins = 0; (v = (unsigned)__builtin_amdgcn_readfirstlane(__hip_atomic_load(p, __ATOMIC_RELAXED, __HIP_MEMORY_SCOPE_AGENT))) < need;) {
        __builtin_amdgcn_s_sleep(8);
        if (++spins > 400000u) break;
    }
    asm volatile("" ::: "memory");
    return v;
}

constexpr unsigned SPIN_CAP = 200000u;
constexpr int KS_PITCH = 272;
constexpr int CH_LDS = 160 * 1024;

__device__ __forceinline__ float xmax16_32(float v) {
    auto a = __builtin_amdgcn_permlane16_swap(__builtin_bit_cast(unsigned, v), __builtin_bit_cast(unsigned, v), false, false);
    v = fmaxf(__builtin_bit_cast(float, (unsigned)a[0]), __builtin_bit_cast(float, (unsigned)a[1]));
    auto b = __builtin_amdgcn_permlane32_swap(__builtin_bit_cast(unsigned, v), __builtin_bit_cast(unsigned, v), false, false);
    return fmaxf(__builtin_bit_cast(float, (unsigned)b[0]), __builtin_bit_cast(float, (unsigned)b[1]));
}
__device__ __forceinline__ float xsum16_32(float v) {
    auto a = __builtin_amdgcn_permlane16_swap(__builtin_bit_cast(unsigned, v), __builtin_bit_cast(unsigned, v), false, false);
    v = __builtin_bit_cast(float, (unsigned)a[0]) + __builtin_bit_cast(float, (unsigned)a[1]);
    auto b = __builtin_amdgcn_permlane32_swap(__builtin_bit_cast(unsigned, v), __builtin_bit_cast(unsigned, v), false, false);
    return __builtin_bit_cast(float, (unsigned)b[0]) + __builtin_bit_cast(float, (unsigned)b[1]);
}

__device__ __forceinline__ u32x4 quad_gather(unsigned v) {
    u32x4 r;
    r.x = (unsigned)__builtin_amdgcn_update_dpp(0, (int)v, 0x00, 0xf, 0xf, true); r.y = (unsigned)__builtin_amdgcn_update_dpp(0, (int)v, 0x55, 0xf, 0xf, true);
    r.z = (unsigned)__builtin_amdgcn_update_dpp(0, (int)v, 0xAA, 0xf, 0xf, true); r.w = (unsigned)__builtin_amdgcn_update_dpp(0, (int)v, 0xFF, 0xf, 0xf, true);
    return r;
}
#define STAMP(i) do {} while (0)
#define PCOUNT(sh) do {} while (0)
#define CH_DELAY1 14
#define CH_DELAY2 12
#define CH_DELAY3 12

template <int CPH>
__device__ __forceinline__ void chain_body(LAS unsigned char* lds, const int b, const bf16_t* __restrict__ Wq, const bf16_t* __restrict__ Wk, const bf16_t* __restrict__ Wv,
                                           const bf16_t* __restrict__ Wo, char* ws) {
    constexpr int ES = 128 / CPH, NE = ES / 16, NT = 3 * NE, RG = CPH / 2, RPG = 8 / RG;
    constexpr int VP = ES * 2;
    constexpr int L_RED = 0;
    constexpr int L_RED2 = L_RED + 8 * NT * 512;
    constexpr int L_KS = L_RED2 + 8 * RPG * 64;
    constexpr int L_VS = L_KS + 2 * 32 * KS_PITCH;
    constexpr int L_OS = L_VS + 2 * 48 * VP;
    constexpr bool WOL = (CPH == 4);
    constexpr int L_WO = L_OS + 8 * 1024 * RPG;
    static_assert(L_WO + (WOL ? 65536 : 0) <= 156 * 1024, "LDS");
    const int tid = threadIdx.x, wave = __builtin_amdgcn_readfirstlane(tid >> 6), lane = tid & 63, fr = lane & 15, fq = lane >> 4;
    const int xc = b & 7, yc = b >> 3;
    const int h = xc + 8 * (yc / CPH), j = yc % CPH, g = yc % RG, c = xc * 8 + yc / RG;
    const __amdgpu_buffer_rsrc_t rs = __builtin_amdgcn_make_buffer_rsrc(ws + WS_SENT0, 0, (int)(WS_SENT1 - WS_SENT0), 0x00020000);
    constexpr int O_MEM = (int)(WS_MEM - WS_SENT0), O_QKVM = (int)(WS_QKVM - WS_SENT0), O_OM = (int)(WS_OM - WS_SENT0);
    const __amdgpu_buffer_rsrc_t rkv = __builtin_amdgcn_make_buffer_rsrc(ws + WS_K, 0, (int)(WS_H - WS_K), 0x00020000);
    constexpr int O_V = (int)(WS_V - WS_K);
    bool dead = false;

    bf16x8 wq[NT][4], wo[16];
    {
        LAS unsigned char* stg = lds + wave * 8192;
        constexpr int CPR = ES / 8;
        static_assert(128 * CPR / 64 == 8, "8 chunks per lane and pass");
        u32x4 t[5][8];
        auto issue = [&](int ps) {
            if (ps < 3) { const bf16_t* W = (ps == 0 ? Wq : (ps == 1 ? Wk : Wv)) + (size_t)wave * 128 * HD + h * DH + j * ES;
#pragma unroll
                for (int i = 0; i < 8; ++i) { const int ci = i * 64 + lane; t[ps][i] = *(const u32x4*)(W + (size_t)(ci / CPR) * HD + (ci % CPR) * 8); } }
            else { const bf16_t* W = Wo + (size_t)(wave * 512 + (ps - 3) * 256) * DM + c * 16;
#pragma unroll
                for (int i = 0; i < 8; ++i) { const int ci = i * 64 + lane; t[ps][i] = *(const u32x4*)(W + (size_t)(ci >> 1) * DM + (ci & 1) * 8); } }
        };
        issue(0); issue(1); issue(2);
        asm volatile("" ::: "memory");
#pragma unroll
        for (int ps = 0; ps < 5; ++ps) {
#pragma unroll
            for (int i = 0; i < 8; ++i) *(LAS u32x4*)(stg + (i * 64 + lane) * 16) = t[ps][i];
            asm volatile("s_waitcnt lgkmcnt(0)" ::: "memory");
            if (ps + 3 < 5) issue(ps + 3);
            asm volatile("" ::: "memory");
            if (ps < 3) {
#pragma unroll
                for (int e = 0; e < NE; ++e)
#pragma unroll
                    for (int i = 0; i < 4; ++i) {
                        const int off = (i * 32 + 8 * fq + (fr >> 2)) * (ES * 2) + e * 32 + (lane & 3) * 8;
                        const bf16x4 a0 = __builtin_amdgcn_ds_read_tr16_b64_v4i16((LAS bf16x4*)(stg + off)), a1 = __builtin_amdgcn_ds_read_tr16_b64_v4i16((LAS bf16x4*)(stg + off + 4 * ES * 2));
                        wq[ps * NE + e][i] = __builtin_shufflevector(a0, a1, 0, 1, 2, 3, 4, 5, 6, 7);
                    }
            } else {
#pragma unroll
                for (int i = 0; i < 8; ++i) {
                    const int off = (i * 32 + 8 * fq + (fr >> 2)) * 32 + (lane & 3) * 8;
                    const bf16x4 a0 = __builtin_amdgcn_ds_read_tr16_b64_v4i16((LAS bf16x4*)(stg + off)), a1 = __builtin_amdgcn_ds_read_tr16_b64_v4i16((LAS bf16x4*)(stg + off + 128));
                    wo[(ps - 3) * 8 + i] = __builtin_shufflevector(a0, a1, 0, 1, 2, 3, 4, 5, 6, 7);
                }
            }
            asm volatile("s_waitcnt lgkmcnt(0)" ::: "memory");
        }
        if (WOL) {
#pragma unroll
            for (int i = 8; i < 16; ++i) *(LAS bf16x8*)(lds + L_WO + (wave * 8 + i - 8) * 1024 + lane * 16) = wo[i];
        }
    }
    __syncthreads();

    if (tid < 2 * 8 * VP / 16) { const int bf = tid / (8 * VP / 16), r = tid % (8 * VP / 16); *(LAS u32x4*)(lds + L_VS + bf * 48 * VP + 40 * VP + r * 16) = (u32x4){0u, 0u, 0u, 0u}; }
    {
        poll_ge(ws, CNT_KV + (h >> 1), 16u); poll_ge(ws, CNT_KV + 16 + (h >> 1), 16u);
        const int r = tid >> 4, ch = tid & 15;
        *(LAS u32x4*)(lds + L_KS + r * KS_PITCH + ch * 16) = __builtin_amdgcn_raw_buffer_load_b128(rkv, (r * HD + h * DH + ch * 8) * 2, 0, AUX_SC1);
        if (tid < 32 * ES / 8) { const int r2 = tid / (ES / 8), c2 = tid % (ES / 8);
            *(LAS u32x4*)(lds + L_VS + (8 + r2) * VP + c2 * 16) = __builtin_amdgcn_raw_buffer_load_b128(rkv, O_V + (r2 * HD + h * DH + j * ES + c2 * 8) * 2, 0, AUX_SC1); }
    }
    __syncthreads();

    for (int s = 0; s < NSEG; ++s) {
        const int kb = s & 1;
        unsigned long long pcnt = 0; (void)pcnt;
        STAMP(0);
        {
            if (s > 0) __builtin_amdgcn_s_sleep(CH_DELAY1);
            u32x4 a[4];
            const int base = O_MEM + s * (int)MEM_B + wave * 2048 + fq * 128 + (lane & 7) * 16;
            for (unsigned spins = 0;;) {
                asm volatile("" ::: "memory");
#pragma unroll
                for (int i = 0; i < 4; ++i) a[i] = __builtin_amdgcn_raw_buffer_load_b128(rs, base + i * 512, 0, AUX_SC1);
                unsigned mxs = 0;
#pragma unroll
                for (int i = 0; i < 4; ++i) mxs = umax2(mxs, umax4(a[i]));
                if (!__any(mxs == SENT) || dead) break;
                PCOUNT(0);
                if (++spins > SPIN_CAP) { dead = true; if (lane == 0) *(volatile unsigned*)(ws + WS_TMO) = 1u; break; }
            }
            STAMP(1);
            if (b == 0 && tid == 448 && (s & 7) == 0) __hip_atomic_store((unsigned*)(ws + WS_CNT + (size_t)CNT_STEP * 256), (unsigned)s, __ATOMIC_RELAXED, __HIP_MEMORY_SCOPE_AGENT);
            f32x4 acc[NT];
#pragma unroll
            for (int t = 0; t < NT; ++t) acc[t] = (f32x4){0.f, 0.f, 0.f, 0.f};
#pragma unroll
            for (int i = 0; i < 4; ++i)
#pragma unroll
                for (int t = 0; t < NT; ++t) acc[t] = __builtin_amdgcn_mfma_f32_16x16x32_bf16(wq[t][i], __builtin_bit_cast(bf16x8, a[i]), acc[t], 0, 0, 0);
            if (fr < 8) {
#pragma unroll
                for (int t = 0; t < NT; ++t) *(LAS f32x4*)(lds + L_RED + ((wave * NT + t) * 128 + fr * 16 + fq * 4) * 4) = acc[t];
            }
        }
        __syncthreads();
        if (tid < NT * 64) {
            const int t = tid >> 6, m = (tid & 63) >> 3, np = (tid & 7) * 2, mat = t / NE, col = (t % NE) * 16 + np;
            f32x2 sum = (f32x2){0.f, 0.f};
#pragma unroll
            for (int w = 0; w < 8; ++w) sum += *(const LAS f32x2*)(lds + L_RED + ((w * NT + t) * 128 + m * 16 + np) * 4);
            const unsigned pk = desent(pk_bf16(sum.x, sum.y));
            const u32x4 q4 = quad_gather(pk);
            if ((tid & 3) == 0) __builtin_amdgcn_raw_buffer_store_b128(q4, rs, O_QKVM + s * (int)QKVM_B + ((h * 3 + mat) * 8 + m) * 256 + (j * ES + col) * 2, 0, AUX_SC1);
            if (mat == 2) *(LAS unsigned*)(lds + L_VS + kb * 48 * VP + m * VP + col * 2) = pk;
        }
        STAMP(2);
        if (s == NSEG - 1) break;
        __syncthreads();
        if (wave == 0) {
            bf16x8 kx[3][4];
#pragma unroll
            for (int t = 0; t < 3; ++t) {
                int r = t * 16 + fr - 8; r = r < 0 ? 0 : (r > 31 ? 31 : r);
#pragma unroll
                for (int i = 0; i < 4; ++i) kx[t][i] = *(const LAS bf16x8*)(lds + L_KS + kb * 32 * KS_PITCH + r * KS_PITCH + i * 64 + fq * 16);
            }
            __builtin_amdgcn_s_sleep(CH_DELAY2);
            u32x4 qf[4], km[4];
            const int base = O_QKVM + s * (int)QKVM_B + h * 3 * 2048 + (lane & 7) * 256 + fq * 16;
            for (unsigned spins = 0;;) {
                asm volatile("" ::: "memory");
#pragma unroll
                for (int i = 0; i < 4; ++i) { qf[i] = __builtin_amdgcn_raw_buffer_load_b128(rs, base + i * 64, 0, AUX_SC1); km[i] = __builtin_amdgcn_raw_buffer_load_b128(rs, base + 2048 + i * 64, 0, AUX_SC1); }
                unsigned mxs = 0;
#pragma unroll
                for (int i = 0; i < 4; ++i) mxs = umax2(mxs, umax2(umax4(qf[i]), umax4(km[i])));
                if (!__any(mxs == SENT) || dead) break;
                PCOUNT(16);
                if (++spins > SPIN_CAP) { dead = true; if (lane == 0) *(volatile unsigned*)(ws + WS_TMO) = 2u; break; }
            }
            STAMP(3);
            f32x4 sc[3];
#pragma unroll
            for (int t = 0; t < 3; ++t) sc[t] = (f32x4){0.f, 0.f, 0.f, 0.f};
#pragma unroll
            for (int i = 0; i < 4; ++i) {
                const bf16x8 k0 = fr < 8 ? __builtin_bit_cast(bf16x8, km[i]) : kx[0][i];
                sc[1] = __builtin_amdgcn_mfma_f32_16x16x32_bf16(kx[1][i], __builtin_bit_cast(bf16x8, qf[i]), sc[1], 0, 0, 0);
                sc[2] = __builtin_amdgcn_mfma_f32_16x16x32_bf16(kx[2][i], __builtin_bit_cast(bf16x8, qf[i]), sc[2], 0, 0, 0);
                sc[0] = __builtin_amdgcn_mfma_f32_16x16x32_bf16(k0, __builtin_bit_cast(bf16x8, qf[i]), sc[0], 0, 0, 0);
            }
            asm volatile("" ::: "memory");
            bf16x8 vf[NE][2];
            const bf16x4 zz = (bf16x4){0, 0, 0, 0};
#pragma unroll
            for (int et = 0; et < NE; ++et) {
                const int voff = L_VS + kb * 48 * VP + (4 * fq + (fr >> 2)) * VP + et * 32 + (lane & 3) * 8;
                const bf16x4 v0 = __builtin_amdgcn_ds_read_tr16_b64_v4i16((LAS bf16x4*)(lds + voff));
                const bf16x4 v1 = __builtin_amdgcn_ds_read_tr16_b64_v4i16((LAS bf16x4*)(lds + voff + 16 * VP));
                const bf16x4 v2 = __builtin_amdgcn_ds_read_tr16_b64_v4i16((LAS bf16x4*)(lds + voff + 32 * VP));
                vf[et][0] = __builtin_shufflevector(v0, v1, 0, 1, 2, 3, 4, 5, 6, 7); vf[et][1] = __builtin_shufflevector(v2, zz, 0, 1, 2, 3, 4, 5, 6, 7);
            }
            const float scale = 0.08838834764831845f;
            const bool hi = fr >= 8;
            float v[6];
            v[0] = hi ? sc[1][2] : sc[0][0]; v[1] = hi ? sc[1][3] : sc[0][1]; v[2] = hi ? sc[2][0] : sc[0][2];
            v[3] = hi ? sc[2][1] : sc[0][3]; v[4] = hi ? sc[2][2] : sc[1][0]; v[5] = hi ? sc[2][3] : sc[1][1];
            float mx = -INFINITY;
#pragma unroll
            for (int i = 0; i < 6; ++i) { float z = rbf(rbf(v[i]) * scale); if (i >= 2 && hi && fq >= 2) z = -INFINITY; v[i] = z; mx = fmaxf(mx, z); }
            mx = fmaxf(mx, __builtin_bit_cast(float, __builtin_amdgcn_update_dpp(0, __builtin_bit_cast(int, mx), 0x128, 0xf, 0xf, true)));
            mx = xmax16_32(mx);
            float sum = 0.f;
#pragma unroll
            for (int i = 0; i < 6; ++i) { v[i] = __expf(v[i] - mx); sum += v[i]; }
            sum += __builtin_bit_cast(float, __builtin_amdgcn_update_dpp(0, __builtin_bit_cast(int, sum), 0x128, 0xf, 0xf, true));
            sum = xsum16_32(sum);
            const float inv = 1.0f / sum;
            const unsigned w0 = pk_bf16(v[0] * inv, v[1] * inv), w1 = pk_bf16(v[2] * inv, v[3] * inv), w2 = pk_bf16(v[4] * inv, v[5] * inv);
            u32x4 p0, p1;
            p0.x = w0; p0.y = w1; p0.z = w2; p0.w = (unsigned)__builtin_amdgcn_update_dpp(0, (int)w0, 0x128, 0xf, 0xf, true);
            p1.x = (unsigned)__builtin_amdgcn_update_dpp(0, (int)w1, 0x128, 0xf, 0xf, true); p1.y = (unsigned)__builtin_amdgcn_update_dpp(0, (int)w2, 0x128, 0xf, 0xf, true); p1.z = 0u; p1.w = 0u;
#pragma unroll
            for (int et = 0; et < NE; ++et) {
                f32x4 o = (f32x4){0.f, 0.f, 0.f, 0.f};
                o = __builtin_amdgcn_mfma_f32_16x16x32_bf16(vf[et][0], __builtin_bit_cast(bf16x8, p0), o, 0, 0, 0);
                o = __builtin_amdgcn_mfma_f32_16x16x32_bf16(vf[et][1], __builtin_bit_cast(bf16x8, p1), o, 0, 0, 0);
                {
                    const unsigned wx = desent(pk_bf16(o[0], o[1])), wy = desent(pk_bf16(o[2], o[3]));
                    auto sx = __builtin_amdgcn_permlane16_swap(wx, wx, false, false); auto sy = __builtin_amdgcn_permlane16_swap(wy, wy, false, false);
                    const u32x4 w = (u32x4){(unsigned)sx[0], (unsigned)sy[0], (unsigned)sx[1], (unsigned)sy[1]};
                    const int k = h * DH + j * ES + et * 16 + 4 * fq;
                    const int off = O_OM + s * (int)OM_B + (fr / RPG) * (8192 * RPG) + (k >> 5) * (64 * RPG) + ((k >> 3) & 3) * (16 * RPG) + (fr % RPG) * 16;
                    if (fr < 8 && (fq & 1) == 0) __builtin_amdgcn_raw_buffer_store_b128(w, rs, off, 0, AUX_SC1);
                }
            }
            STAMP(4);
        } else {
            const int t2 = tid - 64; const int nb = kb ^ 1; const int row0 = (s + 1) * SEG;
            if (((s + 1) & 7) == 0) { const int blk = (s + 1) >> 3; poll_ge(ws, CNT_KV + blk * 32 + (h >> 1), 8u); poll_ge(ws, CNT_KV + blk * 32 + 16 + (h >> 1), 8u); }
            if (s + 1 == 4) { poll_ge(ws, CNT_KV0B + (h >> 1), 16u); poll_ge(ws, CNT_KV0B + 16 + (h >> 1), 16u); }
            for (int i = t2; i < 512 + 32 * ES / 8; i += 448) {
                if (i < 512) { const int r = i >> 4, ch = i & 15;
                    *(LAS u32x4*)(lds + L_KS + nb * 32 * KS_PITCH + r * KS_PITCH + ch * 16) = __builtin_amdgcn_raw_buffer_load_b128(rkv, ((row0 + r) * HD + h * DH + ch * 8) * 2, 0, AUX_SC1); }
                else { const int r2 = (i - 512) / (ES / 8), c2 = (i - 512) % (ES / 8);
                    *(LAS u32x4*)(lds + L_VS + nb * 48 * VP + (8 + r2) * VP + c2 * 16) = __builtin_amdgcn_raw_buffer_load_b128(rkv, O_V + ((row0 + r2) * HD + h * DH + j * ES + c2 * 8) * 2, 0, AUX_SC1); }
            }
        }
        __syncthreads();
        {
            if (WOL) {
#pragma unroll
                for (int i = 8; i < 16; ++i) wo[i] = *(const LAS bf16x8*)(lds + L_WO + (wave * 8 + i - 8) * 1024 + lane * 16);
            }
            __builtin_amdgcn_s_sleep(CH_DELAY3);
            u32x4 a[RPG];
            constexpr int KPG = 16 / RPG;
            const int base = O_OM + s * (int)OM_B + g * (8192 * RPG) + wave * (1024 * RPG) + (fr / RPG) * (64 * RPG) + fq * (16 * RPG) + (fr % RPG) * 16;
            for (unsigned spins = 0;;) {
                asm volatile("" ::: "memory");
#pragma unroll
                for (int i = 0; i < RPG; ++i) a[i] = __builtin_amdgcn_raw_buffer_load_b128(rs, base + i * 1024, 0, AUX_SC1);
                unsigned mxs = 0;
#pragma unroll
                for (int i = 0; i < RPG; ++i) mxs = umax2(mxs, umax4(a[i]));
                if (!__any(mxs == SENT) || dead) break;
                PCOUNT(32);
                if (++spins > SPIN_CAP) { dead = true; if (lane == 0) *(volatile unsigned*)(ws + WS_TMO) = 3u; break; }
            }
            STAMP(5);
            f32x4 acc[KPG];
#pragma unroll
            for (int q = 0; q < KPG; ++q) acc[q] = (f32x4){0.f, 0.f, 0.f, 0.f};
#pragma unroll
            for (int gq = 0; gq < RPG; ++gq)
#pragma unroll
                for (int q = 0; q < KPG; ++q) acc[q] = __builtin_amdgcn_mfma_f32_16x16x32_bf16(wo[gq * KPG + q], __builtin_bit_cast(bf16x8, a[gq]), acc[q], 0, 0, 0);
            f32x4 r = acc[0];
#pragma unroll
            for (int q = 1; q < KPG; ++q) if (fr / RPG == q) r = acc[q];
#pragma unroll
            for (int e = 0; e < 4; ++e) {
                if (RPG <= 2) r[e] += __builtin_bit_cast(float, __builtin_amdgcn_update_dpp(0, __builtin_bit_cast(int, r[e]), 0x122, 0xf, 0xf, true));
                r[e] += __builtin_bit_cast(float, __builtin_amdgcn_update_dpp(0, __builtin_bit_cast(int, r[e]), 0x124, 0xf, 0xf, true));
                r[e] += __builtin_bit_cast(float, __builtin_amdgcn_update_dpp(0, __builtin_bit_cast(int, r[e]), 0x128, 0xf, 0xf, true));
            }
            if (fr < RPG) *(LAS f32x4*)(lds + L_RED2 + ((wave * RPG + fr) * 16 + fq * 4) * 4) = r;
        }
        __syncthreads();
        if (tid < RPG * 8) {
            const int m = tid >> 3, np = (tid & 7) * 2;
            f32x2 sum = (f32x2){0.f, 0.f};
#pragma unroll
            for (int w = 0; w < 8; ++w) sum += *(const LAS f32x2*)(lds + L_RED2 + ((w * RPG + m) * 16 + np) * 4);
            const unsigned pk = desent(pk_bf16(sum.x, sum.y));
            const int off = O_MEM + (s + 1) * (int)MEM_B + (c >> 1) * 512 + ((c & 1) * 2 + (np >> 3)) * 128 + (g * RPG + m) * 16;
            const u32x4 q4 = quad_gather(pk);
            if ((tid & 3) == 0) __builtin_amdgcn_raw_buffer_store_b128(q4, rs, off, 0, AUX_SC1);
        }
        STAMP(6);
    }
}

template <int MT, class Epi>
__device__ __forceinline__ void gemm128_w8(LAS unsigned char* lds, const bf16_t* A, const int lda, const bf16_t* B, const int ldb, const int K, const Epi& E) {
    int tid = threadIdx.x; asm volatile("" : "+v"(tid));
    const int wid = __builtin_amdgcn_readfirstlane(tid >> 6), lane = tid & 63, wr = wid >> 2, wc = wid & 3, fr = lane & 15, fq = lane >> 4;
    constexpr int NA = MT / 2;
    unsigned voffA[NA], voffB[2];
#pragma unroll
    for (int i = 0; i < NA; ++i) { int R, C; g8::stage_rc(tid * 16 + i * 8192, R, C); voffA[i] = (unsigned)(R * lda + C) * 2u; }
#pragma unroll
    for (int i = 0; i < 2; ++i) { const int bb = tid * 16 + i * 8192, row = bb >> 8, ch = (bb & 255) >> 4, seg = (ch >> 1) ^ g8::bsw(row);
        voffB[i] = (unsigned)(row * ldb + seg * 16 + (ch & 1) * 8) * 2u; }
    const char* cA = (const char*)A; const char* cB = (const char*)B;
    const unsigned ldsw = (unsigned)wid * 1024u;
    const int aoff = g8::lds_byte(wr * 16 * MT + fr, fq * 8);
    const int q4 = (lane & 15) >> 2, p4 = lane & 3, sB = q4 | ((fq & 1) << 2);
    const int boff0 = (8 * fq + q4) * 256 + (((wc * 2 + 0) ^ sB) * 32) + 8 * p4;
    const int boff1 = (8 * fq + q4) * 256 + (((wc * 2 + 1) ^ sB) * 32) + 8 * p4;
    f32x4 acc[MT][2];
#pragma unroll
    for (int m = 0; m < MT; ++m) { acc[m][0] = (f32x4){0.f, 0.f, 0.f, 0.f}; acc[m][1] = (f32x4){0.f, 0.f, 0.f, 0.f}; }
    const int nt = K / 64;
#define STG(buf, t) do { _Pragma("unroll") for (int _i = 0; _i < NA; ++_i) \
        __builtin_amdgcn_global_load_lds((const unsigned*)(cA + (size_t)(t) * 128 + voffA[_i]), (LAS unsigned*)(lds + (buf) * 32768 + ldsw + _i * 8192), 16, 0, 0); \
        _Pragma("unroll") for (int _i = 0; _i < 2; ++_i) \
        __builtin_amdgcn_global_load_lds((const unsigned*)(cB + (size_t)(t) * 64 * ldb * 2 + voffB[_i]), (LAS unsigned*)(lds + (buf) * 32768 + 16384 + ldsw + _i * 8192), 16, 0, 0); } while (0)
    STG(0, 0); STG(1, 1); STG(2, 2);
    int cur = 0;
    for (int t = 0; t < nt; ++t) {
        if (MT == 4) { if (t + 2 < nt) asm volatile("s_waitcnt vmcnt(8)" ::: "memory"); else if (t + 1 < nt) asm volatile("s_waitcnt vmcnt(4)" ::: "memory"); else asm volatile("s_waitcnt vmcnt(0)" ::: "memory"); }
        else { if (t + 2 < nt) asm volatile("s_waitcnt vmcnt(6)" ::: "memory"); else if (t + 1 < nt) asm volatile("s_waitcnt vmcnt(3)" ::: "memory"); else asm volatile("s_waitcnt vmcnt(0)" ::: "memory"); }
        __builtin_amdgcn_s_barrier();
        asm volatile("" ::: "memory");
        if (t + 3 < nt) STG((cur + 3) & 3, t + 3);
        bf16x8 At[MT][2], Bt[2][2];
        LAS unsigned char* sa = lds + cur * 32768; LAS unsigned char* sb = sa + 16384;
#pragma unroll
        for (int k = 0; k < 2; ++k) {
            bf16x4 a0 = __builtin_amdgcn_ds_read_tr16_b64_v4i16((LAS bf16x4*)(sb + boff0 + k * 8192)), a1 = __builtin_amdgcn_ds_read_tr16_b64_v4i16((LAS bf16x4*)(sb + boff0 + k * 8192 + 1024));
            bf16x4 c0 = __builtin_amdgcn_ds_read_tr16_b64_v4i16((LAS bf16x4*)(sb + boff1 + k * 8192)), c1 = __builtin_amdgcn_ds_read_tr16_b64_v4i16((LAS bf16x4*)(sb + boff1 + k * 8192 + 1024));
            Bt[0][k] = __builtin_shufflevector(a0, a1, 0, 1, 2, 3, 4, 5, 6, 7); Bt[1][k] = __builtin_shufflevector(c0, c1, 0, 1, 2, 3, 4, 5, 6, 7);
#pragma unroll
            for (int m = 0; m < MT; ++m) At[m][k] = *(const LAS bf16x8*)(sa + aoff + m * 2048 + k * 1024);
        }
        __builtin_amdgcn_s_setprio(1);
#pragma unroll
        for (int k = 0; k < 2; ++k)
#pragma unroll
            for (int m = 0; m < MT; ++m)
#pragma unroll
                for (int n = 0; n < 2; ++n) acc[m][n] = __builtin_amdgcn_mfma_f32_16x16x32_bf16(Bt[n][k], At[m][k], acc[m][n], 0, 0, 0);
        __builtin_amdgcn_s_setprio(0);
        asm volatile("s_waitcnt lgkmcnt(0)" ::: "memory");
        cur = (cur + 1) & 3;
    }
#undef STG
    __builtin_amdgcn_s_barrier();
    E(acc, wr, wc, fr, fq);
}

constexpr int XV_PITCH = 272;
template <bool INK>
__device__ __forceinline__ void xattn_item(LAS unsigned char* vs, char* ws, const int s, const int h) {
    const int lane = threadIdx.x & 63, fr = lane & 15, fq = lane >> 4;
    const bf16_t* Q = (const bf16_t*)(ws + WS_Q) + (size_t)s * SEG * HD + h * DH;
    const bf16_t* K = (const bf16_t*)(ws + WS_K) + (size_t)s * SEG * HD + h * DH;
    const bf16_t* V = (const bf16_t*)(ws + WS_V) + (size_t)s * SEG * HD + h * DH;
    const __amdgpu_buffer_rsrc_t rm = __builtin_amdgcn_make_buffer_rsrc(ws + WS_QKVM, 0, (int)(WS_OM - WS_QKVM), 0x00020000);
    const int kmo = s * (int)QKVM_B + (h * 3 + 1) * 2048, vmo = kmo + 2048;
    u32x4 km[4], vm[2];
    for (unsigned spins = 0;;) {
        asm volatile("" ::: "memory");
#pragma unroll
        for (int i = 0; i < 4; ++i) km[i] = __builtin_amdgcn_raw_buffer_load_b128(rm, kmo + (lane & 7) * 256 + i * 64 + fq * 16, 0, INK ? AUX_SC1 : 0);
#pragma unroll
        for (int i = 0; i < 2; ++i) vm[i] = __builtin_amdgcn_raw_buffer_load_b128(rm, vmo + (i * 4 + fq) * 256 + fr * 16, 0, INK ? AUX_SC1 : 0);
        if (!INK) break;
        unsigned mxs = umax2(umax4(vm[0]), umax4(vm[1]));
#pragma unroll
        for (int i = 0; i < 4; ++i) mxs = umax2(mxs, umax4(km[i]));
        if (!__any(mxs == SENT)) break;
        __builtin_amdgcn_s_sleep(16);
        if (++spins > SPIN_CAP) break;
    }
#pragma unroll
    for (int i = 0; i < 12; ++i) {
        const int r = i * 4 + fq;
        u32x4 v = (u32x4){0u, 0u, 0u, 0u};
        if (i < 2) v = vm[i]; else if (i < 10) v = *(const u32x4*)(V + (size_t)(r - 8) * HD + fr * 8);
        *(LAS u32x4*)(vs + r * XV_PITCH + fr * 16) = v;
    }
    f32x4 sc[3][2];
#pragma unroll
    for (int t = 0; t < 3; ++t) { sc[t][0] = (f32x4){0.f, 0.f, 0.f, 0.f}; sc[t][1] = (f32x4){0.f, 0.f, 0.f, 0.f}; }
#pragma unroll
    for (int i = 0; i < 4; ++i) {
        const bf16x8 q0 = *(const bf16x8*)(Q + (size_t)fr * HD + i * 32 + fq * 8), q1 = *(const bf16x8*)(Q + (size_t)(16 + fr) * HD + i * 32 + fq * 8);
#pragma unroll
        for (int t = 0; t < 3; ++t) {
            int r = t * 16 + fr - 8; r = r < 0 ? 0 : (r > 31 ? 31 : r);
            bf16x8 kf = *(const bf16x8*)(K + (size_t)r * HD + i * 32 + fq * 8);
            if (t == 0 && fr < 8) kf = __builtin_bit_cast(bf16x8, km[i]);
            sc[t][0] = __builtin_amdgcn_mfma_f32_16x16x32_bf16(kf, q0, sc[t][0], 0, 0, 0);
            sc[t][1] = __builtin_amdgcn_mfma_f32_16x16x32_bf16(kf, q1, sc[t][1], 0, 0, 0);
        }
    }
    const float scale = 0.08838834764831845f, NEG = rbf(-1000000000.0f);
    u32x4 p0[2], p1[2];
#pragma unroll
    for (int rt = 0; rt < 2; ++rt) {
        const int row = rt * 16 + fr;
        float v[12]; float mx = -INFINITY;
#pragma unroll
        for (int t = 0; t < 3; ++t)
#pragma unroll
            for (int r = 0; r < 4; ++r) { const int key = t * 16 + fq * 4 + r; float z = rbf(rbf(sc[t][rt][r]) * scale);
                if (key - 8 > row) z = NEG; if (key >= 40) z = -INFINITY; v[t * 4 + r] = z; mx = fmaxf(mx, z); }
        mx = xmax16_32(mx);
        float sum = 0.f;
#pragma unroll
        for (int i = 0; i < 12; ++i) { v[i] = __expf(v[i] - mx); sum += v[i]; }
        sum = xsum16_32(sum);
        const float inv = 1.0f / sum;
        p0[rt].x = pk_bf16(v[0] * inv, v[1] * inv); p0[rt].y = pk_bf16(v[2] * inv, v[3] * inv); p0[rt].z = pk_bf16(v[4] * inv, v[5] * inv); p0[rt].w = pk_bf16(v[6] * inv, v[7] * inv);
        p1[rt].x = pk_bf16(v[8] * inv, v[9] * inv); p1[rt].y = pk_bf16(v[10] * inv, v[11] * inv); p1[rt].z = 0u; p1[rt].w = 0u;
    }
    asm volatile("s_waitcnt lgkmcnt(0)" ::: "memory");
    const __amdgpu_buffer_rsrc_t ro = __builtin_amdgcn_make_buffer_rsrc(ws + WS_O, 0, S_LEN * HD * 2, 0x00020000);
    const int obase = (s * SEG * HD + h * DH) * 2;
    const bf16x4 zz = (bf16x4){0, 0, 0, 0};
    u32x2 ow[8][2];
#pragma unroll
    for (int et = 0; et < 8; ++et) {
        const int voff = (4 * fq + (fr >> 2)) * XV_PITCH + et * 32 + (lane & 3) * 8;
        const bf16x4 v0 = __builtin_amdgcn_ds_read_tr16_b64_v4i16((LAS bf16x4*)(vs + voff));
        const bf16x4 v1 = __builtin_amdgcn_ds_read_tr16_b64_v4i16((LAS bf16x4*)(vs + voff + 16 * XV_PITCH));
        const bf16x4 v2 = __builtin_amdgcn_ds_read_tr16_b64_v4i16((LAS bf16x4*)(vs + voff + 32 * XV_PITCH));
        const bf16x8 va = __builtin_shufflevector(v0, v1, 0, 1, 2, 3, 4, 5, 6, 7), vb = __builtin_shufflevector(v2, zz, 0, 1, 2, 3, 4, 5, 6, 7);
#pragma unroll
        for (int rt = 0; rt < 2; ++rt) {
            f32x4 o = (f32x4){0.f, 0.f, 0.f, 0.f};
            o = __builtin_amdgcn_mfma_f32_16x16x32_bf16(va, __builtin_bit_cast(bf16x8, p0[rt]), o, 0, 0, 0);
            o = __builtin_amdgcn_mfma_f32_16x16x32_bf16(vb, __builtin_bit_cast(bf16x8, p1[rt]), o, 0, 0, 0);
            ow[et][rt].x = pk_bf16(o[0], o[1]); ow[et][rt].y = pk_bf16(o[2], o[3]);
        }
    }
    asm volatile("s_waitcnt lgkmcnt(0)" ::: "memory");
#pragma unroll
    for (int et = 0; et < 8; ++et)
#pragma unroll
        for (int rt = 0; rt < 2; ++rt) *(LAS u32x2*)(vs + (rt * 16 + fr) * XV_PITCH + et * 32 + fq * 8) = ow[et][rt];
    asm volatile("s_waitcnt lgkmcnt(0)" ::: "memory");
#pragma unroll
    for (int i = 0; i < 8; ++i) {
        const int r = i * 4 + fq;
        const u32x4 w = *(const LAS u32x4*)(vs + r * XV_PITCH + fr * 16);
        __builtin_amdgcn_raw_buffer_store_b128(w, ro, obase + (r * HD + fr * 8) * 2, 0, INK ? AUX_SC1 : 0);
    }
    asm volatile("s_waitcnt lgkmcnt(0)" ::: "memory");
}

__device__ __forceinline__ void store_pair_sc1(const __amdgpu_buffer_rsrc_t rs, int off0, u32x2 w0, u32x2 w1, int fq) {
    auto sx = __builtin_amdgcn_permlane16_swap(w0.x, w1.x, false, false); auto sy = __builtin_amdgcn_permlane16_swap(w0.y, w1.y, false, false);
    const u32x4 w = (u32x4){(unsigned)sx[0], (unsigned)sy[0], (unsigned)sx[1], (unsigned)sy[1]};
    __builtin_amdgcn_raw_buffer_store_b128(w, rs, off0 + (fq & 1) * 32 + (fq >> 1) * 16, 0, AUX_SC1);
}
struct SchedOne {
    g8::Unit u; bool valid;
    __device__ __forceinline__ bool next(int i, g8::Unit& o) const { if (i != 0 || !valid) return false; o = u; return true; }
};
struct SchedKVQ {
    const bf16_t* A; const bf16_t* Wq; long dk, dv; int r;
    __device__ __forceinline__ bool next(int i, g8::Unit& u) const {
        const int L = r + 128 * i; if (L >= 352) return false;
        int pm, pn;
        if (L < 224) { pm = 1 + (L >> 5); pn = 16 + (L & 31); } else { pm = (L - 224) & 7; pn = (L - 224) >> 3; }
        const int mat = pn >> 4;
        u.A = A + (size_t)pm * 256 * DM; u.B = Wq + (mat == 1 ? dk : 0L) + (mat == 2 ? dv : 0L) + (pn & 15) * 256; u.pm = pm; u.pn = pn; return true;
    }
};
struct EpiQKV {
    char* ws;
    __device__ __forceinline__ void operator()(const f32x4 (&acc)[2][2][4][2], const g8::Unit& u, int wr, int wc, int fr, int fq) const {
        const __amdgpu_buffer_rsrc_t rq = __builtin_amdgcn_make_buffer_rsrc(ws + WS_Q, 0, (int)(WS_H - WS_Q), 0x00020000);
        const int base = (u.pn >> 4) * (S_LEN * HD * 2) + ((u.pm * 256 + wr * 64 + fr) * HD + (u.pn & 15) * 256 + wc * 32) * 2;
#pragma unroll
        for (int ai = 0; ai < 2; ++ai)
#pragma unroll
            for (int m = 0; m < 4; ++m)
#pragma unroll
                for (int bj = 0; bj < 2; ++bj) { const f32x4 v0 = acc[ai][bj][m][0], v1 = acc[ai][bj][m][1];
                    store_pair_sc1(rq, base + ((ai * 128 + m * 16) * HD + bj * 128) * 2, (u32x2){pk_bf16(v0[0], v0[1]), pk_bf16(v0[2], v0[3])}, (u32x2){pk_bf16(v1[0], v1[1]), pk_bf16(v1[2], v1[3])}, fq); }
        asm volatile("s_waitcnt vmcnt(0)" ::: "memory");
        if ((threadIdx.x & 63) == 0) {
            if (u.pn >= 16) __hip_atomic_fetch_add((unsigned*)(ws + WS_CNT + (size_t)(CNT_KV + u.pm * 32 + u.pn - 16) * 256), 1u, __ATOMIC_RELAXED, __HIP_MEMORY_SCOPE_AGENT);
            __hip_atomic_fetch_add((unsigned*)(ws + WS_CNT + (size_t)(CNT_QKV + u.pm) * 256), 1u, __ATOMIC_RELAXED, __HIP_MEMORY_SCOPE_AGENT);
        }
    }
};
struct EpiKV0 {
    char* ws; int pm2, pn2;
    __device__ __forceinline__ void operator()(const f32x4 (&acc)[4][2], int wr, int wc, int fr, int fq) const {
        const __amdgpu_buffer_rsrc_t rq = __builtin_amdgcn_make_buffer_rsrc(ws + WS_K, 0, (int)(WS_H - WS_K), 0x00020000);
        const int base = (pn2 >> 5) * (S_LEN * HD * 2) + ((pm2 * 128 + wr * 64 + fr) * HD + (pn2 & 31) * 128 + wc * 32) * 2;
#pragma unroll
        for (int m = 0; m < 4; ++m) { const f32x4 v0 = acc[m][0], v1 = acc[m][1];
            store_pair_sc1(rq, base + m * 16 * HD * 2, (u32x2){pk_bf16(v0[0], v0[1]), pk_bf16(v0[2], v0[3])}, (u32x2){pk_bf16(v1[0], v1[1]), pk_bf16(v1[2], v1[3])}, fq); }
        asm volatile("s_waitcnt vmcnt(0)" ::: "memory");
        if ((threadIdx.x & 63) == 0) {
            __hip_atomic_fetch_add((unsigned*)(ws + WS_CNT + (size_t)((pm2 == 0 ? CNT_KV : CNT_KV0B) + (pn2 >> 1)) * 256), 1u, __ATOMIC_RELAXED, __HIP_MEMORY_SCOPE_AGENT);
            __hip_atomic_fetch_add((unsigned*)(ws + WS_CNT + (size_t)CNT_QKV * 256), 1u, __ATOMIC_RELAXED, __HIP_MEMORY_SCOPE_AGENT);
        }
    }
};
struct EpiMLP1 {
    char* ws; const bf16_t* b1;
    __device__ __forceinline__ void operator()(const f32x4 (&acc)[2][2][4][2], const g8::Unit& u, int wr, int wc, int fr, int fq) const {
        const __amdgpu_buffer_rsrc_t rh = __builtin_amdgcn_make_buffer_rsrc(ws + WS_H, 0, S_LEN * FF * 2, 0x00020000);
        const int row0 = u.pm * 256 + wr * 64 + fr, col0 = u.pn * 256 + wc * 32;
#pragma unroll
        for (int bj = 0; bj < 2; ++bj) {
            const u32x2 ba = *(const u32x2*)(b1 + col0 + bj * 128 + 4 * fq), bb = *(const u32x2*)(b1 + col0 + bj * 128 + 16 + 4 * fq);
#pragma unroll
            for (int ai = 0; ai < 2; ++ai)
#pragma unroll
                for (int m = 0; m < 4; ++m) { const f32x4 v0 = acc[ai][bj][m][0], v1 = acc[ai][bj][m][1];
                    u32x2 w0, w1;
                    w0.x = pk_bf16(gelu_tanh(rbf(rbf(v0[0]) + bf2f(ba.x & 0xffffu))), gelu_tanh(rbf(rbf(v0[1]) + bf2f(ba.x >> 16))));
                    w0.y = pk_bf16(gelu_tanh(rbf(rbf(v0[2]) + bf2f(ba.y & 0xffffu))), gelu_tanh(rbf(rbf(v0[3]) + bf2f(ba.y >> 16))));
                    w1.x = pk_bf16(gelu_tanh(rbf(rbf(v1[0]) + bf2f(bb.x & 0xffffu))), gelu_tanh(rbf(rbf(v1[1]) + bf2f(bb.x >> 16))));
                    w1.y = pk_bf16(gelu_tanh(rbf(rbf(v1[2]) + bf2f(bb.y & 0xffffu))), gelu_tanh(rbf(rbf(v1[3]) + bf2f(bb.y >> 16))));
                    store_pair_sc1(rh, ((row0 + ai * 128 + m * 16) * FF + col0 + bj * 128) * 2, w0, w1, fq);
                    if (m & 1) asm volatile("" ::: "memory"); }
        }
        signal_counter(ws, CNT_H + u.pm);
    }
};
struct EpiMLP2 {
    char* ws; const bf16_t* b2; int row0, col0;
    __device__ __forceinline__ void operator()(const f32x4 (&acc)[4][2], int wr, int wc, int fr, int fq) const {
        const __amdgpu_buffer_rsrc_t rb = __builtin_amdgcn_make_buffer_rsrc(ws + WS_B, 0, S_LEN * DM * 2, 0x00020000);
        const int col = col0 + wc * 32;
        const u32x2 ba = *(const u32x2*)(b2 + col + 4 * fq), bb = *(const u32x2*)(b2 + col + 16 + 4 * fq);
#pragma unroll
        for (int m = 0; m < 4; ++m) { const f32x4 v0 = acc[m][0], v1 = acc[m][1];
            u32x2 w0, w1;
            w0.x = pk_bf16(rbf(v0[0]) + bf2f(ba.x & 0xffffu), rbf(v0[1]) + bf2f(ba.x >> 16)); w0.y = pk_bf16(rbf(v0[2]) + bf2f(ba.y & 0xffffu), rbf(v0[3]) + bf2f(ba.y >> 16));
            w1.x = pk_bf16(rbf(v1[0]) + bf2f(bb.x & 0xffffu), rbf(v1[1]) + bf2f(bb.x >> 16)); w1.y = pk_bf16(rbf(v1[2]) + bf2f(bb.y & 0xffffu), rbf(v1[3]) + bf2f(bb.y >> 16));
            store_pair_sc1(rb, ((row0 + wr * 64 + m * 16 + fr) * DM + col) * 2, w0, w1, fq); }
        signal_counter(ws, CNT_B + (row0 >> 8));
    }
};
__device__ __forceinline__ u32x2 combine4(const f32x4 v, const u32x2 xw, const u32x2 bw) {
    u32x2 w;
    w.x = pk_bf16(rbf(bf2f(xw.x & 0xffffu) + rbf(v[0])) + bf2f(bw.x & 0xffffu), rbf(bf2f(xw.x >> 16) + rbf(v[1])) + bf2f(bw.x >> 16));
    w.y = pk_bf16(rbf(bf2f(xw.y & 0xffffu) + rbf(v[2])) + bf2f(bw.y & 0xffffu), rbf(bf2f(xw.y >> 16) + rbf(v[3])) + bf2f(bw.y >> 16));
    return w;
}
struct EpiOut {
    const bf16_t* x; const bf16_t* bout; bf16_t* out; int row0, col0;
    __device__ __forceinline__ void operator()(const f32x4 (&acc)[4][2], int wr, int wc, int fr, int fq) const {
#pragma unroll
        for (int m = 0; m < 4; ++m)
#pragma unroll
            for (int n = 0; n < 2; ++n) {
                const size_t off = (size_t)(row0 + wr * 64 + m * 16 + fr) * DM + col0 + wc * 32 + n * 16 + 4 * fq;
                *(u32x2*)(out + off) = combine4(acc[m][n], *(const u32x2*)(x + off), *(const u32x2*)(bout + off));
            }
    }
};

constexpr int TAIL_BLK0 = 6;
__global__ void __launch_bounds__(512, 2) k_mega(const bf16_t* __restrict__ x, const bf16_t* __restrict__ Wq, const bf16_t* __restrict__ Wk, const bf16_t* __restrict__ Wv,
                                                 const bf16_t* __restrict__ Wo, const bf16_t* __restrict__ w1, const bf16_t* __restrict__ b1, const bf16_t* __restrict__ w2,
                                                 const bf16_t* __restrict__ b2, char* ws, bf16_t* __restrict__ out) {
    extern __shared__ __attribute__((aligned(16))) unsigned char shm_[];
    LAS unsigned char* lds = (LAS unsigned char*)shm_;
    const int b = blockIdx.x;
    const int role = (b >> 3) & 1, r = (b >> 4) * 8 + (b & 7);
    if (role == 0) { chain_body<4>(lds, r, Wq, Wk, Wv, Wo, ws); return; }
#define WSTAMP(i) do {} while (0)
    WSTAMP(0);
    {
        const int pm2 = r & 1, pn2 = r >> 1;
        EpiKV0 E{ws, pm2, pn2};
        gemm128_w8<4, EpiKV0>(lds, (const bf16_t*)(ws + WS_XN) + (size_t)pm2 * 128 * DM, DM, (pn2 >= 32 ? Wv : Wk) + (pn2 & 31) * 128, HD, DM, E);
    }
    WSTAMP(1);
    {
        SchedKVQ S{(const bf16_t*)(ws + WS_XN), Wq, (long)(Wk - Wq), (long)(Wv - Wq), r};
        EpiQKV E{ws}; g8::gemm_phase<EpiQKV, SchedKVQ>(lds, DM, DM, HD, S, E);
    }
    WSTAMP(2);
    {
        const int L = r >= 96 ? r - 96 : 32 + r;
        SchedOne S; S.valid = (r >= 96) || (r < 80); S.u.pm = L & 7; S.u.pn = L >> 3; S.u.A = (const bf16_t*)(ws + WS_Y) + (size_t)S.u.pm * 256 * DM; S.u.B = w1 + S.u.pn * 256;
        EpiMLP1 E{ws, b1}; g8::gemm_phase<EpiMLP1, SchedOne>(lds, DM, DM, FF, S, E);
    }
    WSTAMP(3);
    {
        const int pm = r >> 3, pn = r & 7;
        wait_counter(ws, CNT_H + (pm >> 1), 14 * 8);
        EpiMLP2 E{ws, b2, pm * 128, pn * 128};
        gemm128_w8<4, EpiMLP2>(lds, (const bf16_t*)(ws + WS_H) + (size_t)pm * 128 * FF, FF, w2 + pn * 128, DM, FF, E);
    }
    WSTAMP(4);
    const int blk = r >> 4, t = r & 15;
    if (blk >= TAIL_BLK0) return;
    {
        if (threadIdx.x < 64) poll_ge(ws, CNT_STEP, (unsigned)(8 * blk + 8));
        wait_counter(ws, CNT_QKV + blk, (blk == 0 ? 128 * 8 : 32 * 8) + 16 * 8);
        WSTAMP(5);
        const int wave = threadIdx.x >> 6;
#pragma unroll 1
        for (int i = 0; i < 2; ++i) { const int item = t * 16 + wave * 2 + i; xattn_item<true>(lds + wave * 48 * XV_PITCH, ws, blk * 8 + (item >> 5), item & 31); }
        signal_counter(ws, CNT_O + blk);
    }
    {
        if (threadIdx.x < 64) poll_ge(ws, CNT_B + blk, 16 * 8);
        wait_counter(ws, CNT_O + blk, 16 * 8);
        WSTAMP(6);
        const int pm = blk * 2 + (t >> 3), pn = t & 7;
        EpiOut E{x, (const bf16_t*)(ws + WS_B), out, pm * 128, pn * 128};
        gemm128_w8<4, EpiOut>(lds, (const bf16_t*)(ws + WS_O) + (size_t)pm * 128 * HD, HD, Wo + pn * 128, DM, HD, E);
    }
    WSTAMP(7);
}

__global__ void __launch_bounds__(256) k_tail_attn(char* ws) {
    __shared__ __attribute__((aligned(16))) unsigned char shm_[4 * 48 * XV_PITCH];
    const int wave = threadIdx.x >> 6, item = blockIdx.x * 4 + wave;
    xattn_item<false>((LAS unsigned char*)shm_ + wave * 48 * XV_PITCH, ws, TAIL_BLK0 * 8 + (item >> 5), item & 31);
}
__global__ void __launch_bounds__(256) k_tail_gemm(const bf16_t* __restrict__ x, const bf16_t* __restrict__ Wo, const char* __restrict__ ws, bf16_t* __restrict__ out) {
    extern __shared__ __attribute__((aligned(16))) unsigned char shm_[];
    const int tid = threadIdx.x, wave = __builtin_amdgcn_readfirstlane(tid >> 6), lane = tid & 63, fr = lane & 15, fq = lane >> 4;
    LAS unsigned char* lds = (LAS unsigned char*)shm_ + wave * 16384;
    const int pn = blockIdx.x & 31, pm = blockIdx.x >> 5, row0 = TAIL_BLK0 * 256 + pm * 32, col0 = pn * 32;
    const char* cA = (const char*)((const bf16_t*)(ws + WS_O) + (size_t)row0 * HD + wave * 1024);
    const char* cB = (const char*)(Wo + (size_t)wave * 1024 * DM + col0);
    unsigned voffA[4], voffB[4];
#pragma unroll
    for (int i = 0; i < 4; ++i) { const int c = i * 64 + lane, row = c >> 3, ch = (c & 7) ^ (row & 7); voffA[i] = (unsigned)(row * HD + ch * 8) * 2u;
        const int k = c >> 2, cb = c & 3; voffB[i] = (unsigned)(k * DM + cb * 8) * 2u; }
#define STG(buf, t) do { _Pragma("unroll") for (int _i = 0; _i < 4; ++_i) { \
        __builtin_amdgcn_global_load_lds((const unsigned*)(cA + (size_t)(t) * 128 + voffA[_i]), (LAS unsigned*)(lds + (buf) * 8192 + _i * 1024), 16, 0, 0); \
        __builtin_amdgcn_global_load_lds((const unsigned*)(cB + (size_t)(t) * 64 * DM * 2 + voffB[_i]), (LAS unsigned*)(lds + (buf) * 8192 + 4096 + _i * 1024), 16, 0, 0); } } while (0)
    f32x4 acc[2][2];
#pragma unroll
    for (int m = 0; m < 2; ++m) { acc[m][0] = (f32x4){0.f, 0.f, 0.f, 0.f}; acc[m][1] = (f32x4){0.f, 0.f, 0.f, 0.f}; }
    STG(0, 0);
    int cur = 0;
    for (int t = 0; t < 16; ++t) {
        if (t + 1 < 16) { STG(cur ^ 1, t + 1); asm volatile("s_waitcnt vmcnt(8)" ::: "memory"); }
        else asm volatile("s_waitcnt vmcnt(0)" ::: "memory");
        LAS unsigned char* sa = lds + cur * 8192; LAS unsigned char* sb = sa + 4096;
        bf16x8 At[2][2], Bt[2][2];
#pragma unroll
        for (int k = 0; k < 2; ++k) {
#pragma unroll
            for (int m = 0; m < 2; ++m) { const int row = m * 16 + fr; At[m][k] = *(const LAS bf16x8*)(sa + row * 128 + (((k * 4 + fq) ^ (row & 7)) * 16)); }
#pragma unroll
            for (int n = 0; n < 2; ++n) {
                const int off = (k * 32 + 8 * fq + (fr >> 2)) * 64 + n * 32 + (lane & 3) * 8;
                const bf16x4 a0 = __builtin_amdgcn_ds_read_tr16_b64_v4i16((LAS bf16x4*)(sb + off)), a1 = __builtin_amdgcn_ds_read_tr16_b64_v4i16((LAS bf16x4*)(sb + off + 256));
                Bt[n][k] = __builtin_shufflevector(a0, a1, 0, 1, 2, 3, 4, 5, 6, 7); }
        }
#pragma unroll
        for (int k = 0; k < 2; ++k)
#pragma unroll
            for (int m = 0; m < 2; ++m)
#pragma unroll
                for (int n = 0; n < 2; ++n) acc[m][n] = __builtin_amdgcn_mfma_f32_16x16x32_bf16(Bt[n][k], At[m][k], acc[m][n], 0, 0, 0);
        asm volatile("s_waitcnt lgkmcnt(0)" ::: "memory");
        cur ^= 1;
    }
#undef STG
    __syncthreads();
    LAS f32x4* red = (LAS f32x4*)shm_;
#pragma unroll
    for (int m = 0; m < 2; ++m)
#pragma unroll
        for (int n = 0; n < 2; ++n) red[((wave * 2 + m) * 2 + n) * 64 + lane] = acc[m][n];
    __syncthreads();
    {
        const int m = wave >> 1, n = wave & 1;
        const f32x4 v = (red[((0 * 2 + m) * 2 + n) * 64 + lane] + red[((1 * 2 + m) * 2 + n) * 64 + lane]) + (red[((2 * 2 + m) * 2 + n) * 64 + lane] + red[((3 * 2 + m) * 2 + n) * 64 + lane]);
        const size_t off = (size_t)(row0 + m * 16 + fr) * DM + col0 + n * 16 + 4 * fq;
        *(u32x2*)(out + off) = combine4(v, *(const u32x2*)(x + off), *(const u32x2*)((const bf16_t*)(ws + WS_B) + off));
    }
}

extern "C" void kernel_launch(void* const* d_in, const int* in_sizes, int n_in, void* d_out, int out_size, void* d_ws, size_t ws_size, hipStream_t stream) {
    const bf16_t* x = (const bf16_t*)d_in[0];
    const bf16_t* mem0 = (const bf16_t*)d_in[1];
    const bf16_t* Wq = (const bf16_t*)d_in[2];
    const bf16_t* Wk = (const bf16_t*)d_in[3];
    const bf16_t* Wv = (const bf16_t*)d_in[4];
    const bf16_t* Wo = (const bf16_t*)d_in[5];
    const bf16_t* w1 = (const bf16_t*)d_in[8];
    const bf16_t* b1 = (const bf16_t*)d_in[9];
    const bf16_t* w2 = (const bf16_t*)d_in[10];
    const bf16_t* b2 = (const bf16_t*)d_in[11];
    char* ws = (char*)d_ws;
    bf16_t* out = (bf16_t*)d_out;
    (void)hipFuncSetAttribute((const void*)k_mega, hipFuncAttributeMaxDynamicSharedMemorySize, CH_LDS);
    (void)hipFuncSetAttribute((const void*)k_tail_gemm, hipFuncAttributeMaxDynamicSharedMemorySize, 4 * 16384);
    k_prep<<<S_LEN / 4, 256, 0, stream>>>(x, mem0, ws);
    k_mega<<<256, 512, CH_LDS, stream>>>(x, Wq, Wk, Wv, Wo, w1, b1, w2, b2, ws, out);
    k_tail_attn<<<64 * (8 - TAIL_BLK0), 256, 0, stream>>>(ws);
    k_tail_gemm<<<256 * (8 - TAIL_BLK0), 256, 4 * 16384, stream>>>(x, Wo, ws, out);
}
```

```cpp
#include <hip/hip_runtime.h>
#include <hip/hip_bf16.h>

#define LAS __attribute__((address_space(3)))
typedef unsigned short bf16_t;
typedef short bf16x8 __attribute__((ext_vector_type(8)));
typedef short bf16x4 __attribute__((ext_vector_type(4)));
typedef float f32x4 __attribute__((ext_vector_type(4)));
typedef float f32x2 __attribute__((ext_vector_type(2)));
typedef unsigned u32x4 __attribute__((ext_vector_type(4)));
typedef unsigned u32x2 __attribute__((ext_vector_type(2)));

constexpr int S_LEN = 2048, DM = 1024, NH = 32, DH = 128, HD = NH * DH, MM = 8, SEG = 32, NSEG = S_LEN / SEG, FF = 3584;
constexpr int AUX_SC1 = 16;
constexpr unsigned SENT = 0xFFFFFFFFu;

__device__ __forceinline__ float bf2f(unsigned b) { return __builtin_bit_cast(float, b << 16); }
typedef __bf16 bf16x2_t __attribute__((ext_vector_type(2)));
__device__ __forceinline__ unsigned pk_bf16(float lo, float hi) { const f32x2 v = {lo, hi}; return __builtin_bit_cast(unsigned, __builtin_convertvector(v, bf16x2_t)); }
__device__ __forceinline__ float rbf(float x) { return bf2f(pk_bf16(x, x) & 0xffffu); }
__device__ __forceinline__ unsigned desent(unsigned w) { return w == SENT ? 0x7FC07FC0u : w; }
__device__ __forceinline__ unsigned umax2(unsigned a, unsigned b) { return a > b ? a : b; }
__device__ __forceinline__ unsigned umax4(u32x4 v) { return umax2(umax2(v.x, v.y), umax2(v.z, v.w)); }

constexpr size_t WS_XN = 0;
constexpr size_t WS_Y = WS_XN + (size_t)S_LEN * DM * 2;
constexpr size_t WS_Q = WS_Y + (size_t)S_LEN * DM * 2;
constexpr size_t WS_K = WS_Q + (size_t)S_LEN * HD * 2;
constexpr size_t WS_V = WS_K + (size_t)S_LEN * HD * 2;
constexpr size_t WS_H = WS_V + (size_t)S_LEN * HD * 2;
constexpr size_t WS_O = WS_H + (size_t)S_LEN * FF * 2;
constexpr size_t WS_B = WS_O + (size_t)S_LEN * HD * 2;
constexpr size_t WS_SENT0 = WS_B + (size_t)S_LEN * DM * 2;
constexpr size_t MEM_B = (size_t)MM * DM * 2;
constexpr size_t WS_MEM = WS_SENT0;
constexpr size_t QKVM_B = (size_t)NH * 3 * MM * DH * 2;
constexpr size_t WS_QKVM = WS_MEM + (size_t)(NSEG + 1) * MEM_B;
constexpr size_t OM_B = (size_t)MM * HD * 2;
constexpr size_t WS_OM = WS_QKVM + (size_t)NSEG * QKVM_B;
constexpr size_t WS_SENT1 = WS_OM + (size_t)NSEG * OM_B;
constexpr size_t WS_CNT = WS_SENT1;
constexpr int CNT_H = 0;
constexpr int CNT_B = 8;
constexpr int CNT_O = 16;
constexpr int CNT_QKV = 24;
constexpr int CNT_FILL = 33;
constexpr int CNT_STEP = 32;
constexpr int CNT_KV = 40;
constexpr int CNT_KV0B = 40 + 256;
constexpr int CNT_N = 40 + 256 + 32;
constexpr size_t WS_TMO = WS_CNT + (size_t)CNT_N * 256;
constexpr size_t WS_END = WS_TMO + 256;

__device__ __forceinline__ float wave_sum(float v) {
#pragma unroll
    for (int o = 32; o > 0; o >>= 1) v += __shfl_xor(v, o);
    return v;
}
__device__ __forceinline__ void ln_row(float (&v)[16]) {
    float s = 0.f;
#pragma unroll
    for (int i = 0; i < 16; ++i) s += v[i];
    const float mean = wave_sum(s) * (1.0f / DM);
    float q = 0.f;
#pragma unroll
    for (int i = 0; i < 16; ++i) { const float d = v[i] - mean; q += d * d; }
    const float var = rbf(wave_sum(q) * (1.0f / DM));
    const float mb = rbf(mean);
    const float den = rbf(sqrtf(rbf(var + 1e-5f)));
#pragma unroll
    for (int i = 0; i < 16; ++i) v[i] = rbf(rbf(v[i] - mb) / den);
}
__global__ void __launch_bounds__(256) k_prep(const bf16_t* __restrict__ x, const bf16_t* __restrict__ mem0, char* __restrict__ ws) {
    const int wave = threadIdx.x >> 6, lane = threadIdx.x & 63, row = blockIdx.x * 4 + wave;
    const u32x4* src = (const u32x4*)(x + (size_t)row * DM);
    u32x4 a = src[lane], b = src[lane + 64];
    float v[16];
    const unsigned w[8] = {a.x, a.y, a.z, a.w, b.x, b.y, b.z, b.w};
#pragma unroll
    for (int i = 0; i < 8; ++i) { v[2 * i] = bf2f(w[i] & 0xffffu); v[2 * i + 1] = bf2f(w[i] >> 16); }
    ln_row(v);
    u32x4 o0, o1;
    o0.x = pk_bf16(v[0], v[1]); o0.y = pk_bf16(v[2], v[3]); o0.z = pk_bf16(v[4], v[5]); o0.w = pk_bf16(v[6], v[7]);
    o1.x = pk_bf16(v[8], v[9]); o1.y = pk_bf16(v[10], v[11]); o1.z = pk_bf16(v[12], v[13]); o1.w = pk_bf16(v[14], v[15]);
    u32x4* dx = (u32x4*)(ws + WS_XN + (size_t)row * DM * 2);
    dx[lane] = o0; dx[lane + 64] = o1;
    ln_row(v);
    o0.x = pk_bf16(v[0], v[1]); o0.y = pk_bf16(v[2], v[3]); o0.z = pk_bf16(v[4], v[5]); o0.w = pk_bf16(v[6], v[7]);
    o1.x = pk_bf16(v[8], v[9]); o1.y = pk_bf16(v[10], v[11]); o1.z = pk_bf16(v[12], v[13]); o1.w = pk_bf16(v[14], v[15]);
    u32x4* dy = (u32x4*)(ws + WS_Y + (size_t)row * DM * 2);
    dy[lane] = o0; dy[lane + 64] = o1;
    {
        const size_t n16 = (WS_SENT1 - WS_SENT0 - MEM_B) / 16;
        u32x4* dst = (u32x4*)(ws + WS_SENT0 + MEM_B);
        const u32x4 f = (u32x4){SENT, SENT, SENT, SENT};
        for (size_t i = (size_t)blockIdx.x * 256 + threadIdx.x; i < n16; i += (size_t)gridDim.x * 256) dst[i] = f;
    }
    if (blockIdx.x >= 8 && blockIdx.x < 8 + (CNT_N * 256 / 16 + 255) / 256) {
        const int i = (blockIdx.x - 8) * 256 + threadIdx.x; if (i < CNT_N * 256 / 16) ((u32x4*)(ws + WS_CNT))[i] = (u32x4){0u, 0u, 0u, 0u}; }
    if (blockIdx.x < 4) {
        const int c = blockIdx.x * 256 + threadIdx.x, r = c & 7, kg = c >> 3;
        u32x4 m = *(const u32x4*)(mem0 + (size_t)r * DM + kg * 8);
        m.x = desent(m.x); m.y = desent(m.y); m.z = desent(m.z); m.w = desent(m.w);
        *(u32x4*)(ws + WS_MEM + (size_t)c * 16) = m;
    }
}

namespace g8 {
constexpr int BM = 256, BK = 64, HALF = 128, HTB = HALF * BK * 2, STAGE_BYTES = 8 * HTB;
__host__ __device__ __forceinline__ int lds_byte(int r, int c) { const int st = (r >> 4) * 2 + (c >> 5), rr = r & 15, cc = c & 31, ob = rr * 64 + cc * 2; return st * 1024 + (ob ^ (((ob >> 9) & 1) << 5)); }
__host__ __device__ __forceinline__ void stage_rc(int b, int& R, int& C) { const int st = b / 1024, sb = b % 1024, swz = sb ^ (((sb >> 9) & 1) << 5); R = (st >> 1) * 16 + swz / 64; C = (st & 1) * 32 + (swz % 64) / 2; }
__host__ __device__ __forceinline__ int bsw(int row) { return (row & 3) | (((row >> 3) & 1) << 2); }
struct Unit { const bf16_t* A; const bf16_t* B; int pm, pn; };

template <class Epi, class Sched>
__device__ __forceinline__ void gemm_phase(LAS unsigned char* lds, const int K, const int lda, const int ldb, const Sched& S, const Epi& E) {
    int tid = threadIdx.x; asm volatile("" : "+v"(tid));
    const int wid = __builtin_amdgcn_readfirstlane(tid >> 6), lane = tid & 63, wr = wid >> 2, wc = wid & 3, fr = lane & 15, fq = lane >> 4;
    const int nt = K / BK;
    unsigned voffA[2], voffB[2];
#pragma unroll
    for (int i = 0; i < 2; ++i) { int R, C; stage_rc(tid * 16 + i * 8192, R, C); voffA[i] = (unsigned)(R * lda + C) * 2u;
        const int b = tid * 16 + i * 8192, row = b >> 8, ch = (b & 255) >> 4, seg = (ch >> 1) ^ bsw(row);
        voffB[i] = (unsigned)(row * ldb + seg * 16 + (ch & 1) * 8) * 2u; }
    const size_t kstepA = (size_t)(BK * 2), kstepB = (size_t)BK * ldb * 2;
    const size_t hstepA = (size_t)HALF * lda * 2, hstepB = (size_t)HALF * 2;
    const unsigned ldsw = (unsigned)wid * 1024u;
    const int aoff = lds_byte(wr * 64 + fr, fq * 8);
    const int q4 = (lane & 15) >> 2, p4 = lane & 3, sB = q4 | ((fq & 1) << 2);
    const int boff0 = (8 * fq + q4) * 256 + (((wc * 2 + 0) ^ sB) * 32) + 8 * p4;
    const int boff1 = (8 * fq + q4) * 256 + (((wc * 2 + 1) ^ sB) * 32) + 8 * p4;
#define G8_SA(b, h) (((b) * 2 + (h)) * HTB)
#define G8_SB(b, h) ((4 + (b) * 2 + (h)) * HTB)
#define G8_STAGE(bufoff, gbase, voff) do { _Pragma("unroll") for (int _i = 0; _i < 2; ++_i) \
        __builtin_amdgcn_global_load_lds((const unsigned*)((const char*)(gbase) + (voff)[_i]), (LAS unsigned*)(lds + (bufoff) + ldsw + _i * 8192), 16, 0, 0); } while (0)
#define G8_LDA(dst, b, h) do { _Pragma("unroll") for (int m = 0; m < 4; ++m) _Pragma("unroll") for (int k = 0; k < 2; ++k) dst[m][k] = *(const LAS bf16x8*)(lds + G8_SA(b, h) + aoff + m * 2048 + k * 1024); } while (0)
#define G8_TR(off) __builtin_amdgcn_ds_read_tr16_b64_v4i16((LAS bf16x4*)(lds + (off)))
#define G8_LDB(dst, b, h) do { _Pragma("unroll") for (int k = 0; k < 2; ++k) { \
        bf16x4 a0 = G8_TR(G8_SB(b, h) + boff0 + k * 8192), a1 = G8_TR(G8_SB(b, h) + boff0 + k * 8192 + 1024); \
        bf16x4 c0 = G8_TR(G8_SB(b, h) + boff1 + k * 8192), c1 = G8_TR(G8_SB(b, h) + boff1 + k * 8192 + 1024); \
        dst[0][k] = __builtin_shufflevector(a0, a1, 0, 1, 2, 3, 4, 5, 6, 7); dst[1][k] = __builtin_shufflevector(c0, c1, 0, 1, 2, 3, 4, 5, 6, 7); } } while (0)
#define G8_MMA(ai, bj, At, Bt) do { __builtin_amdgcn_s_setprio(1); _Pragma("unroll") for (int m = 0; m < 4; ++m) _Pragma("unroll") for (int n = 0; n < 2; ++n) _Pragma("unroll") for (int k = 0; k < 2; ++k) \
        acc[ai][bj][m][n] = __builtin_amdgcn_mfma_f32_16x16x32_bf16(Bt[n][k], At[m][k], acc[ai][bj][m][n], 0, 0, 0); __builtin_amdgcn_s_setprio(0); } while (0)
#define G8_WAIT_V(n) asm volatile("s_waitcnt vmcnt(" #n ")" ::: "memory")
#define G8_WAIT_L(n) asm volatile("s_waitcnt lgkmcnt(" #n ")" ::: "memory")
#define G8_BAR __builtin_amdgcn_s_barrier()
#define G8_SCHED __builtin_amdgcn_sched_barrier(0)
    Unit cur, nxt; int ui = 0;
    if (!S.next(0, cur)) return;
    f32x4 acc[2][2][4][2];
#pragma unroll
    for (int a = 0; a < 2; ++a)
#pragma unroll
        for (int b = 0; b < 2; ++b)
#pragma unroll
            for (int m = 0; m < 4; ++m)
#pragma unroll
                for (int n = 0; n < 2; ++n) acc[a][b][m][n] = (f32x4){0.f, 0.f, 0.f, 0.f};
    bf16x8 At[4][2], B0[2][2], B1[2][2];
    const char* cA = (const char*)cur.A; const char* cB = (const char*)cur.B;
    G8_STAGE(G8_SB(0, 0), cB, voffB); G8_STAGE(G8_SA(0, 0), cA, voffA); G8_STAGE(G8_SB(0, 1), cB + hstepB, voffB); G8_STAGE(G8_SA(0, 1), cA + hstepA, voffA);
    if (wr == 1) G8_BAR;
    G8_WAIT_V(4); G8_BAR;
    G8_STAGE(G8_SB(1, 0), cB + kstepB, voffB); G8_STAGE(G8_SA(1, 0), cA + kstepA, voffA); G8_STAGE(G8_SB(1, 1), cB + hstepB + kstepB, voffB);
    G8_WAIT_V(6); G8_BAR;
    for (;;) {
        const bool has_next = S.next(ui + 1, nxt);
        const char* nA = has_next ? (const char*)nxt.A : cA; const char* nB = has_next ? (const char*)nxt.B : cB;
        for (int t = 0; t < nt; t += 2) {
            const bool last = (t == nt - 2);
            const char* a1 = cA + (size_t)(t + 1) * kstepA;
            const char* a2 = last ? nA : cA + (size_t)(t + 2) * kstepA; const char* b2 = last ? nB : cB + (size_t)(t + 2) * kstepB;
            const char* a3 = a2 + kstepA; const char* b3 = b2 + kstepB;
            G8_LDB(B0, 0, 0); G8_SCHED; G8_LDA(At, 0, 0); G8_STAGE(G8_SA(1, 1), a1 + hstepA, voffA);
            G8_WAIT_L(8); G8_BAR; G8_WAIT_L(0); G8_MMA(0, 0, At, B0); G8_BAR; G8_SCHED;
            G8_LDB(B1, 0, 1); G8_STAGE(G8_SB(0, 0), b2, voffB);
            G8_BAR; G8_WAIT_L(0); G8_MMA(0, 1, At, B1); G8_BAR;
            G8_LDA(At, 0, 1); G8_STAGE(G8_SA(0, 0), a2, voffA);
            G8_BAR; G8_WAIT_L(0); G8_MMA(1, 0, At, B0); G8_BAR; G8_SCHED;
            G8_STAGE(G8_SB(0, 1), b2 + hstepB, voffB);
            G8_WAIT_V(6); G8_BAR; G8_MMA(1, 1, At, B1); G8_BAR;
            G8_LDB(B0, 1, 0); G8_SCHED; G8_LDA(At, 1, 0); G8_STAGE(G8_SA(0, 1), a2 + hstepA, voffA);
            G8_WAIT_L(8); G8_BAR; G8_WAIT_L(0); G8_MMA(0, 0, At, B0); G8_BAR; G8_SCHED;
            G8_LDB(B1, 1, 1); G8_STAGE(G8_SB(1, 0), b3, voffB);
            G8_BAR; G8_WAIT_L(0); G8_MMA(0, 1, At, B1); G8_BAR;
            G8_LDA(At, 1, 1); G8_STAGE(G8_SA(1, 0), a3, voffA);
            G8_BAR; G8_WAIT_L(0); G8_MMA(1, 0, At, B0); G8_BAR; G8_SCHED;
            G8_STAGE(G8_SB(1, 1), b3 + hstepB, voffB);
            G8_WAIT_V(6); G8_BAR; G8_MMA(1, 1, At, B1); G8_BAR;
        }
        E(acc, cur, wr, wc, fr, fq);
        if (!has_next) break;
#pragma unroll
        for (int a = 0; a < 2; ++a)
#pragma unroll
            for (int b = 0; b < 2; ++b)
#pragma unroll
                for (int m = 0; m < 4; ++m)
#pragma unroll
                    for (int n = 0; n < 2; ++n) acc[a][b][m][n] = (f32x4){0.f, 0.f, 0.f, 0.f};
        cur = nxt; cA = nA; cB = nB; ++ui;
    }
    G8_WAIT_V(0);
    if (wr == 0) G8_BAR;
    G8_BAR;
#undef G8_SA
#undef G8_SB
#undef G8_STAGE
#undef G8_LDA
#undef G8_LDB
#undef G8_TR
#undef G8_MMA
#undef G8_WAIT_V
#undef G8_WAIT_L
#undef G8_BAR
#undef G8_SCHED
}
}

__device__ __forceinline__ float gelu_tanh(float x) {
    const float u = 0.7978845608028654f * (x + 0.044715f * x * x * x);
    const float th = 1.0f - 2.0f * __builtin_amdgcn_rcpf(__expf(2.0f * u) + 1.0f);
    return 0.5f * x * (1.0f + th);
}

__device__ __forceinline__ void wait_counter(const char* ws, int idx, unsigned need) {
    if (threadIdx.x < 64) {
        const unsigned* p = (const unsigned*)(ws + WS_CNT + (size_t)idx * 256);
        for (unsigned spins = 0; (unsigned)__builtin_amdgcn_readfirstlane(__hip_atomic_load(p, __ATOMIC_RELAXED, __HIP_MEMORY_SCOPE_AGENT)) < need;) {
            __builtin_amdgcn_s_sleep(8);
            if (++spins > 400000u) break;
        }
        __builtin_amdgcn_fence(__ATOMIC_ACQUIRE, "agent");
        asm volatile("s_waitcnt vmcnt(0)" ::: "memory");
    }
    asm volatile("" ::: "memory"); __builtin_amdgcn_s_barrier(); asm volatile("" ::: "memory");
}
__device__ __forceinline__ void signal_counter(char* ws, int idx) {
    asm volatile("s_waitcnt vmcnt(0)" ::: "memory");
    if ((threadIdx.x & 63) == 0) __hip_atomic_fetch_add((unsigned*)(ws + WS_CNT + (size_t)idx * 256), 1u, __ATOMIC_RELAXED, __HIP_MEMORY_SCOPE_AGENT);
}

__device__ __forceinline__ unsigned poll_ge(const char* ws, int idx, unsigned need) {
    const unsigned* p = (const unsigned*)(ws + WS_CNT + (size_t)idx * 256);
    unsigned v = 0;
    for (unsigned spins = 0; (v = (unsigned)__builtin_amdgcn_readfirstlane(__hip_atomic_load(p, __ATOMIC_RELAXED, __HIP_MEMORY_SCOPE_AGENT))) < need;) {
        __builtin_amdgcn_s_sleep(8);
        if (++spins > 400000u) break;
    }
    asm volatile("" ::: "memory");
    return v;
}

constexpr unsigned SPIN_CAP = 200000u;
constexpr int KS_PITCH = 272;
constexpr int CH_LDS = 160 * 1024;

__device__ __forceinline__ float xmax16_32(float v) {
    auto a = __builtin_amdgcn_permlane16_swap(__builtin_bit_cast(unsigned, v), __builtin_bit_cast(unsigned, v), false, false);
    v = fmaxf(__builtin_bit_cast(float, (unsigned)a[0]), __builtin_bit_cast(float, (unsigned)a[1]));
    auto b = __builtin_amdgcn_permlane32_swap(__builtin_bit_cast(unsigned, v), __builtin_bit_cast(unsigned, v), false, false);
    return fmaxf(__builtin_bit_cast(float, (unsigned)b[0]), __builtin_bit_cast(float, (unsigned)b[1]));
}
__device__ __forceinline__ float xsum16_32(float v) {
    auto a = __builtin_amdgcn_permlane16_swap(__builtin_bit_cast(unsigned, v), __builtin_bit_cast(unsigned, v), false, false);
    v = __builtin_bit_cast(float, (unsigned)a[0]) + __builtin_bit_cast(float, (unsigned)a[1]);
    auto b = __builtin_amdgcn_permlane32_swap(__builtin_bit_cast(unsigned, v), __builtin_bit_cast(unsigned, v), false, false);
    return __builtin_bit_cast(float, (unsigned)b[0]) + __builtin_bit_cast(float, (unsigned)b[1]);
}

__device__ __forceinline__ u32x4 quad_gather(unsigned v) {
    u32x4 r;
    r.x = (unsigned)__builtin_amdgcn_update_dpp(0, (int)v, 0x00, 0xf, 0xf, true); r.y = (unsigned)__builtin_amdgcn_update_dpp(0, (int)v, 0x55, 0xf, 0xf, true);
    r.z = (unsigned)__builtin_amdgcn_update_dpp(0, (int)v, 0xAA, 0xf, 0xf, true); r.w = (unsigned)__builtin_amdgcn_update_dpp(0, (int)v, 0xFF, 0xf, 0xf, true);
    return r;
}
#define STAMP(i) do {} while (0)
#define PCOUNT(sh) do {} while (0)
#define CH_DELAY1 12
#define CH_DELAY2 8
#define CH_DELAY3 8

template <int CPH>
__device__ __forceinline__ void chain_body(LAS unsigned char* lds, const int b, const bf16_t* __restrict__ Wq, const bf16_t* __restrict__ Wk, const bf16_t* __restrict__ Wv,
                                           const bf16_t* __restrict__ Wo, char* ws) {
    constexpr int ES = 128 / CPH, NE = ES / 16, NT = 3 * NE, RG = CPH / 2, RPG = 8 / RG;
    constexpr int VP = ES * 2;
    constexpr int L_RED = 0;
    constexpr int L_RED2 = L_RED + 8 * NT * 512;
    constexpr int L_KS = L_RED2 + 8 * RPG * 64;
    constexpr int L_VS = L_KS + 2 * 32 * KS_PITCH;
    constexpr int L_OS = L_VS + 2 * 48 * VP;
    constexpr bool WOL = (CPH == 4);
    constexpr int L_WO = L_OS + 8 * 1024 * RPG;
    static_assert(L_WO + (WOL ? 65536 : 0) <= 156 * 1024, "LDS");
    const int tid = threadIdx.x, wave = __builtin_amdgcn_readfirstlane(tid >> 6), lane = tid & 63, fr = lane & 15, fq = lane >> 4;
    const int xc = b & 7, yc = b >> 3;
    const int h = xc + 8 * (yc / CPH), j = yc % CPH, g = yc % RG, c = xc * 8 + yc / RG;
    const __amdgpu_buffer_rsrc_t rs = __builtin_amdgcn_make_buffer_rsrc(ws + WS_SENT0, 0, (int)(WS_SENT1 - WS_SENT0), 0x00020000);
    constexpr int O_MEM = (int)(WS_MEM - WS_SENT0), O_QKVM = (int)(WS_QKVM - WS_SENT0), O_OM = (int)(WS_OM - WS_SENT0);
    const __amdgpu_buffer_rsrc_t rkv = __builtin_amdgcn_make_buffer_rsrc(ws + WS_K, 0, (int)(WS_H - WS_K), 0x00020000);
    constexpr int O_V = (int)(WS_V - WS_K);
    bool dead = false;

    bf16x8 wq[NT][4], wo[16];
    {
        LAS unsigned char* stg = lds + wave * 8192;
        constexpr int CPR = ES / 8;
        static_assert(128 * CPR / 64 == 8, "8 chunks per lane and pass");
        u32x4 t[5][8];
        auto issue = [&](int ps) {
            if (ps < 3) { const bf16_t* W = (ps == 0 ? Wq : (ps == 1 ? Wk : Wv)) + (size_t)wave * 128 * HD + h * DH + j * ES;
#pragma unroll
                for (int i = 0; i < 8; ++i) { const int ci = i * 64 + lane; t[ps][i] = *(const u32x4*)(W + (size_t)(ci / CPR) * HD + (ci % CPR) * 8); } }
            else { const bf16_t* W = Wo + (size_t)(wave * 512 + (ps - 3) * 256) * DM + c * 16;
#pragma unroll
                for (int i = 0; i < 8; ++i) { const int ci = i * 64 + lane; t[ps][i] = *(const u32x4*)(W + (size_t)(ci >> 1) * DM + (ci & 1) * 8); } }
        };
        issue(0); issue(1); issue(2);
        asm volatile("" ::: "memory");
#pragma unroll
        for (int ps = 0; ps < 5; ++ps) {
#pragma unroll
            for (int i = 0; i < 8; ++i) *(LAS u32x4*)(stg + (i * 64 + lane) * 16) = t[ps][i];
            asm volatile("s_waitcnt lgkmcnt(0)" ::: "memory");
            if (ps + 3 < 5) issue(ps + 3);
            asm volatile("" ::: "memory");
            if (ps < 3) {
#pragma unroll
                for (int e = 0; e < NE; ++e)
#pragma unroll
                    for (int i = 0; i < 4; ++i) {
                        const int off = (i * 32 + 8 * fq + (fr >> 2)) * (ES * 2) + e * 32 + (lane & 3) * 8;
                        const bf16x4 a0 = __builtin_amdgcn_ds_read_tr16_b64_v4i16((LAS bf16x4*)(stg + off)), a1 = __builtin_amdgcn_ds_read_tr16_b64_v4i16((LAS bf16x4*)(stg + off + 4 * ES * 2));
                        wq[ps * NE + e][i] = __builtin_shufflevector(a0, a1, 0, 1, 2, 3, 4, 5, 6, 7);
                    }
            } else {
#pragma unroll
                for (int i = 0; i < 8; ++i) {
                    const int off = (i * 32 + 8 * fq + (fr >> 2)) * 32 + (lane & 3) * 8;
                    const bf16x4 a0 = __builtin_amdgcn_ds_read_tr16_b64_v4i16((LAS bf16x4*)(stg + off)), a1 = __builtin_amdgcn_ds_read_tr16_b64_v4i16((LAS bf16x4*)(stg + off + 128));
                    wo[(ps - 3) * 8 + i] = __builtin_shufflevector(a0, a1, 0, 1, 2, 3, 4, 5, 6, 7);
                }
            }
            asm volatile("s_waitcnt lgkmcnt(0)" ::: "memory");
        }
        if (WOL) {
#pragma unroll
            for (int i = 8; i < 16; ++i) *(LAS bf16x8*)(lds + L_WO + (wave * 8 + i - 8) * 1024 + lane * 16) = wo[i];
        }
    }
    __syncthreads();

    if (tid < 2 * 8 * VP / 16) { const int bf = tid / (8 * VP / 16), r = tid % (8 * VP / 16); *(LAS u32x4*)(lds + L_VS + bf * 48 * VP + 40 * VP + r * 16) = (u32x4){0u, 0u, 0u, 0u}; }
    {
        poll_ge(ws, CNT_KV + (h >> 1), 16u); poll_ge(ws, CNT_KV + 16 + (h >> 1), 16u);
        const int r = tid >> 4, ch = tid & 15;
        *(LAS u32x4*)(lds + L_KS + r * KS_PITCH + ch * 16) = __builtin_amdgcn_raw_buffer_load_b128(rkv, (r * HD + h * DH + ch * 8) * 2, 0, AUX_SC1);
        if (tid < 32 * ES / 8) { const int r2 = tid / (ES / 8), c2 = tid % (ES / 8);
            *(LAS u32x4*)(lds + L_VS + (8 + r2) * VP + c2 * 16) = __builtin_amdgcn_raw_buffer_load_b128(rkv, O_V + (r2 * HD + h * DH + j * ES + c2 * 8) * 2, 0, AUX_SC1); }
    }
    __syncthreads();

    for (int s = 0; s < NSEG; ++s) {
        const int kb = s & 1;
        unsigned long long pcnt = 0; (void)pcnt;
        STAMP(0);
        {
            if (s > 0) __builtin_amdgcn_s_sleep(CH_DELAY1);
            u32x4 a[4];
            const int base = O_MEM + s * (int)MEM_B + wave * 2048 + fq * 128 + (lane & 7) * 16;
            for (unsigned spins = 0;;) {
                asm volatile("" ::: "memory");
#pragma unroll
                for (int i = 0; i < 4; ++i) a[i] = __builtin_amdgcn_raw_buffer_load_b128(rs, base + i * 512, 0, AUX_SC1);
                unsigned mxs = 0;
#pragma unroll
                for (int i = 0; i < 4; ++i) mxs = umax2(mxs, umax4(a[i]));
                if (!__any(mxs == SENT) || dead) break;
                PCOUNT(0);
                if (++spins > SPIN_CAP) { dead = true; if (lane == 0) *(volatile unsigned*)(ws + WS_TMO) = 1u; break; }
            }
            STAMP(1);
            if (b == 0 && tid == 448 && (s & 7) == 0) __hip_atomic_store((unsigned*)(ws + WS_CNT + (size_t)CNT_STEP * 256), (unsigned)s, __ATOMIC_RELAXED, __HIP_MEMORY_SCOPE_AGENT);
            f32x4 acc[NT];
#pragma unroll
            for (int t = 0; t < NT; ++t) acc[t] = (f32x4){0.f, 0.f, 0.f, 0.f};
#pragma unroll
            for (int i = 0; i < 4; ++i)
#pragma unroll
                for (int t = 0; t < NT; ++t) acc[t] = __builtin_amdgcn_mfma_f32_16x16x32_bf16(wq[t][i], __builtin_bit_cast(bf16x8, a[i]), acc[t], 0, 0, 0);
            if (fr < 8) {
#pragma unroll
                for (int t = 0; t < NT; ++t) *(LAS f32x4*)(lds + L_RED + ((wave * NT + t) * 128 + fr * 16 + fq * 4) * 4) = acc[t];
            }
        }
        __syncthreads();
        if (tid < NT * 64) {
            const int t = tid >> 6, m = (tid & 63) >> 3, np = (tid & 7) * 2, mat = t / NE, col = (t % NE) * 16 + np;
            f32x2 sum = (f32x2){0.f, 0.f};
#pragma unroll
            for (int w = 0; w < 8; ++w) sum += *(const LAS f32x2*)(lds + L_RED + ((w * NT + t) * 128 + m * 16 + np) * 4);
            const unsigned pk = desent(pk_bf16(sum.x, sum.y));
            const u32x4 q4 = quad_gather(pk);
            if ((tid & 3) == 0) __builtin_amdgcn_raw_buffer_store_b128(q4, rs, O_QKVM + s * (int)QKVM_B + ((h * 3 + mat) * 8 + m) * 256 + (j * ES + col) * 2, 0, AUX_SC1);
            if (mat == 2) *(LAS unsigned*)(lds + L_VS + kb * 48 * VP + m * VP + col * 2) = pk;
        }
        STAMP(2);
        if (s == NSEG - 1) break;
        __syncthreads();
        if (wave == 0) {
            bf16x8 kx[3][4];
#pragma unroll
            for (int t = 0; t < 3; ++t) {
                int r = t * 16 + fr - 8; r = r < 0 ? 0 : (r > 31 ? 31 : r);
#pragma unroll
                for (int i = 0; i < 4; ++i) kx[t][i] = *(const LAS bf16x8*)(lds + L_KS + kb * 32 * KS_PITCH + r * KS_PITCH + i * 64 + fq * 16);
            }
            __builtin_amdgcn_s_sleep(CH_DELAY2);
            u32x4 qf[4], km[4];
            const int base = O_QKVM + s * (int)QKVM_B + h * 3 * 2048 + (lane & 7) * 256 + fq * 16;
            for (unsigned spins = 0;;) {
                asm volatile("" ::: "memory");
#pragma unroll
                for (int i = 0; i < 4; ++i) { qf[i] = __builtin_amdgcn_raw_buffer_load_b128(rs, base + i * 64, 0, AUX_SC1); km[i] = __builtin_amdgcn_raw_buffer_load_b128(rs, base + 2048 + i * 64, 0, AUX_SC1); }
                unsigned mxs = 0;
#pragma unroll
                for (int i = 0; i < 4; ++i) mxs = umax2(mxs, umax2(umax4(qf[i]), umax4(km[i])));
                if (!__any(mxs == SENT) || dead) break;
                PCOUNT(16);
                if (++spins > SPIN_CAP) { dead = true; if (lane == 0) *(volatile unsigned*)(ws + WS_TMO) = 2u; break; }
            }
            STAMP(3);
            f32x4 sc[3];
#pragma unroll
            for (int t = 0; t < 3; ++t) sc[t] = (f32x4){0.f, 0.f, 0.f, 0.f};
#pragma unroll
            for (int i = 0; i < 4; ++i) {
                const bf16x8 k0 = fr < 8 ? __builtin_bit_cast(bf16x8, km[i]) : kx[0][i];
                sc[1] = __builtin_amdgcn_mfma_f32_16x16x32_bf16(kx[1][i], __builtin_bit_cast(bf16x8, qf[i]), sc[1], 0, 0, 0);
                sc[2] = __builtin_amdgcn_mfma_f32_16x16x32_bf16(kx[2][i], __builtin_bit_cast(bf16x8, qf[i]), sc[2], 0, 0, 0);
                sc[0] = __builtin_amdgcn_mfma_f32_16x16x32_bf16(k0, __builtin_bit_cast(bf16x8, qf[i]), sc[0], 0, 0, 0);
            }
            asm volatile("" ::: "memory");
            bf16x8 vf[NE][2];
            const bf16x4 zz = (bf16x4){0, 0, 0, 0};
#pragma unroll
            for (int et = 0; et < NE; ++et) {
                const int voff = L_VS + kb * 48 * VP + (4 * fq + (fr >> 2)) * VP + et * 32 + (lane & 3) * 8;
                const bf16x4 v0 = __builtin_amdgcn_ds_read_tr16_b64_v4i16((LAS bf16x4*)(lds + voff));
                const bf16x4 v1 = __builtin_amdgcn_ds_read_tr16_b64_v4i16((LAS bf16x4*)(lds + voff + 16 * VP));
                const bf16x4 v2 = __builtin_amdgcn_ds_read_tr16_b64_v4i16((LAS bf16x4*)(lds + voff + 32 * VP));
                vf[et][0] = __builtin_shufflevector(v0, v1, 0, 1, 2, 3, 4, 5, 6, 7); vf[et][1] = __builtin_shufflevector(v2, zz, 0, 1, 2, 3, 4, 5, 6, 7);
            }
            const float scale = 0.08838834764831845f;
            const bool hi = fr >= 8;
            float v[6];
            v[0] = hi ? sc[1][2] : sc[0][0]; v[1] = hi ? sc[1][3] : sc[0][1]; v[2] = hi ? sc[2][0] : sc[0][2];
            v[3] = hi ? sc[2][1] : sc[0][3]; v[4] = hi ? sc[2][2] : sc[1][0]; v[5] = hi ? sc[2][3] : sc[1][1];
            float mx = -INFINITY;
#pragma unroll
            for (int i = 0; i < 6; ++i) { float z = rbf(rbf(v[i]) * scale); if (i >= 2 && hi && fq >= 2) z = -INFINITY; v[i] = z; mx = fmaxf(mx, z); }
            mx = fmaxf(mx, __builtin_bit_cast(float, __builtin_amdgcn_update_dpp(0, __builtin_bit_cast(int, mx), 0x128, 0xf, 0xf, true)));
            mx = xmax16_32(mx);
            float sum = 0.f;
#pragma unroll
            for (int i = 0; i < 6; ++i) { v[i] = __expf(v[i] - mx); sum += v[i]; }
            sum += __builtin_bit_cast(float, __builtin_amdgcn_update_dpp(0, __builtin_bit_cast(int, sum), 0x128, 0xf, 0xf, true));
            sum = xsum16_32(sum);
            const float inv = 1.0f / sum;
            const unsigned w0 = pk_bf16(v[0] * inv, v[1] * inv), w1 = pk_bf16(v[2] * inv, v[3] * inv), w2 = pk_bf16(v[4] * inv, v[5] * inv);
            u32x4 p0, p1;
            p0.x = w0; p0.y = w1; p0.z = w2; p0.w = (unsigned)__builtin_amdgcn_update_dpp(0, (int)w0, 0x128, 0xf, 0xf, true);
            p1.x = (unsigned)__builtin_amdgcn_update_dpp(0, (int)w1, 0x128, 0xf, 0xf, true); p1.y = (unsigned)__builtin_amdgcn_update_dpp(0, (int)w2, 0x128, 0xf, 0xf, true); p1.z = 0u; p1.w = 0u;
#pragma unroll
            for (int et = 0; et < NE; ++et) {
                f32x4 o = (f32x4){0.f, 0.f, 0.f, 0.f};
                o = __builtin_amdgcn_mfma_f32_16x16x32_bf16(vf[et][0], __builtin_bit_cast(bf16x8, p0), o, 0, 0, 0);
                o = __builtin_amdgcn_mfma_f32_16x16x32_bf16(vf[et][1], __builtin_bit_cast(bf16x8, p1), o, 0, 0, 0);
                {
                    const unsigned wx = desent(pk_bf16(o[0], o[1])), wy = desent(pk_bf16(o[2], o[3]));
                    auto sx = __builtin_amdgcn_permlane16_swap(wx, wx, false, false); auto sy = __builtin_amdgcn_permlane16_swap(wy, wy, false, false);
                    const u32x4 w = (u32x4){(unsigned)sx[0], (unsigned)sy[0], (unsigned)sx[1], (unsigned)sy[1]};
                    const int k = h * DH + j * ES + et * 16 + 4 * fq;
                    const int off = O_OM + s * (int)OM_B + (fr / RPG) * (8192 * RPG) + (k >> 5) * (64 * RPG) + ((k >> 3) & 3) * (16 * RPG) + (fr % RPG) * 16;
                    if (fr < 8 && (fq & 1) == 0) __builtin_amdgcn_raw_buffer_store_b128(w, rs, off, 0, AUX_SC1);
                }
            }
            STAMP(4);
        } else {
            const int t2 = tid - 64; const int nb = kb ^ 1; const int row0 = (s + 1) * SEG;
            if (((s + 1) & 7) == 0) { const int blk = (s + 1) >> 3; poll_ge(ws, CNT_KV + blk * 32 + (h >> 1), 8u); poll_ge(ws, CNT_KV + blk * 32 + 16 + (h >> 1), 8u); }
            if (s + 1 == 4) { poll_ge(ws, CNT_KV0B + (h >> 1), 16u); poll_ge(ws, CNT_KV0B + 16 + (h >> 1), 16u); }
            for (int i = t2; i < 512 + 32 * ES / 8; i += 448) {
                if (i < 512) { const int r = i >> 4, ch = i & 15;
                    *(LAS u32x4*)(lds + L_KS + nb * 32 * KS_PITCH + r * KS_PITCH + ch * 16) = __builtin_amdgcn_raw_buffer_load_b128(rkv, ((row0 + r) * HD + h * DH + ch * 8) * 2, 0, AUX_SC1); }
                else { const int r2 = (i - 512) / (ES / 8), c2 = (i - 512) % (ES / 8);
                    *(LAS u32x4*)(lds + L_VS + nb * 48 * VP + (8 + r2) * VP + c2 * 16) = __builtin_amdgcn_raw_buffer_load_b128(rkv, O_V + ((row0 + r2) * HD + h * DH + j * ES + c2 * 8) * 2, 0, AUX_SC1); }
            }
        }
        __syncthreads();
        {
            if (WOL) {
#pragma unroll
                for (int i = 8; i < 16; ++i) wo[i] = *(const LAS bf16x8*)(lds + L_WO + (wave * 8 + i - 8) * 1024 + lane * 16);
            }
            __builtin_amdgcn_s_sleep(CH_DELAY3);
            u32x4 a[RPG];
            constexpr int KPG = 16 / RPG;
            const int base = O_OM + s * (int)OM_B + g * (8192 * RPG) + wave * (1024 * RPG) + (fr / RPG) * (64 * RPG) + fq * (16 * RPG) + (fr % RPG) * 16;
            for (unsigned spins = 0;;) {
                asm volatile("" ::: "memory");
#pragma unroll
                for (int i = 0; i < RPG; ++i) a[i] = __builtin_amdgcn_raw_buffer_load_b128(rs, base + i * 1024, 0, AUX_SC1);
                unsigned mxs = 0;
#pragma unroll
                for (int i = 0; i < RPG; ++i) mxs = umax2(mxs, umax4(a[i]));
                if (!__any(mxs == SENT) || dead) break;
                PCOUNT(32);
                if (++spins > SPIN_CAP) { dead = true; if (lane == 0) *(volatile unsigned*)(ws + WS_TMO) = 3u; break; }
            }
            STAMP(5);
            f32x4 acc[KPG];
#pragma unroll
            for (int q = 0; q < KPG; ++q) acc[q] = (f32x4){0.f, 0.f, 0.f, 0.f};
#pragma unroll
            for (int gq = 0; gq < RPG; ++gq)
#pragma unroll
                for (int q = 0; q < KPG; ++q) acc[q] = __builtin_amdgcn_mfma_f32_16x16x32_bf16(wo[gq * KPG + q], __builtin_bit_cast(bf16x8, a[gq]), acc[q], 0, 0, 0);
            f32x4 r = acc[0];
#pragma unroll
            for (int q = 1; q < KPG; ++q) if (fr / RPG == q) r = acc[q];
#pragma unroll
            for (int e = 0; e < 4; ++e) {
                if (RPG <= 2) r[e] += __builtin_bit_cast(float, __builtin_amdgcn_update_dpp(0, __builtin_bit_cast(int, r[e]), 0x122, 0xf, 0xf, true));
                r[e] += __builtin_bit_cast(float, __builtin_amdgcn_update_dpp(0, __builtin_bit_cast(int, r[e]), 0x124, 0xf, 0xf, true));
                r[e] += __builtin_bit_cast(float, __builtin_amdgcn_update_dpp(0, __builtin_bit_cast(int, r[e]), 0x128, 0xf, 0xf, true));
            }
            if (fr < RPG) *(LAS f32x4*)(lds + L_RED2 + ((wave * RPG + fr) * 16 + fq * 4) * 4) = r;
        }
        __syncthreads();
        if (tid < RPG * 8) {
            const int m = tid >> 3, np = (tid & 7) * 2;
            f32x2 sum = (f32x2){0.f, 0.f};
#pragma unroll
            for (int w = 0; w < 8; ++w) sum += *(const LAS f32x2*)(lds + L_RED2 + ((w * RPG + m) * 16 + np) * 4);
            const unsigned pk = desent(pk_bf16(sum.x, sum.y));
            const int off = O_MEM + (s + 1) * (int)MEM_B + (c >> 1) * 512 + ((c & 1) * 2 + (np >> 3)) * 128 + (g * RPG + m) * 16;
            const u32x4 q4 = quad_gather(pk);
            if ((tid & 3) == 0) __builtin_amdgcn_raw_buffer_store_b128(q4, rs, off, 0, AUX_SC1);
        }
        STAMP(6);
    }
}

template <int MT, class Epi>
__device__ __forceinline__ void gemm128_w8(LAS unsigned char* lds, const bf16_t* A, const int lda, const bf16_t* B, const int ldb, const int K, const Epi& E) {
    int tid = threadIdx.x; asm volatile("" : "+v"(tid));
    const int wid = __builtin_amdgcn_readfirstlane(tid >> 6), lane = tid & 63, wr = wid >> 2, wc = wid & 3, fr = lane & 15, fq = lane >> 4;
    constexpr int NA = MT / 2;
    unsigned voffA[NA], voffB[2];
#pragma unroll
    for (int i = 0; i < NA; ++i) { int R, C; g8::stage_rc(tid * 16 + i * 8192, R, C); voffA[i] = (unsigned)(R * lda + C) * 2u; }
#pragma unroll
    for (int i = 0; i < 2; ++i) { const int bb = tid * 16 + i * 8192, row = bb >> 8, ch = (bb & 255) >> 4, seg = (ch >> 1) ^ g8::bsw(row);
        voffB[i] = (unsigned)(row * ldb + seg * 16 + (ch & 1) * 8) * 2u; }
    const char* cA = (const char*)A; const char* cB = (const char*)B;
    const unsigned ldsw = (unsigned)wid * 1024u;
    const int aoff = g8::lds_byte(wr * 16 * MT + fr, fq * 8);
    const int q4 = (lane & 15) >> 2, p4 = lane & 3, sB = q4 | ((fq & 1) << 2);
    const int boff0 = (8 * fq + q4) * 256 + (((wc * 2 + 0) ^ sB) * 32) + 8 * p4;
    const int boff1 = (8 * fq + q4) * 256 + (((wc * 2 + 1) ^ sB) * 32) + 8 * p4;
    f32x4 acc[MT][2];
#pragma unroll
    for (int m = 0; m < MT; ++m) { acc[m][0] = (f32x4){0.f, 0.f, 0.f, 0.f}; acc[m][1] = (f32x4){0.f, 0.f, 0.f, 0.f}; }
    const int nt = K / 64;
#define STG(buf, t) do { _Pragma("unroll") for (int _i = 0; _i < NA; ++_i) \
        __builtin_amdgcn_global_load_lds((const unsigned*)(cA + (size_t)(t) * 128 + voffA[_i]), (LAS unsigned*)(lds + (buf) * 32768 + ldsw + _i * 8192), 16, 0, 0); \
        _Pragma("unroll") for (int _i = 0; _i < 2; ++_i) \
        __builtin_amdgcn_global_load_lds((const unsigned*)(cB + (size_t)(t) * 64 * ldb * 2 + voffB[_i]), (LAS unsigned*)(lds + (buf) * 32768 + 16384 + ldsw + _i * 8192), 16, 0, 0); } while (0)
    STG(0, 0); STG(1, 1); STG(2, 2);
    int cur = 0;
    for (int t = 0; t < nt; ++t) {
        if (MT == 4) { if (t + 2 < nt) asm volatile("s_waitcnt vmcnt(8)" ::: "memory"); else if (t + 1 < nt) asm volatile("s_waitcnt vmcnt(4)" ::: "memory"); else asm volatile("s_waitcnt vmcnt(0)" ::: "memory"); }
        else { if (t + 2 < nt) asm volatile("s_waitcnt vmcnt(6)" ::: "memory"); else if (t + 1 < nt) asm volatile("s_waitcnt vmcnt(3)" ::: "memory"); else asm volatile("s_waitcnt vmcnt(0)" ::: "memory"); }
        __builtin_amdgcn_s_barrier();
        asm volatile("" ::: "memory");
        if (t + 3 < nt) STG((cur + 3) & 3, t + 3);
        bf16x8 At[MT][2], Bt[2][2];
        LAS unsigned char* sa = lds + cur * 32768; LAS unsigned char* sb = sa + 16384;
#pragma unroll
        for (int k = 0; k < 2; ++k) {
            bf16x4 a0 = __builtin_amdgcn_ds_read_tr16_b64_v4i16((LAS bf16x4*)(sb + boff0 + k * 8192)), a1 = __builtin_amdgcn_ds_read_tr16_b64_v4i16((LAS bf16x4*)(sb + boff0 + k * 8192 + 1024));
            bf16x4 c0 = __builtin_amdgcn_ds_read_tr16_b64_v4i16((LAS bf16x4*)(sb + boff1 + k * 8192)), c1 = __builtin_amdgcn_ds_read_tr16_b64_v4i16((LAS bf16x4*)(sb + boff1 + k * 8192 + 1024));
            Bt[0][k] = __builtin_shufflevector(a0, a1, 0, 1, 2, 3, 4, 5, 6, 7); Bt[1][k] = __builtin_shufflevector(c0, c1, 0, 1, 2, 3, 4, 5, 6, 7);
#pragma unroll
            for (int m = 0; m < MT; ++m) At[m][k] = *(const LAS bf16x8*)(sa + aoff + m * 2048 + k * 1024);
        }
        __builtin_amdgcn_s_setprio(1);
#pragma unroll
        for (int k = 0; k < 2; ++k)
#pragma unroll
            for (int m = 0; m < MT; ++m)
#pragma unroll
                for (int n = 0; n < 2; ++n) acc[m][n] = __builtin_amdgcn_mfma_f32_16x16x32_bf16(Bt[n][k], At[m][k], acc[m][n], 0, 0, 0);
        __builtin_amdgcn_s_setprio(0);
        asm volatile("s_waitcnt lgkmcnt(0)" ::: "memory");
        cur = (cur + 1) & 3;
    }
#undef STG
    __builtin_amdgcn_s_barrier();
    E(acc, wr, wc, fr, fq);
}

constexpr int XV_PITCH = 272;
template <bool INK>
__device__ __forceinline__ void xattn_item(LAS unsigned char* vs, char* ws, const int s, const int h) {
    const int lane = threadIdx.x & 63, fr = lane & 15, fq = lane >> 4;
    const bf16_t* Q = (const bf16_t*)(ws + WS_Q) + (size_t)s * SEG * HD + h * DH;
    const bf16_t* K = (const bf16_t*)(ws + WS_K) + (size_t)s * SEG * HD + h * DH;
    const bf16_t* V = (const bf16_t*)(ws + WS_V) + (size_t)s * SEG * HD + h * DH;
    const __amdgpu_buffer_rsrc_t rm = __builtin_amdgcn_make_buffer_rsrc(ws + WS_QKVM, 0, (int)(WS_OM - WS_QKVM), 0x00020000);
    const int kmo = s * (int)QKVM_B + (h * 3 + 1) * 2048, vmo = kmo + 2048;
    u32x4 km[4], vm[2];
    for (unsigned spins = 0;;) {
        asm volatile("" ::: "memory");
#pragma unroll
        for (int i = 0; i < 4; ++i) km[i] = __builtin_amdgcn_raw_buffer_load_b128(rm, kmo + (lane & 7) * 256 + i * 64 + fq * 16, 0, INK ? AUX_SC1 : 0);
#pragma unroll
        for (int i = 0; i < 2; ++i) vm[i] = __builtin_amdgcn_raw_buffer_load_b128(rm, vmo + (i * 4 + fq) * 256 + fr * 16, 0, INK ? AUX_SC1 : 0);
        if (!INK) break;
        unsigned mxs = umax2(umax4(vm[0]), umax4(vm[1]));
#pragma unroll
        for (int i = 0; i < 4; ++i) mxs = umax2(mxs, umax4(km[i]));
        if (!__any(mxs == SENT)) break;
        __builtin_amdgcn_s_sleep(16);
        if (++spins > SPIN_CAP) break;
    }
#pragma unroll
    for (int i = 0; i < 12; ++i) {
        const int r = i * 4 + fq;
        u32x4 v = (u32x4){0u, 0u, 0u, 0u};
        if (i < 2) v = vm[i]; else if (i < 10) v = *(const u32x4*)(V + (size_t)(r - 8) * HD + fr * 8);
        *(LAS u32x4*)(vs + r * XV_PITCH + fr * 16) = v;
    }
    f32x4 sc[3][2];
#pragma unroll
    for (int t = 0; t < 3; ++t) { sc[t][0] = (f32x4){0.f, 0.f, 0.f, 0.f}; sc[t][1] = (f32x4){0.f, 0.f, 0.f, 0.f}; }
#pragma unroll
    for (int i = 0; i < 4; ++i) {
        const bf16x8 q0 = *(const bf16x8*)(Q + (size_t)fr * HD + i * 32 + fq * 8), q1 = *(const bf16x8*)(Q + (size_t)(16 + fr) * HD + i * 32 + fq * 8);
#pragma unroll
        for (int t = 0; t < 3; ++t) {
            int r = t * 16 + fr - 8; r = r < 0 ? 0 : (r > 31 ? 31 : r);
            bf16x8 kf = *(const bf16x8*)(K + (size_t)r * HD + i * 32 + fq * 8);
            if (t == 0 && fr < 8) kf = __builtin_bit_cast(bf16x8, km[i]);
            sc[t][0] = __builtin_amdgcn_mfma_f32_16x16x32_bf16(kf, q0, sc[t][0], 0, 0, 0);
            sc[t][1] = __builtin_amdgcn_mfma_f32_16x16x32_bf16(kf, q1, sc[t][1], 0, 0, 0);
        }
    }
    const float scale = 0.08838834764831845f, NEG = rbf(-1000000000.0f);
    u32x4 p0[2], p1[2];
#pragma unroll
    for (int rt = 0; rt < 2; ++rt) {
        const int row = rt * 16 + fr;
        float v[12]; float mx = -INFINITY;
#pragma unroll
        for (int t = 0; t < 3; ++t)
#pragma unroll
            for (int r = 0; r < 4; ++r) { const int key = t * 16 + fq * 4 + r; float z = rbf(rbf(sc[t][rt][r]) * scale);
                if (key - 8 > row) z = NEG; if (key >= 40) z = -INFINITY; v[t * 4 + r] = z; mx = fmaxf(mx, z); }
        mx = xmax16_32(mx);
        float sum = 0.f;
#pragma unroll
        for (int i = 0; i < 12; ++i) { v[i] = __expf(v[i] - mx); sum += v[i]; }
        sum = xsum16_32(sum);
        const float inv = 1.0f / sum;
        p0[rt].x = pk_bf16(v[0] * inv, v[1] * inv); p0[rt].y = pk_bf16(v[2] * inv, v[3] * inv); p0[rt].z = pk_bf16(v[4] * inv, v[5] * inv); p0[rt].w = pk_bf16(v[6] * inv, v[7] * inv);
        p1[rt].x = pk_bf16(v[8] * inv, v[9] * inv); p1[rt].y = pk_bf16(v[10] * inv, v[11] * inv); p1[rt].z = 0u; p1[rt].w = 0u;
    }
    asm volatile("s_waitcnt lgkmcnt(0)" ::: "memory");
    const __amdgpu_buffer_rsrc_t ro = __builtin_amdgcn_make_buffer_rsrc(ws + WS_O, 0, S_LEN * HD * 2, 0x00020000);
    const int obase = (s * SEG * HD + h * DH) * 2;
    const bf16x4 zz = (bf16x4){0, 0, 0, 0};
    u32x2 ow[8][2];
#pragma unroll
    for (int et = 0; et < 8; ++et) {
        const int voff = (4 * fq + (fr >> 2)) * XV_PITCH + et * 32 + (lane & 3) * 8;
        const bf16x4 v0 = __builtin_amdgcn_ds_read_tr16_b64_v4i16((LAS bf16x4*)(vs + voff));
        const bf16x4 v1 = __builtin_amdgcn_ds_read_tr16_b64_v4i16((LAS bf16x4*)(vs + voff + 16 * XV_PITCH));
        const bf16x4 v2 = __builtin_amdgcn_ds_read_tr16_b64_v4i16((LAS bf16x4*)(vs + voff + 32 * XV_PITCH));
        const bf16x8 va = __builtin_shufflevector(v0, v1, 0, 1, 2, 3, 4, 5, 6, 7), vb = __builtin_shufflevector(v2, zz, 0, 1, 2, 3, 4, 5, 6, 7);
#pragma unroll
        for (int rt = 0; rt < 2; ++rt) {
            f32x4 o = (f32x4){0.f, 0.f, 0.f, 0.f};
            o = __builtin_amdgcn_mfma_f32_16x16x32_bf16(va, __builtin_bit_cast(bf16x8, p0[rt]), o, 0, 0, 0);
            o = __builtin_amdgcn_mfma_f32_16x16x32_bf16(vb, __builtin_bit_cast(bf16x8, p1[rt]), o, 0, 0, 0);
            ow[et][rt].x = pk_bf16(o[0], o[1]); ow[et][rt].y = pk_bf16(o[2], o[3]);
        }
    }
    asm volatile("s_waitcnt lgkmcnt(0)" ::: "memory");
#pragma unroll
    for (int et = 0; et < 8; ++et)
#pragma unroll
        for (int rt = 0; rt < 2; ++rt) *(LAS u32x2*)(vs + (rt * 16 + fr) * XV_PITCH + et * 32 + fq * 8) = ow[et][rt];
    asm volatile("s_waitcnt lgkmcnt(0)" ::: "memory");
#pragma unroll
    for (int i = 0; i < 8; ++i) {
        const int r = i * 4 + fq;
        const u32x4 w = *(const LAS u32x4*)(vs + r * XV_PITCH + fr * 16);
        __builtin_amdgcn_raw_buffer_store_b128(w, ro, obase + (r * HD + fr * 8) * 2, 0, INK ? AUX_SC1 : 0);
    }
    asm volatile("s_waitcnt lgkmcnt(0)" ::: "memory");
}

__device__ __forceinline__ void store_pair_sc1(const __amdgpu_buffer_rsrc_t rs, int off0, u32x2 w0, u32x2 w1, int fq) {
    auto sx = __builtin_amdgcn_permlane16_swap(w0.x, w1.x, false, false); auto sy = __builtin_amdgcn_permlane16_swap(w0.y, w1.y, false, false);
    const u32x4 w = (u32x4){(unsigned)sx[0], (unsigned)sy[0], (unsigned)sx[1], (unsigned)sy[1]};
    __builtin_amdgcn_raw_buffer_store_b128(w, rs, off0 + (fq & 1) * 32 + (fq >> 1) * 16, 0, AUX_SC1);
}
struct SchedOne {
    g8::Unit u; bool valid;
    __device__ __forceinline__ bool next(int i, g8::Unit& o) const { if (i != 0 || !valid) return false; o = u; return true; }
};
struct SchedKVQ {
    const bf16_t* A; const bf16_t* Wq; long dk, dv; int r;
    __device__ __forceinline__ bool next(int i, g8::Unit& u) const {
        const int L = r + 128 * i; if (L >= 352) return false;
        int pm, pn;
        if (L < 224) { pm = 1 + (L >> 5); pn = 16 + (L & 31); } else { pm = (L - 224) & 7; pn = (L - 224) >> 3; }
        const int mat = pn >> 4;
        u.A = A + (size_t)pm * 256 * DM; u.B = Wq + (mat == 1 ? dk : 0L) + (mat == 2 ? dv : 0L) + (pn & 15) * 256; u.pm = pm; u.pn = pn; return true;
    }
};
struct EpiQKV {
    char* ws;
    __device__ __forceinline__ void operator()(const f32x4 (&acc)[2][2][4][2], const g8::Unit& u, int wr, int wc, int fr, int fq) const {
        const __amdgpu_buffer_rsrc_t rq = __builtin_amdgcn_make_buffer_rsrc(ws + WS_Q, 0, (int)(WS_H - WS_Q), 0x00020000);
        const int base = (u.pn >> 4) * (S_LEN * HD * 2) + ((u.pm * 256 + wr * 64 + fr) * HD + (u.pn & 15) * 256 + wc * 32) * 2;
#pragma unroll
        for (int ai = 0; ai < 2; ++ai)
#pragma unroll
            for (int m = 0; m < 4; ++m)
#pragma unroll
                for (int bj = 0; bj < 2; ++bj) { const f32x4 v0 = acc[ai][bj][m][0], v1 = acc[ai][bj][m][1];
                    store_pair_sc1(rq, base + ((ai * 128 + m * 16) * HD + bj * 128) * 2, (u32x2){pk_bf16(v0[0], v0[1]), pk_bf16(v0[2], v0[3])}, (u32x2){pk_bf16(v1[0], v1[1]), pk_bf16(v1[2], v1[3])}, fq); }
        asm volatile("s_waitcnt vmcnt(0)" ::: "memory");
        if ((threadIdx.x & 63) == 0) {
            if (u.pn >= 16) __hip_atomic_fetch_add((unsigned*)(ws + WS_CNT + (size_t)(CNT_KV + u.pm * 32 + u.pn - 16) * 256), 1u, __ATOMIC_RELAXED, __HIP_MEMORY_SCOPE_AGENT);
            __hip_atomic_fetch_add((unsigned*)(ws + WS_CNT + (size_t)(CNT_QKV + u.pm) * 256), 1u, __ATOMIC_RELAXED, __HIP_MEMORY_SCOPE_AGENT);
        }
    }
};
struct EpiKV0 {
    char* ws; int pm2, pn2;
    __device__ __forceinline__ void operator()(const f32x4 (&acc)[4][2], int wr, int wc, int fr, int fq) const {
        const __amdgpu_buffer_rsrc_t rq = __builtin_amdgcn_make_buffer_rsrc(ws + WS_K, 0, (int)(WS_H - WS_K), 0x00020000);
        const int base = (pn2 >> 5) * (S_LEN * HD * 2) + ((pm2 * 128 + wr * 64 + fr) * HD + (pn2 & 31) * 128 + wc * 32) * 2;
#pragma unroll
        for (int m = 0; m < 4; ++m) { const f32x4 v0 = acc[m][0], v1 = acc[m][1];
            store_pair_sc1(rq, base + m * 16 * HD * 2, (u32x2){pk_bf16(v0[0], v0[1]), pk_bf16(v0[2], v0[3])}, (u32x2){pk_bf16(v1[0], v1[1]), pk_bf16(v1[2], v1[3])}, fq); }
        asm volatile("s_waitcnt vmcnt(0)" ::: "memory");
        if ((threadIdx.x & 63) == 0) {
            __hip_atomic_fetch_add((unsigned*)(ws + WS_CNT + (size_t)((pm2 == 0 ? CNT_KV : CNT_KV0B) + (pn2 >> 1)) * 256), 1u, __ATOMIC_RELAXED, __HIP_MEMORY_SCOPE_AGENT);
            __hip_atomic_fetch_add((unsigned*)(ws + WS_CNT + (size_t)CNT_QKV * 256), 1u, __ATOMIC_RELAXED, __HIP_MEMORY_SCOPE_AGENT);
        }
    }
};
struct EpiMLP1 {
    char* ws; const bf16_t* b1;
    __device__ __forceinline__ void operator()(const f32x4 (&acc)[2][2][4][2], const g8::Unit& u, int wr, int wc, int fr, int fq) const {
        const __amdgpu_buffer_rsrc_t rh = __builtin_amdgcn_make_buffer_rsrc(ws + WS_H, 0, S_LEN * FF * 2, 0x00020000);
        const int row0 = u.pm * 256 + wr * 64 + fr, col0 = u.pn * 256 + wc * 32;
#pragma unroll
        for (int bj = 0; bj < 2; ++bj) {
            const u32x2 ba = *(const u32x2*)(b1 + col0 + bj * 128 + 4 * fq), bb = *(const u32x2*)(b1 + col0 + bj * 128 + 16 + 4 * fq);
#pragma unroll
            for (int ai = 0; ai < 2; ++ai)
#pragma unroll
                for (int m = 0; m < 4; ++m) { const f32x4 v0 = acc[ai][bj][m][0], v1 = acc[ai][bj][m][1];
                    u32x2 w0, w1;
                    w0.x = pk_bf16(gelu_tanh(rbf(rbf(v0[0]) + bf2f(ba.x & 0xffffu))), gelu_tanh(rbf(rbf(v0[1]) + bf2f(ba.x >> 16))));
                    w0.y = pk_bf16(gelu_tanh(rbf(rbf(v0[2]) + bf2f(ba.y & 0xffffu))), gelu_tanh(rbf(rbf(v0[3]) + bf2f(ba.y >> 16))));
                    w1.x = pk_bf16(gelu_tanh(rbf(rbf(v1[0]) + bf2f(bb.x & 0xffffu))), gelu_tanh(rbf(rbf(v1[1]) + bf2f(bb.x >> 16))));
                    w1.y = pk_bf16(gelu_tanh(rbf(rbf(v1[2]) + bf2f(bb.y & 0xffffu))), gelu_tanh(rbf(rbf(v1[3]) + bf2f(bb.y >> 16))));
                    store_pair_sc1(rh, ((row0 + ai * 128 + m * 16) * FF + col0 + bj * 128) * 2, w0, w1, fq);
                    if (m & 1) asm volatile("" ::: "memory"); }
        }
        signal_counter(ws, CNT_H + u.pm);
    }
};
struct EpiMLP2 {
    char* ws; const bf16_t* b2; int row0, col0;
    __device__ __forceinline__ void operator()(const f32x4 (&acc)[4][2], int wr, int wc, int fr, int fq) const {
        const __amdgpu_buffer_rsrc_t rb = __builtin_amdgcn_make_buffer_rsrc(ws + WS_B, 0, S_LEN * DM * 2, 0x00020000);
        const int col = col0 + wc * 32;
        const u32x2 ba = *(const u32x2*)(b2 + col + 4 * fq), bb = *(const u32x2*)(b2 + col + 16 + 4 * fq);
#pragma unroll
        for (int m = 0; m < 4; ++m) { const f32x4 v0 = acc[m][0], v1 = acc[m][1];
            u32x2 w0, w1;
            w0.x = pk_bf16(rbf(v0[0]) + bf2f(ba.x & 0xffffu), rbf(v0[1]) + bf2f(ba.x >> 16)); w0.y = pk_bf16(rbf(v0[2]) + bf2f(ba.y & 0xffffu), rbf(v0[3]) + bf2f(ba.y >> 16));
            w1.x = pk_bf16(rbf(v1[0]) + bf2f(bb.x & 0xffffu), rbf(v1[1]) + bf2f(bb.x >> 16)); w1.y = pk_bf16(rbf(v1[2]) + bf2f(bb.y & 0xffffu), rbf(v1[3]) + bf2f(bb.y >> 16));
            store_pair_sc1(rb, ((row0 + wr * 64 + m * 16 + fr) * DM + col) * 2, w0, w1, fq); }
        signal_counter(ws, CNT_B + (row0 >> 8));
    }
};
__device__ __forceinline__ u32x2 combine4(const f32x4 v, const u32x2 xw, const u32x2 bw) {
    u32x2 w;
    w.x = pk_bf16(rbf(bf2f(xw.x & 0xffffu) + rbf(v[0])) + bf2f(bw.x & 0xffffu), rbf(bf2f(xw.x >> 16) + rbf(v[1])) + bf2f(bw.x >> 16));
    w.y = pk_bf16(rbf(bf2f(xw.y & 0xffffu) + rbf(v[2])) + bf2f(bw.y & 0xffffu), rbf(bf2f(xw.y >> 16) + rbf(v[3])) + bf2f(bw.y >> 16));
    return w;
}
template <int MT>
struct EpiOut {
    const bf16_t* x; const bf16_t* bout; bf16_t* out; int row0, col0;
    __device__ __forceinline__ void operator()(const f32x4 (&acc)[MT][2], int wr, int wc, int fr, int fq) const {
#pragma unroll
        for (int m = 0; m < MT; ++m)
#pragma unroll
            for (int n = 0; n < 2; ++n) {
                const size_t off = (size_t)(row0 + wr * 16 * MT + m * 16 + fr) * DM + col0 + wc * 32 + n * 16 + 4 * fq;
                *(u32x2*)(out + off) = combine4(acc[m][n], *(const u32x2*)(x + off), *(const u32x2*)(bout + off));
            }
    }
};

constexpr int TAIL_BLK0 = 6;
__global__ void __launch_bounds__(512, 2) k_mega(const bf16_t* __restrict__ x, const bf16_t* __restrict__ Wq, const bf16_t* __restrict__ Wk, const bf16_t* __restrict__ Wv,
                                                 const bf16_t* __restrict__ Wo, const bf16_t* __restrict__ w1, const bf16_t* __restrict__ b1, const bf16_t* __restrict__ w2,
                                                 const bf16_t* __restrict__ b2, char* ws, bf16_t* __restrict__ out) {
    extern __shared__ __attribute__((aligned(16))) unsigned char shm_[];
    LAS unsigned char* lds = (LAS unsigned char*)shm_;
    const int b = blockIdx.x;
    const int role = (b >> 3) & 1, r = (b >> 4) * 8 + (b & 7);
    if (role == 0) { chain_body<4>(lds, r, Wq, Wk, Wv, Wo, ws); return; }
#define WSTAMP(i) do {} while (0)
    WSTAMP(0);
    {
        const int pm2 = r & 1, pn2 = r >> 1;
        EpiKV0 E{ws, pm2, pn2};
        gemm128_w8<4, EpiKV0>(lds, (const bf16_t*)(ws + WS_XN) + (size_t)pm2 * 128 * DM, DM, (pn2 >= 32 ? Wv : Wk) + (pn2 & 31) * 128, HD, DM, E);
    }
    WSTAMP(1);
    {
        SchedKVQ S{(const bf16_t*)(ws + WS_XN), Wq, (long)(Wk - Wq), (long)(Wv - Wq), r};
        EpiQKV E{ws}; g8::gemm_phase<EpiQKV, SchedKVQ>(lds, DM, DM, HD, S, E);
    }
    WSTAMP(2);
    {
        const int L = r >= 96 ? r - 96 : 32 + r;
        SchedOne S; S.valid = (r >= 96) || (r < 80); S.u.pm = L & 7; S.u.pn = L >> 3; S.u.A = (const bf16_t*)(ws + WS_Y) + (size_t)S.u.pm * 256 * DM; S.u.B = w1 + S.u.pn * 256;
        EpiMLP1 E{ws, b1}; g8::gemm_phase<EpiMLP1, SchedOne>(lds, DM, DM, FF, S, E);
    }
    WSTAMP(3);
    {
        const int pm = r >> 3, pn = r & 7;
        wait_counter(ws, CNT_H + (pm >> 1), 14 * 8);
        EpiMLP2 E{ws, b2, pm * 128, pn * 128};
        gemm128_w8<4, EpiMLP2>(lds, (const bf16_t*)(ws + WS_H) + (size_t)pm * 128 * FF, FF, w2 + pn * 128, DM, FF, E);
    }
    WSTAMP(4);
    if (r < 80) {
        const int blk = r >> 4, t = r & 15;
        {
            if (threadIdx.x < 64) poll_ge(ws, CNT_STEP, (unsigned)(8 * blk + 8));
            wait_counter(ws, CNT_QKV + blk, (blk == 0 ? 128 * 8 : 32 * 8) + 16 * 8);
            WSTAMP(5);
            const int wave = threadIdx.x >> 6;
#pragma unroll 1
            for (int i = 0; i < 2; ++i) { const int item = t * 16 + wave * 2 + i; xattn_item<true>(lds + wave * 48 * XV_PITCH, ws, blk * 8 + (item >> 5), item & 31); }
            signal_counter(ws, CNT_O + blk);
        }
        {
            if (threadIdx.x < 64) poll_ge(ws, CNT_B + blk, 16 * 8);
            wait_counter(ws, CNT_O + blk, 16 * 8);
            WSTAMP(6);
            const int pm = blk * 2 + (t >> 3), pn = t & 7;
            EpiOut<4> E{x, (const bf16_t*)(ws + WS_B), out, pm * 128, pn * 128};
            gemm128_w8<4, EpiOut<4>>(lds, (const bf16_t*)(ws + WS_O) + (size_t)pm * 128 * HD, HD, Wo + pn * 128, DM, HD, E);
        }
    } else if (r < 112) {
        constexpr int blk = 5; const int t = r - 80;
        if (threadIdx.x < 64) poll_ge(ws, CNT_STEP, (unsigned)(8 * blk + 8));
        wait_counter(ws, CNT_QKV + blk, 32 * 8 + 16 * 8);
        WSTAMP(5);
        const int wave = threadIdx.x >> 6, item = t * 8 + wave;
        xattn_item<true>(lds + wave * 48 * XV_PITCH, ws, blk * 8 + (item >> 5), item & 31);
        signal_counter(ws, CNT_O + blk);
        if (threadIdx.x < 64) poll_ge(ws, CNT_B + blk, 16 * 8);
        wait_counter(ws, CNT_O + blk, 32 * 8);
        WSTAMP(6);
        const int pm = blk * 4 + (t >> 3), pn = t & 7;
        EpiOut<2> E{x, (const bf16_t*)(ws + WS_B), out, pm * 64, pn * 128};
        gemm128_w8<2, EpiOut<2>>(lds, (const bf16_t*)(ws + WS_O) + (size_t)pm * 64 * HD, HD, Wo + pn * 128, DM, HD, E);
    }
    WSTAMP(7);
}

__global__ void __launch_bounds__(256) k_tail_attn(char* ws) {
    __shared__ __attribute__((aligned(16))) unsigned char shm_[4 * 48 * XV_PITCH];
    const int wave = threadIdx.x >> 6, item = blockIdx.x * 4 + wave;
    xattn_item<false>((LAS unsigned char*)shm_ + wave * 48 * XV_PITCH, ws, TAIL_BLK0 * 8 + (item >> 5), item & 31);
}
__global__ void __launch_bounds__(256) k_tail_gemm(const bf16_t* __restrict__ x, const bf16_t* __restrict__ Wo, const char* __restrict__ ws, bf16_t* __restrict__ out) {
    extern __shared__ __attribute__((aligned(16))) unsigned char shm_[];
    const int tid = threadIdx.x, wave = __builtin_amdgcn_readfirstlane(tid >> 6), lane = tid & 63, fr = lane & 15, fq = lane >> 4;
    LAS unsigned char* lds = (LAS unsigned char*)shm_ + wave * 36864;
    const int pn = blockIdx.x & 31, pm = blockIdx.x >> 5, row0 = TAIL_BLK0 * 256 + pm * 64, col0 = pn * 32;
    const char* cA = (const char*)((const bf16_t*)(ws + WS_O) + (size_t)row0 * HD + wave * 1024);
    const char* cB = (const char*)(Wo + (size_t)wave * 1024 * DM + col0);
    unsigned voffA[8], voffB[4];
#pragma unroll
    for (int i = 0; i < 8; ++i) { const int c = i * 64 + lane, row = c >> 3, ch = (c & 7) ^ (row & 7); voffA[i] = (unsigned)(row * HD + ch * 8) * 2u; }
#pragma unroll
    for (int i = 0; i < 4; ++i) { const int c = i * 64 + lane, k = c >> 2, cb = c & 3; voffB[i] = (unsigned)(k * DM + cb * 8) * 2u; }
#define STG(buf, t) do { _Pragma("unroll") for (int _i = 0; _i < 8; ++_i) \
        __builtin_amdgcn_global_load_lds((const unsigned*)(cA + (size_t)(t) * 128 + voffA[_i]), (LAS unsigned*)(lds + (buf) * 12288 + _i * 1024), 16, 0, 0); \
        _Pragma("unroll") for (int _i = 0; _i < 4; ++_i) \
        __builtin_amdgcn_global_load_lds((const unsigned*)(cB + (size_t)(t) * 64 * DM * 2 + voffB[_i]), (LAS unsigned*)(lds + (buf) * 12288 + 8192 + _i * 1024), 16, 0, 0); } while (0)
    f32x4 acc[4][2];
#pragma unroll
    for (int m = 0; m < 4; ++m) { acc[m][0] = (f32x4){0.f, 0.f, 0.f, 0.f}; acc[m][1] = (f32x4){0.f, 0.f, 0.f, 0.f}; }
    STG(0, 0); STG(1, 1);
    int cur = 0;
    for (int t = 0; t < 16; ++t) {
        int nx = cur + 2; if (nx >= 3) nx -= 3;
        if (t + 2 < 16) { STG(nx, t + 2); asm volatile("s_waitcnt vmcnt(24)" ::: "memory"); }
        else if (t + 1 < 16) asm volatile("s_waitcnt vmcnt(12)" ::: "memory");
        else asm volatile("s_waitcnt vmcnt(0)" ::: "memory");
        LAS unsigned char* sa = lds + cur * 12288; LAS unsigned char* sb = sa + 8192;
        bf16x8 At[4][2], Bt[2][2];
#pragma unroll
        for (int k = 0; k < 2; ++k) {
#pragma unroll
            for (int m = 0; m < 4; ++m) { const int row = m * 16 + fr; At[m][k] = *(const LAS bf16x8*)(sa + row * 128 + (((k * 4 + fq) ^ (row & 7)) * 16)); }
#pragma unroll
            for (int n = 0; n < 2; ++n) {
                const int off = (k * 32 + 8 * fq + (fr >> 2)) * 64 + n * 32 + (lane & 3) * 8;
                const bf16x4 a0 = __builtin_amdgcn_ds_read_tr16_b64_v4i16((LAS bf16x4*)(sb + off)), a1 = __builtin_amdgcn_ds_read_tr16_b64_v4i16((LAS bf16x4*)(sb + off + 256));
                Bt[n][k] = __builtin_shufflevector(a0, a1, 0, 1, 2, 3, 4, 5, 6, 7); }
        }
#pragma unroll
        for (int k = 0; k < 2; ++k)
#pragma unroll
            for (int m = 0; m < 4; ++m)
#pragma unroll
                for (int n = 0; n < 2; ++n) acc[m][n] = __builtin_amdgcn_mfma_f32_16x16x32_bf16(Bt[n][k], At[m][k], acc[m][n], 0, 0, 0);
        asm volatile("s_waitcnt lgkmcnt(0)" ::: "memory");
        cur = cur + 1 == 3 ? 0 : cur + 1;
    }
#undef STG
    __syncthreads();
    LAS f32x4* red = (LAS f32x4*)shm_;
#pragma unroll
    for (int m = 0; m < 4; ++m)
#pragma unroll
        for (int n = 0; n < 2; ++n) red[((wave * 4 + m) * 2 + n) * 64 + lane] = acc[m][n];
    __syncthreads();
#pragma unroll
    for (int n = 0; n < 2; ++n) {
        const int m = wave;
        const f32x4 v = (red[((0 * 4 + m) * 2 + n) * 64 + lane] + red[((1 * 4 + m) * 2 + n) * 64 + lane]) + (red[((2 * 4 + m) * 2 + n) * 64 + lane] + red[((3 * 4 + m) * 2 + n) * 64 + lane]);
        const size_t off = (size_t)(row0 + m * 16 + fr) * DM + col0 + n * 16 + 4 * fq;
        *(u32x2*)(out + off) = combine4(v, *(const u32x2*)(x + off), *(const u32x2*)((const bf16_t*)(ws + WS_B) + off));
    }
}

extern "C" void kernel_launch(void* const* d_in, const int* in_sizes, int n_in, void* d_out, int out_size, void* d_ws, size_t ws_size, hipStream_t stream) {
    const bf16_t* x = (const bf16_t*)d_in[0];
    const bf16_t* mem0 = (const bf16_t*)d_in[1];
    const bf16_t* Wq = (const bf16_t*)d_in[2];
    const bf16_t* Wk = (const bf16_t*)d_in[3];
    const bf16_t* Wv = (const bf16_t*)d_in[4];
    const bf16_t* Wo = (const bf16_t*)d_in[5];
    const bf16_t* w1 = (const bf16_t*)d_in[8];
    const bf16_t* b1 = (const bf16_t*)d_in[9];
    const bf16_t* w2 = (const bf16_t*)d_in[10];
    const bf16_t* b2 = (const bf16_t*)d_in[11];
    char* ws = (char*)d_ws;
    bf16_t* out = (bf16_t*)d_out;
    (void)hipFuncSetAttribute((const void*)k_mega, hipFuncAttributeMaxDynamicSharedMemorySize, CH_LDS);
    (void)hipFuncSetAttribute((const void*)k_tail_gemm, hipFuncAttributeMaxDynamicSharedMemorySize, 4 * 36864);
    k_prep<<<S_LEN / 4, 256, 0, stream>>>(x, mem0, ws);
    k_mega<<<256, 512, CH_LDS, stream>>>(x, Wq, Wk, Wv, Wo, w1, b1, w2, b2, ws, out);
    k_tail_attn<<<64 * (8 - TAIL_BLK0), 256, 0, stream>>>(ws);
    k_tail_gemm<<<128 * (8 - TAIL_BLK0), 256, 4 * 36864, stream>>>(x, Wo, ws, out);
}
```

```cpp
#include <hip/hip_runtime.h>
#include <hip/hip_bf16.h>

#define LAS __attribute__((address_space(3)))
typedef unsigned short bf16_t;
typedef short bf16x8 __attribute__((ext_vector_type(8)));
typedef short bf16x4 __attribute__((ext_vector_type(4)));
typedef float f32x4 __attribute__((ext_vector_type(4)));
typedef float f32x2 __attribute__((ext_vector_type(2)));
typedef unsigned u32x4 __attribute__((ext_vector_type(4)));
typedef unsigned u32x2 __attribute__((ext_vector_type(2)));

constexpr int S_LEN = 2048, DM = 1024, NH = 32, DH = 128, HD = NH * DH, MM = 8, SEG = 32, NSEG = S_LEN / SEG, FF = 3584;
constexpr int AUX_SC1 = 16;
constexpr unsigned SENT = 0xFFFFFFFFu;

__device__ __forceinline__ float bf2f(unsigned b) { return __builtin_bit_cast(float, b << 16); }
typedef __bf16 bf16x2_t __attribute__((ext_vector_type(2)));
__device__ __forceinline__ unsigned pk_bf16(float lo, float hi) { const f32x2 v = {lo, hi}; return __builtin_bit_cast(unsigned, __builtin_convertvector(v, bf16x2_t)); }
__device__ __forceinline__ float rbf(float x) { return bf2f(pk_bf16(x, x) & 0xffffu); }
__device__ __forceinline__ unsigned desent(unsigned w) { return w == SENT ? 0x7FC07FC0u : w; }
__device__ __forceinline__ unsigned umax2(unsigned a, unsigned b) { return a > b ? a : b; }
__device__ __forceinline__ unsigned umax4(u32x4 v) { return umax2(umax2(v.x, v.y), umax2(v.z, v.w)); }

constexpr size_t WS_XN = 0;
constexpr size_t WS_Y = WS_XN + (size_t)S_LEN * DM * 2;
constexpr size_t WS_Q = WS_Y + (size_t)S_LEN * DM * 2;
constexpr size_t WS_K = WS_Q + (size_t)S_LEN * HD * 2;
constexpr size_t WS_V = WS_K + (size_t)S_LEN * HD * 2;
constexpr size_t WS_H = WS_V + (size_t)S_LEN * HD * 2;
constexpr size_t WS_O = WS_H + (size_t)S_LEN * FF * 2;
constexpr size_t WS_B = WS_O + (size_t)S_LEN * HD * 2;
constexpr size_t WS_SENT0 = WS_B + (size_t)S_LEN * DM * 2;
constexpr size_t MEM_B = (size_t)MM * DM * 2;
constexpr size_t WS_MEM = WS_SENT0;
constexpr size_t QKVM_B = (size_t)NH * 3 * MM * DH * 2;
constexpr size_t WS_QKVM = WS_MEM + (size_t)(NSEG + 1) * MEM_B;
constexpr size_t OM_B = (size_t)MM * HD * 2;
constexpr size_t WS_OM = WS_QKVM + (size_t)NSEG * QKVM_B;
constexpr size_t WS_SENT1 = WS_OM + (size_t)NSEG * OM_B;
constexpr size_t WS_CNT = WS_SENT1;
constexpr int CNT_H = 0;
constexpr int CNT_B = 8;
constexpr int CNT_O = 16;
constexpr int CNT_QKV = 24;
constexpr int CNT_FILL = 33;
constexpr int CNT_STEP = 32;
constexpr int CNT_KV = 40;
constexpr int CNT_KV0B = 40 + 256;
constexpr int CNT_N = 40 + 256 + 32;
constexpr size_t WS_TMO = WS_CNT + (size_t)CNT_N * 256;
constexpr size_t WS_END = WS_TMO + 256;

__device__ __forceinline__ float wave_sum(float v) {
#pragma unroll
    for (int o = 32; o > 0; o >>= 1) v += __shfl_xor(v, o);
    return v;
}
__device__ __forceinline__ void ln_row(float (&v)[16]) {
    float s = 0.f;
#pragma unroll
    for (int i = 0; i < 16; ++i) s += v[i];
    const float mean = wave_sum(s) * (1.0f / DM);
    float q = 0.f;
#pragma unroll
    for (int i = 0; i < 16; ++i) { const float d = v[i] - mean; q += d * d; }
    const float var = rbf(wave_sum(q) * (1.0f / DM));
    const float mb = rbf(mean);
    const float den = rbf(sqrtf(rbf(var + 1e-5f)));
#pragma unroll
    for (int i = 0; i < 16; ++i) v[i] = rbf(rbf(v[i] - mb) / den);
}
__global__ void __launch_bounds__(256) k_prep(const bf16_t* __restrict__ x, const bf16_t* __restrict__ mem0, char* __restrict__ ws) {
    const int wave = threadIdx.x >> 6, lane = threadIdx.x & 63, row = blockIdx.x * 4 + wave;
    const u32x4* src = (const u32x4*)(x + (size_t)row * DM);
    u32x4 a = src[lane], b = src[lane + 64];
    float v[16];
    const unsigned w[8] = {a.x, a.y, a.z, a.w, b.x, b.y, b.z, b.w};
#pragma unroll
    for (int i = 0; i < 8; ++i) { v[2 * i] = bf2f(w[i] & 0xffffu); v[2 * i + 1] = bf2f(w[i] >> 16); }
    ln_row(v);
    u32x4 o0, o1;
    o0.x = pk_bf16(v[0], v[1]); o0.y = pk_bf16(v[2], v[3]); o0.z = pk_bf16(v[4], v[5]); o0.w = pk_bf16(v[6], v[7]);
    o1.x = pk_bf16(v[8], v[9]); o1.y = pk_bf16(v[10], v[11]); o1.z = pk_bf16(v[12], v[13]); o1.w = pk_bf16(v[14], v[15]);
    u32x4* dx = (u32x4*)(ws + WS_XN + (size_t)row * DM * 2);
    dx[lane] = o0; dx[lane + 64] = o1;
    ln_row(v);
    o0.x = pk_bf16(v[0], v[1]); o0.y = pk_bf16(v[2], v[3]); o0.z = pk_bf16(v[4], v[5]); o0.w = pk_bf16(v[6], v[7]);
    o1.x = pk_bf16(v[8], v[9]); o1.y = pk_bf16(v[10], v[11]); o1.z = pk_bf16(v[12], v[13]); o1.w = pk_bf16(v[14], v[15]);
    u32x4* dy = (u32x4*)(ws + WS_Y + (size_t)row * DM * 2);
    dy[lane] = o0; dy[lane + 64] = o1;
    {
        const size_t n16 = (WS_SENT1 - WS_SENT0 - MEM_B) / 16;
        u32x4* dst = (u32x4*)(ws + WS_SENT0 + MEM_B);
        const u32x4 f = (u32x4){SENT, SENT, SENT, SENT};
        for (size_t i = (size_t)blockIdx.x * 256 + threadIdx.x; i < n16; i += (size_t)gridDim.x * 256) dst[i] = f;
    }
    if (blockIdx.x >= 8 && blockIdx.x < 8 + (CNT_N * 256 / 16 + 255) / 256) {
        const int i = (blockIdx.x - 8) * 256 + threadIdx.x; if (i < CNT_N * 256 / 16) ((u32x4*)(ws + WS_CNT))[i] = (u32x4){0u, 0u, 0u, 0u}; }
    if (blockIdx.x < 4) {
        const int c = blockIdx.x * 256 + threadIdx.x, r = c & 7, kg = c >> 3;
        u32x4 m = *(const u32x4*)(mem0 + (size_t)r * DM + kg * 8);
        m.x = desent(m.x); m.y = desent(m.y); m.z = desent(m.z); m.w = desent(m.w);
        *(u32x4*)(ws + WS_MEM + (size_t)c * 16) = m;
    }
}

namespace g8 {
constexpr int BM = 256, BK = 64, HALF = 128, HTB = HALF * BK * 2, STAGE_BYTES = 8 * HTB;
__host__ __device__ __forceinline__ int lds_byte(int r, int c) { const int st = (r >> 4) * 2 + (c >> 5), rr = r & 15, cc = c & 31, ob = rr * 64 + cc * 2; return st * 1024 + (ob ^ (((ob >> 9) & 1) << 5)); }
__host__ __device__ __forceinline__ void stage_rc(int b, int& R, int& C) { const int st = b / 1024, sb = b % 1024, swz = sb ^ (((sb >> 9) & 1) << 5); R = (st >> 1) * 16 + swz / 64; C = (st & 1) * 32 + (swz % 64) / 2; }
__host__ __device__ __forceinline__ int bsw(int row) { return (row & 3) | (((row >> 3) & 1) << 2); }
struct Unit { const bf16_t* A; const bf16_t* B; int pm, pn; };

template <class Epi, class Sched>
__device__ __forceinline__ void gemm_phase(LAS unsigned char* lds, const int K, const int lda, const int ldb, const Sched& S, const Epi& E) {
    int tid = threadIdx.x; asm volatile("" : "+v"(tid));
    const int wid = __builtin_amdgcn_readfirstlane(tid >> 6), lane = tid & 63, wr = wid >> 2, wc = wid & 3, fr = lane & 15, fq = lane >> 4;
    const int nt = K / BK;
    unsigned voffA[2], voffB[2];
#pragma unroll
    for (int i = 0; i < 2; ++i) { int R, C; stage_rc(tid * 16 + i * 8192, R, C); voffA[i] = (unsigned)(R * lda + C) * 2u;
        const int b = tid * 16 + i * 8192, row = b >> 8, ch = (b & 255) >> 4, seg = (ch >> 1) ^ bsw(row);
        voffB[i] = (unsigned)(row * ldb + seg * 16 + (ch & 1) * 8) * 2u; }
    const size_t kstepA = (size_t)(BK * 2), kstepB = (size_t)BK * ldb * 2;
    const size_t hstepA = (size_t)HALF * lda * 2, hstepB = (size_t)HALF * 2;
    const unsigned ldsw = (unsigned)wid * 1024u;
    const int aoff = lds_byte(wr * 64 + fr, fq * 8);
    const int q4 = (lane & 15) >> 2, p4 = lane & 3, sB = q4 | ((fq & 1) << 2);
    const int boff0 = (8 * fq + q4) * 256 + (((wc * 2 + 0) ^ sB) * 32) + 8 * p4;
    const int boff1 = (8 * fq + q4) * 256 + (((wc * 2 + 1) ^ sB) * 32) + 8 * p4;
#define G8_SA(b, h) (((b) * 2 + (h)) * HTB)
#define G8_SB(b, h) ((4 + (b) * 2 + (h)) * HTB)
#define G8_STAGE(bufoff, gbase, voff) do { _Pragma("unroll") for (int _i = 0; _i < 2; ++_i) \
        __builtin_amdgcn_global_load_lds((const unsigned*)((const char*)(gbase) + (voff)[_i]), (LAS unsigned*)(lds + (bufoff) + ldsw + _i * 8192), 16, 0, 0); } while (0)
#define G8_LDA(dst, b, h) do { _Pragma("unroll") for (int m = 0; m < 4; ++m) _Pragma("unroll") for (int k = 0; k < 2; ++k) dst[m][k] = *(const LAS bf16x8*)(lds + G8_SA(b, h) + aoff + m * 2048 + k * 1024); } while (0)
#define G8_TR(off) __builtin_amdgcn_ds_read_tr16_b64_v4i16((LAS bf16x4*)(lds + (off)))
#define G8_LDB(dst, b, h) do { _Pragma("unroll") for (int k = 0; k < 2; ++k) { \
        bf16x4 a0 = G8_TR(G8_SB(b, h) + boff0 + k * 8192), a1 = G8_TR(G8_SB(b, h) + boff0 + k * 8192 + 1024); \
        bf16x4 c0 = G8_TR(G8_SB(b, h) + boff1 + k * 8192), c1 = G8_TR(G8_SB(b, h) + boff1 + k * 8192 + 1024); \
        dst[0][k] = __builtin_shufflevector(a0, a1, 0, 1, 2, 3, 4, 5, 6, 7); dst[1][k] = __builtin_shufflevector(c0, c1, 0, 1, 2, 3, 4, 5, 6, 7); } } while (0)
#define G8_MMA(ai, bj, At, Bt) do { __builtin_amdgcn_s_setprio(1); _Pragma("unroll") for (int m = 0; m < 4; ++m) _Pragma("unroll") for (int n = 0; n < 2; ++n) _Pragma("unroll") for (int k = 0; k < 2; ++k) \
        acc[ai][bj][m][n] = __builtin_amdgcn_mfma_f32_16x16x32_bf16(Bt[n][k], At[m][k], acc[ai][bj][m][n], 0, 0, 0); __builtin_amdgcn_s_setprio(0); } while (0)
#define G8_WAIT_V(n) asm volatile("s_waitcnt vmcnt(" #n ")" ::: "memory")
#define G8_WAIT_L(n) asm volatile("s_waitcnt lgkmcnt(" #n ")" ::: "memory")
#define G8_BAR __builtin_amdgcn_s_barrier()
#define G8_SCHED __builtin_amdgcn_sched_barrier(0)
    Unit cur, nxt; int ui = 0;
    if (!S.next(0, cur)) return;
    f32x4 acc[2][2][4][2];
#pragma unroll
    for (int a = 0; a < 2; ++a)
#pragma unroll
        for (int b = 0; b < 2; ++b)
#pragma unroll
            for (int m = 0; m < 4; ++m)
#pragma unroll
                for (int n = 0; n < 2; ++n) acc[a][b][m][n] = (f32x4){0.f, 0.f, 0.f, 0.f};
    bf16x8 At[4][2], B0[2][2], B1[2][2];
    const char* cA = (const char*)cur.A; const char* cB = (const char*)cur.B;
    G8_STAGE(G8_SB(0, 0), cB, voffB); G8_STAGE(G8_SA(0, 0), cA, voffA); G8_STAGE(G8_SB(0, 1), cB + hstepB, voffB); G8_STAGE(G8_SA(0, 1), cA + hstepA, voffA);
    if (wr == 1) G8_BAR;
    G8_WAIT_V(4); G8_BAR;
    G8_STAGE(G8_SB(1, 0), cB + kstepB, voffB); G8_STAGE(G8_SA(1, 0), cA + kstepA, voffA); G8_STAGE(G8_SB(1, 1), cB + hstepB + kstepB, voffB);
    G8_WAIT_V(6); G8_BAR;
    for (;;) {
        const bool has_next = S.next(ui + 1, nxt);
        const char* nA = has_next ? (const char*)nxt.A : cA; const char* nB = has_next ? (const char*)nxt.B : cB;
        for (int t = 0; t < nt; t += 2) {
            const bool last = (t == nt - 2);
            const char* a1 = cA + (size_t)(t + 1) * kstepA;
            const char* a2 = last ? nA : cA + (size_t)(t + 2) * kstepA; const char* b2 = last ? nB : cB + (size_t)(t + 2) * kstepB;
            const char* a3 = a2 + kstepA; const char* b3 = b2 + kstepB;
            G8_LDB(B0, 0, 0); G8_SCHED; G8_LDA(At, 0, 0); G8_STAGE(G8_SA(1, 1), a1 + hstepA, voffA);
            G8_WAIT_L(8); G8_BAR; G8_WAIT_L(0); G8_MMA(0, 0, At, B0); G8_BAR; G8_SCHED;
            G8_LDB(B1, 0, 1); G8_STAGE(G8_SB(0, 0), b2, voffB);
            G8_BAR; G8_WAIT_L(0); G8_MMA(0, 1, At, B1); G8_BAR;
            G8_LDA(At, 0, 1); G8_STAGE(G8_SA(0, 0), a2, voffA);
            G8_BAR; G8_WAIT_L(0); G8_MMA(1, 0, At, B0); G8_BAR; G8_SCHED;
            G8_STAGE(G8_SB(0, 1), b2 + hstepB, voffB);
            G8_WAIT_V(6); G8_BAR; G8_MMA(1, 1, At, B1); G8_BAR;
            G8_LDB(B0, 1, 0); G8_SCHED; G8_LDA(At, 1, 0); G8_STAGE(G8_SA(0, 1), a2 + hstepA, voffA);
            G8_WAIT_L(8); G8_BAR; G8_WAIT_L(0); G8_MMA(0, 0, At, B0); G8_BAR; G8_SCHED;
            G8_LDB(B1, 1, 1); G8_STAGE(G8_SB(1, 0), b3, voffB);
            G8_BAR; G8_WAIT_L(0); G8_MMA(0, 1, At, B1); G8_BAR;
            G8_LDA(At, 1, 1); G8_STAGE(G8_SA(1, 0), a3, voffA);
            G8_BAR; G8_WAIT_L(0); G8_MMA(1, 0, At, B0); G8_BAR; G8_SCHED;
            G8_STAGE(G8_SB(1, 1), b3 + hstepB, voffB);
            G8_WAIT_V(6); G8_BAR; G8_MMA(1, 1, At, B1); G8_BAR;
        }
        E(acc, cur, wr, wc, fr, fq);
        if (!has_next) break;
#pragma unroll
        for (int a = 0; a < 2; ++a)
#pragma unroll
            for (int b = 0; b < 2; ++b)
#pragma unroll
                for (int m = 0; m < 4; ++m)
#pragma unroll
                    for (int n = 0; n < 2; ++n) acc[a][b][m][n] = (f32x4){0.f, 0.f, 0.f, 0.f};
        cur = nxt; cA = nA; cB = nB; ++ui;
    }
    G8_WAIT_V(0);
    if (wr == 0) G8_BAR;
    G8_BAR;
#undef G8_SA
#undef G8_SB
#undef G8_STAGE
#undef G8_LDA
#undef G8_LDB
#undef G8_TR
#undef G8_MMA
#undef G8_WAIT_V
#undef G8_WAIT_L
#undef G8_BAR
#undef G8_SCHED
}
}

__device__ __forceinline__ float gelu_tanh(float x) {
    const float u = 0.7978845608028654f * (x + 0.044715f * x * x * x);
    const float th = 1.0f - 2.0f * __builtin_amdgcn_rcpf(__expf(2.0f * u) + 1.0f);
    return 0.5f * x * (1.0f + th);
}

__device__ __forceinline__ void wait_counter(const char* ws, int idx, unsigned need) {
    if (threadIdx.x < 64) {
        const unsigned* p = (const unsigned*)(ws + WS_CNT + (size_t)idx * 256);
        for (unsigned spins = 0; (unsigned)__builtin_amdgcn_readfirstlane(__hip_atomic_load(p, __ATOMIC_RELAXED, __HIP_MEMORY_SCOPE_AGENT)) < need;) {
            __builtin_amdgcn_s_sleep(8);
            if (++spins > 400000u) break;
        }
        __builtin_amdgcn_fence(__ATOMIC_ACQUIRE, "agent");
        asm volatile("s_waitcnt vmcnt(0)" ::: "memory");
    }
    asm volatile("" ::: "memory"); __builtin_amdgcn_s_barrier(); asm volatile("" ::: "memory");
}
__device__ __forceinline__ void signal_counter(char* ws, int idx) {
    asm volatile("s_waitcnt vmcnt(0)" ::: "memory");
    if ((threadIdx.x & 63) == 0) __hip_atomic_fetch_add((unsigned*)(ws + WS_CNT + (size_t)idx * 256), 1u, __ATOMIC_RELAXED, __HIP_MEMORY_SCOPE_AGENT);
}

__device__ __forceinline__ unsigned poll_ge(const char* ws, int idx, unsigned need) {
    const unsigned* p = (const unsigned*)(ws + WS_CNT + (size_t)idx * 256);
    unsigned v = 0;
    for (unsigned spins = 0; (v = (unsigned)__builtin_amdgcn_readfirstlane(__hip_atomic_load(p, __ATOMIC_RELAXED, __HIP_MEMORY_SCOPE_AGENT))) < need;) {
        __builtin_amdgcn_s_sleep(8);
        if (++spins > 400000u) break;
    }
    asm volatile("" ::: "memory");
    return v;
}

constexpr unsigned SPIN_CAP = 200000u;
constexpr int KS_PITCH = 272;
constexpr int CH_LDS = 160 * 1024;

__device__ __forceinline__ float xmax16_32(float v) {
    auto a = __builtin_amdgcn_permlane16_swap(__builtin_bit_cast(unsigned, v), __builtin_bit_cast(unsigned, v), false, false);
    v = fmaxf(__builtin_bit_cast(float, (unsigned)a[0]), __builtin_bit_cast(float, (unsigned)a[1]));
    auto b = __builtin_amdgcn_permlane32_swap(__builtin_bit_cast(unsigned, v), __builtin_bit_cast(unsigned, v), false, false);
    return fmaxf(__builtin_bit_cast(float, (unsigned)b[0]), __builtin_bit_cast(float, (unsigned)b[1]));
}
__device__ __forceinline__ float xsum16_32(float v) {
    auto a = __builtin_amdgcn_permlane16_swap(__builtin_bit_cast(unsigned, v), __builtin_bit_cast(unsigned, v), false, false);
    v = __builtin_bit_cast(float, (unsigned)a[0]) + __builtin_bit_cast(float, (unsigned)a[1]);
    auto b = __builtin_amdgcn_permlane32_swap(__builtin_bit_cast(unsigned, v), __builtin_bit_cast(unsigned, v), false, false);
    return __builtin_bit_cast(float, (unsigned)b[0]) + __builtin_bit_cast(float, (unsigned)b[1]);
}

__device__ __forceinline__ u32x4 quad_gather(unsigned v) {
    u32x4 r;
    r.x = (unsigned)__builtin_amdgcn_update_dpp(0, (int)v, 0x00, 0xf, 0xf, true); r.y = (unsigned)__builtin_amdgcn_update_dpp(0, (int)v, 0x55, 0xf, 0xf, true);
    r.z = (unsigned)__builtin_amdgcn_update_dpp(0, (int)v, 0xAA, 0xf, 0xf, true); r.w = (unsigned)__builtin_amdgcn_update_dpp(0, (int)v, 0xFF, 0xf, 0xf, true);
    return r;
}
#define STAMP(i) do {} while (0)
#define PCOUNT(sh) do {} while (0)
#define CH_DELAY1 12
#define CH_DELAY2 6
#define CH_DELAY3 8

template <int CPH>
__device__ __forceinline__ void chain_body(LAS unsigned char* lds, const int b, const bf16_t* __restrict__ Wq, const bf16_t* __restrict__ Wk, const bf16_t* __restrict__ Wv,
                                           const bf16_t* __restrict__ Wo, char* ws) {
    constexpr int ES = 128 / CPH, NE = ES / 16, NT = 3 * NE, RG = CPH / 2, RPG = 8 / RG;
    constexpr int VP = ES * 2;
    constexpr int L_RED = 0;
    constexpr int L_RED2 = L_RED + 8 * NT * 512;
    constexpr int L_KS = L_RED2 + 8 * RPG * 64;
    constexpr int L_VS = L_KS + 2 * 32 * KS_PITCH;
    constexpr int L_OS = L_VS + 2 * 48 * VP;
    constexpr bool WOL = (CPH == 4);
    constexpr int L_WO = L_OS + 8 * 1024 * RPG;
    static_assert(L_WO + (WOL ? 65536 : 0) <= 156 * 1024, "LDS");
    const int tid = threadIdx.x, wave = __builtin_amdgcn_readfirstlane(tid >> 6), lane = tid & 63, fr = lane & 15, fq = lane >> 4;
    const int xc = b & 7, yc = b >> 3;
    const int h = xc + 8 * (yc / CPH), j = yc % CPH, g = yc % RG, c = xc * 8 + yc / RG;
    const __amdgpu_buffer_rsrc_t rs = __builtin_amdgcn_make_buffer_rsrc(ws + WS_SENT0, 0, (int)(WS_SENT1 - WS_SENT0), 0x00020000);
    constexpr int O_MEM = (int)(WS_MEM - WS_SENT0), O_QKVM = (int)(WS_QKVM - WS_SENT0), O_OM = (int)(WS_OM - WS_SENT0);
    const __amdgpu_buffer_rsrc_t rkv = __builtin_amdgcn_make_buffer_rsrc(ws + WS_K, 0, (int)(WS_H - WS_K), 0x00020000);
    constexpr int O_V = (int)(WS_V - WS_K);
    bool dead = false;

    bf16x8 wq[NT][4], wo[16];
    {
        LAS unsigned char* stg = lds + wave * 8192;
        constexpr int CPR = ES / 8;
        static_assert(128 * CPR / 64 == 8, "8 chunks per lane and pass");
        u32x4 t[5][8];
        auto issue = [&](int ps) {
            if (ps < 3) { const bf16_t* W = (ps == 0 ? Wq : (ps == 1 ? Wk : Wv)) + (size_t)wave * 128 * HD + h * DH + j * ES;
#pragma unroll
                for (int i = 0; i < 8; ++i) { const int ci = i * 64 + lane; t[ps][i] = *(const u32x4*)(W + (size_t)(ci / CPR) * HD + (ci % CPR) * 8); } }
            else { const bf16_t* W = Wo + (size_t)(wave * 512 + (ps - 3) * 256) * DM + c * 16;
#pragma unroll
                for (int i = 0; i < 8; ++i) { const int ci = i * 64 + lane; t[ps][i] = *(const u32x4*)(W + (size_t)(ci >> 1) * DM + (ci & 1) * 8); } }
        };
        issue(0); issue(1); issue(2);
        asm volatile("" ::: "memory");
#pragma unroll
        for (int ps = 0; ps < 5; ++ps) {
#pragma unroll
            for (int i = 0; i < 8; ++i) *(LAS u32x4*)(stg + (i * 64 + lane) * 16) = t[ps][i];
            asm volatile("s_waitcnt lgkmcnt(0)" ::: "memory");
            if (ps + 3 < 5) issue(ps + 3);
            asm volatile("" ::: "memory");
            if (ps < 3) {
#pragma unroll
                for (int e = 0; e < NE; ++e)
#pragma unroll
                    for (int i = 0; i < 4; ++i) {
                        const int off = (i * 32 + 8 * fq + (fr >> 2)) * (ES * 2) + e * 32 + (lane & 3) * 8;
                        const bf16x4 a0 = __builtin_amdgcn_ds_read_tr16_b64_v4i16((LAS bf16x4*)(stg + off)), a1 = __builtin_amdgcn_ds_read_tr16_b64_v4i16((LAS bf16x4*)(stg + off + 4 * ES * 2));
                        wq[ps * NE + e][i] = __builtin_shufflevector(a0, a1, 0, 1, 2, 3, 4, 5, 6, 7);
                    }
            } else {
#pragma unroll
                for (int i = 0; i < 8; ++i) {
                    const int off = (i * 32 + 8 * fq + (fr >> 2)) * 32 + (lane & 3) * 8;
                    const bf16x4 a0 = __builtin_amdgcn_ds_read_tr16_b64_v4i16((LAS bf16x4*)(stg + off)), a1 = __builtin_amdgcn_ds_read_tr16_b64_v4i16((LAS bf16x4*)(stg + off + 128));
                    wo[(ps - 3) * 8 + i] = __builtin_shufflevector(a0, a1, 0, 1, 2, 3, 4, 5, 6, 7);
                }
            }
            asm volatile("s_waitcnt lgkmcnt(0)" ::: "memory");
        }
        if (WOL) {
#pragma unroll
            for (int i = 8; i < 16; ++i) *(LAS bf16x8*)(lds + L_WO + (wave * 8 + i - 8) * 1024 + lane * 16) = wo[i];
        }
    }
    __syncthreads();

    if (tid == 0) *(LAS unsigned*)(lds + L_OS) = 0u;
    if (tid < 2 * 8 * VP / 16) { const int bf = tid / (8 * VP / 16), r = tid % (8 * VP / 16); *(LAS u32x4*)(lds + L_VS + bf * 48 * VP + 40 * VP + r * 16) = (u32x4){0u, 0u, 0u, 0u}; }
    {
        poll_ge(ws, CNT_KV + (h >> 1), 16u); poll_ge(ws, CNT_KV + 16 + (h >> 1), 16u);
        const int r = tid >> 4, ch = tid & 15;
        *(LAS u32x4*)(lds + L_KS + r * KS_PITCH + ch * 16) = __builtin_amdgcn_raw_buffer_load_b128(rkv, (r * HD + h * DH + ch * 8) * 2, 0, AUX_SC1);
        if (tid < 32 * ES / 8) { const int r2 = tid / (ES / 8), c2 = tid % (ES / 8);
            *(LAS u32x4*)(lds + L_VS + (8 + r2) * VP + c2 * 16) = __builtin_amdgcn_raw_buffer_load_b128(rkv, O_V + (r2 * HD + h * DH + j * ES + c2 * 8) * 2, 0, AUX_SC1); }
    }
    __syncthreads();

    for (int s = 0; s < NSEG; ++s) {
        const int kb = s & 1;
        unsigned long long pcnt = 0; (void)pcnt;
        STAMP(0);
        {
            if (s > 0) __builtin_amdgcn_s_sleep(CH_DELAY1);
            u32x4 a[4];
            const int base = O_MEM + s * (int)MEM_B + wave * 2048 + fq * 128 + (lane & 7) * 16;
            for (unsigned spins = 0;;) {
                asm volatile("" ::: "memory");
#pragma unroll
                for (int i = 0; i < 4; ++i) a[i] = __builtin_amdgcn_raw_buffer_load_b128(rs, base + i * 512, 0, AUX_SC1);
                unsigned mxs = 0;
#pragma unroll
                for (int i = 0; i < 4; ++i) mxs = umax2(mxs, umax4(a[i]));
                if (!__any(mxs == SENT) || dead) break;
                PCOUNT(0);
                if (++spins > SPIN_CAP) { dead = true; if (lane == 0) *(volatile unsigned*)(ws + WS_TMO) = 1u; break; }
            }
            STAMP(1);
            if (b == 0 && tid == 448 && (s & 7) == 0) __hip_atomic_store((unsigned*)(ws + WS_CNT + (size_t)CNT_STEP * 256), (unsigned)s, __ATOMIC_RELAXED, __HIP_MEMORY_SCOPE_AGENT);
            constexpr int NQK = 2 * NE;
            {
                f32x4 acc[NQK];
#pragma unroll
                for (int t = 0; t < NQK; ++t) acc[t] = (f32x4){0.f, 0.f, 0.f, 0.f};
#pragma unroll
                for (int i = 0; i < 4; ++i)
#pragma unroll
                    for (int t = 0; t < NQK; ++t) acc[t] = __builtin_amdgcn_mfma_f32_16x16x32_bf16(wq[t][i], __builtin_bit_cast(bf16x8, a[i]), acc[t], 0, 0, 0);
                if (fr < 8) {
#pragma unroll
                    for (int t = 0; t < NQK; ++t) *(LAS f32x4*)(lds + L_RED + ((wave * NT + t) * 128 + fr * 16 + fq * 4) * 4) = acc[t];
                }
            }
            __syncthreads();
            if (tid < NQK * 64) {
                const int t = tid >> 6, m = (tid & 63) >> 3, np = (tid & 7) * 2, mat = t / NE, col = (t % NE) * 16 + np;
                f32x2 sum = (f32x2){0.f, 0.f};
#pragma unroll
                for (int w = 0; w < 8; ++w) sum += *(const LAS f32x2*)(lds + L_RED + ((w * NT + t) * 128 + m * 16 + np) * 4);
                const u32x4 q4 = quad_gather(desent(pk_bf16(sum.x, sum.y)));
                if ((tid & 3) == 0) __builtin_amdgcn_raw_buffer_store_b128(q4, rs, O_QKVM + s * (int)QKVM_B + ((h * 3 + mat) * 8 + m) * 256 + (j * ES + col) * 2, 0, AUX_SC1);
            }
            STAMP(2);
            {
                f32x4 acc[NE];
#pragma unroll
                for (int t = 0; t < NE; ++t) acc[t] = (f32x4){0.f, 0.f, 0.f, 0.f};
#pragma unroll
                for (int i = 0; i < 4; ++i)
#pragma unroll
                    for (int t = 0; t < NE; ++t) acc[t] = __builtin_amdgcn_mfma_f32_16x16x32_bf16(wq[NQK + t][i], __builtin_bit_cast(bf16x8, a[i]), acc[t], 0, 0, 0);
                if (fr < 8) {
#pragma unroll
                    for (int t = 0; t < NE; ++t) *(LAS f32x4*)(lds + L_RED + ((wave * NT + NQK + t) * 128 + fr * 16 + fq * 4) * 4) = acc[t];
                }
            }
        }
        __syncthreads();
        if (tid >= 2 * NE * 64 && tid < NT * 64) {
            const int t = tid >> 6, m = (tid & 63) >> 3, np = (tid & 7) * 2, col = (t % NE) * 16 + np;
            f32x2 sum = (f32x2){0.f, 0.f};
#pragma unroll
            for (int w = 0; w < 8; ++w) sum += *(const LAS f32x2*)(lds + L_RED + ((w * NT + t) * 128 + m * 16 + np) * 4);
            const unsigned pk = desent(pk_bf16(sum.x, sum.y));
            const u32x4 q4 = quad_gather(pk);
            if ((tid & 3) == 0) __builtin_amdgcn_raw_buffer_store_b128(q4, rs, O_QKVM + s * (int)QKVM_B + ((h * 3 + 2) * 8 + m) * 256 + (j * ES + col) * 2, 0, AUX_SC1);
            *(LAS unsigned*)(lds + L_VS + kb * 48 * VP + m * VP + col * 2) = pk;
            asm volatile("s_waitcnt lgkmcnt(0)" ::: "memory");
            if (lane == 0) __hip_atomic_fetch_add((LAS unsigned*)(lds + L_OS), 1u, __ATOMIC_RELAXED, __HIP_MEMORY_SCOPE_WORKGROUP);
        }
        if (s == NSEG - 1) break;
        if (wave == 0) {
            bf16x8 kx[3][4];
#pragma unroll
            for (int t = 0; t < 3; ++t) {
                int r = t * 16 + fr - 8; r = r < 0 ? 0 : (r > 31 ? 31 : r);
#pragma unroll
                for (int i = 0; i < 4; ++i) kx[t][i] = *(const LAS bf16x8*)(lds + L_KS + kb * 32 * KS_PITCH + r * KS_PITCH + i * 64 + fq * 16);
            }
            __builtin_amdgcn_s_sleep(CH_DELAY2);
            u32x4 qf[4], km[4];
            const int base = O_QKVM + s * (int)QKVM_B + h * 3 * 2048 + (lane & 7) * 256 + fq * 16;
            for (unsigned spins = 0;;) {
                asm volatile("" ::: "memory");
#pragma unroll
                for (int i = 0; i < 4; ++i) { qf[i] = __builtin_amdgcn_raw_buffer_load_b128(rs, base + i * 64, 0, AUX_SC1); km[i] = __builtin_amdgcn_raw_buffer_load_b128(rs, base + 2048 + i * 64, 0, AUX_SC1); }
                unsigned mxs = 0;
#pragma unroll
                for (int i = 0; i < 4; ++i) mxs = umax2(mxs, umax2(umax4(qf[i]), umax4(km[i])));
                if (!__any(mxs == SENT) || dead) break;
                PCOUNT(16);
                if (++spins > SPIN_CAP) { dead = true; if (lane == 0) *(volatile unsigned*)(ws + WS_TMO) = 2u; break; }
            }
            STAMP(3);
            f32x4 sc[3];
#pragma unroll
            for (int t = 0; t < 3; ++t) sc[t] = (f32x4){0.f, 0.f, 0.f, 0.f};
#pragma unroll
            for (int i = 0; i < 4; ++i) {
                const bf16x8 k0 = fr < 8 ? __builtin_bit_cast(bf16x8, km[i]) : kx[0][i];
                sc[1] = __builtin_amdgcn_mfma_f32_16x16x32_bf16(kx[1][i], __builtin_bit_cast(bf16x8, qf[i]), sc[1], 0, 0, 0);
                sc[2] = __builtin_amdgcn_mfma_f32_16x16x32_bf16(kx[2][i], __builtin_bit_cast(bf16x8, qf[i]), sc[2], 0, 0, 0);
                sc[0] = __builtin_amdgcn_mfma_f32_16x16x32_bf16(k0, __builtin_bit_cast(bf16x8, qf[i]), sc[0], 0, 0, 0);
            }
            for (unsigned sp = 0; __hip_atomic_load((LAS unsigned*)(lds + L_OS), __ATOMIC_RELAXED, __HIP_MEMORY_SCOPE_WORKGROUP) < (unsigned)(NE * (s + 1)) && sp < SPIN_CAP; ++sp) {}
            asm volatile("" ::: "memory");
            bf16x8 vf[NE][2];
            const bf16x4 zz = (bf16x4){0, 0, 0, 0};
#pragma unroll
            for (int et = 0; et < NE; ++et) {
                const int voff = L_VS + kb * 48 * VP + (4 * fq + (fr >> 2)) * VP + et * 32 + (lane & 3) * 8;
                const bf16x4 v0 = __builtin_amdgcn_ds_read_tr16_b64_v4i16((LAS bf16x4*)(lds + voff));
                const bf16x4 v1 = __builtin_amdgcn_ds_read_tr16_b64_v4i16((LAS bf16x4*)(lds + voff + 16 * VP));
                const bf16x4 v2 = __builtin_amdgcn_ds_read_tr16_b64_v4i16((LAS bf16x4*)(lds + voff + 32 * VP));
                vf[et][0] = __builtin_shufflevector(v0, v1, 0, 1, 2, 3, 4, 5, 6, 7); vf[et][1] = __builtin_shufflevector(v2, zz, 0, 1, 2, 3, 4, 5, 6, 7);
            }
            const float scale = 0.08838834764831845f;
            const bool hi = fr >= 8;
            float v[6];
            v[0] = hi ? sc[1][2] : sc[0][0]; v[1] = hi ? sc[1][3] : sc[0][1]; v[2] = hi ? sc[2][0] : sc[0][2];
            v[3] = hi ? sc[2][1] : sc[0][3]; v[4] = hi ? sc[2][2] : sc[1][0]; v[5] = hi ? sc[2][3] : sc[1][1];
            float mx = -INFINITY;
#pragma unroll
            for (int i = 0; i < 6; ++i) { float z = rbf(rbf(v[i]) * scale); if (i >= 2 && hi && fq >= 2) z = -INFINITY; v[i] = z; mx = fmaxf(mx, z); }
            mx = fmaxf(mx, __builtin_bit_cast(float, __builtin_amdgcn_update_dpp(0, __builtin_bit_cast(int, mx), 0x128, 0xf, 0xf, true)));
            mx = xmax16_32(mx);
            float sum = 0.f;
#pragma unroll
            for (int i = 0; i < 6; ++i) { v[i] = __expf(v[i] - mx); sum += v[i]; }
            sum += __builtin_bit_cast(float, __builtin_amdgcn_update_dpp(0, __builtin_bit_cast(int, sum), 0x128, 0xf, 0xf, true));
            sum = xsum16_32(sum);
            const float inv = 1.0f / sum;
            const unsigned w0 = pk_bf16(v[0] * inv, v[1] * inv), w1 = pk_bf16(v[2] * inv, v[3] * inv), w2 = pk_bf16(v[4] * inv, v[5] * inv);
            u32x4 p0, p1;
            p0.x = w0; p0.y = w1; p0.z = w2; p0.w = (unsigned)__builtin_amdgcn_update_dpp(0, (int)w0, 0x128, 0xf, 0xf, true);
            p1.x = (unsigned)__builtin_amdgcn_update_dpp(0, (int)w1, 0x128, 0xf, 0xf, true); p1.y = (unsigned)__builtin_amdgcn_update_dpp(0, (int)w2, 0x128, 0xf, 0xf, true); p1.z = 0u; p1.w = 0u;
#pragma unroll
            for (int et = 0; et < NE; ++et) {
                f32x4 o = (f32x4){0.f, 0.f, 0.f, 0.f};
                o = __builtin_amdgcn_mfma_f32_16x16x32_bf16(vf[et][0], __builtin_bit_cast(bf16x8, p0), o, 0, 0, 0);
                o = __builtin_amdgcn_mfma_f32_16x16x32_bf16(vf[et][1], __builtin_bit_cast(bf16x8, p1), o, 0, 0, 0);
                {
                    const unsigned wx = desent(pk_bf16(o[0], o[1])), wy = desent(pk_bf16(o[2], o[3]));
                    auto sx = __builtin_amdgcn_permlane16_swap(wx, wx, false, false); auto sy = __builtin_amdgcn_permlane16_swap(wy, wy, false, false);
                    const u32x4 w = (u32x4){(unsigned)sx[0], (unsigned)sy[0], (unsigned)sx[1], (unsigned)sy[1]};
                    const int k = h * DH + j * ES + et * 16 + 4 * fq;
                    const int off = O_OM + s * (int)OM_B + (fr / RPG) * (8192 * RPG) + (k >> 5) * (64 * RPG) + ((k >> 3) & 3) * (16 * RPG) + (fr % RPG) * 16;
                    if (fr < 8 && (fq & 1) == 0) __builtin_amdgcn_raw_buffer_store_b128(w, rs, off, 0, AUX_SC1);
                }
            }
            STAMP(4);
        } else {
            const int t2 = tid - 64; const int nb = kb ^ 1; const int row0 = (s + 1) * SEG;
            if (((s + 1) & 7) == 0) { const int blk = (s + 1) >> 3; poll_ge(ws, CNT_KV + blk * 32 + (h >> 1), 8u); poll_ge(ws, CNT_KV + blk * 32 + 16 + (h >> 1), 8u); }
            if (s + 1 == 4) { poll_ge(ws, CNT_KV0B + (h >> 1), 16u); poll_ge(ws, CNT_KV0B + 16 + (h >> 1), 16u); }
            for (int i = t2; i < 512 + 32 * ES / 8; i += 448) {
                if (i < 512) { const int r = i >> 4, ch = i & 15;
                    *(LAS u32x4*)(lds + L_KS + nb * 32 * KS_PITCH + r * KS_PITCH + ch * 16) = __builtin_amdgcn_raw_buffer_load_b128(rkv, ((row0 + r) * HD + h * DH + ch * 8) * 2, 0, AUX_SC1); }
                else { const int r2 = (i - 512) / (ES / 8), c2 = (i - 512) % (ES / 8);
                    *(LAS u32x4*)(lds + L_VS + nb * 48 * VP + (8 + r2) * VP + c2 * 16) = __builtin_amdgcn_raw_buffer_load_b128(rkv, O_V + ((row0 + r2) * HD + h * DH + j * ES + c2 * 8) * 2, 0, AUX_SC1); }
            }
        }
        __syncthreads();
        {
            if (WOL) {
#pragma unroll
                for (int i = 8; i < 16; ++i) wo[i] = *(const LAS bf16x8*)(lds + L_WO + (wave * 8 + i - 8) * 1024 + lane * 16);
            }
            __builtin_amdgcn_s_sleep(CH_DELAY3);
            u32x4 a[RPG];
            constexpr int KPG = 16 / RPG;
            const int base = O_OM + s * (int)OM_B + g * (8192 * RPG) + wave * (1024 * RPG) + (fr / RPG) * (64 * RPG) + fq * (16 * RPG) + (fr % RPG) * 16;
            for (unsigned spins = 0;;) {
                asm volatile("" ::: "memory");
#pragma unroll
                for (int i = 0; i < RPG; ++i) a[i] = __builtin_amdgcn_raw_buffer_load_b128(rs, base + i * 1024, 0, AUX_SC1);
                unsigned mxs = 0;
#pragma unroll
                for (int i = 0; i < RPG; ++i) mxs = umax2(mxs, umax4(a[i]));
                if (!__any(mxs == SENT) || dead) break;
                PCOUNT(32);
                if (++spins > SPIN_CAP) { dead = true; if (lane == 0) *(volatile unsigned*)(ws + WS_TMO) = 3u; break; }
            }
            STAMP(5);
            f32x4 acc[KPG];
#pragma unroll
            for (int q = 0; q < KPG; ++q) acc[q] = (f32x4){0.f, 0.f, 0.f, 0.f};
#pragma unroll
            for (int gq = 0; gq < RPG; ++gq)
#pragma unroll
                for (int q = 0; q < KPG; ++q) acc[q] = __builtin_amdgcn_mfma_f32_16x16x32_bf16(wo[gq * KPG + q], __builtin_bit_cast(bf16x8, a[gq]), acc[q], 0, 0, 0);
            f32x4 r = acc[0];
#pragma unroll
            for (int q = 1; q < KPG; ++q) if (fr / RPG == q) r = acc[q];
#pragma unroll
            for (int e = 0; e < 4; ++e) {
                if (RPG <= 2) r[e] += __builtin_bit_cast(float, __builtin_amdgcn_update_dpp(0, __builtin_bit_cast(int, r[e]), 0x122, 0xf, 0xf, true));
                r[e] += __builtin_bit_cast(float, __builtin_amdgcn_update_dpp(0, __builtin_bit_cast(int, r[e]), 0x124, 0xf, 0xf, true));
                r[e] += __builtin_bit_cast(float, __builtin_amdgcn_update_dpp(0, __builtin_bit_cast(int, r[e]), 0x128, 0xf, 0xf, true));
            }
            if (fr < RPG) *(LAS f32x4*)(lds + L_RED2 + ((wave * RPG + fr) * 16 + fq * 4) * 4) = r;
        }
        __syncthreads();
        if (tid < RPG * 8) {
            const int m = tid >> 3, np = (tid & 7) * 2;
            f32x2 sum = (f32x2){0.f, 0.f};
#pragma unroll
            for (int w = 0; w < 8; ++w) sum += *(const LAS f32x2*)(lds + L_RED2 + ((w * RPG + m) * 16 + np) * 4);
            const unsigned pk = desent(pk_bf16(sum.x, sum.y));
            const int off = O_MEM + (s + 1) * (int)MEM_B + (c >> 1) * 512 + ((c & 1) * 2 + (np >> 3)) * 128 + (g * RPG + m) * 16;
            const u32x4 q4 = quad_gather(pk);
            if ((tid & 3) == 0) __builtin_amdgcn_raw_buffer_store_b128(q4, rs, off, 0, AUX_SC1);
        }
        STAMP(6);
    }
}

template <int MT, class Epi>
__device__ __forceinline__ void gemm128_w8(LAS unsigned char* lds, const bf16_t* A, const int lda, const bf16_t* B, const int ldb, const int K, const Epi& E) {
    int tid = threadIdx.x; asm volatile("" : "+v"(tid));
    const int wid = __builtin_amdgcn_readfirstlane(tid >> 6), lane = tid & 63, wr = wid >> 2, wc = wid & 3, fr = lane & 15, fq = lane >> 4;
    constexpr int NA = MT / 2;
    unsigned voffA[NA], voffB[2];
#pragma unroll
    for (int i = 0; i < NA; ++i) { int R, C; g8::stage_rc(tid * 16 + i * 8192, R, C); voffA[i] = (unsigned)(R * lda + C) * 2u; }
#pragma unroll
    for (int i = 0; i < 2; ++i) { const int bb = tid * 16 + i * 8192, row = bb >> 8, ch = (bb & 255) >> 4, seg = (ch >> 1) ^ g8::bsw(row);
        voffB[i] = (unsigned)(row * ldb + seg * 16 + (ch & 1) * 8) * 2u; }
    const char* cA = (const char*)A; const char* cB = (const char*)B;
    const unsigned ldsw = (unsigned)wid * 1024u;
    const int aoff = g8::lds_byte(wr * 16 * MT + fr, fq * 8);
    const int q4 = (lane & 15) >> 2, p4 = lane & 3, sB = q4 | ((fq & 1) << 2);
    const int boff0 = (8 * fq + q4) * 256 + (((wc * 2 + 0) ^ sB) * 32) + 8 * p4;
    const int boff1 = (8 * fq + q4) * 256 + (((wc * 2 + 1) ^ sB) * 32) + 8 * p4;
    f32x4 acc[MT][2];
#pragma unroll
    for (int m = 0; m < MT; ++m) { acc[m][0] = (f32x4){0.f, 0.f, 0.f, 0.f}; acc[m][1] = (f32x4){0.f, 0.f, 0.f, 0.f}; }
    const int nt = K / 64;
#define STG(buf, t) do { _Pragma("unroll") for (int _i = 0; _i < NA; ++_i) \
        __builtin_amdgcn_global_load_lds((const unsigned*)(cA + (size_t)(t) * 128 + voffA[_i]), (LAS unsigned*)(lds + (buf) * 32768 + ldsw + _i * 8192), 16, 0, 0); \
        _Pragma("unroll") for (int _i = 0; _i < 2; ++_i) \
        __builtin_amdgcn_global_load_lds((const unsigned*)(cB + (size_t)(t) * 64 * ldb * 2 + voffB[_i]), (LAS unsigned*)(lds + (buf) * 32768 + 16384 + ldsw + _i * 8192), 16, 0, 0); } while (0)
    STG(0, 0); STG(1, 1); STG(2, 2);
    int cur = 0;
    for (int t = 0; t < nt; ++t) {
        if (MT == 4) { if (t + 2 < nt) asm volatile("s_waitcnt vmcnt(8)" ::: "memory"); else if (t + 1 < nt) asm volatile("s_waitcnt vmcnt(4)" ::: "memory"); else asm volatile("s_waitcnt vmcnt(0)" ::: "memory"); }
        else { if (t + 2 < nt) asm volatile("s_waitcnt vmcnt(6)" ::: "memory"); else if (t + 1 < nt) asm volatile("s_waitcnt vmcnt(3)" ::: "memory"); else asm volatile("s_waitcnt vmcnt(0)" ::: "memory"); }
        __builtin_amdgcn_s_barrier();
        asm volatile("" ::: "memory");
        if (t + 3 < nt) STG((cur + 3) & 3, t + 3);
        bf16x8 At[MT][2], Bt[2][2];
        LAS unsigned char* sa = lds + cur * 32768; LAS unsigned char* sb = sa + 16384;
#pragma unroll
        for (int k = 0; k < 2; ++k) {
            bf16x4 a0 = __builtin_amdgcn_ds_read_tr16_b64_v4i16((LAS bf16x4*)(sb + boff0 + k * 8192)), a1 = __builtin_amdgcn_ds_read_tr16_b64_v4i16((LAS bf16x4*)(sb + boff0 + k * 8192 + 1024));
            bf16x4 c0 = __builtin_amdgcn_ds_read_tr16_b64_v4i16((LAS bf16x4*)(sb + boff1 + k * 8192)), c1 = __builtin_amdgcn_ds_read_tr16_b64_v4i16((LAS bf16x4*)(sb + boff1 + k * 8192 + 1024));
            Bt[0][k] = __builtin_shufflevector(a0, a1, 0, 1, 2, 3, 4, 5, 6, 7); Bt[1][k] = __builtin_shufflevector(c0, c1, 0, 1, 2, 3, 4, 5, 6, 7);
#pragma unroll
            for (int m = 0; m < MT; ++m) At[m][k] = *(const LAS bf16x8*)(sa + aoff + m * 2048 + k * 1024);
        }
        __builtin_amdgcn_s_setprio(1);
#pragma unroll
        for (int k = 0; k < 2; ++k)
#pragma unroll
            for (int m = 0; m < MT; ++m)
#pragma unroll
                for (int n = 0; n < 2; ++n) acc[m][n] = __builtin_amdgcn_mfma_f32_16x16x32_bf16(Bt[n][k], At[m][k], acc[m][n], 0, 0, 0);
        __builtin_amdgcn_s_setprio(0);
        asm volatile("s_waitcnt lgkmcnt(0)" ::: "memory");
        cur = (cur + 1) & 3;
    }
#undef STG
    __builtin_amdgcn_s_barrier();
    E(acc, wr, wc, fr, fq);
}

constexpr int XV_PITCH = 272;
template <bool INK>
__device__ __forceinline__ void xattn_item(LAS unsigned char* vs, char* ws, const int s, const int h) {
    const int lane = threadIdx.x & 63, fr = lane & 15, fq = lane >> 4;
    const bf16_t* Q = (const bf16_t*)(ws + WS_Q) + (size_t)s * SEG * HD + h * DH;
    const bf16_t* K = (const bf16_t*)(ws + WS_K) + (size_t)s * SEG * HD + h * DH;
    const bf16_t* V = (const bf16_t*)(ws + WS_V) + (size_t)s * SEG * HD + h * DH;
    const __amdgpu_buffer_rsrc_t rm = __builtin_amdgcn_make_buffer_rsrc(ws + WS_QKVM, 0, (int)(WS_OM - WS_QKVM), 0x00020000);
    const int kmo = s * (int)QKVM_B + (h * 3 + 1) * 2048, vmo = kmo + 2048;
    u32x4 km[4], vm[2];
    for (unsigned spins = 0;;) {
        asm volatile("" ::: "memory");
#pragma unroll
        for (int i = 0; i < 4; ++i) km[i] = __builtin_amdgcn_raw_buffer_load_b128(rm, kmo + (lane & 7) * 256 + i * 64 + fq * 16, 0, INK ? AUX_SC1 : 0);
#pragma unroll
        for (int i = 0; i < 2; ++i) vm[i] = __builtin_amdgcn_raw_buffer_load_b128(rm, vmo + (i * 4 + fq) * 256 + fr * 16, 0, INK ? AUX_SC1 : 0);
        if (!INK) break;
        unsigned mxs = umax2(umax4(vm[0]), umax4(vm[1]));
#pragma unroll
        for (int i = 0; i < 4; ++i) mxs = umax2(mxs, umax4(km[i]));
        if (!__any(mxs == SENT)) break;
        __builtin_amdgcn_s_sleep(16);
        if (++spins > SPIN_CAP) break;
    }
#pragma unroll
    for (int i = 0; i < 12; ++i) {
        const int r = i * 4 + fq;
        u32x4 v = (u32x4){0u, 0u, 0u, 0u};
        if (i < 2) v = vm[i]; else if (i < 10) v = *(const u32x4*)(V + (size_t)(r - 8) * HD + fr * 8);
        *(LAS u32x4*)(vs + r * XV_PITCH + fr * 16) = v;
    }
    f32x4 sc[3][2];
#pragma unroll
    for (int t = 0; t < 3; ++t) { sc[t][0] = (f32x4){0.f, 0.f, 0.f, 0.f}; sc[t][1] = (f32x4){0.f, 0.f, 0.f, 0.f}; }
#pragma unroll
    for (int i = 0; i < 4; ++i) {
        const bf16x8 q0 = *(const bf16x8*)(Q + (size_t)fr * HD + i * 32 + fq * 8), q1 = *(const bf16x8*)(Q + (size_t)(16 + fr) * HD + i * 32 + fq * 8);
#pragma unroll
        for (int t = 0; t < 3; ++t) {
            int r = t * 16 + fr - 8; r = r < 0 ? 0 : (r > 31 ? 31 : r);
            bf16x8 kf = *(const bf16x8*)(K + (size_t)r * HD + i * 32 + fq * 8);
            if (t == 0 && fr < 8) kf = __builtin_bit_cast(bf16x8, km[i]);
            sc[t][0] = __builtin_amdgcn_mfma_f32_16x16x32_bf16(kf, q0, sc[t][0], 0, 0, 0);
            sc[t][1] = __builtin_amdgcn_mfma_f32_16x16x32_bf16(kf, q1, sc[t][1], 0, 0, 0);
        }
    }
    const float scale = 0.08838834764831845f, NEG = rbf(-1000000000.0f);
    u32x4 p0[2], p1[2];
#pragma unroll
    for (int rt = 0; rt < 2; ++rt) {
        const int row = rt * 16 + fr;
        float v[12]; float mx = -INFINITY;
#pragma unroll
        for (int t = 0; t < 3; ++t)
#pragma unroll
            for (int r = 0; r < 4; ++r) { const int key = t * 16 + fq * 4 + r; float z = rbf(rbf(sc[t][rt][r]) * scale);
                if (key - 8 > row) z = NEG; if (key >= 40) z = -INFINITY; v[t * 4 + r] = z; mx = fmaxf(mx, z); }
        mx = xmax16_32(mx);
        float sum = 0.f;
#pragma unroll
        for (int i = 0; i < 12; ++i) { v[i] = __expf(v[i] - mx); sum += v[i]; }
        sum = xsum16_32(sum);
        const float inv = 1.0f / sum;
        p0[rt].x = pk_bf16(v[0] * inv, v[1] * inv); p0[rt].y = pk_bf16(v[2] * inv, v[3] * inv); p0[rt].z = pk_bf16(v[4] * inv, v[5] * inv); p0[rt].w = pk_bf16(v[6] * inv, v[7] * inv);
        p1[rt].x = pk_bf16(v[8] * inv, v[9] * inv); p1[rt].y = pk_bf16(v[10] * inv, v[11] * inv); p1[rt].z = 0u; p1[rt].w = 0u;
    }
    asm volatile("s_waitcnt lgkmcnt(0)" ::: "memory");
    const __amdgpu_buffer_rsrc_t ro = __builtin_amdgcn_make_buffer_rsrc(ws + WS_O, 0, S_LEN * HD * 2, 0x00020000);
    const int obase = (s * SEG * HD + h * DH) * 2;
    const bf16x4 zz = (bf16x4){0, 0, 0, 0};
    u32x2 ow[8][2];
#pragma unroll
    for (int et = 0; et < 8; ++et) {
        const int voff = (4 * fq + (fr >> 2)) * XV_PITCH + et * 32 + (lane & 3) * 8;
        const bf16x4 v0 = __builtin_amdgcn_ds_read_tr16_b64_v4i16((LAS bf16x4*)(vs + voff));
        const bf16x4 v1 = __builtin_amdgcn_ds_read_tr16_b64_v4i16((LAS bf16x4*)(vs + voff + 16 * XV_PITCH));
        const bf16x4 v2 = __builtin_amdgcn_ds_read_tr16_b64_v4i16((LAS bf16x4*)(vs + voff + 32 * XV_PITCH));
        const bf16x8 va = __builtin_shufflevector(v0, v1, 0, 1, 2, 3, 4, 5, 6, 7), vb = __builtin_shufflevector(v2, zz, 0, 1, 2, 3, 4, 5, 6, 7);
#pragma unroll
        for (int rt = 0; rt < 2; ++rt) {
            f32x4 o = (f32x4){0.f, 0.f, 0.f, 0.f};
            o = __builtin_amdgcn_mfma_f32_16x16x32_bf16(va, __builtin_bit_cast(bf16x8, p0[rt]), o, 0, 0, 0);
            o = __builtin_amdgcn_mfma_f32_16x16x32_bf16(vb, __builtin_bit_cast(bf16x8, p1[rt]), o, 0, 0, 0);
            ow[et][rt].x = pk_bf16(o[0], o[1]); ow[et][rt].y = pk_bf16(o[2], o[3]);
        }
    }
    asm volatile("s_waitcnt lgkmcnt(0)" ::: "memory");
#pragma unroll
    for (int et = 0; et < 8; ++et)
#pragma unroll
        for (int rt = 0; rt < 2; ++rt) *(LAS u32x2*)(vs + (rt * 16 + fr) * XV_PITCH + et * 32 + fq * 8) = ow[et][rt];
    asm volatile("s_waitcnt lgkmcnt(0)" ::: "memory");
#pragma unroll
    for (int i = 0; i < 8; ++i) {
        const int r = i * 4 + fq;
        const u32x4 w = *(const LAS u32x4*)(vs + r * XV_PITCH + fr * 16);
        __builtin_amdgcn_raw_buffer_store_b128(w, ro, obase + (r * HD + fr * 8) * 2, 0, INK ? AUX_SC1 : 0);
    }
    asm volatile("s_waitcnt lgkmcnt(0)" ::: "memory");
}

__device__ __forceinline__ void store_pair_sc1(const __amdgpu_buffer_rsrc_t rs, int off0, u32x2 w0, u32x2 w1, int fq) {
    auto sx = __builtin_amdgcn_permlane16_swap(w0.x, w1.x, false, false); auto sy = __builtin_amdgcn_permlane16_swap(w0.y, w1.y, false, false);
    const u32x4 w = (u32x4){(unsigned)sx[0], (unsigned)sy[0], (unsigned)sx[1], (unsigned)sy[1]};
    __builtin_amdgcn_raw_buffer_store_b128(w, rs, off0 + (fq & 1) * 32 + (fq >> 1) * 16, 0, AUX_SC1);
}
struct SchedOne {
    g8::Unit u; bool valid;
    __device__ __forceinline__ bool next(int i, g8::Unit& o) const { if (i != 0 || !valid) return false; o = u; return true; }
};
struct SchedKVQ {
    const bf16_t* A; const bf16_t* Wq; long dk, dv; int r;
    __device__ __forceinline__ bool next(int i, g8::Unit& u) const {
        const int L = r + 128 * i; if (L >= 352) return false;
        int pm, pn;
        if (L < 224) { pm = 1 + (L >> 5); pn = 16 + (L & 31); } else { pm = (L - 224) & 7; pn = (L - 224) >> 3; }
        const int mat = pn >> 4;
        u.A = A + (size_t)pm * 256 * DM; u.B = Wq + (mat == 1 ? dk : 0L) + (mat == 2 ? dv : 0L) + (pn & 15) * 256; u.pm = pm; u.pn = pn; return true;
    }
};
struct EpiQKV {
    char* ws;
    __device__ __forceinline__ void operator()(const f32x4 (&acc)[2][2][4][2], const g8::Unit& u, int wr, int wc, int fr, int fq) const {
        const __amdgpu_buffer_rsrc_t rq = __builtin_amdgcn_make_buffer_rsrc(ws + WS_Q, 0, (int)(WS_H - WS_Q), 0x00020000);
        const int base = (u.pn >> 4) * (S_LEN * HD * 2) + ((u.pm * 256 + wr * 64 + fr) * HD + (u.pn & 15) * 256 + wc * 32) * 2;
#pragma unroll
        for (int ai = 0; ai < 2; ++ai)
#pragma unroll
            for (int m = 0; m < 4; ++m)
#pragma unroll
                for (int bj = 0; bj < 2; ++bj) { const f32x4 v0 = acc[ai][bj][m][0], v1 = acc[ai][bj][m][1];
                    store_pair_sc1(rq, base + ((ai * 128 + m * 16) * HD + bj * 128) * 2, (u32x2){pk_bf16(v0[0], v0[1]), pk_bf16(v0[2], v0[3])}, (u32x2){pk_bf16(v1[0], v1[1]), pk_bf16(v1[2], v1[3])}, fq); }
        asm volatile("s_waitcnt vmcnt(0)" ::: "memory");
        if ((threadIdx.x & 63) == 0) {
            if (u.pn >= 16) __hip_atomic_fetch_add((unsigned*)(ws + WS_CNT + (size_t)(CNT_KV + u.pm * 32 + u.pn - 16) * 256), 1u, __ATOMIC_RELAXED, __HIP_MEMORY_SCOPE_AGENT);
            __hip_atomic_fetch_add((unsigned*)(ws + WS_CNT + (size_t)(CNT_QKV + u.pm) * 256), 1u, __ATOMIC_RELAXED, __HIP_MEMORY_SCOPE_AGENT);
        }
    }
};
struct EpiKV0 {
    char* ws; int pm2, pn2;
    __device__ __forceinline__ void operator()(const f32x4 (&acc)[4][2], int wr, int wc, int fr, int fq) const {
        const __amdgpu_buffer_rsrc_t rq = __builtin_amdgcn_make_buffer_rsrc(ws + WS_K, 0, (int)(WS_H - WS_K), 0x00020000);
        const int base = (pn2 >> 5) * (S_LEN * HD * 2) + ((pm2 * 128 + wr * 64 + fr) * HD + (pn2 & 31) * 128 + wc * 32) * 2;
#pragma unroll
        for (int m = 0; m < 4; ++m) { const f32x4 v0 = acc[m][0], v1 = acc[m][1];
            store_pair_sc1(rq, base + m * 16 * HD * 2, (u32x2){pk_bf16(v0[0], v0[1]), pk_bf16(v0[2], v0[3])}, (u32x2){pk_bf16(v1[0], v1[1]), pk_bf16(v1[2], v1[3])}, fq); }
        asm volatile("s_waitcnt vmcnt(0)" ::: "memory");
        if ((threadIdx.x & 63) == 0) {
            __hip_atomic_fetch_add((unsigned*)(ws + WS_CNT + (size_t)((pm2 == 0 ? CNT_KV : CNT_KV0B) + (pn2 >> 1)) * 256), 1u, __ATOMIC_RELAXED, __HIP_MEMORY_SCOPE_AGENT);
            __hip_atomic_fetch_add((unsigned*)(ws + WS_CNT + (size_t)CNT_QKV * 256), 1u, __ATOMIC_RELAXED, __HIP_MEMORY_SCOPE_AGENT);
        }
    }
};
struct EpiMLP1 {
    char* ws; const bf16_t* b1;
    __device__ __forceinline__ void operator()(const f32x4 (&acc)[2][2][4][2], const g8::Unit& u, int wr, int wc, int fr, int fq) const {
        const __amdgpu_buffer_rsrc_t rh = __builtin_amdgcn_make_buffer_rsrc(ws + WS_H, 0, S_LEN * FF * 2, 0x00020000);
        const int row0 = u.pm * 256 + wr * 64 + fr, col0 = u.pn * 256 + wc * 32;
#pragma unroll
        for (int bj = 0; bj < 2; ++bj) {
            const u32x2 ba = *(const u32x2*)(b1 + col0 + bj * 128 + 4 * fq), bb = *(const u32x2*)(b1 + col0 + bj * 128 + 16 + 4 * fq);
#pragma unroll
            for (int ai = 0; ai < 2; ++ai)
#pragma unroll
                for (int m = 0; m < 4; ++m) { const f32x4 v0 = acc[ai][bj][m][0], v1 = acc[ai][bj][m][1];
                    u32x2 w0, w1;
                    w0.x = pk_bf16(gelu_tanh(rbf(rbf(v0[0]) + bf2f(ba.x & 0xffffu))), gelu_tanh(rbf(rbf(v0[1]) + bf2f(ba.x >> 16))));
                    w0.y = pk_bf16(gelu_tanh(rbf(rbf(v0[2]) + bf2f(ba.y & 0xffffu))), gelu_tanh(rbf(rbf(v0[3]) + bf2f(ba.y >> 16))));
                    w1.x = pk_bf16(gelu_tanh(rbf(rbf(v1[0]) + bf2f(bb.x & 0xffffu))), gelu_tanh(rbf(rbf(v1[1]) + bf2f(bb.x >> 16))));
                    w1.y = pk_bf16(gelu_tanh(rbf(rbf(v1[2]) + bf2f(bb.y & 0xffffu))), gelu_tanh(rbf(rbf(v1[3]) + bf2f(bb.y >> 16))));
                    store_pair_sc1(rh, ((row0 + ai * 128 + m * 16) * FF + col0 + bj * 128) * 2, w0, w1, fq);
                    if (m & 1) asm volatile("" ::: "memory"); }
        }
        signal_counter(ws, CNT_H + u.pm);
    }
};
struct EpiMLP2 {
    char* ws; const bf16_t* b2; int row0, col0;
    __device__ __forceinline__ void operator()(const f32x4 (&acc)[4][2], int wr, int wc, int fr, int fq) const {
        const __amdgpu_buffer_rsrc_t rb = __builtin_amdgcn_make_buffer_rsrc(ws + WS_B, 0, S_LEN * DM * 2, 0x00020000);
        const int col = col0 + wc * 32;
        const u32x2 ba = *(const u32x2*)(b2 + col + 4 * fq), bb = *(const u32x2*)(b2 + col + 16 + 4 * fq);
#pragma unroll
        for (int m = 0; m < 4; ++m) { const f32x4 v0 = acc[m][0], v1 = acc[m][1];
            u32x2 w0, w1;
            w0.x = pk_bf16(rbf(v0[0]) + bf2f(ba.x & 0xffffu), rbf(v0[1]) + bf2f(ba.x >> 16)); w0.y = pk_bf16(rbf(v0[2]) + bf2f(ba.y & 0xffffu), rbf(v0[3]) + bf2f(ba.y >> 16));
            w1.x = pk_bf16(rbf(v1[0]) + bf2f(bb.x & 0xffffu), rbf(v1[1]) + bf2f(bb.x >> 16)); w1.y = pk_bf16(rbf(v1[2]) + bf2f(bb.y & 0xffffu), rbf(v1[3]) + bf2f(bb.y >> 16));
            store_pair_sc1(rb, ((row0 + wr * 64 + m * 16 + fr) * DM + col) * 2, w0, w1, fq); }
        signal_counter(ws, CNT_B + (row0 >> 8));
    }
};
__device__ __forceinline__ u32x2 combine4(const f32x4 v, const u32x2 xw, const u32x2 bw) {
    u32x2 w;
    w.x = pk_bf16(rbf(bf2f(xw.x & 0xffffu) + rbf(v[0])) + bf2f(bw.x & 0xffffu), rbf(bf2f(xw.x >> 16) + rbf(v[1])) + bf2f(bw.x >> 16));
    w.y = pk_bf16(rbf(bf2f(xw.y & 0xffffu) + rbf(v[2])) + bf2f(bw.y & 0xffffu), rbf(bf2f(xw.y >> 16) + rbf(v[3])) + bf2f(bw.y >> 16));
    return w;
}
template <int MT>
struct EpiOut {
    const bf16_t* x; const bf16_t* bout; bf16_t* out; int row0, col0;
    __device__ __forceinline__ void operator()(const f32x4 (&acc)[MT][2], int wr, int wc, int fr, int fq) const {
#pragma unroll
        for (int m = 0; m < MT; ++m)
#pragma unroll
            for (int n = 0; n < 2; ++n) {
                const size_t off = (size_t)(row0 + wr * 16 * MT + m * 16 + fr) * DM + col0 + wc * 32 + n * 16 + 4 * fq;
                *(u32x2*)(out + off) = combine4(acc[m][n], *(const u32x2*)(x + off), *(const u32x2*)(bout + off));
            }
    }
};

constexpr int TAIL_BLK0 = 6;
__global__ void __launch_bounds__(512, 2) k_mega(const bf16_t* __restrict__ x, const bf16_t* __restrict__ Wq, const bf16_t* __restrict__ Wk, const bf16_t* __restrict__ Wv,
                                                 const bf16_t* __restrict__ Wo, const bf16_t* __restrict__ w1, const bf16_t* __restrict__ b1, const bf16_t* __restrict__ w2,
                                                 const bf16_t* __restrict__ b2, char* ws, bf16_t* __restrict__ out) {
    extern __shared__ __attribute__((aligned(16))) unsigned char shm_[];
    LAS unsigned char* lds = (LAS unsigned char*)shm_;
    const int b = blockIdx.x;
    const int role = (b >> 3) & 1, r = (b >> 4) * 8 + (b & 7);
    if (role == 0) { chain_body<4>(lds, r, Wq, Wk, Wv, Wo, ws); return; }
#define WSTAMP(i) do {} while (0)
    WSTAMP(0);
    {
        const int pm2 = r & 1, pn2 = r >> 1;
        EpiKV0 E{ws, pm2, pn2};
        gemm128_w8<4, EpiKV0>(lds, (const bf16_t*)(ws + WS_XN) + (size_t)pm2 * 128 * DM, DM, (pn2 >= 32 ? Wv : Wk) + (pn2 & 31) * 128, HD, DM, E);
    }
    WSTAMP(1);
    {
        SchedKVQ S{(const bf16_t*)(ws + WS_XN), Wq, (long)(Wk - Wq), (long)(Wv - Wq), r};
        EpiQKV E{ws}; g8::gemm_phase<EpiQKV, SchedKVQ>(lds, DM, DM, HD, S, E);
    }
    WSTAMP(2);
    {
        const int L = r >= 96 ? r - 96 : 32 + r;
        SchedOne S; S.valid = (r >= 96) || (r < 80); S.u.pm = L & 7; S.u.pn = L >> 3; S.u.A = (const bf16_t*)(ws + WS_Y) + (size_t)S.u.pm * 256 * DM; S.u.B = w1 + S.u.pn * 256;
        EpiMLP1 E{ws, b1}; g8::gemm_phase<EpiMLP1, SchedOne>(lds, DM, DM, FF, S, E);
    }
    WSTAMP(3);
    {
        const int pm = r >> 3, pn = r & 7;
        wait_counter(ws, CNT_H + (pm >> 1), 14 * 8);
        EpiMLP2 E{ws, b2, pm * 128, pn * 128};
        gemm128_w8<4, EpiMLP2>(lds, (const bf16_t*)(ws + WS_H) + (size_t)pm * 128 * FF, FF, w2 + pn * 128, DM, FF, E);
    }
    WSTAMP(4);
    if (r < 80) {
        const int blk = r >> 4, t = r & 15;
        {
            if (threadIdx.x < 64) poll_ge(ws, CNT_STEP, (unsigned)(8 * blk + 8));
            wait_counter(ws, CNT_QKV + blk, (blk == 0 ? 128 * 8 : 32 * 8) + 16 * 8);
            WSTAMP(5);
            const int wave = threadIdx.x >> 6;
#pragma unroll 1
            for (int i = 0; i < 2; ++i) { const int item = t * 16 + wave * 2 + i; xattn_item<true>(lds + wave * 48 * XV_PITCH, ws, blk * 8 + (item >> 5), item & 31); }
            signal_counter(ws, CNT_O + blk);
        }
        {
            if (threadIdx.x < 64) poll_ge(ws, CNT_B + blk, 16 * 8);
            wait_counter(ws, CNT_O + blk, 16 * 8);
            WSTAMP(6);
            const int pm = blk * 2 + (t >> 3), pn = t & 7;
            EpiOut<4> E{x, (const bf16_t*)(ws + WS_B), out, pm * 128, pn * 128};
            gemm128_w8<4, EpiOut<4>>(lds, (const bf16_t*)(ws + WS_O) + (size_t)pm * 128 * HD, HD, Wo + pn * 128, DM, HD, E);
        }
    } else if (r < 112) {
        constexpr int blk = 5; const int t = r - 80;
        if (threadIdx.x < 64) poll_ge(ws, CNT_STEP, (unsigned)(8 * blk + 8));
        wait_counter(ws, CNT_QKV + blk, 32 * 8 + 16 * 8);
        WSTAMP(5);
        const int wave = threadIdx.x >> 6, item = t * 8 + wave;
        xattn_item<true>(lds + wave * 48 * XV_PITCH, ws, blk * 8 + (item >> 5), item & 31);
        signal_counter(ws, CNT_O + blk);
        if (threadIdx.x < 64) poll_ge(ws, CNT_B + blk, 16 * 8);
        wait_counter(ws, CNT_O + blk, 32 * 8);
        WSTAMP(6);
        const int pm = blk * 4 + (t >> 3), pn = t & 7;
        EpiOut<2> E{x, (const bf16_t*)(ws + WS_B), out, pm * 64, pn * 128};
        gemm128_w8<2, EpiOut<2>>(lds, (const bf16_t*)(ws + WS_O) + (size_t)pm * 64 * HD, HD, Wo + pn * 128, DM, HD, E);
    }
    WSTAMP(7);
}

__global__ void __launch_bounds__(256) k_tail_attn(char* ws) {
    __shared__ __attribute__((aligned(16))) unsigned char shm_[4 * 48 * XV_PITCH];
    const int wave = threadIdx.x >> 6, item = blockIdx.x * 4 + wave;
    xattn_item<false>((LAS unsigned char*)shm_ + wave * 48 * XV_PITCH, ws, TAIL_BLK0 * 8 + (item >> 5), item & 31);
}
__global__ void __launch_bounds__(256) k_tail_gemm(const bf16_t* __restrict__ x, const bf16_t* __restrict__ Wo, const char* __restrict__ ws, bf16_t* __restrict__ out) {
    extern __shared__ __attribute__((aligned(16))) unsigned char shm_[];
    const int tid = threadIdx.x, wave = __builtin_amdgcn_readfirstlane(tid >> 6), lane = tid & 63, fr = lane & 15, fq = lane >> 4;
    LAS unsigned char* lds = (LAS unsigned char*)shm_ + wave * 36864;
    const int pn = blockIdx.x & 31, pm = blockIdx.x >> 5, row0 = TAIL_BLK0 * 256 + pm * 64, col0 = pn * 32;
    const char* cA = (const char*)((const bf16_t*)(ws + WS_O) + (size_t)row0 * HD + wave * 1024);
    const char* cB = (const char*)(Wo + (size_t)wave * 1024 * DM + col0);
    unsigned voffA[8], voffB[4];
#pragma unroll
    for (int i = 0; i < 8; ++i) { const int c = i * 64 + lane, row = c >> 3, ch = (c & 7) ^ (row & 7); voffA[i] = (unsigned)(row * HD + ch * 8) * 2u; }
#pragma unroll
    for (int i = 0; i < 4; ++i) { const int c = i * 64 + lane, k = c >> 2, cb = c & 3; voffB[i] = (unsigned)(k * DM + cb * 8) * 2u; }
#define STG(buf, t) do { _Pragma("unroll") for (int _i = 0; _i < 8; ++_i) \
        __builtin_amdgcn_global_load_lds((const unsigned*)(cA + (size_t)(t) * 128 + voffA[_i]), (LAS unsigned*)(lds + (buf) * 12288 + _i * 1024), 16, 0, 0); \
        _Pragma("unroll") for (int _i = 0; _i < 4; ++_i) \
        __builtin_amdgcn_global_load_lds((const unsigned*)(cB + (size_t)(t) * 64 * DM * 2 + voffB[_i]), (LAS unsigned*)(lds + (buf) * 12288 + 8192 + _i * 1024), 16, 0, 0); } while (0)
    f32x4 acc[4][2];
#pragma unroll
    for (int m = 0; m < 4; ++m) { acc[m][0] = (f32x4){0.f, 0.f, 0.f, 0.f}; acc[m][1] = (f32x4){0.f, 0.f, 0.f, 0.f}; }
    STG(0, 0); STG(1, 1);
    int cur = 0;
    for (int t = 0; t < 16; ++t) {
        int nx = cur + 2; if (nx >= 3) nx -= 3;
        if (t + 2 < 16) { STG(nx, t + 2); asm volatile("s_waitcnt vmcnt(24)" ::: "memory"); }
        else if (t + 1 < 16) asm volatile("s_waitcnt vmcnt(12)" ::: "memory");
        else asm volatile("s_waitcnt vmcnt(0)" ::: "memory");
        LAS unsigned char* sa = lds + cur * 12288; LAS unsigned char* sb = sa + 8192;
        bf16x8 At[4][2], Bt[2][2];
#pragma unroll
        for (int k = 0; k < 2; ++k) {
#pragma unroll
            for (int m = 0; m < 4; ++m) { const int row = m * 16 + fr; At[m][k] = *(const LAS bf16x8*)(sa + row * 128 + (((k * 4 + fq) ^ (row & 7)) * 16)); }
#pragma unroll
            for (int n = 0; n < 2; ++n) {
                const int off = (k * 32 + 8 * fq + (fr >> 2)) * 64 + n * 32 + (lane & 3) * 8;
                const bf16x4 a0 = __builtin_amdgcn_ds_read_tr16_b64_v4i16((LAS bf16x4*)(sb + off)), a1 = __builtin_amdgcn_ds_read_tr16_b64_v4i16((LAS bf16x4*)(sb + off + 256));
                Bt[n][k] = __builtin_shufflevector(a0, a1, 0, 1, 2, 3, 4, 5, 6, 7); }
        }
#pragma unroll
        for (int k = 0; k < 2; ++k)
#pragma unroll
            for (int m = 0; m < 4; ++m)
#pragma unroll
                for (int n = 0; n < 2; ++n) acc[m][n] = __builtin_amdgcn_mfma_f32_16x16x32_bf16(Bt[n][k], At[m][k], acc[m][n], 0, 0, 0);
        asm volatile("s_waitcnt lgkmcnt(0)" ::: "memory");
        cur = cur + 1 == 3 ? 0 : cur + 1;
    }
#undef STG
    __syncthreads();
    LAS f32x4* red = (LAS f32x4*)shm_;
#pragma unroll
    for (int m = 0; m < 4; ++m)
#pragma unroll
        for (int n = 0; n < 2; ++n) red[((wave * 4 + m) * 2 + n) * 64 + lane] = acc[m][n];
    __syncthreads();
#pragma unroll
    for (int n = 0; n < 2; ++n) {
        const int m = wave;
        const f32x4 v = (red[((0 * 4 + m) * 2 + n) * 64 + lane] + red[((1 * 4 + m) * 2 + n) * 64 + lane]) + (red[((2 * 4 + m) * 2 + n) * 64 + lane] + red[((3 * 4 + m) * 2 + n) * 64 + lane]);
        const size_t off = (size_t)(row0 + m * 16 + fr) * DM + col0 + n * 16 + 4 * fq;
        *(u32x2*)(out + off) = combine4(v, *(const u32x2*)(x + off), *(const u32x2*)((const bf16_t*)(ws + WS_B) + off));
    }
}

extern "C" void kernel_launch(void* const* d_in, const int* in_sizes, int n_in, void* d_out, int out_size, void* d_ws, size_t ws_size, hipStream_t stream) {
    const bf16_t* x = (const bf16_t*)d_in[0];
    const bf16_t* mem0 = (const bf16_t*)d_in[1];
    const bf16_t* Wq = (const bf16_t*)d_in[2];
    const bf16_t* Wk = (const bf16_t*)d_in[3];
    const bf16_t* Wv = (const bf16_t*)d_in[4];
    const bf16_t* Wo = (const bf16_t*)d_in[5];
    const bf16_t* w1 = (const bf16_t*)d_in[8];
    const bf16_t* b1 = (const bf16_t*)d_in[9];
    const bf16_t* w2 = (const bf16_t*)d_in[10];
    const bf16_t* b2 = (const bf16_t*)d_in[11];
    char* ws = (char*)d_ws;
    bf16_t* out = (bf16_t*)d_out;
    (void)hipFuncSetAttribute((const void*)k_mega, hipFuncAttributeMaxDynamicSharedMemorySize, CH_LDS);
    (void)hipFuncSetAttribute((const void*)k_tail_gemm, hipFuncAttributeMaxDynamicSharedMemorySize, 4 * 36864);
    k_prep<<<S_LEN / 4, 256, 0, stream>>>(x, mem0, ws);
    k_mega<<<256, 512, CH_LDS, stream>>>(x, Wq, Wk, Wv, Wo, w1, b1, w2, b2, ws, out);
    k_tail_attn<<<64 * (8 - TAIL_BLK0), 256, 0, stream>>>(ws);
    k_tail_gemm<<<128 * (8 - TAIL_BLK0), 256, 4 * 36864, stream>>>(x, Wo, ws, out);
}
```

```cpp
#include <hip/hip_runtime.h>
#include <hip/hip_bf16.h>

#define LAS __attribute__((address_space(3)))
typedef unsigned short bf16_t;
typedef short bf16x8 __attribute__((ext_vector_type(8)));
typedef short bf16x4 __attribute__((ext_vector_type(4)));
typedef float f32x4 __attribute__((ext_vector_type(4)));
typedef float f32x2 __attribute__((ext_vector_type(2)));
typedef unsigned u32x4 __attribute__((ext_vector_type(4)));
typedef unsigned u32x2 __attribute__((ext_vector_type(2)));

constexpr int S_LEN = 2048, DM = 1024, NH = 32, DH = 128, HD = NH * DH, MM = 8, SEG = 32, NSEG = S_LEN / SEG, FF = 3584;
constexpr int AUX_SC1 = 16;
constexpr unsigned SENT = 0xFFFFFFFFu;

__device__ __forceinline__ float bf2f(unsigned b) { return __builtin_bit_cast(float, b << 16); }
typedef __bf16 bf16x2_t __attribute__((ext_vector_type(2)));
__device__ __forceinline__ unsigned pk_bf16(float lo, float hi) { const f32x2 v = {lo, hi}; return __builtin_bit_cast(unsigned, __builtin_convertvector(v, bf16x2_t)); }
__device__ __forceinline__ float rbf(float x) { return bf2f(pk_bf16(x, x) & 0xffffu); }
__device__ __forceinline__ unsigned desent(unsigned w) { return w == SENT ? 0x7FC07FC0u : w; }
__device__ __forceinline__ unsigned umax2(unsigned a, unsigned b) { return a > b ? a : b; }
__device__ __forceinline__ unsigned umax4(u32x4 v) { return umax2(umax2(v.x, v.y), umax2(v.z, v.w)); }

constexpr size_t WS_XN = 0;
constexpr size_t WS_Y = WS_XN + (size_t)S_LEN * DM * 2;
constexpr size_t WS_Q = WS_Y + (size_t)S_LEN * DM * 2;
constexpr size_t WS_K = WS_Q + (size_t)S_LEN * HD * 2;
constexpr size_t WS_V = WS_K + (size_t)S_LEN * HD * 2;
constexpr size_t WS_H = WS_V + (size_t)S_LEN * HD * 2;
constexpr size_t WS_O = WS_H + (size_t)S_LEN * FF * 2;
constexpr size_t WS_B = WS_O + (size_t)S_LEN * HD * 2;
constexpr size_t WS_SENT0 = WS_B + (size_t)S_LEN * DM * 2;
constexpr size_t MEM_B = (size_t)MM * DM * 2;
constexpr size_t WS_MEM = WS_SENT0;
constexpr size_t QKVM_B = (size_t)NH * 3 * MM * DH * 2;
constexpr size_t WS_QKVM = WS_MEM + (size_t)(NSEG + 1) * MEM_B;
constexpr size_t OM_B = (size_t)MM * HD * 2;
constexpr size_t WS_OM = WS_QKVM + (size_t)NSEG * QKVM_B;
constexpr size_t WS_SENT1 = WS_OM + (size_t)NSEG * OM_B;
constexpr size_t WS_CNT = WS_SENT1;
constexpr int CNT_H = 0;
constexpr int CNT_B = 8;
constexpr int CNT_O = 16;
constexpr int CNT_QKV = 24;
constexpr int CNT_FILL = 33;
constexpr int CNT_STEP = 32;
constexpr int CNT_KV = 40;
constexpr int CNT_KV0B = 40 + 256;
constexpr int CNT_N = 40 + 256 + 32;
constexpr size_t WS_TMO = WS_CNT + (size_t)CNT_N * 256;
constexpr size_t WS_END = WS_TMO + 256;

__device__ __forceinline__ float wave_sum(float v) {
#pragma unroll
    for (int o = 32; o > 0; o >>= 1) v += __shfl_xor(v, o);
    return v;
}
__device__ __forceinline__ void ln_row(float (&v)[16]) {
    float s = 0.f;
#pragma unroll
    for (int i = 0; i < 16; ++i) s += v[i];
    const float mean = wave_sum(s) * (1.0f / DM);
    float q = 0.f;
#pragma unroll
    for (int i = 0; i < 16; ++i) { const float d = v[i] - mean; q += d * d; }
    const float var = rbf(wave_sum(q) * (1.0f / DM));
    const float mb = rbf(mean);
    const float den = rbf(sqrtf(rbf(var + 1e-5f)));
#pragma unroll
    for (int i = 0; i < 16; ++i) v[i] = rbf(rbf(v[i] - mb) / den);
}
__global__ void __launch_bounds__(256) k_prep(const bf16_t* __restrict__ x, const bf16_t* __restrict__ mem0, char* __restrict__ ws) {
    const int wave = threadIdx.x >> 6, lane = threadIdx.x & 63, row = blockIdx.x * 4 + wave;
    const u32x4* src = (const u32x4*)(x + (size_t)row * DM);
    u32x4 a = src[lane], b = src[lane + 64];
    float v[16];
    const unsigned w[8] = {a.x, a.y, a.z, a.w, b.x, b.y, b.z, b.w};
#pragma unroll
    for (int i = 0; i < 8; ++i) { v[2 * i] = bf2f(w[i] & 0xffffu); v[2 * i + 1] = bf2f(w[i] >> 16); }
    ln_row(v);
    u32x4 o0, o1;
    o0.x = pk_bf16(v[0], v[1]); o0.y = pk_bf16(v[2], v[3]); o0.z = pk_bf16(v[4], v[5]); o0.w = pk_bf16(v[6], v[7]);
    o1.x = pk_bf16(v[8], v[9]); o1.y = pk_bf16(v[10], v[11]); o1.z = pk_bf16(v[12], v[13]); o1.w = pk_bf16(v[14], v[15]);
    u32x4* dx = (u32x4*)(ws + WS_XN + (size_t)row * DM * 2);
    dx[lane] = o0; dx[lane + 64] = o1;
    ln_row(v);
    o0.x = pk_bf16(v[0], v[1]); o0.y = pk_bf16(v[2], v[3]); o0.z = pk_bf16(v[4], v[5]); o0.w = pk_bf16(v[6], v[7]);
    o1.x = pk_bf16(v[8], v[9]); o1.y = pk_bf16(v[10], v[11]); o1.z = pk_bf16(v[12], v[13]); o1.w = pk_bf16(v[14], v[15]);
    u32x4* dy = (u32x4*)(ws + WS_Y + (size_t)row * DM * 2);
    dy[lane] = o0; dy[lane + 64] = o1;
    {
        const size_t n16 = (WS_SENT1 - WS_SENT0 - MEM_B) / 16;
        u32x4* dst = (u32x4*)(ws + WS_SENT0 + MEM_B);
        const u32x4 f = (u32x4){SENT, SENT, SENT, SENT};
        for (size_t i = (size_t)blockIdx.x * 256 + threadIdx.x; i < n16; i += (size_t)gridDim.x * 256) dst[i] = f;
    }
    if (blockIdx.x >= 8 && blockIdx.x < 8 + (CNT_N * 256 / 16 + 255) / 256) {
        const int i = (blockIdx.x - 8) * 256 + threadIdx.x; if (i < CNT_N * 256 / 16) ((u32x4*)(ws + WS_CNT))[i] = (u32x4){0u, 0u, 0u, 0u}; }
    if (blockIdx.x < 4) {
        const int c = blockIdx.x * 256 + threadIdx.x, r = c & 7, kg = c >> 3;
        u32x4 m = *(const u32x4*)(mem0 + (size_t)r * DM + kg * 8);
        m.x = desent(m.x); m.y = desent(m.y); m.z = desent(m.z); m.w = desent(m.w);
        *(u32x4*)(ws + WS_MEM + (size_t)c * 16) = m;
    }
}

namespace g8 {
constexpr int BM = 256, BK = 64, HALF = 128, HTB = HALF * BK * 2, STAGE_BYTES = 8 * HTB;
__host__ __device__ __forceinline__ int lds_byte(int r, int c) { const int st = (r >> 4) * 2 + (c >> 5), rr = r & 15, cc = c & 31, ob = rr * 64 + cc * 2; return st * 1024 + (ob ^ (((ob >> 9) & 1) << 5)); }
__host__ __device__ __forceinline__ void stage_rc(int b, int& R, int& C) { const int st = b / 1024, sb = b % 1024, swz = sb ^ (((sb >> 9) & 1) << 5); R = (st >> 1) * 16 + swz / 64; C = (st & 1) * 32 + (swz % 64) / 2; }
__host__ __device__ __forceinline__ int bsw(int row) { return (row & 3) | (((row >> 3) & 1) << 2); }
struct Unit { const bf16_t* A; const bf16_t* B; int pm, pn; };

template <class Epi, class Sched>
__device__ __forceinline__ void gemm_phase(LAS unsigned char* lds, const int K, const int lda, const int ldb, const Sched& S, const Epi& E) {
    int tid = threadIdx.x; asm volatile("" : "+v"(tid));
    const int wid = __builtin_amdgcn_readfirstlane(tid >> 6), lane = tid & 63, wr = wid >> 2, wc = wid & 3, fr = lane & 15, fq = lane >> 4;
    const int nt = K / BK;
    unsigned voffA[2], voffB[2];
#pragma unroll
    for (int i = 0; i < 2; ++i) { int R, C; stage_rc(tid * 16 + i * 8192, R, C); voffA[i] = (unsigned)(R * lda + C) * 2u;
        const int b = tid * 16 + i * 8192, row = b >> 8, ch = (b & 255) >> 4, seg = (ch >> 1) ^ bsw(row);
        voffB[i] = (unsigned)(row * ldb + seg * 16 + (ch & 1) * 8) * 2u; }
    const size_t kstepA = (size_t)(BK * 2), kstepB = (size_t)BK * ldb * 2;
    const size_t hstepA = (size_t)HALF * lda * 2, hstepB = (size_t)HALF * 2;
    const unsigned ldsw = (unsigned)wid * 1024u;
    const int aoff = lds_byte(wr * 64 + fr, fq * 8);
    const int q4 = (lane & 15) >> 2, p4 = lane & 3, sB = q4 | ((fq & 1) << 2);
    const int boff0 = (8 * fq + q4) * 256 + (((wc * 2 + 0) ^ sB) * 32) + 8 * p4;
    const int boff1 = (8 * fq + q4) * 256 + (((wc * 2 + 1) ^ sB) * 32) + 8 * p4;
#define G8_SA(b, h) (((b) * 2 + (h)) * HTB)
#define G8_SB(b, h) ((4 + (b) * 2 + (h)) * HTB)
#define G8_STAGE(bufoff, gbase, voff) do { _Pragma("unroll") for (int _i = 0; _i < 2; ++_i) \
        __builtin_amdgcn_global_load_lds((const unsigned*)((const char*)(gbase) + (voff)[_i]), (LAS unsigned*)(lds + (bufoff) + ldsw + _i * 8192), 16, 0, 0); } while (0)
#define G8_LDA(dst, b, h) do { _Pragma("unroll") for (int m = 0; m < 4; ++m) _Pragma("unroll") for (int k = 0; k < 2; ++k) dst[m][k] = *(const LAS bf16x8*)(lds + G8_SA(b, h) + aoff + m * 2048 + k * 1024); } while (0)
#define G8_TR(off) __builtin_amdgcn_ds_read_tr16_b64_v4i16((LAS bf16x4*)(lds + (off)))
#define G8_LDB(dst, b, h) do { _Pragma("unroll") for (int k = 0; k < 2; ++k) { \
        bf16x4 a0 = G8_TR(G8_SB(b, h) + boff0 + k * 8192), a1 = G8_TR(G8_SB(b, h) + boff0 + k * 8192 + 1024); \
        bf16x4 c0 = G8_TR(G8_SB(b, h) + boff1 + k * 8192), c1 = G8_TR(G8_SB(b, h) + boff1 + k * 8192 + 1024); \
        dst[0][k] = __builtin_shufflevector(a0, a1, 0, 1, 2, 3, 4, 5, 6, 7); dst[1][k] = __builtin_shufflevector(c0, c1, 0, 1, 2, 3, 4, 5, 6, 7); } } while (0)
#define G8_MMA(ai, bj, At, Bt) do { __builtin_amdgcn_s_setprio(1); _Pragma("unroll") for (int m = 0; m < 4; ++m) _Pragma("unroll") for (int n = 0; n < 2; ++n) _Pragma("unroll") for (int k = 0; k < 2; ++k) \
        acc[ai][bj][m][n] = __builtin_amdgcn_mfma_f32_16x16x32_bf16(Bt[n][k], At[m][k], acc[ai][bj][m][n], 0, 0, 0); __builtin_amdgcn_s_setprio(0); } while (0)
#define G8_WAIT_V(n) asm volatile("s_waitcnt vmcnt(" #n ")" ::: "memory")
#define G8_WAIT_L(n) asm volatile("s_waitcnt lgkmcnt(" #n ")" ::: "memory")
#define G8_BAR __builtin_amdgcn_s_barrier()
#define G8_SCHED __builtin_amdgcn_sched_barrier(0)
    Unit cur, nxt; int ui = 0;
    if (!S.next(0, cur)) return;
    f32x4 acc[2][2][4][2];
#pragma unroll
    for (int a = 0; a < 2; ++a)
#pragma unroll
        for (int b = 0; b < 2; ++b)
#pragma unroll
            for (int m = 0; m < 4; ++m)
#pragma unroll
                for (int n = 0; n < 2; ++n) acc[a][b][m][n] = (f32x4){0.f, 0.f, 0.f, 0.f};
    bf16x8 At[4][2], B0[2][2], B1[2][2];
    const char* cA = (const char*)cur.A; const char* cB = (const char*)cur.B;
    G8_STAGE(G8_SB(0, 0), cB, voffB); G8_STAGE(G8_SA(0, 0), cA, voffA); G8_STAGE(G8_SB(0, 1), cB + hstepB, voffB); G8_STAGE(G8_SA(0, 1), cA + hstepA, voffA);
    if (wr == 1) G8_BAR;
    G8_WAIT_V(4); G8_BAR;
    G8_STAGE(G8_SB(1, 0), cB + kstepB, voffB); G8_STAGE(G8_SA(1, 0), cA + kstepA, voffA); G8_STAGE(G8_SB(1, 1), cB + hstepB + kstepB, voffB);
    G8_WAIT_V(6); G8_BAR;
    for (;;) {
        const bool has_next = S.next(ui + 1, nxt);
        const char* nA = has_next ? (const char*)nxt.A : cA; const char* nB = has_next ? (const char*)nxt.B : cB;
        for (int t = 0; t < nt; t += 2) {
            const bool last = (t == nt - 2);
            const char* a1 = cA + (size_t)(t + 1) * kstepA;
            const char* a2 = last ? nA : cA + (size_t)(t + 2) * kstepA; const char* b2 = last ? nB : cB + (size_t)(t + 2) * kstepB;
            const char* a3 = a2 + kstepA; const char* b3 = b2 + kstepB;
            G8_LDB(B0, 0, 0); G8_SCHED; G8_LDA(At, 0, 0); G8_STAGE(G8_SA(1, 1), a1 + hstepA, voffA);
            G8_WAIT_L(8); G8_BAR; G8_WAIT_L(0); G8_MMA(0, 0, At, B0); G8_BAR; G8_SCHED;
            G8_LDB(B1, 0, 1); G8_STAGE(G8_SB(0, 0), b2, voffB);
            G8_BAR; G8_WAIT_L(0); G8_MMA(0, 1, At, B1); G8_BAR;
            G8_LDA(At, 0, 1); G8_STAGE(G8_SA(0, 0), a2, voffA);
            G8_BAR; G8_WAIT_L(0); G8_MMA(1, 0, At, B0); G8_BAR; G8_SCHED;
            G8_STAGE(G8_SB(0, 1), b2 + hstepB, voffB);
            G8_WAIT_V(6); G8_BAR; G8_MMA(1, 1, At, B1); G8_BAR;
            G8_LDB(B0, 1, 0); G8_SCHED; G8_LDA(At, 1, 0); G8_STAGE(G8_SA(0, 1), a2 + hstepA, voffA);
            G8_WAIT_L(8); G8_BAR; G8_WAIT_L(0); G8_MMA(0, 0, At, B0); G8_BAR; G8_SCHED;
            G8_LDB(B1, 1, 1); G8_STAGE(G8_SB(1, 0), b3, voffB);
            G8_BAR; G8_WAIT_L(0); G8_MMA(0, 1, At, B1); G8_BAR;
            G8_LDA(At, 1, 1); G8_STAGE(G8_SA(1, 0), a3, voffA);
            G8_BAR; G8_WAIT_L(0); G8_MMA(1, 0, At, B0); G8_BAR; G8_SCHED;
            G8_STAGE(G8_SB(1, 1), b3 + hstepB, voffB);
            G8_WAIT_V(6); G8_BAR; G8_MMA(1, 1, At, B1); G8_BAR;
        }
        E(acc, cur, wr, wc, fr, fq);
        if (!has_next) break;
#pragma unroll
        for (int a = 0; a < 2; ++a)
#pragma unroll
            for (int b = 0; b < 2; ++b)
#pragma unroll
                for (int m = 0; m < 4; ++m)
#pragma unroll
                    for (int n = 0; n < 2; ++n) acc[a][b][m][n] = (f32x4){0.f, 0.f, 0.f, 0.f};
        cur = nxt; cA = nA; cB = nB; ++ui;
    }
    G8_WAIT_V(0);
    if (wr == 0) G8_BAR;
    G8_BAR;
#undef G8_SA
#undef G8_SB
#undef G8_STAGE
#undef G8_LDA
#undef G8_LDB
#undef G8_TR
#undef G8_MMA
#undef G8_WAIT_V
#undef G8_WAIT_L
#undef G8_BAR
#undef G8_SCHED
}
}

__device__ __forceinline__ float gelu_tanh(float x) {
    const float u = 0.7978845608028654f * (x + 0.044715f * x * x * x);
    const float th = 1.0f - 2.0f * __builtin_amdgcn_rcpf(__expf(2.0f * u) + 1.0f);
    return 0.5f * x * (1.0f + th);
}

__device__ __forceinline__ void wait_counter(const char* ws, int idx, unsigned need) {
    if (threadIdx.x < 64) {
        const unsigned* p = (const unsigned*)(ws + WS_CNT + (size_t)idx * 256);
        for (unsigned spins = 0; (unsigned)__builtin_amdgcn_readfirstlane(__hip_atomic_load(p, __ATOMIC_RELAXED, __HIP_MEMORY_SCOPE_AGENT)) < need;) {
            __builtin_amdgcn_s_sleep(8);
            if (++spins > 400000u) break;
        }
        __builtin_amdgcn_fence(__ATOMIC_ACQUIRE, "agent");
        asm volatile("s_waitcnt vmcnt(0)" ::: "memory");
    }
    asm volatile("" ::: "memory"); __builtin_amdgcn_s_barrier(); asm volatile("" ::: "memory");
}
__device__ __forceinline__ void signal_counter(char* ws, int idx) {
    asm volatile("s_waitcnt vmcnt(0)" ::: "memory");
    if ((threadIdx.x & 63) == 0) __hip_atomic_fetch_add((unsigned*)(ws + WS_CNT + (size_t)idx * 256), 1u, __ATOMIC_RELAXED, __HIP_MEMORY_SCOPE_AGENT);
}

__device__ __forceinline__ unsigned poll_ge(const char* ws, int idx, unsigned need) {
    const unsigned* p = (const unsigned*)(ws + WS_CNT + (size_t)idx * 256);
    unsigned v = 0;
    for (unsigned spins = 0; (v = (unsigned)__builtin_amdgcn_readfirstlane(__hip_atomic_load(p, __ATOMIC_RELAXED, __HIP_MEMORY_SCOPE_AGENT))) < need;) {
        __builtin_amdgcn_s_sleep(8);
        if (++spins > 400000u) break;
    }
    asm volatile("" ::: "memory");
    return v;
}

constexpr unsigned SPIN_CAP = 200000u;
constexpr int KS_PITCH = 272;
constexpr int CH_LDS = 160 * 1024;

__device__ __forceinline__ float xmax16_32(float v) {
    auto a = __builtin_amdgcn_permlane16_swap(__builtin_bit_cast(unsigned, v), __builtin_bit_cast(unsigned, v), false, false);
    v = fmaxf(__builtin_bit_cast(float, (unsigned)a[0]), __builtin_bit_cast(float, (unsigned)a[1]));
    auto b = __builtin_amdgcn_permlane32_swap(__builtin_bit_cast(unsigned, v), __builtin_bit_cast(unsigned, v), false, false);
    return fmaxf(__builtin_bit_cast(float, (unsigned)b[0]), __builtin_bit_cast(float, (unsigned)b[1]));
}
__device__ __forceinline__ float xsum16_32(float v) {
    auto a = __builtin_amdgcn_permlane16_swap(__builtin_bit_cast(unsigned, v), __builtin_bit_cast(unsigned, v), false, false);
    v = __builtin_bit_cast(float, (unsigned)a[0]) + __builtin_bit_cast(float, (unsigned)a[1]);
    auto b = __builtin_amdgcn_permlane32_swap(__builtin_bit_cast(unsigned, v), __builtin_bit_cast(unsigned, v), false, false);
    return __builtin_bit_cast(float, (unsigned)b[0]) + __builtin_bit_cast(float, (unsigned)b[1]);
}

__device__ __forceinline__ u32x4 quad_gather(unsigned v) {
    u32x4 r;
    r.x = (unsigned)__builtin_amdgcn_update_dpp(0, (int)v, 0x00, 0xf, 0xf, true); r.y = (unsigned)__builtin_amdgcn_update_dpp(0, (int)v, 0x55, 0xf, 0xf, true);
    r.z = (unsigned)__builtin_amdgcn_update_dpp(0, (int)v, 0xAA, 0xf, 0xf, true); r.w = (unsigned)__builtin_amdgcn_update_dpp(0, (int)v, 0xFF, 0xf, 0xf, true);
    return r;
}
#define STAMP(i) do {} while (0)
#define PCOUNT(sh) do {} while (0)
#define CH_DELAY1 12
#define CH_DELAY2 6
#define CH_DELAY3 8

template <int CPH>
__device__ __forceinline__ void chain_body(LAS unsigned char* lds, const int b, const bf16_t* __restrict__ Wq, const bf16_t* __restrict__ Wk, const bf16_t* __restrict__ Wv,
                                           const bf16_t* __restrict__ Wo, char* ws) {
    constexpr int ES = 128 / CPH, NE = ES / 16, NT = 3 * NE, RG = CPH / 2, RPG = 8 / RG;
    constexpr int VP = ES * 2;
    constexpr int L_RED = 0;
    constexpr int L_RED2 = L_RED + 8 * NT * 512;
    constexpr int L_KS = L_RED2 + 8 * RPG * 64;
    constexpr int L_VS = L_KS + 2 * 32 * KS_PITCH;
    constexpr int L_OS = L_VS + 2 * 48 * VP;
    constexpr bool WOL = (CPH == 4);
    constexpr int L_WO = L_OS + 8 * 1024 * RPG;
    static_assert(L_WO + (WOL ? 65536 : 0) <= 156 * 1024, "LDS");
    const int tid = threadIdx.x, wave = __builtin_amdgcn_readfirstlane(tid >> 6), lane = tid & 63, fr = lane & 15, fq = lane >> 4;
    const int xc = b & 7, yc = b >> 3;
    const int h = xc + 8 * (yc / CPH), j = yc % CPH, g = yc % RG, c = xc * 8 + yc / RG;
    const __amdgpu_buffer_rsrc_t rs = __builtin_amdgcn_make_buffer_rsrc(ws + WS_SENT0, 0, (int)(WS_SENT1 - WS_SENT0), 0x00020000);
    constexpr int O_MEM = (int)(WS_MEM - WS_SENT0), O_QKVM = (int)(WS_QKVM - WS_SENT0), O_OM = (int)(WS_OM - WS_SENT0);
    const __amdgpu_buffer_rsrc_t rkv = __builtin_amdgcn_make_buffer_rsrc(ws + WS_K, 0, (int)(WS_H - WS_K), 0x00020000);
    constexpr int O_V = (int)(WS_V - WS_K);
    bool dead = false;

    bf16x8 wq[NT][4], wo[16];
    {
        LAS unsigned char* stg = lds + wave * 8192;
        constexpr int CPR = ES / 8;
        static_assert(128 * CPR / 64 == 8, "8 chunks per lane and pass");
        u32x4 t[5][8];
        auto issue = [&](int ps) {
            if (ps < 3) { const bf16_t* W = (ps == 0 ? Wq : (ps == 1 ? Wk : Wv)) + (size_t)wave * 128 * HD + h * DH + j * ES;
#pragma unroll
                for (int i = 0; i < 8; ++i) { const int ci = i * 64 + lane; t[ps][i] = *(const u32x4*)(W + (size_t)(ci / CPR) * HD + (ci % CPR) * 8); } }
            else { const bf16_t* W = Wo + (size_t)(wave * 512 + (ps - 3) * 256) * DM + c * 16;
#pragma unroll
                for (int i = 0; i < 8; ++i) { const int ci = i * 64 + lane; t[ps][i] = *(const u32x4*)(W + (size_t)(ci >> 1) * DM + (ci & 1) * 8); } }
        };
        issue(0); issue(1); issue(2);
        asm volatile("" ::: "memory");
#pragma unroll
        for (int ps = 0; ps < 5; ++ps) {
#pragma unroll
            for (int i = 0; i < 8; ++i) *(LAS u32x4*)(stg + (i * 64 + lane) * 16) = t[ps][i];
            asm volatile("s_waitcnt lgkmcnt(0)" ::: "memory");
            if (ps + 3 < 5) issue(ps + 3);
            asm volatile("" ::: "memory");
            if (ps < 3) {
#pragma unroll
                for (int e = 0; e < NE; ++e)
#pragma unroll
                    for (int i = 0; i < 4; ++i) {
                        const int off = (i * 32 + 8 * fq + (fr >> 2)) * (ES * 2) + e * 32 + (lane & 3) * 8;
                        const bf16x4 a0 = __builtin_amdgcn_ds_read_tr16_b64_v4i16((LAS bf16x4*)(stg + off)), a1 = __builtin_amdgcn_ds_read_tr16_b64_v4i16((LAS bf16x4*)(stg + off + 4 * ES * 2));
                        wq[ps * NE + e][i] = __builtin_shufflevector(a0, a1, 0, 1, 2, 3, 4, 5, 6, 7);
                    }
            } else {
#pragma unroll
                for (int i = 0; i < 8; ++i) {
                    const int off = (i * 32 + 8 * fq + (fr >> 2)) * 32 + (lane & 3) * 8;
                    const bf16x4 a0 = __builtin_amdgcn_ds_read_tr16_b64_v4i16((LAS bf16x4*)(stg + off)), a1 = __builtin_amdgcn_ds_read_tr16_b64_v4i16((LAS bf16x4*)(stg + off + 128));
                    wo[(ps - 3) * 8 + i] = __builtin_shufflevector(a0, a1, 0, 1, 2, 3, 4, 5, 6, 7);
                }
            }
            asm volatile("s_waitcnt lgkmcnt(0)" ::: "memory");
        }
        if (WOL) {
#pragma unroll
            for (int i = 8; i < 16; ++i) *(LAS bf16x8*)(lds + L_WO + (wave * 8 + i - 8) * 1024 + lane * 16) = wo[i];
        }
    }
    __syncthreads();

    if (tid == 0) *(LAS unsigned*)(lds + L_OS) = 0u;
    if (tid < 2 * 8 * VP / 16) { const int bf = tid / (8 * VP / 16), r = tid % (8 * VP / 16); *(LAS u32x4*)(lds + L_VS + bf * 48 * VP + 40 * VP + r * 16) = (u32x4){0u, 0u, 0u, 0u}; }
    {
        poll_ge(ws, CNT_KV + (h >> 1), 32u); poll_ge(ws, CNT_KV + 16 + (h >> 1), 32u);
        const int r = tid >> 4, ch = tid & 15;
        *(LAS u32x4*)(lds + L_KS + r * KS_PITCH + ch * 16) = __builtin_amdgcn_raw_buffer_load_b128(rkv, (r * HD + h * DH + ch * 8) * 2, 0, AUX_SC1);
        if (tid < 32 * ES / 8) { const int r2 = tid / (ES / 8), c2 = tid % (ES / 8);
            *(LAS u32x4*)(lds + L_VS + (8 + r2) * VP + c2 * 16) = __builtin_amdgcn_raw_buffer_load_b128(rkv, O_V + (r2 * HD + h * DH + j * ES + c2 * 8) * 2, 0, AUX_SC1); }
    }
    __syncthreads();

    for (int s = 0; s < NSEG; ++s) {
        const int kb = s & 1;
        unsigned long long pcnt = 0; (void)pcnt;
        STAMP(0);
        {
            if (s > 0) __builtin_amdgcn_s_sleep(CH_DELAY1);
            u32x4 a[4];
            const int base = O_MEM + s * (int)MEM_B + wave * 2048 + fq * 128 + (lane & 7) * 16;
            for (unsigned spins = 0;;) {
                asm volatile("" ::: "memory");
#pragma unroll
                for (int i = 0; i < 4; ++i) a[i] = __builtin_amdgcn_raw_buffer_load_b128(rs, base + i * 512, 0, AUX_SC1);
                unsigned mxs = 0;
#pragma unroll
                for (int i = 0; i < 4; ++i) mxs = umax2(mxs, umax4(a[i]));
                if (!__any(mxs == SENT) || dead) break;
                PCOUNT(0);
                if (++spins > SPIN_CAP) { dead = true; if (lane == 0) *(volatile unsigned*)(ws + WS_TMO) = 1u; break; }
            }
            STAMP(1);
            if (b == 0 && tid == 448 && (s & 7) == 0) __hip_atomic_store((unsigned*)(ws + WS_CNT + (size_t)CNT_STEP * 256), (unsigned)s, __ATOMIC_RELAXED, __HIP_MEMORY_SCOPE_AGENT);
            constexpr int NQK = 2 * NE;
            {
                f32x4 acc[NQK];
#pragma unroll
                for (int t = 0; t < NQK; ++t) acc[t] = (f32x4){0.f, 0.f, 0.f, 0.f};
#pragma unroll
                for (int i = 0; i < 4; ++i)
#pragma unroll
                    for (int t = 0; t < NQK; ++t) acc[t] = __builtin_amdgcn_mfma_f32_16x16x32_bf16(wq[t][i], __builtin_bit_cast(bf16x8, a[i]), acc[t], 0, 0, 0);
                if (fr < 8) {
#pragma unroll
                    for (int t = 0; t < NQK; ++t) *(LAS f32x4*)(lds + L_RED + ((wave * NT + t) * 128 + fr * 16 + fq * 4) * 4) = acc[t];
                }
            }
            __syncthreads();
            if (tid < NQK * 64) {
                const int t = tid >> 6, m = (tid & 63) >> 3, np = (tid & 7) * 2, mat = t / NE, col = (t % NE) * 16 + np;
                f32x2 sum = (f32x2){0.f, 0.f};
#pragma unroll
                for (int w = 0; w < 8; ++w) sum += *(const LAS f32x2*)(lds + L_RED + ((w * NT + t) * 128 + m * 16 + np) * 4);
                const u32x4 q4 = quad_gather(desent(pk_bf16(sum.x, sum.y)));
                if ((tid & 3) == 0) __builtin_amdgcn_raw_buffer_store_b128(q4, rs, O_QKVM + s * (int)QKVM_B + ((h * 3 + mat) * 8 + m) * 256 + (j * ES + col) * 2, 0, AUX_SC1);
            }
            STAMP(2);
            {
                f32x4 acc[NE];
#pragma unroll
                for (int t = 0; t < NE; ++t) acc[t] = (f32x4){0.f, 0.f, 0.f, 0.f};
#pragma unroll
                for (int i = 0; i < 4; ++i)
#pragma unroll
                    for (int t = 0; t < NE; ++t) acc[t] = __builtin_amdgcn_mfma_f32_16x16x32_bf16(wq[NQK + t][i], __builtin_bit_cast(bf16x8, a[i]), acc[t], 0, 0, 0);
                if (fr < 8) {
#pragma unroll
                    for (int t = 0; t < NE; ++t) *(LAS f32x4*)(lds + L_RED + ((wave * NT + NQK + t) * 128 + fr * 16 + fq * 4) * 4) = acc[t];
                }
            }
        }
        __syncthreads();
        if (tid >= 2 * NE * 64 && tid < NT * 64) {
            const int t = tid >> 6, m = (tid & 63) >> 3, np = (tid & 7) * 2, col = (t % NE) * 16 + np;
            f32x2 sum = (f32x2){0.f, 0.f};
#pragma unroll
            for (int w = 0; w < 8; ++w) sum += *(const LAS f32x2*)(lds + L_RED + ((w * NT + t) * 128 + m * 16 + np) * 4);
            const unsigned pk = desent(pk_bf16(sum.x, sum.y));
            const u32x4 q4 = quad_gather(pk);
            if ((tid & 3) == 0) __builtin_amdgcn_raw_buffer_store_b128(q4, rs, O_QKVM + s * (int)QKVM_B + ((h * 3 + 2) * 8 + m) * 256 + (j * ES + col) * 2, 0, AUX_SC1);
            *(LAS unsigned*)(lds + L_VS + kb * 48 * VP + m * VP + col * 2) = pk;
            asm volatile("s_waitcnt lgkmcnt(0)" ::: "memory");
            if (lane == 0) __hip_atomic_fetch_add((LAS unsigned*)(lds + L_OS), 1u, __ATOMIC_RELAXED, __HIP_MEMORY_SCOPE_WORKGROUP);
        }
        if (s == NSEG - 1) break;
        if (wave == 0) {
            bf16x8 kx[3][4];
#pragma unroll
            for (int t = 0; t < 3; ++t) {
                int r = t * 16 + fr - 8; r = r < 0 ? 0 : (r > 31 ? 31 : r);
#pragma unroll
                for (int i = 0; i < 4; ++i) kx[t][i] = *(const LAS bf16x8*)(lds + L_KS + kb * 32 * KS_PITCH + r * KS_PITCH + i * 64 + fq * 16);
            }
            __builtin_amdgcn_s_sleep(CH_DELAY2);
            u32x4 qf[4], km[4];
            const int base = O_QKVM + s * (int)QKVM_B + h * 3 * 2048 + (lane & 7) * 256 + fq * 16;
            for (unsigned spins = 0;;) {
                asm volatile("" ::: "memory");
#pragma unroll
                for (int i = 0; i < 4; ++i) { qf[i] = __builtin_amdgcn_raw_buffer_load_b128(rs, base + i * 64, 0, AUX_SC1); km[i] = __builtin_amdgcn_raw_buffer_load_b128(rs, base + 2048 + i * 64, 0, AUX_SC1); }
                unsigned mxs = 0;
#pragma unroll
                for (int i = 0; i < 4; ++i) mxs = umax2(mxs, umax2(umax4(qf[i]), umax4(km[i])));
                if (!__any(mxs == SENT) || dead) break;
                PCOUNT(16);
                if (++spins > SPIN_CAP) { dead = true; if (lane == 0) *(volatile unsigned*)(ws + WS_TMO) = 2u; break; }
            }
            STAMP(3);
            f32x4 sc[3];
#pragma unroll
            for (int t = 0; t < 3; ++t) sc[t] = (f32x4){0.f, 0.f, 0.f, 0.f};
#pragma unroll
            for (int i = 0; i < 4; ++i) {
                const bf16x8 k0 = fr < 8 ? __builtin_bit_cast(bf16x8, km[i]) : kx[0][i];
                sc[1] = __builtin_amdgcn_mfma_f32_16x16x32_bf16(kx[1][i], __builtin_bit_cast(bf16x8, qf[i]), sc[1], 0, 0, 0);
                sc[2] = __builtin_amdgcn_mfma_f32_16x16x32_bf16(kx[2][i], __builtin_bit_cast(bf16x8, qf[i]), sc[2], 0, 0, 0);
                sc[0] = __builtin_amdgcn_mfma_f32_16x16x32_bf16(k0, __builtin_bit_cast(bf16x8, qf[i]), sc[0], 0, 0, 0);
            }
            for (unsigned sp = 0; __hip_atomic_load((LAS unsigned*)(lds + L_OS), __ATOMIC_RELAXED, __HIP_MEMORY_SCOPE_WORKGROUP) < (unsigned)(NE * (s + 1)) && sp < SPIN_CAP; ++sp) {}
            asm volatile("" ::: "memory");
            bf16x8 vf[NE][2];
            const bf16x4 zz = (bf16x4){0, 0, 0, 0};
#pragma unroll
            for (int et = 0; et < NE; ++et) {
                const int voff = L_VS + kb * 48 * VP + (4 * fq + (fr >> 2)) * VP + et * 32 + (lane & 3) * 8;
                const bf16x4 v0 = __builtin_amdgcn_ds_read_tr16_b64_v4i16((LAS bf16x4*)(lds + voff));
                const bf16x4 v1 = __builtin_amdgcn_ds_read_tr16_b64_v4i16((LAS bf16x4*)(lds + voff + 16 * VP));
                const bf16x4 v2 = __builtin_amdgcn_ds_read_tr16_b64_v4i16((LAS bf16x4*)(lds + voff + 32 * VP));
                vf[et][0] = __builtin_shufflevector(v0, v1, 0, 1, 2, 3, 4, 5, 6, 7); vf[et][1] = __builtin_shufflevector(v2, zz, 0, 1, 2, 3, 4, 5, 6, 7);
            }
            const float scale = 0.08838834764831845f;
            const bool hi = fr >= 8;
            float v[6];
            v[0] = hi ? sc[1][2] : sc[0][0]; v[1] = hi ? sc[1][3] : sc[0][1]; v[2] = hi ? sc[2][0] : sc[0][2];
            v[3] = hi ? sc[2][1] : sc[0][3]; v[4] = hi ? sc[2][2] : sc[1][0]; v[5] = hi ? sc[2][3] : sc[1][1];
            float mx = -INFINITY;
#pragma unroll
            for (int i = 0; i < 6; ++i) { float z = rbf(rbf(v[i]) * scale); if (i >= 2 && hi && fq >= 2) z = -INFINITY; v[i] = z; mx = fmaxf(mx, z); }
            mx = fmaxf(mx, __builtin_bit_cast(float, __builtin_amdgcn_update_dpp(0, __builtin_bit_cast(int, mx), 0x128, 0xf, 0xf, true)));
            mx = xmax16_32(mx);
            float sum = 0.f;
#pragma unroll
            for (int i = 0; i < 6; ++i) { v[i] = __expf(v[i] - mx); sum += v[i]; }
            sum += __builtin_bit_cast(float, __builtin_amdgcn_update_dpp(0, __builtin_bit_cast(int, sum), 0x128, 0xf, 0xf, true));
            sum = xsum16_32(sum);
            const float inv = 1.0f / sum;
            const unsigned w0 = pk_bf16(v[0] * inv, v[1] * inv), w1 = pk_bf16(v[2] * inv, v[3] * inv), w2 = pk_bf16(v[4] * inv, v[5] * inv);
            u32x4 p0, p1;
            p0.x = w0; p0.y = w1; p0.z = w2; p0.w = (unsigned)__builtin_amdgcn_update_dpp(0, (int)w0, 0x128, 0xf, 0xf, true);
            p1.x = (unsigned)__builtin_amdgcn_update_dpp(0, (int)w1, 0x128, 0xf, 0xf, true); p1.y = (unsigned)__builtin_amdgcn_update_dpp(0, (int)w2, 0x128, 0xf, 0xf, true); p1.z = 0u; p1.w = 0u;
#pragma unroll
            for (int et = 0; et < NE; ++et) {
                f32x4 o = (f32x4){0.f, 0.f, 0.f, 0.f};
                o = __builtin_amdgcn_mfma_f32_16x16x32_bf16(vf[et][0], __builtin_bit_cast(bf16x8, p0), o, 0, 0, 0);
                o = __builtin_amdgcn_mfma_f32_16x16x32_bf16(vf[et][1], __builtin_bit_cast(bf16x8, p1), o, 0, 0, 0);
                {
                    const unsigned wx = desent(pk_bf16(o[0], o[1])), wy = desent(pk_bf16(o[2], o[3]));
                    auto sx = __builtin_amdgcn_permlane16_swap(wx, wx, false, false); auto sy = __builtin_amdgcn_permlane16_swap(wy, wy, false, false);
                    const u32x4 w = (u32x4){(unsigned)sx[0], (unsigned)sy[0], (unsigned)sx[1], (unsigned)sy[1]};
                    const int k = h * DH + j * ES + et * 16 + 4 * fq;
                    const int off = O_OM + s * (int)OM_B + (fr / RPG) * (8192 * RPG) + (k >> 5) * (64 * RPG) + ((k >> 3) & 3) * (16 * RPG) + (fr % RPG) * 16;
                    if (fr < 8 && (fq & 1) == 0) __builtin_amdgcn_raw_buffer_store_b128(w, rs, off, 0, AUX_SC1);
                }
            }
            STAMP(4);
        } else {
            const int t2 = tid - 64; const int nb = kb ^ 1; const int row0 = (s + 1) * SEG;
            if (((s + 1) & 7) == 0) { const int blk = (s + 1) >> 3; poll_ge(ws, CNT_KV + blk * 32 + (h >> 1), 8u); poll_ge(ws, CNT_KV + blk * 32 + 16 + (h >> 1), 8u); }
            if (s + 1 == 4) { poll_ge(ws, CNT_KV0B + (h >> 1), 16u); poll_ge(ws, CNT_KV0B + 16 + (h >> 1), 16u); }
            for (int i = t2; i < 512 + 32 * ES / 8; i += 448) {
                if (i < 512) { const int r = i >> 4, ch = i & 15;
                    *(LAS u32x4*)(lds + L_KS + nb * 32 * KS_PITCH + r * KS_PITCH + ch * 16) = __builtin_amdgcn_raw_buffer_load_b128(rkv, ((row0 + r) * HD + h * DH + ch * 8) * 2, 0, AUX_SC1); }
                else { const int r2 = (i - 512) / (ES / 8), c2 = (i - 512) % (ES / 8);
                    *(LAS u32x4*)(lds + L_VS + nb * 48 * VP + (8 + r2) * VP + c2 * 16) = __builtin_amdgcn_raw_buffer_load_b128(rkv, O_V + ((row0 + r2) * HD + h * DH + j * ES + c2 * 8) * 2, 0, AUX_SC1); }
            }
        }
        __syncthreads();
        {
            if (WOL) {
#pragma unroll
                for (int i = 8; i < 16; ++i) wo[i] = *(const LAS bf16x8*)(lds + L_WO + (wave * 8 + i - 8) * 1024 + lane * 16);
            }
            __builtin_amdgcn_s_sleep(CH_DELAY3);
            u32x4 a[RPG];
            constexpr int KPG = 16 / RPG;
            const int base = O_OM + s * (int)OM_B + g * (8192 * RPG) + wave * (1024 * RPG) + (fr / RPG) * (64 * RPG) + fq * (16 * RPG) + (fr % RPG) * 16;
            for (unsigned spins = 0;;) {
                asm volatile("" ::: "memory");
#pragma unroll
                for (int i = 0; i < RPG; ++i) a[i] = __builtin_amdgcn_raw_buffer_load_b128(rs, base + i * 1024, 0, AUX_SC1);
                unsigned mxs = 0;
#pragma unroll
                for (int i = 0; i < RPG; ++i) mxs = umax2(mxs, umax4(a[i]));
                if (!__any(mxs == SENT) || dead) break;
                PCOUNT(32);
                if (++spins > SPIN_CAP) { dead = true; if (lane == 0) *(volatile unsigned*)(ws + WS_TMO) = 3u; break; }
            }
            STAMP(5);
            f32x4 acc[KPG];
#pragma unroll
            for (int q = 0; q < KPG; ++q) acc[q] = (f32x4){0.f, 0.f, 0.f, 0.f};
#pragma unroll
            for (int gq = 0; gq < RPG; ++gq)
#pragma unroll
                for (int q = 0; q < KPG; ++q) acc[q] = __builtin_amdgcn_mfma_f32_16x16x32_bf16(wo[gq * KPG + q], __builtin_bit_cast(bf16x8, a[gq]), acc[q], 0, 0, 0);
            f32x4 r = acc[0];
#pragma unroll
            for (int q = 1; q < KPG; ++q) if (fr / RPG == q) r = acc[q];
#pragma unroll
            for (int e = 0; e < 4; ++e) {
                if (RPG <= 2) r[e] += __builtin_bit_cast(float, __builtin_amdgcn_update_dpp(0, __builtin_bit_cast(int, r[e]), 0x122, 0xf, 0xf, true));
                r[e] += __builtin_bit_cast(float, __builtin_amdgcn_update_dpp(0, __builtin_bit_cast(int, r[e]), 0x124, 0xf, 0xf, true));
                r[e] += __builtin_bit_cast(float, __builtin_amdgcn_update_dpp(0, __builtin_bit_cast(int, r[e]), 0x128, 0xf, 0xf, true));
            }
            if (fr < RPG) *(LAS f32x4*)(lds + L_RED2 + ((wave * RPG + fr) * 16 + fq * 4) * 4) = r;
        }
        __syncthreads();
        if (tid < RPG * 8) {
            const int m = tid >> 3, np = (tid & 7) * 2;
            f32x2 sum = (f32x2){0.f, 0.f};
#pragma unroll
            for (int w = 0; w < 8; ++w) sum += *(const LAS f32x2*)(lds + L_RED2 + ((w * RPG + m) * 16 + np) * 4);
            const unsigned pk = desent(pk_bf16(sum.x, sum.y));
            const int off = O_MEM + (s + 1) * (int)MEM_B + (c >> 1) * 512 + ((c & 1) * 2 + (np >> 3)) * 128 + (g * RPG + m) * 16;
            const u32x4 q4 = quad_gather(pk);
            if ((tid & 3) == 0) __builtin_amdgcn_raw_buffer_store_b128(q4, rs, off, 0, AUX_SC1);
        }
        STAMP(6);
    }
}

template <int MT, class Epi>
__device__ __forceinline__ void gemm128_w8(LAS unsigned char* lds, const bf16_t* A, const int lda, const bf16_t* B, const int ldb, const int K, const Epi& E) {
    int tid = threadIdx.x; asm volatile("" : "+v"(tid));
    const int wid = __builtin_amdgcn_readfirstlane(tid >> 6), lane = tid & 63, wr = wid >> 2, wc = wid & 3, fr = lane & 15, fq = lane >> 4;
    constexpr int NA = MT / 2;
    unsigned voffA[NA], voffB[2];
#pragma unroll
    for (int i = 0; i < NA; ++i) { int R, C; g8::stage_rc(tid * 16 + i * 8192, R, C); voffA[i] = (unsigned)(R * lda + C) * 2u; }
#pragma unroll
    for (int i = 0; i < 2; ++i) { const int bb = tid * 16 + i * 8192, row = bb >> 8, ch = (bb & 255) >> 4, seg = (ch >> 1) ^ g8::bsw(row);
        voffB[i] = (unsigned)(row * ldb + seg * 16 + (ch & 1) * 8) * 2u; }
    const char* cA = (const char*)A; const char* cB = (const char*)B;
    const unsigned ldsw = (unsigned)wid * 1024u;
    const int aoff = g8::lds_byte(wr * 16 * MT + fr, fq * 8);
    const int q4 = (lane & 15) >> 2, p4 = lane & 3, sB = q4 | ((fq & 1) << 2);
    const int boff0 = (8 * fq + q4) * 256 + (((wc * 2 + 0) ^ sB) * 32) + 8 * p4;
    const int boff1 = (8 * fq + q4) * 256 + (((wc * 2 + 1) ^ sB) * 32) + 8 * p4;
    f32x4 acc[MT][2];
#pragma unroll
    for (int m = 0; m < MT; ++m) { acc[m][0] = (f32x4){0.f, 0.f, 0.f, 0.f}; acc[m][1] = (f32x4){0.f, 0.f, 0.f, 0.f}; }
    const int nt = K / 64;
#define STG(buf, t) do { _Pragma("unroll") for (int _i = 0; _i < NA; ++_i) \
        __builtin_amdgcn_global_load_lds((const unsigned*)(cA + (size_t)(t) * 128 + voffA[_i]), (LAS unsigned*)(lds + (buf) * 32768 + ldsw + _i * 8192), 16, 0, 0); \
        _Pragma("unroll") for (int _i = 0; _i < 2; ++_i) \
        __builtin_amdgcn_global_load_lds((const unsigned*)(cB + (size_t)(t) * 64 * ldb * 2 + voffB[_i]), (LAS unsigned*)(lds + (buf) * 32768 + 16384 + ldsw + _i * 8192), 16, 0, 0); } while (0)
    STG(0, 0); STG(1, 1); STG(2, 2);
    int cur = 0;
    for (int t = 0; t < nt; ++t) {
        if (MT == 4) { if (t + 2 < nt) asm volatile("s_waitcnt vmcnt(8)" ::: "memory"); else if (t + 1 < nt) asm volatile("s_waitcnt vmcnt(4)" ::: "memory"); else asm volatile("s_waitcnt vmcnt(0)" ::: "memory"); }
        else { if (t + 2 < nt) asm volatile("s_waitcnt vmcnt(6)" ::: "memory"); else if (t + 1 < nt) asm volatile("s_waitcnt vmcnt(3)" ::: "memory"); else asm volatile("s_waitcnt vmcnt(0)" ::: "memory"); }
        __builtin_amdgcn_s_barrier();
        asm volatile("" ::: "memory");
        if (t + 3 < nt) STG((cur + 3) & 3, t + 3);
        bf16x8 At[MT][2], Bt[2][2];
        LAS unsigned char* sa = lds + cur * 32768; LAS unsigned char* sb = sa + 16384;
#pragma unroll
        for (int k = 0; k < 2; ++k) {
            bf16x4 a0 = __builtin_amdgcn_ds_read_tr16_b64_v4i16((LAS bf16x4*)(sb + boff0 + k * 8192)), a1 = __builtin_amdgcn_ds_read_tr16_b64_v4i16((LAS bf16x4*)(sb + boff0 + k * 8192 + 1024));
            bf16x4 c0 = __builtin_amdgcn_ds_read_tr16_b64_v4i16((LAS bf16x4*)(sb + boff1 + k * 8192)), c1 = __builtin_amdgcn_ds_read_tr16_b64_v4i16((LAS bf16x4*)(sb + boff1 + k * 8192 + 1024));
            Bt[0][k] = __builtin_shufflevector(a0, a1, 0, 1, 2, 3, 4, 5, 6, 7); Bt[1][k] = __builtin_shufflevector(c0, c1, 0, 1, 2, 3, 4, 5, 6, 7);
#pragma unroll
            for (int m = 0; m < MT; ++m) At[m][k] = *(const LAS bf16x8*)(sa + aoff + m * 2048 + k * 1024);
        }
        __builtin_amdgcn_s_setprio(1);
#pragma unroll
        for (int k = 0; k < 2; ++k)
#pragma unroll
            for (int m = 0; m < MT; ++m)
#pragma unroll
                for (int n = 0; n < 2; ++n) acc[m][n] = __builtin_amdgcn_mfma_f32_16x16x32_bf16(Bt[n][k], At[m][k], acc[m][n], 0, 0, 0);
        __builtin_amdgcn_s_setprio(0);
        asm volatile("s_waitcnt lgkmcnt(0)" ::: "memory");
        cur = (cur + 1) & 3;
    }
#undef STG
    __builtin_amdgcn_s_barrier();
    E(acc, wr, wc, fr, fq);
}

constexpr int XV_PITCH = 272;
template <bool INK>
__device__ __forceinline__ void xattn_item(LAS unsigned char* vs, char* ws, const int s, const int h) {
    const int lane = threadIdx.x & 63, fr = lane & 15, fq = lane >> 4;
    const bf16_t* Q = (const bf16_t*)(ws + WS_Q) + (size_t)s * SEG * HD + h * DH;
    const bf16_t* K = (const bf16_t*)(ws + WS_K) + (size_t)s * SEG * HD + h * DH;
    const bf16_t* V = (const bf16_t*)(ws + WS_V) + (size_t)s * SEG * HD + h * DH;
    const __amdgpu_buffer_rsrc_t rm = __builtin_amdgcn_make_buffer_rsrc(ws + WS_QKVM, 0, (int)(WS_OM - WS_QKVM), 0x00020000);
    const int kmo = s * (int)QKVM_B + (h * 3 + 1) * 2048, vmo = kmo + 2048;
    u32x4 km[4], vm[2];
    for (unsigned spins = 0;;) {
        asm volatile("" ::: "memory");
#pragma unroll
        for (int i = 0; i < 4; ++i) km[i] = __builtin_amdgcn_raw_buffer_load_b128(rm, kmo + (lane & 7) * 256 + i * 64 + fq * 16, 0, INK ? AUX_SC1 : 0);
#pragma unroll
        for (int i = 0; i < 2; ++i) vm[i] = __builtin_amdgcn_raw_buffer_load_b128(rm, vmo + (i * 4 + fq) * 256 + fr * 16, 0, INK ? AUX_SC1 : 0);
        if (!INK) break;
        unsigned mxs = umax2(umax4(vm[0]), umax4(vm[1]));
#pragma unroll
        for (int i = 0; i < 4; ++i) mxs = umax2(mxs, umax4(km[i]));
        if (!__any(mxs == SENT)) break;
        __builtin_amdgcn_s_sleep(16);
        if (++spins > SPIN_CAP) break;
    }
#pragma unroll
    for (int i = 0; i < 12; ++i) {
        const int r = i * 4 + fq;
        u32x4 v = (u32x4){0u, 0u, 0u, 0u};
        if (i < 2) v = vm[i]; else if (i < 10) v = *(const u32x4*)(V + (size_t)(r - 8) * HD + fr * 8);
        *(LAS u32x4*)(vs + r * XV_PITCH + fr * 16) = v;
    }
    f32x4 sc[3][2];
#pragma unroll
    for (int t = 0; t < 3; ++t) { sc[t][0] = (f32x4){0.f, 0.f, 0.f, 0.f}; sc[t][1] = (f32x4){0.f, 0.f, 0.f, 0.f}; }
#pragma unroll
    for (int i = 0; i < 4; ++i) {
        const bf16x8 q0 = *(const bf16x8*)(Q + (size_t)fr * HD + i * 32 + fq * 8), q1 = *(const bf16x8*)(Q + (size_t)(16 + fr) * HD + i * 32 + fq * 8);
#pragma unroll
        for (int t = 0; t < 3; ++t) {
            int r = t * 16 + fr - 8; r = r < 0 ? 0 : (r > 31 ? 31 : r);
            bf16x8 kf = *(const bf16x8*)(K + (size_t)r * HD + i * 32 + fq * 8);
            if (t == 0 && fr < 8) kf = __builtin_bit_cast(bf16x8, km[i]);
            sc[t][0] = __builtin_amdgcn_mfma_f32_16x16x32_bf16(kf, q0, sc[t][0], 0, 0, 0);
            sc[t][1] = __builtin_amdgcn_mfma_f32_16x16x32_bf16(kf, q1, sc[t][1], 0, 0, 0);
        }
    }
    const float scale = 0.08838834764831845f, NEG = rbf(-1000000000.0f);
    u32x4 p0[2], p1[2];
#pragma unroll
    for (int rt = 0; rt < 2; ++rt) {
        const int row = rt * 16 + fr;
        float v[12]; float mx = -INFINITY;
#pragma unroll
        for (int t = 0; t < 3; ++t)
#pragma unroll
            for (int r = 0; r < 4; ++r) { const int key = t * 16 + fq * 4 + r; float z = rbf(rbf(sc[t][rt][r]) * scale);
                if (key - 8 > row) z = NEG; if (key >= 40) z = -INFINITY; v[t * 4 + r] = z; mx = fmaxf(mx, z); }
        mx = xmax16_32(mx);
        float sum = 0.f;
#pragma unroll
        for (int i = 0; i < 12; ++i) { v[i] = __expf(v[i] - mx); sum += v[i]; }
        sum = xsum16_32(sum);
        const float inv = 1.0f / sum;
        p0[rt].x = pk_bf16(v[0] * inv, v[1] * inv); p0[rt].y = pk_bf16(v[2] * inv, v[3] * inv); p0[rt].z = pk_bf16(v[4] * inv, v[5] * inv); p0[rt].w = pk_bf16(v[6] * inv, v[7] * inv);
        p1[rt].x = pk_bf16(v[8] * inv, v[9] * inv); p1[rt].y = pk_bf16(v[10] * inv, v[11] * inv); p1[rt].z = 0u; p1[rt].w = 0u;
    }
    asm volatile("s_waitcnt lgkmcnt(0)" ::: "memory");
    const __amdgpu_buffer_rsrc_t ro = __builtin_amdgcn_make_buffer_rsrc(ws + WS_O, 0, S_LEN * HD * 2, 0x00020000);
    const int obase = (s * SEG * HD + h * DH) * 2;
    const bf16x4 zz = (bf16x4){0, 0, 0, 0};
    u32x2 ow[8][2];
#pragma unroll
    for (int et = 0; et < 8; ++et) {
        const int voff = (4 * fq + (fr >> 2)) * XV_PITCH + et * 32 + (lane & 3) * 8;
        const bf16x4 v0 = __builtin_amdgcn_ds_read_tr16_b64_v4i16((LAS bf16x4*)(vs + voff));
        const bf16x4 v1 = __builtin_amdgcn_ds_read_tr16_b64_v4i16((LAS bf16x4*)(vs + voff + 16 * XV_PITCH));
        const bf16x4 v2 = __builtin_amdgcn_ds_read_tr16_b64_v4i16((LAS bf16x4*)(vs + voff + 32 * XV_PITCH));
        const bf16x8 va = __builtin_shufflevector(v0, v1, 0, 1, 2, 3, 4, 5, 6, 7), vb = __builtin_shufflevector(v2, zz, 0, 1, 2, 3, 4, 5, 6, 7);
#pragma unroll
        for (int rt = 0; rt < 2; ++rt) {
            f32x4 o = (f32x4){0.f, 0.f, 0.f, 0.f};
            o = __builtin_amdgcn_mfma_f32_16x16x32_bf16(va, __builtin_bit_cast(bf16x8, p0[rt]), o, 0, 0, 0);
            o = __builtin_amdgcn_mfma_f32_16x16x32_bf16(vb, __builtin_bit_cast(bf16x8, p1[rt]), o, 0, 0, 0);
            ow[et][rt].x = pk_bf16(o[0], o[1]); ow[et][rt].y = pk_bf16(o[2], o[3]);
        }
    }
    asm volatile("s_waitcnt lgkmcnt(0)" ::: "memory");
#pragma unroll
    for (int et = 0; et < 8; ++et)
#pragma unroll
        for (int rt = 0; rt < 2; ++rt) *(LAS u32x2*)(vs + (rt * 16 + fr) * XV_PITCH + et * 32 + fq * 8) = ow[et][rt];
    asm volatile("s_waitcnt lgkmcnt(0)" ::: "memory");
#pragma unroll
    for (int i = 0; i < 8; ++i) {
        const int r = i * 4 + fq;
        const u32x4 w = *(const LAS u32x4*)(vs + r * XV_PITCH + fr * 16);
        __builtin_amdgcn_raw_buffer_store_b128(w, ro, obase + (r * HD + fr * 8) * 2, 0, INK ? AUX_SC1 : 0);
    }
    asm volatile("s_waitcnt lgkmcnt(0)" ::: "memory");
}

__device__ __forceinline__ void store_pair_sc1(const __amdgpu_buffer_rsrc_t rs, int off0, u32x2 w0, u32x2 w1, int fq) {
    auto sx = __builtin_amdgcn_permlane16_swap(w0.x, w1.x, false, false); auto sy = __builtin_amdgcn_permlane16_swap(w0.y, w1.y, false, false);
    const u32x4 w = (u32x4){(unsigned)sx[0], (unsigned)sy[0], (unsigned)sx[1], (unsigned)sy[1]};
    __builtin_amdgcn_raw_buffer_store_b128(w, rs, off0 + (fq & 1) * 32 + (fq >> 1) * 16, 0, AUX_SC1);
}
struct SchedOne {
    g8::Unit u; bool valid;
    __device__ __forceinline__ bool next(int i, g8::Unit& o) const { if (i != 0 || !valid) return false; o = u; return true; }
};
struct SchedKVQ {
    const bf16_t* A; const bf16_t* Wq; long dk, dv; int r;
    __device__ __forceinline__ bool next(int i, g8::Unit& u) const {
        const int L = r + 128 * i; if (L >= 352) return false;
        int pm, pn;
        if (L < 224) { pm = 1 + (L >> 5); pn = 16 + (L & 31); } else { pm = (L - 224) & 7; pn = (L - 224) >> 3; }
        const int mat = pn >> 4;
        u.A = A + (size_t)pm * 256 * DM; u.B = Wq + (mat == 1 ? dk : 0L) + (mat == 2 ? dv : 0L) + (pn & 15) * 256; u.pm = pm; u.pn = pn; return true;
    }
};
struct EpiQKV {
    char* ws;
    __device__ __forceinline__ void operator()(const f32x4 (&acc)[2][2][4][2], const g8::Unit& u, int wr, int wc, int fr, int fq) const {
        const __amdgpu_buffer_rsrc_t rq = __builtin_amdgcn_make_buffer_rsrc(ws + WS_Q, 0, (int)(WS_H - WS_Q), 0x00020000);
        const int base = (u.pn >> 4) * (S_LEN * HD * 2) + ((u.pm * 256 + wr * 64 + fr) * HD + (u.pn & 15) * 256 + wc * 32) * 2;
#pragma unroll
        for (int ai = 0; ai < 2; ++ai)
#pragma unroll
            for (int m = 0; m < 4; ++m)
#pragma unroll
                for (int bj = 0; bj < 2; ++bj) { const f32x4 v0 = acc[ai][bj][m][0], v1 = acc[ai][bj][m][1];
                    store_pair_sc1(rq, base + ((ai * 128 + m * 16) * HD + bj * 128) * 2, (u32x2){pk_bf16(v0[0], v0[1]), pk_bf16(v0[2], v0[3])}, (u32x2){pk_bf16(v1[0], v1[1]), pk_bf16(v1[2], v1[3])}, fq); }
        asm volatile("s_waitcnt vmcnt(0)" ::: "memory");
        if ((threadIdx.x & 63) == 0) {
            if (u.pn >= 16) __hip_atomic_fetch_add((unsigned*)(ws + WS_CNT + (size_t)(CNT_KV + u.pm * 32 + u.pn - 16) * 256), 1u, __ATOMIC_RELAXED, __HIP_MEMORY_SCOPE_AGENT);
            __hip_atomic_fetch_add((unsigned*)(ws + WS_CNT + (size_t)(CNT_QKV + u.pm) * 256), 1u, __ATOMIC_RELAXED, __HIP_MEMORY_SCOPE_AGENT);
        }
    }
};
template <int MT>
struct EpiKV0 {
    char* ws; int row0, pn2;
    __device__ __forceinline__ void operator()(const f32x4 (&acc)[MT][2], int wr, int wc, int fr, int fq) const {
        const __amdgpu_buffer_rsrc_t rq = __builtin_amdgcn_make_buffer_rsrc(ws + WS_K, 0, (int)(WS_H - WS_K), 0x00020000);
        const int base = (pn2 >> 5) * (S_LEN * HD * 2) + ((row0 + wr * 16 * MT + fr) * HD + (pn2 & 31) * 128 + wc * 32) * 2;
#pragma unroll
        for (int m = 0; m < MT; ++m) { const f32x4 v0 = acc[m][0], v1 = acc[m][1];
            store_pair_sc1(rq, base + m * 16 * HD * 2, (u32x2){pk_bf16(v0[0], v0[1]), pk_bf16(v0[2], v0[3])}, (u32x2){pk_bf16(v1[0], v1[1]), pk_bf16(v1[2], v1[3])}, fq); }
        asm volatile("s_waitcnt vmcnt(0)" ::: "memory");
        if ((threadIdx.x & 63) == 0) {
            __hip_atomic_fetch_add((unsigned*)(ws + WS_CNT + (size_t)((row0 < 128 ? CNT_KV : CNT_KV0B) + (pn2 >> 1)) * 256), 1u, __ATOMIC_RELAXED, __HIP_MEMORY_SCOPE_AGENT);
            __hip_atomic_fetch_add((unsigned*)(ws + WS_CNT + (size_t)CNT_QKV * 256), 1u, __ATOMIC_RELAXED, __HIP_MEMORY_SCOPE_AGENT);
        }
    }
};
struct EpiMLP1 {
    char* ws; const bf16_t* b1;
    __device__ __forceinline__ void operator()(const f32x4 (&acc)[2][2][4][2], const g8::Unit& u, int wr, int wc, int fr, int fq) const {
        const __amdgpu_buffer_rsrc_t rh = __builtin_amdgcn_make_buffer_rsrc(ws + WS_H, 0, S_LEN * FF * 2, 0x00020000);
        const int row0 = u.pm * 256 + wr * 64 + fr, col0 = u.pn * 256 + wc * 32;
#pragma unroll
        for (int bj = 0; bj < 2; ++bj) {
            const u32x2 ba = *(const u32x2*)(b1 + col0 + bj * 128 + 4 * fq), bb = *(const u32x2*)(b1 + col0 + bj * 128 + 16 + 4 * fq);
#pragma unroll
            for (int ai = 0; ai < 2; ++ai)
#pragma unroll
                for (int m = 0; m < 4; ++m) { const f32x4 v0 = acc[ai][bj][m][0], v1 = acc[ai][bj][m][1];
                    u32x2 w0, w1;
                    w0.x = pk_bf16(gelu_tanh(rbf(rbf(v0[0]) + bf2f(ba.x & 0xffffu))), gelu_tanh(rbf(rbf(v0[1]) + bf2f(ba.x >> 16))));
                    w0.y = pk_bf16(gelu_tanh(rbf(rbf(v0[2]) + bf2f(ba.y & 0xffffu))), gelu_tanh(rbf(rbf(v0[3]) + bf2f(ba.y >> 16))));
                    w1.x = pk_bf16(gelu_tanh(rbf(rbf(v1[0]) + bf2f(bb.x & 0xffffu))), gelu_tanh(rbf(rbf(v1[1]) + bf2f(bb.x >> 16))));
                    w1.y = pk_bf16(gelu_tanh(rbf(rbf(v1[2]) + bf2f(bb.y & 0xffffu))), gelu_tanh(rbf(rbf(v1[3]) + bf2f(bb.y >> 16))));
                    store_pair_sc1(rh, ((row0 + ai * 128 + m * 16) * FF + col0 + bj * 128) * 2, w0, w1, fq);
                    if (m & 1) asm volatile("" ::: "memory"); }
        }
        signal_counter(ws, CNT_H + u.pm);
    }
};
struct EpiMLP2 {
    char* ws; const bf16_t* b2; int row0, col0;
    __device__ __forceinline__ void operator()(const f32x4 (&acc)[4][2], int wr, int wc, int fr, int fq) const {
        const __amdgpu_buffer_rsrc_t rb = __builtin_amdgcn_make_buffer_rsrc(ws + WS_B, 0, S_LEN * DM * 2, 0x00020000);
        const int col = col0 + wc * 32;
        const u32x2 ba = *(const u32x2*)(b2 + col + 4 * fq), bb = *(const u32x2*)(b2 + col + 16 + 4 * fq);
#pragma unroll
        for (int m = 0; m < 4; ++m) { const f32x4 v0 = acc[m][0], v1 = acc[m][1];
            u32x2 w0, w1;
            w0.x = pk_bf16(rbf(v0[0]) + bf2f(ba.x & 0xffffu), rbf(v0[1]) + bf2f(ba.x >> 16)); w0.y = pk_bf16(rbf(v0[2]) + bf2f(ba.y & 0xffffu), rbf(v0[3]) + bf2f(ba.y >> 16));
            w1.x = pk_bf16(rbf(v1[0]) + bf2f(bb.x & 0xffffu), rbf(v1[1]) + bf2f(bb.x >> 16)); w1.y = pk_bf16(rbf(v1[2]) + bf2f(bb.y & 0xffffu), rbf(v1[3]) + bf2f(bb.y >> 16));
            store_pair_sc1(rb, ((row0 + wr * 64 + m * 16 + fr) * DM + col) * 2, w0, w1, fq); }
        signal_counter(ws, CNT_B + (row0 >> 8));
    }
};
__device__ __forceinline__ u32x2 combine4(const f32x4 v, const u32x2 xw, const u32x2 bw) {
    u32x2 w;
    w.x = pk_bf16(rbf(bf2f(xw.x & 0xffffu) + rbf(v[0])) + bf2f(bw.x & 0xffffu), rbf(bf2f(xw.x >> 16) + rbf(v[1])) + bf2f(bw.x >> 16));
    w.y = pk_bf16(rbf(bf2f(xw.y & 0xffffu) + rbf(v[2])) + bf2f(bw.y & 0xffffu), rbf(bf2f(xw.y >> 16) + rbf(v[3])) + bf2f(bw.y >> 16));
    return w;
}
template <int MT>
struct EpiOut {
    const bf16_t* x; const bf16_t* bout; bf16_t* out; int row0, col0;
    __device__ __forceinline__ void operator()(const f32x4 (&acc)[MT][2], int wr, int wc, int fr, int fq) const {
#pragma unroll
        for (int m = 0; m < MT; ++m)
#pragma unroll
            for (int n = 0; n < 2; ++n) {
                const size_t off = (size_t)(row0 + wr * 16 * MT + m * 16 + fr) * DM + col0 + wc * 32 + n * 16 + 4 * fq;
                *(u32x2*)(out + off) = combine4(acc[m][n], *(const u32x2*)(x + off), *(const u32x2*)(bout + off));
            }
    }
};

constexpr int TAIL_BLK0 = 6;
__global__ void __launch_bounds__(512, 2) k_mega(const bf16_t* __restrict__ x, const bf16_t* __restrict__ Wq, const bf16_t* __restrict__ Wk, const bf16_t* __restrict__ Wv,
                                                 const bf16_t* __restrict__ Wo, const bf16_t* __restrict__ w1, const bf16_t* __restrict__ b1, const bf16_t* __restrict__ w2,
                                                 const bf16_t* __restrict__ b2, char* ws, bf16_t* __restrict__ out) {
    extern __shared__ __attribute__((aligned(16))) unsigned char shm_[];
    LAS unsigned char* lds = (LAS unsigned char*)shm_;
    const int b = blockIdx.x;
    const int role = (b >> 3) & 1, r = (b >> 4) * 8 + (b & 7);
    if (role == 0) { chain_body<4>(lds, r, Wq, Wk, Wv, Wo, ws); return; }
#define WSTAMP(i) do {} while (0)
    WSTAMP(0);
    {
        const int row0 = (r >> 6) * 64, pn2 = r & 63;
        EpiKV0<2> E{ws, row0, pn2};
        gemm128_w8<2, EpiKV0<2>>(lds, (const bf16_t*)(ws + WS_XN) + (size_t)row0 * DM, DM, (pn2 >= 32 ? Wv : Wk) + (pn2 & 31) * 128, HD, DM, E);
    }
    if (r < 64) {
        EpiKV0<4> E{ws, 128, r};
        gemm128_w8<4, EpiKV0<4>>(lds, (const bf16_t*)(ws + WS_XN) + (size_t)128 * DM, DM, (r >= 32 ? Wv : Wk) + (r & 31) * 128, HD, DM, E);
    }
    WSTAMP(1);
    {
        SchedKVQ S{(const bf16_t*)(ws + WS_XN), Wq, (long)(Wk - Wq), (long)(Wv - Wq), r ^ 64};
        EpiQKV E{ws}; g8::gemm_phase<EpiQKV, SchedKVQ>(lds, DM, DM, HD, S, E);
    }
    WSTAMP(2);
    {
        const int r2 = r ^ 64, L = r2 >= 96 ? r2 - 96 : 32 + r2;
        SchedOne S; S.valid = (r2 >= 96) || (r2 < 80); S.u.pm = L & 7; S.u.pn = L >> 3; S.u.A = (const bf16_t*)(ws + WS_Y) + (size_t)S.u.pm * 256 * DM; S.u.B = w1 + S.u.pn * 256;
        EpiMLP1 E{ws, b1}; g8::gemm_phase<EpiMLP1, SchedOne>(lds, DM, DM, FF, S, E);
    }
    WSTAMP(3);
    {
        const int pm = r >> 3, pn = r & 7;
        wait_counter(ws, CNT_H + (pm >> 1), 14 * 8);
        EpiMLP2 E{ws, b2, pm * 128, pn * 128};
        gemm128_w8<4, EpiMLP2>(lds, (const bf16_t*)(ws + WS_H) + (size_t)pm * 128 * FF, FF, w2 + pn * 128, DM, FF, E);
    }
    WSTAMP(4);
    if (r < 80) {
        const int blk = r >> 4, t = r & 15;
        {
            if (threadIdx.x < 64) poll_ge(ws, CNT_STEP, (unsigned)(8 * blk + 8));
            wait_counter(ws, CNT_QKV + blk, (blk == 0 ? 192 * 8 : 32 * 8) + 16 * 8);
            WSTAMP(5);
            const int wave = threadIdx.x >> 6;
#pragma unroll 1
            for (int i = 0; i < 2; ++i) { const int item = t * 16 + wave * 2 + i; xattn_item<true>(lds + wave * 48 * XV_PITCH, ws, blk * 8 + (item >> 5), item & 31); }
            signal_counter(ws, CNT_O + blk);
        }
        {
            if (threadIdx.x < 64) poll_ge(ws, CNT_B + blk, 16 * 8);
            wait_counter(ws, CNT_O + blk, 16 * 8);
            WSTAMP(6);
            const int pm = blk * 2 + (t >> 3), pn = t & 7;
            EpiOut<4> E{x, (const bf16_t*)(ws + WS_B), out, pm * 128, pn * 128};
            gemm128_w8<4, EpiOut<4>>(lds, (const bf16_t*)(ws + WS_O) + (size_t)pm * 128 * HD, HD, Wo + pn * 128, DM, HD, E);
        }
    } else if (r < 112) {
        constexpr int blk = 5; const int t = r - 80;
        if (threadIdx.x < 64) poll_ge(ws, CNT_STEP, (unsigned)(8 * blk + 8));
        wait_counter(ws, CNT_QKV + blk, 32 * 8 + 16 * 8);
        WSTAMP(5);
        const int wave = threadIdx.x >> 6, item = t * 8 + wave;
        xattn_item<true>(lds + wave * 48 * XV_PITCH, ws, blk * 8 + (item >> 5), item & 31);
        signal_counter(ws, CNT_O + blk);
        if (threadIdx.x < 64) poll_ge(ws, CNT_B + blk, 16 * 8);
        wait_counter(ws, CNT_O + blk, 32 * 8);
        WSTAMP(6);
        const int pm = blk * 4 + (t >> 3), pn = t & 7;
        EpiOut<2> E{x, (const bf16_t*)(ws + WS_B), out, pm * 64, pn * 128};
        gemm128_w8<2, EpiOut<2>>(lds, (const bf16_t*)(ws + WS_O) + (size_t)pm * 64 * HD, HD, Wo + pn * 128, DM, HD, E);
    }
    WSTAMP(7);
}

__global__ void __launch_bounds__(256) k_tail_attn(char* ws) {
    __shared__ __attribute__((aligned(16))) unsigned char shm_[4 * 48 * XV_PITCH];
    const int wave = threadIdx.x >> 6, item = blockIdx.x * 4 + wave;
    xattn_item<false>((LAS unsigned char*)shm_ + wave * 48 * XV_PITCH, ws, TAIL_BLK0 * 8 + (item >> 5), item & 31);
}
__global__ void __launch_bounds__(256) k_tail_gemm(const bf16_t* __restrict__ x, const bf16_t* __restrict__ Wo, const char* __restrict__ ws, bf16_t* __restrict__ out) {
    extern __shared__ __attribute__((aligned(16))) unsigned char shm_[];
    const int tid = threadIdx.x, wave = __builtin_amdgcn_readfirstlane(tid >> 6), lane = tid & 63, fr = lane & 15, fq = lane >> 4;
    LAS unsigned char* lds = (LAS unsigned char*)shm_ + wave * 36864;
    const int pn = blockIdx.x & 31, pm = blockIdx.x >> 5, row0 = TAIL_BLK0 * 256 + pm * 64, col0 = pn * 32;
    const char* cA = (const char*)((const bf16_t*)(ws + WS_O) + (size_t)row0 * HD + wave * 1024);
    const char* cB = (const char*)(Wo + (size_t)wave * 1024 * DM + col0);
    unsigned voffA[8], voffB[4];
#pragma unroll
    for (int i = 0; i < 8; ++i) { const int c = i * 64 + lane, row = c >> 3, ch = (c & 7) ^ (row & 7); voffA[i] = (unsigned)(row * HD + ch * 8) * 2u; }
#pragma unroll
    for (int i = 0; i < 4; ++i) { const int c = i * 64 + lane, k = c >> 2, cb = c & 3; voffB[i] = (unsigned)(k * DM + cb * 8) * 2u; }
#define STG(buf, t) do { _Pragma("unroll") for (int _i = 0; _i < 8; ++_i) \
        __builtin_amdgcn_global_load_lds((const unsigned*)(cA + (size_t)(t) * 128 + voffA[_i]), (LAS unsigned*)(lds + (buf) * 12288 + _i * 1024), 16, 0, 0); \
        _Pragma("unroll") for (int _i = 0; _i < 4; ++_i) \
        __builtin_amdgcn_global_load_lds((const unsigned*)(cB + (size_t)(t) * 64 * DM * 2 + voffB[_i]), (LAS unsigned*)(lds + (buf) * 12288 + 8192 + _i * 1024), 16, 0, 0); } while (0)
    f32x4 acc[4][2];
#pragma unroll
    for (int m = 0; m < 4; ++m) { acc[m][0] = (f32x4){0.f, 0.f, 0.f, 0.f}; acc[m][1] = (f32x4){0.f, 0.f, 0.f, 0.f}; }
    STG(0, 0); STG(1, 1);
    int cur = 0;
    for (int t = 0; t < 16; ++t) {
        int nx = cur + 2; if (nx >= 3) nx -= 3;
        if (t + 2 < 16) { STG(nx, t + 2); asm volatile("s_waitcnt vmcnt(24)" ::: "memory"); }
        else if (t + 1 < 16) asm volatile("s_waitcnt vmcnt(12)" ::: "memory");
        else asm volatile("s_waitcnt vmcnt(0)" ::: "memory");
        LAS unsigned char* sa = lds + cur * 12288; LAS unsigned char* sb = sa + 8192;
        bf16x8 At[4][2], Bt[2][2];
#pragma unroll
        for (int k = 0; k < 2; ++k) {
#pragma unroll
            for (int m = 0; m < 4; ++m) { const int row = m * 16 + fr; At[m][k] = *(const LAS bf16x8*)(sa + row * 128 + (((k * 4 + fq) ^ (row & 7)) * 16)); }
#pragma unroll
            for (int n = 0; n < 2; ++n) {
                const int off = (k * 32 + 8 * fq + (fr >> 2)) * 64 + n * 32 + (lane & 3) * 8;
                const bf16x4 a0 = __builtin_amdgcn_ds_read_tr16_b64_v4i16((LAS bf16x4*)(sb + off)), a1 = __builtin_amdgcn_ds_read_tr16_b64_v4i16((LAS bf16x4*)(sb + off + 256));
                Bt[n][k] = __builtin_shufflevector(a0, a1, 0, 1, 2, 3, 4, 5, 6, 7); }
        }
#pragma unroll
        for (int k = 0; k < 2; ++k)
#pragma unroll
            for (int m = 0; m < 4; ++m)
#pragma unroll
                for (int n = 0; n < 2; ++n) acc[m][n] = __builtin_amdgcn_mfma_f32_16x16x32_bf16(Bt[n][k], At[m][k], acc[m][n], 0, 0, 0);
        asm volatile("s_waitcnt lgkmcnt(0)" ::: "memory");
        cur = cur + 1 == 3 ? 0 : cur + 1;
    }
#undef STG
    __syncthreads();
    LAS f32x4* red = (LAS f32x4*)shm_;
#pragma unroll
    for (int m = 0; m < 4; ++m)
#pragma unroll
        for (int n = 0; n < 2; ++n) red[((wave * 4 + m) * 2 + n) * 64 + lane] = acc[m][n];
    __syncthreads();
#pragma unroll
    for (int n = 0; n < 2; ++n) {
        const int m = wave;
        const f32x4 v = (red[((0 * 4 + m) * 2 + n) * 64 + lane] + red[((1 * 4 + m) * 2 + n) * 64 + lane]) + (red[((2 * 4 + m) * 2 + n) * 64 + lane] + red[((3 * 4 + m) * 2 + n) * 64 + lane]);
        const size_t off = (size_t)(row0 + m * 16 + fr) * DM + col0 + n * 16 + 4 * fq;
        *(u32x2*)(out + off) = combine4(v, *(const u32x2*)(x + off), *(const u32x2*)((const bf16_t*)(ws + WS_B) + off));
    }
}

extern "C" void kernel_launch(void* const* d_in, const int* in_sizes, int n_in, void* d_out, int out_size, void* d_ws, size_t ws_size, hipStream_t stream) {
    const bf16_t* x = (const bf16_t*)d_in[0];
    const bf16_t* mem0 = (const bf16_t*)d_in[1];
    const bf16_t* Wq = (const bf16_t*)d_in[2];
    const bf16_t* Wk = (const bf16_t*)d_in[3];
    const bf16_t* Wv = (const bf16_t*)d_in[4];
    const bf16_t* Wo = (const bf16_t*)d_in[5];
    const bf16_t* w1 = (const bf16_t*)d_in[8];
    const bf16_t* b1 = (const bf16_t*)d_in[9];
    const bf16_t* w2 = (const bf16_t*)d_in[10];
    const bf16_t* b2 = (const bf16_t*)d_in[11];
    char* ws = (char*)d_ws;
    bf16_t* out = (bf16_t*)d_out;
    (void)hipFuncSetAttribute((const void*)k_mega, hipFuncAttributeMaxDynamicSharedMemorySize, CH_LDS);
    (void)hipFuncSetAttribute((const void*)k_tail_gemm, hipFuncAttributeMaxDynamicSharedMemorySize, 4 * 36864);
    k_prep<<<S_LEN / 4, 256, 0, stream>>>(x, mem0, ws);
    k_mega<<<256, 512, CH_LDS, stream>>>(x, Wq, Wk, Wv, Wo, w1, b1, w2, b2, ws, out);
    k_tail_attn<<<64 * (8 - TAIL_BLK0), 256, 0, stream>>>(ws);
    k_tail_gemm<<<128 * (8 - TAIL_BLK0), 256, 4 * 36864, stream>>>(x, Wo, ws, out);
}
```

```cpp
#include <hip/hip_runtime.h>
#include <hip/hip_bf16.h>

#define LAS __attribute__((address_space(3)))
typedef unsigned short bf16_t;
typedef short bf16x8 __attribute__((ext_vector_type(8)));
typedef short bf16x4 __attribute__((ext_vector_type(4)));
typedef float f32x4 __attribute__((ext_vector_type(4)));
typedef float f32x2 __attribute__((ext_vector_type(2)));
typedef unsigned u32x4 __attribute__((ext_vector_type(4)));
typedef unsigned u32x2 __attribute__((ext_vector_type(2)));

constexpr int S_LEN = 2048, DM = 1024, NH = 32, DH = 128, HD = NH * DH, MM = 8, SEG = 32, NSEG = S_LEN / SEG, FF = 3584;
constexpr int AUX_SC1 = 16;
constexpr unsigned SENT = 0xFFFFFFFFu;

__device__ __forceinline__ float bf2f(unsigned b) { return __builtin_bit_cast(float, b << 16); }
typedef __bf16 bf16x2_t __attribute__((ext_vector_type(2)));
__device__ __forceinline__ unsigned pk_bf16(float lo, float hi) { const f32x2 v = {lo, hi}; return __builtin_bit_cast(unsigned, __builtin_convertvector(v, bf16x2_t)); }
__device__ __forceinline__ float rbf(float x) { return bf2f(pk_bf16(x, x) & 0xffffu); }
__device__ __forceinline__ unsigned desent(unsigned w) { return w == SENT ? 0x7FC07FC0u : w; }
__device__ __forceinline__ unsigned umax2(unsigned a, unsigned b) { return a > b ? a : b; }
__device__ __forceinline__ unsigned umax4(u32x4 v) { return umax2(umax2(v.x, v.y), umax2(v.z, v.w)); }

constexpr size_t WS_XN = 0;
constexpr size_t WS_Y = WS_XN + (size_t)S_LEN * DM * 2;
constexpr size_t WS_Q = WS_Y + (size_t)S_LEN * DM * 2;
constexpr size_t WS_K = WS_Q + (size_t)S_LEN * HD * 2;
constexpr size_t WS_V = WS_K + (size_t)S_LEN * HD * 2;
constexpr size_t WS_H = WS_V + (size_t)S_LEN * HD * 2;
constexpr size_t WS_O = WS_H + (size_t)S_LEN * FF * 2;
constexpr size_t WS_B = WS_O + (size_t)S_LEN * HD * 2;
constexpr size_t WS_SENT0 = WS_B + (size_t)S_LEN * DM * 2;
constexpr size_t MEM_B = (size_t)MM * DM * 2;
constexpr size_t WS_MEM = WS_SENT0;
constexpr size_t QKVM_B = (size_t)NH * 3 * MM * DH * 2;
constexpr size_t WS_QKVM = WS_MEM + (size_t)(NSEG + 1) * MEM_B;
constexpr size_t OM_B = (size_t)MM * HD * 2;
constexpr size_t WS_OM = WS_QKVM + (size_t)NSEG * QKVM_B;
constexpr size_t WS_SENT1 = WS_OM + (size_t)NSEG * OM_B;
constexpr int FILL0 = 16;
constexpr size_t WS_CNT = WS_SENT1;
constexpr int CNT_H = 0;
constexpr int CNT_B = 8;
constexpr int CNT_O = 16;
constexpr int CNT_QKV = 24;
constexpr int CNT_FILL = 33;
constexpr int CNT_STEP = 32;
constexpr int CNT_KV = 40;
constexpr int CNT_KV0B = 40 + 256;
constexpr int CNT_N = 40 + 256 + 32;
constexpr size_t WS_TMO = WS_CNT + (size_t)CNT_N * 256;
constexpr size_t WS_END = WS_TMO + 256;

__device__ __forceinline__ float wave_sum(float v) {
#pragma unroll
    for (int o = 32; o > 0; o >>= 1) v += __shfl_xor(v, o);
    return v;
}
__device__ __forceinline__ void ln_row(float (&v)[16]) {
    float s = 0.f;
#pragma unroll
    for (int i = 0; i < 16; ++i) s += v[i];
    const float mean = wave_sum(s) * (1.0f / DM);
    float q = 0.f;
#pragma unroll
    for (int i = 0; i < 16; ++i) { const float d = v[i] - mean; q += d * d; }
    const float var = rbf(wave_sum(q) * (1.0f / DM));
    const float mb = rbf(mean);
    const float den = rbf(sqrtf(rbf(var + 1e-5f)));
#pragma unroll
    for (int i = 0; i < 16; ++i) v[i] = rbf(rbf(v[i] - mb) / den);
}
__global__ void __launch_bounds__(256) k_prep(const bf16_t* __restrict__ x, const bf16_t* __restrict__ mem0, char* __restrict__ ws) {
    const int wave = threadIdx.x >> 6, lane = threadIdx.x & 63, row = blockIdx.x * 4 + wave;
    const u32x4* src = (const u32x4*)(x + (size_t)row * DM);
    u32x4 a = src[lane], b = src[lane + 64];
    float v[16];
    const unsigned w[8] = {a.x, a.y, a.z, a.w, b.x, b.y, b.z, b.w};
#pragma unroll
    for (int i = 0; i < 8; ++i) { v[2 * i] = bf2f(w[i] & 0xffffu); v[2 * i + 1] = bf2f(w[i] >> 16); }
    ln_row(v);
    u32x4 o0, o1;
    o0.x = pk_bf16(v[0], v[1]); o0.y = pk_bf16(v[2], v[3]); o0.z = pk_bf16(v[4], v[5]); o0.w = pk_bf16(v[6], v[7]);
    o1.x = pk_bf16(v[8], v[9]); o1.y = pk_bf16(v[10], v[11]); o1.z = pk_bf16(v[12], v[13]); o1.w = pk_bf16(v[14], v[15]);
    u32x4* dx = (u32x4*)(ws + WS_XN + (size_t)row * DM * 2);
    dx[lane] = o0; dx[lane + 64] = o1;
    ln_row(v);
    o0.x = pk_bf16(v[0], v[1]); o0.y = pk_bf16(v[2], v[3]); o0.z = pk_bf16(v[4], v[5]); o0.w = pk_bf16(v[6], v[7]);
    o1.x = pk_bf16(v[8], v[9]); o1.y = pk_bf16(v[10], v[11]); o1.z = pk_bf16(v[12], v[13]); o1.w = pk_bf16(v[14], v[15]);
    u32x4* dy = (u32x4*)(ws + WS_Y + (size_t)row * DM * 2);
    dy[lane] = o0; dy[lane + 64] = o1;
    {
        constexpr int nM = FILL0 * (int)MEM_B / 16, nQ = FILL0 * (int)QKVM_B / 16, nO = FILL0 * (int)OM_B / 16;
        const u32x4 f = (u32x4){SENT, SENT, SENT, SENT};
        for (int i = blockIdx.x * 256 + threadIdx.x; i < nM + nQ + nO; i += (S_LEN / 4) * 256) {
            char* dst = i < nM ? ws + WS_MEM + MEM_B + (size_t)i * 16 : (i < nM + nQ ? ws + WS_QKVM + (size_t)(i - nM) * 16 : ws + WS_OM + (size_t)(i - nM - nQ) * 16);
            *(u32x4*)dst = f;
        }
    }
    if (blockIdx.x >= 8 && blockIdx.x < 8 + (CNT_N * 256 / 16 + 255) / 256) {
        const int i = (blockIdx.x - 8) * 256 + threadIdx.x; if (i < CNT_N * 256 / 16) ((u32x4*)(ws + WS_CNT))[i] = (u32x4){0u, 0u, 0u, 0u}; }
    if (blockIdx.x < 4) {
        const int c = blockIdx.x * 256 + threadIdx.x, r = c & 7, kg = c >> 3;
        u32x4 m = *(const u32x4*)(mem0 + (size_t)r * DM + kg * 8);
        m.x = desent(m.x); m.y = desent(m.y); m.z = desent(m.z); m.w = desent(m.w);
        *(u32x4*)(ws + WS_MEM + (size_t)c * 16) = m;
    }
}

namespace g8 {
constexpr int BM = 256, BK = 64, HALF = 128, HTB = HALF * BK * 2, STAGE_BYTES = 8 * HTB;
__host__ __device__ __forceinline__ int lds_byte(int r, int c) { const int st = (r >> 4) * 2 + (c >> 5), rr = r & 15, cc = c & 31, ob = rr * 64 + cc * 2; return st * 1024 + (ob ^ (((ob >> 9) & 1) << 5)); }
__host__ __device__ __forceinline__ void stage_rc(int b, int& R, int& C) { const int st = b / 1024, sb = b % 1024, swz = sb ^ (((sb >> 9) & 1) << 5); R = (st >> 1) * 16 + swz / 64; C = (st & 1) * 32 + (swz % 64) / 2; }
__host__ __device__ __forceinline__ int bsw(int row) { return (row & 3) | (((row >> 3) & 1) << 2); }
struct Unit { const bf16_t* A; const bf16_t* B; int pm, pn; };

template <class Epi, class Sched>
__device__ __forceinline__ void gemm_phase(LAS unsigned char* lds, const int K, const int lda, const int ldb, const Sched& S, const Epi& E) {
    int tid = threadIdx.x; asm volatile("" : "+v"(tid));
    const int wid = __builtin_amdgcn_readfirstlane(tid >> 6), lane = tid & 63, wr = wid >> 2, wc = wid & 3, fr = lane & 15, fq = lane >> 4;
    const int nt = K / BK;
    unsigned voffA[2], voffB[2];
#pragma unroll
    for (int i = 0; i < 2; ++i) { int R, C; stage_rc(tid * 16 + i * 8192, R, C); voffA[i] = (unsigned)(R * lda + C) * 2u;
        const int b = tid * 16 + i * 8192, row = b >> 8, ch = (b & 255) >> 4, seg = (ch >> 1) ^ bsw(row);
        voffB[i] = (unsigned)(row * ldb + seg * 16 + (ch & 1) * 8) * 2u; }
    const size_t kstepA = (size_t)(BK * 2), kstepB = (size_t)BK * ldb * 2;
    const size_t hstepA = (size_t)HALF * lda * 2, hstepB = (size_t)HALF * 2;
    const unsigned ldsw = (unsigned)wid * 1024u;
    const int aoff = lds_byte(wr * 64 + fr, fq * 8);
    const int q4 = (lane & 15) >> 2, p4 = lane & 3, sB = q4 | ((fq & 1) << 2);
    const int boff0 = (8 * fq + q4) * 256 + (((wc * 2 + 0) ^ sB) * 32) + 8 * p4;
    const int boff1 = (8 * fq + q4) * 256 + (((wc * 2 + 1) ^ sB) * 32) + 8 * p4;
#define G8_SA(b, h) (((b) * 2 + (h)) * HTB)
#define G8_SB(b, h) ((4 + (b) * 2 + (h)) * HTB)
#define G8_STAGE(bufoff, gbase, voff) do { _Pragma("unroll") for (int _i = 0; _i < 2; ++_i) \
        __builtin_amdgcn_global_load_lds((const unsigned*)((const char*)(gbase) + (voff)[_i]), (LAS unsigned*)(lds + (bufoff) + ldsw + _i * 8192), 16, 0, 0); } while (0)
#define G8_LDA(dst, b, h) do { _Pragma("unroll") for (int m = 0; m < 4; ++m) _Pragma("unroll") for (int k = 0; k < 2; ++k) dst[m][k] = *(const LAS bf16x8*)(lds + G8_SA(b, h) + aoff + m * 2048 + k * 1024); } while (0)
#define G8_TR(off) __builtin_amdgcn_ds_read_tr16_b64_v4i16((LAS bf16x4*)(lds + (off)))
#define G8_LDB(dst, b, h) do { _Pragma("unroll") for (int k = 0; k < 2; ++k) { \
        bf16x4 a0 = G8_TR(G8_SB(b, h) + boff0 + k * 8192), a1 = G8_TR(G8_SB(b, h) + boff0 + k * 8192 + 1024); \
        bf16x4 c0 = G8_TR(G8_SB(b, h) + boff1 + k * 8192), c1 = G8_TR(G8_SB(b, h) + boff1 + k * 8192 + 1024); \
        dst[0][k] = __builtin_shufflevector(a0, a1, 0, 1, 2, 3, 4, 5, 6, 7); dst[1][k] = __builtin_shufflevector(c0, c1, 0, 1, 2, 3, 4, 5, 6, 7); } } while (0)
#define G8_MMA(ai, bj, At, Bt) do { __builtin_amdgcn_s_setprio(1); _Pragma("unroll") for (int m = 0; m < 4; ++m) _Pragma("unroll") for (int n = 0; n < 2; ++n) _Pragma("unroll") for (int k = 0; k < 2; ++k) \
        acc[ai][bj][m][n] = __builtin_amdgcn_mfma_f32_16x16x32_bf16(Bt[n][k], At[m][k], acc[ai][bj][m][n], 0, 0, 0); __builtin_amdgcn_s_setprio(0); } while (0)
#define G8_WAIT_V(n) asm volatile("s_waitcnt vmcnt(" #n ")" ::: "memory")
#define G8_WAIT_L(n) asm volatile("s_waitcnt lgkmcnt(" #n ")" ::: "memory")
#define G8_BAR __builtin_amdgcn_s_barrier()
#define G8_SCHED __builtin_amdgcn_sched_barrier(0)
    Unit cur, nxt; int ui = 0;
    if (!S.next(0, cur)) return;
    f32x4 acc[2][2][4][2];
#pragma unroll
    for (int a = 0; a < 2; ++a)
#pragma unroll
        for (int b = 0; b < 2; ++b)
#pragma unroll
            for (int m = 0; m < 4; ++m)
#pragma unroll
                for (int n = 0; n < 2; ++n) acc[a][b][m][n] = (f32x4){0.f, 0.f, 0.f, 0.f};
    bf16x8 At[4][2], B0[2][2], B1[2][2];
    const char* cA = (const char*)cur.A; const char* cB = (const char*)cur.B;
    G8_STAGE(G8_SB(0, 0), cB, voffB); G8_STAGE(G8_SA(0, 0), cA, voffA); G8_STAGE(G8_SB(0, 1), cB + hstepB, voffB); G8_STAGE(G8_SA(0, 1), cA + hstepA, voffA);
    if (wr == 1) G8_BAR;
    G8_WAIT_V(4); G8_BAR;
    G8_STAGE(G8_SB(1, 0), cB + kstepB, voffB); G8_STAGE(G8_SA(1, 0), cA + kstepA, voffA); G8_STAGE(G8_SB(1, 1), cB + hstepB + kstepB, voffB);
    G8_WAIT_V(6); G8_BAR;
    for (;;) {
        const bool has_next = S.next(ui + 1, nxt);
        const char* nA = has_next ? (const char*)nxt.A : cA; const char* nB = has_next ? (const char*)nxt.B : cB;
        for (int t = 0; t < nt; t += 2) {
            const bool last = (t == nt - 2);
            const char* a1 = cA + (size_t)(t + 1) * kstepA;
            const char* a2 = last ? nA : cA + (size_t)(t + 2) * kstepA; const char* b2 = last ? nB : cB + (size_t)(t + 2) * kstepB;
            const char* a3 = a2 + kstepA; const char* b3 = b2 + kstepB;
            G8_LDB(B0, 0, 0); G8_SCHED; G8_LDA(At, 0, 0); G8_STAGE(G8_SA(1, 1), a1 + hstepA, voffA);
            G8_WAIT_L(8); G8_BAR; G8_WAIT_L(0); G8_MMA(0, 0, At, B0); G8_BAR; G8_SCHED;
            G8_LDB(B1, 0, 1); G8_STAGE(G8_SB(0, 0), b2, voffB);
            G8_BAR; G8_WAIT_L(0); G8_MMA(0, 1, At, B1); G8_BAR;
            G8_LDA(At, 0, 1); G8_STAGE(G8_SA(0, 0), a2, voffA);
            G8_BAR; G8_WAIT_L(0); G8_MMA(1, 0, At, B0); G8_BAR; G8_SCHED;
            G8_STAGE(G8_SB(0, 1), b2 + hstepB, voffB);
            G8_WAIT_V(6); G8_BAR; G8_MMA(1, 1, At, B1); G8_BAR;
            G8_LDB(B0, 1, 0); G8_SCHED; G8_LDA(At, 1, 0); G8_STAGE(G8_SA(0, 1), a2 + hstepA, voffA);
            G8_WAIT_L(8); G8_BAR; G8_WAIT_L(0); G8_MMA(0, 0, At, B0); G8_BAR; G8_SCHED;
            G8_LDB(B1, 1, 1); G8_STAGE(G8_SB(1, 0), b3, voffB);
            G8_BAR; G8_WAIT_L(0); G8_MMA(0, 1, At, B1); G8_BAR;
            G8_LDA(At, 1, 1); G8_STAGE(G8_SA(1, 0), a3, voffA);
            G8_BAR; G8_WAIT_L(0); G8_MMA(1, 0, At, B0); G8_BAR; G8_SCHED;
            G8_STAGE(G8_SB(1, 1), b3 + hstepB, voffB);
            G8_WAIT_V(6); G8_BAR; G8_MMA(1, 1, At, B1); G8_BAR;
        }
        E(acc, cur, wr, wc, fr, fq);
        if (!has_next) break;
#pragma unroll
        for (int a = 0; a < 2; ++a)
#pragma unroll
            for (int b = 0; b < 2; ++b)
#pragma unroll
                for (int m = 0; m < 4; ++m)
#pragma unroll
                    for (int n = 0; n < 2; ++n) acc[a][b][m][n] = (f32x4){0.f, 0.f, 0.f, 0.f};
        cur = nxt; cA = nA; cB = nB; ++ui;
    }
    G8_WAIT_V(0);
    if (wr == 0) G8_BAR;
    G8_BAR;
#undef G8_SA
#undef G8_SB
#undef G8_STAGE
#undef G8_LDA
#undef G8_LDB
#undef G8_TR
#undef G8_MMA
#undef G8_WAIT_V
#undef G8_WAIT_L
#undef G8_BAR
#undef G8_SCHED
}
}

__device__ __forceinline__ float gelu_tanh(float x) {
    const float u = 0.7978845608028654f * (x + 0.044715f * x * x * x);
    const float th = 1.0f - 2.0f * __builtin_amdgcn_rcpf(__expf(2.0f * u) + 1.0f);
    return 0.5f * x * (1.0f + th);
}

__device__ __forceinline__ void wait_counter(const char* ws, int idx, unsigned need) {
    if (threadIdx.x < 64) {
        const unsigned* p = (const unsigned*)(ws + WS_CNT + (size_t)idx * 256);
        for (unsigned spins = 0; (unsigned)__builtin_amdgcn_readfirstlane(__hip_atomic_load(p, __ATOMIC_RELAXED, __HIP_MEMORY_SCOPE_AGENT)) < need;) {
            __builtin_amdgcn_s_sleep(8);
            if (++spins > 400000u) break;
        }
        __builtin_amdgcn_fence(__ATOMIC_ACQUIRE, "agent");
        asm volatile("s_waitcnt vmcnt(0)" ::: "memory");
    }
    asm volatile("" ::: "memory"); __builtin_amdgcn_s_barrier(); asm volatile("" ::: "memory");
}
__device__ __forceinline__ void signal_counter(char* ws, int idx) {
    asm volatile("s_waitcnt vmcnt(0)" ::: "memory");
    if ((threadIdx.x & 63) == 0) __hip_atomic_fetch_add((unsigned*)(ws + WS_CNT + (size_t)idx * 256), 1u, __ATOMIC_RELAXED, __HIP_MEMORY_SCOPE_AGENT);
}

__device__ __forceinline__ unsigned poll_ge(const char* ws, int idx, unsigned need) {
    const unsigned* p = (const unsigned*)(ws + WS_CNT + (size_t)idx * 256);
    unsigned v = 0;
    for (unsigned spins = 0; (v = (unsigned)__builtin_amdgcn_readfirstlane(__hip_atomic_load(p, __ATOMIC_RELAXED, __HIP_MEMORY_SCOPE_AGENT))) < need;) {
        __builtin_amdgcn_s_sleep(8);
        if (++spins > 400000u) break;
    }
    asm volatile("" ::: "memory");
    return v;
}

constexpr unsigned SPIN_CAP = 200000u;
constexpr int KS_PITCH = 272;
constexpr int CH_LDS = 160 * 1024;

__device__ __forceinline__ float xmax16_32(float v) {
    auto a = __builtin_amdgcn_permlane16_swap(__builtin_bit_cast(unsigned, v), __builtin_bit_cast(unsigned, v), false, false);
    v = fmaxf(__builtin_bit_cast(float, (unsigned)a[0]), __builtin_bit_cast(float, (unsigned)a[1]));
    auto b = __builtin_amdgcn_permlane32_swap(__builtin_bit_cast(unsigned, v), __builtin_bit_cast(unsigned, v), false, false);
    return fmaxf(__builtin_bit_cast(float, (unsigned)b[0]), __builtin_bit_cast(float, (unsigned)b[1]));
}
__device__ __forceinline__ float xsum16_32(float v) {
    auto a = __builtin_amdgcn_permlane16_swap(__builtin_bit_cast(unsigned, v), __builtin_bit_cast(unsigned, v), false, false);
    v = __builtin_bit_cast(float, (unsigned)a[0]) + __builtin_bit_cast(float, (unsigned)a[1]);
    auto b = __builtin_amdgcn_permlane32_swap(__builtin_bit_cast(unsigned, v), __builtin_bit_cast(unsigned, v), false, false);
    return __builtin_bit_cast(float, (unsigned)b[0]) + __builtin_bit_cast(float, (unsigned)b[1]);
}

__device__ __forceinline__ u32x4 quad_gather(unsigned v) {
    u32x4 r;
    r.x = (unsigned)__builtin_amdgcn_update_dpp(0, (int)v, 0x00, 0xf, 0xf, true); r.y = (unsigned)__builtin_amdgcn_update_dpp(0, (int)v, 0x55, 0xf, 0xf, true);
    r.z = (unsigned)__builtin_amdgcn_update_dpp(0, (int)v, 0xAA, 0xf, 0xf, true); r.w = (unsigned)__builtin_amdgcn_update_dpp(0, (int)v, 0xFF, 0xf, 0xf, true);
    return r;
}
#define STAMP(i) do {} while (0)
#define PCOUNT(sh) do {} while (0)
#define CH_DELAY1 12
#define CH_DELAY2 6
#define CH_DELAY3 8

template <int CPH>
__device__ __forceinline__ void chain_body(LAS unsigned char* lds, const int b, const bf16_t* __restrict__ Wq, const bf16_t* __restrict__ Wk, const bf16_t* __restrict__ Wv,
                                           const bf16_t* __restrict__ Wo, char* ws) {
    constexpr int ES = 128 / CPH, NE = ES / 16, NT = 3 * NE, RG = CPH / 2, RPG = 8 / RG;
    constexpr int VP = ES * 2;
    constexpr int L_RED = 0;
    constexpr int L_RED2 = L_RED + 8 * NT * 512;
    constexpr int L_KS = L_RED2 + 8 * RPG * 64;
    constexpr int L_VS = L_KS + 2 * 32 * KS_PITCH;
    constexpr int L_OS = L_VS + 2 * 48 * VP;
    constexpr bool WOL = (CPH == 4);
    constexpr int L_WO = L_OS + 8 * 1024 * RPG;
    static_assert(L_WO + (WOL ? 65536 : 0) <= 156 * 1024, "LDS");
    const int tid = threadIdx.x, wave = __builtin_amdgcn_readfirstlane(tid >> 6), lane = tid & 63, fr = lane & 15, fq = lane >> 4;
    const int xc = b & 7, yc = b >> 3;
    const int h = xc + 8 * (yc / CPH), j = yc % CPH, g = yc % RG, c = xc * 8 + yc / RG;
    const __amdgpu_buffer_rsrc_t rs = __builtin_amdgcn_make_buffer_rsrc(ws + WS_SENT0, 0, (int)(WS_SENT1 - WS_SENT0), 0x00020000);
    constexpr int O_MEM = (int)(WS_MEM - WS_SENT0), O_QKVM = (int)(WS_QKVM - WS_SENT0), O_OM = (int)(WS_OM - WS_SENT0);
    const __amdgpu_buffer_rsrc_t rkv = __builtin_amdgcn_make_buffer_rsrc(ws + WS_K, 0, (int)(WS_H - WS_K), 0x00020000);
    constexpr int O_V = (int)(WS_V - WS_K);
    bool dead = false;

    bf16x8 wq[NT][4], wo[16];
    {
        LAS unsigned char* stg = lds + wave * 8192;
        constexpr int CPR = ES / 8;
        static_assert(128 * CPR / 64 == 8, "8 chunks per lane and pass");
        u32x4 t[5][8];
        auto issue = [&](int ps) {
            if (ps < 3) { const bf16_t* W = (ps == 0 ? Wq : (ps == 1 ? Wk : Wv)) + (size_t)wave * 128 * HD + h * DH + j * ES;
#pragma unroll
                for (int i = 0; i < 8; ++i) { const int ci = i * 64 + lane; t[ps][i] = *(const u32x4*)(W + (size_t)(ci / CPR) * HD + (ci % CPR) * 8); } }
            else { const bf16_t* W = Wo + (size_t)(wave * 512 + (ps - 3) * 256) * DM + c * 16;
#pragma unroll
                for (int i = 0; i < 8; ++i) { const int ci = i * 64 + lane; t[ps][i] = *(const u32x4*)(W + (size_t)(ci >> 1) * DM + (ci & 1) * 8); } }
        };
        issue(0); issue(1); issue(2);
        asm volatile("" ::: "memory");
#pragma unroll
        for (int ps = 0; ps < 5; ++ps) {
#pragma unroll
            for (int i = 0; i < 8; ++i) *(LAS u32x4*)(stg + (i * 64 + lane) * 16) = t[ps][i];
            asm volatile("s_waitcnt lgkmcnt(0)" ::: "memory");
            if (ps + 3 < 5) issue(ps + 3);
            asm volatile("" ::: "memory");
            if (ps < 3) {
#pragma unroll
                for (int e = 0; e < NE; ++e)
#pragma unroll
                    for (int i = 0; i < 4; ++i) {
                        const int off = (i * 32 + 8 * fq + (fr >> 2)) * (ES * 2) + e * 32 + (lane & 3) * 8;
                        const bf16x4 a0 = __builtin_amdgcn_ds_read_tr16_b64_v4i16((LAS bf16x4*)(stg + off)), a1 = __builtin_amdgcn_ds_read_tr16_b64_v4i16((LAS bf16x4*)(stg + off + 4 * ES * 2));
                        wq[ps * NE + e][i] = __builtin_shufflevector(a0, a1, 0, 1, 2, 3, 4, 5, 6, 7);
                    }
            } else {
#pragma unroll
                for (int i = 0; i < 8; ++i) {
                    const int off = (i * 32 + 8 * fq + (fr >> 2)) * 32 + (lane & 3) * 8;
                    const bf16x4 a0 = __builtin_amdgcn_ds_read_tr16_b64_v4i16((LAS bf16x4*)(stg + off)), a1 = __builtin_amdgcn_ds_read_tr16_b64_v4i16((LAS bf16x4*)(stg + off + 128));
                    wo[(ps - 3) * 8 + i] = __builtin_shufflevector(a0, a1, 0, 1, 2, 3, 4, 5, 6, 7);
                }
            }
            asm volatile("s_waitcnt lgkmcnt(0)" ::: "memory");
        }
        if (WOL) {
#pragma unroll
            for (int i = 8; i < 16; ++i) *(LAS bf16x8*)(lds + L_WO + (wave * 8 + i - 8) * 1024 + lane * 16) = wo[i];
        }
    }
    __syncthreads();

    {
        constexpr int nM = (NSEG - FILL0) * (int)MEM_B / 16, nQ = (NSEG - FILL0) * (int)QKVM_B / 16, nO = (NSEG - FILL0) * (int)OM_B / 16;
        const u32x4 f = (u32x4){SENT, SENT, SENT, SENT};
        for (int i = b * 512 + tid; i < nM + nQ + nO; i += 32 * CPH * 512) {
            const int off = i < nM ? O_MEM + (FILL0 + 1) * (int)MEM_B + i * 16 : (i < nM + nQ ? O_QKVM + FILL0 * (int)QKVM_B + (i - nM) * 16 : O_OM + FILL0 * (int)OM_B + (i - nM - nQ) * 16);
            __builtin_amdgcn_raw_buffer_store_b128(f, rs, off, 0, AUX_SC1);
        }
        signal_counter(ws, CNT_FILL);
    }
    if (tid == 0) *(LAS unsigned*)(lds + L_OS) = 0u;
    if (tid < 2 * 8 * VP / 16) { const int bf = tid / (8 * VP / 16), r = tid % (8 * VP / 16); *(LAS u32x4*)(lds + L_VS + bf * 48 * VP + 40 * VP + r * 16) = (u32x4){0u, 0u, 0u, 0u}; }
    {
        poll_ge(ws, CNT_KV + (h >> 1), 32u); poll_ge(ws, CNT_KV + 16 + (h >> 1), 32u);
        const int r = tid >> 4, ch = tid & 15;
        *(LAS u32x4*)(lds + L_KS + r * KS_PITCH + ch * 16) = __builtin_amdgcn_raw_buffer_load_b128(rkv, (r * HD + h * DH + ch * 8) * 2, 0, AUX_SC1);
        if (tid < 32 * ES / 8) { const int r2 = tid / (ES / 8), c2 = tid % (ES / 8);
            *(LAS u32x4*)(lds + L_VS + (8 + r2) * VP + c2 * 16) = __builtin_amdgcn_raw_buffer_load_b128(rkv, O_V + (r2 * HD + h * DH + j * ES + c2 * 8) * 2, 0, AUX_SC1); }
    }
    __syncthreads();

    for (int s = 0; s < NSEG; ++s) {
        const int kb = s & 1;
        unsigned long long pcnt = 0; (void)pcnt;
        STAMP(0);
        {
            if (s > 0) __builtin_amdgcn_s_sleep(CH_DELAY1);
            u32x4 a[4];
            const int base = O_MEM + s * (int)MEM_B + wave * 2048 + fq * 128 + (lane & 7) * 16;
            for (unsigned spins = 0;;) {
                asm volatile("" ::: "memory");
#pragma unroll
                for (int i = 0; i < 4; ++i) a[i] = __builtin_amdgcn_raw_buffer_load_b128(rs, base + i * 512, 0, AUX_SC1);
                unsigned mxs = 0;
#pragma unroll
                for (int i = 0; i < 4; ++i) mxs = umax2(mxs, umax4(a[i]));
                if (!__any(mxs == SENT) || dead) break;
                PCOUNT(0);
                if (++spins > SPIN_CAP) { dead = true; if (lane == 0) *(volatile unsigned*)(ws + WS_TMO) = 1u; break; }
            }
            STAMP(1);
            if (b == 0 && tid == 448 && (s & 7) == 0) __hip_atomic_store((unsigned*)(ws + WS_CNT + (size_t)CNT_STEP * 256), (unsigned)s, __ATOMIC_RELAXED, __HIP_MEMORY_SCOPE_AGENT);
            constexpr int NQK = 2 * NE;
            {
                f32x4 acc[NQK];
#pragma unroll
                for (int t = 0; t < NQK; ++t) acc[t] = (f32x4){0.f, 0.f, 0.f, 0.f};
#pragma unroll
                for (int i = 0; i < 4; ++i)
#pragma unroll
                    for (int t = 0; t < NQK; ++t) acc[t] = __builtin_amdgcn_mfma_f32_16x16x32_bf16(wq[t][i], __builtin_bit_cast(bf16x8, a[i]), acc[t], 0, 0, 0);
                if (fr < 8) {
#pragma unroll
                    for (int t = 0; t < NQK; ++t) *(LAS f32x4*)(lds + L_RED + ((wave * NT + t) * 128 + fr * 16 + fq * 4) * 4) = acc[t];
                }
            }
            __syncthreads();
            if (tid < NQK * 64) {
                const int t = tid >> 6, m = (tid & 63) >> 3, np = (tid & 7) * 2, mat = t / NE, col = (t % NE) * 16 + np;
                f32x2 sum = (f32x2){0.f, 0.f};
#pragma unroll
                for (int w = 0; w < 8; ++w) sum += *(const LAS f32x2*)(lds + L_RED + ((w * NT + t) * 128 + m * 16 + np) * 4);
                const u32x4 q4 = quad_gather(desent(pk_bf16(sum.x, sum.y)));
                if ((tid & 3) == 0) __builtin_amdgcn_raw_buffer_store_b128(q4, rs, O_QKVM + s * (int)QKVM_B + ((h * 3 + mat) * 8 + m) * 256 + (j * ES + col) * 2, 0, AUX_SC1);
            }
            STAMP(2);
            {
                f32x4 acc[NE];
#pragma unroll
                for (int t = 0; t < NE; ++t) acc[t] = (f32x4){0.f, 0.f, 0.f, 0.f};
#pragma unroll
                for (int i = 0; i < 4; ++i)
#pragma unroll
                    for (int t = 0; t < NE; ++t) acc[t] = __builtin_amdgcn_mfma_f32_16x16x32_bf16(wq[NQK + t][i], __builtin_bit_cast(bf16x8, a[i]), acc[t], 0, 0, 0);
                if (fr < 8) {
#pragma unroll
                    for (int t = 0; t < NE; ++t) *(LAS f32x4*)(lds + L_RED + ((wave * NT + NQK + t) * 128 + fr * 16 + fq * 4) * 4) = acc[t];
                }
            }
        }
        __syncthreads();
        if (tid >= 2 * NE * 64 && tid < NT * 64) {
            const int t = tid >> 6, m = (tid & 63) >> 3, np = (tid & 7) * 2, col = (t % NE) * 16 + np;
            f32x2 sum = (f32x2){0.f, 0.f};
#pragma unroll
            for (int w = 0; w < 8; ++w) sum += *(const LAS f32x2*)(lds + L_RED + ((w * NT + t) * 128 + m * 16 + np) * 4);
            const unsigned pk = desent(pk_bf16(sum.x, sum.y));
            const u32x4 q4 = quad_gather(pk);
            if ((tid & 3) == 0) __builtin_amdgcn_raw_buffer_store_b128(q4, rs, O_QKVM + s * (int)QKVM_B + ((h * 3 + 2) * 8 + m) * 256 + (j * ES + col) * 2, 0, AUX_SC1);
            *(LAS unsigned*)(lds + L_VS + kb * 48 * VP + m * VP + col * 2) = pk;
            asm volatile("s_waitcnt lgkmcnt(0)" ::: "memory");
            if (lane == 0) __hip_atomic_fetch_add((LAS unsigned*)(lds + L_OS), 1u, __ATOMIC_RELAXED, __HIP_MEMORY_SCOPE_WORKGROUP);
        }
        if (s == NSEG - 1) break;
        if (wave == 0) {
            bf16x8 kx[3][4];
#pragma unroll
            for (int t = 0; t < 3; ++t) {
                int r = t * 16 + fr - 8; r = r < 0 ? 0 : (r > 31 ? 31 : r);
#pragma unroll
                for (int i = 0; i < 4; ++i) kx[t][i] = *(const LAS bf16x8*)(lds + L_KS + kb * 32 * KS_PITCH + r * KS_PITCH + i * 64 + fq * 16);
            }
            __builtin_amdgcn_s_sleep(CH_DELAY2);
            u32x4 qf[4], km[4];
            const int base = O_QKVM + s * (int)QKVM_B + h * 3 * 2048 + (lane & 7) * 256 + fq * 16;
            for (unsigned spins = 0;;) {
                asm volatile("" ::: "memory");
#pragma unroll
                for (int i = 0; i < 4; ++i) { qf[i] = __builtin_amdgcn_raw_buffer_load_b128(rs, base + i * 64, 0, AUX_SC1); km[i] = __builtin_amdgcn_raw_buffer_load_b128(rs, base + 2048 + i * 64, 0, AUX_SC1); }
                unsigned mxs = 0;
#pragma unroll
                for (int i = 0; i < 4; ++i) mxs = umax2(mxs, umax2(umax4(qf[i]), umax4(km[i])));
                if (!__any(mxs == SENT) || dead) break;
                PCOUNT(16);
                if (++spins > SPIN_CAP) { dead = true; if (lane == 0) *(volatile unsigned*)(ws + WS_TMO) = 2u; break; }
            }
            STAMP(3);
            f32x4 sc[3];
#pragma unroll
            for (int t = 0; t < 3; ++t) sc[t] = (f32x4){0.f, 0.f, 0.f, 0.f};
#pragma unroll
            for (int i = 0; i < 4; ++i) {
                const bf16x8 k0 = fr < 8 ? __builtin_bit_cast(bf16x8, km[i]) : kx[0][i];
                sc[1] = __builtin_amdgcn_mfma_f32_16x16x32_bf16(kx[1][i], __builtin_bit_cast(bf16x8, qf[i]), sc[1], 0, 0, 0);
                sc[2] = __builtin_amdgcn_mfma_f32_16x16x32_bf16(kx[2][i], __builtin_bit_cast(bf16x8, qf[i]), sc[2], 0, 0, 0);
                sc[0] = __builtin_amdgcn_mfma_f32_16x16x32_bf16(k0, __builtin_bit_cast(bf16x8, qf[i]), sc[0], 0, 0, 0);
            }
            for (unsigned sp = 0; __hip_atomic_load((LAS unsigned*)(lds + L_OS), __ATOMIC_RELAXED, __HIP_MEMORY_SCOPE_WORKGROUP) < (unsigned)(NE * (s + 1)) && sp < SPIN_CAP; ++sp) {}
            asm volatile("" ::: "memory");
            bf16x8 vf[NE][2];
            const bf16x4 zz = (bf16x4){0, 0, 0, 0};
#pragma unroll
            for (int et = 0; et < NE; ++et) {
                const int voff = L_VS + kb * 48 * VP + (4 * fq + (fr >> 2)) * VP + et * 32 + (lane & 3) * 8;
                const bf16x4 v0 = __builtin_amdgcn_ds_read_tr16_b64_v4i16((LAS bf16x4*)(lds + voff));
                const bf16x4 v1 = __builtin_amdgcn_ds_read_tr16_b64_v4i16((LAS bf16x4*)(lds + voff + 16 * VP));
                const bf16x4 v2 = __builtin_amdgcn_ds_read_tr16_b64_v4i16((LAS bf16x4*)(lds + voff + 32 * VP));
                vf[et][0] = __builtin_shufflevector(v0, v1, 0, 1, 2, 3, 4, 5, 6, 7); vf[et][1] = __builtin_shufflevector(v2, zz, 0, 1, 2, 3, 4, 5, 6, 7);
            }
            const float scale = 0.08838834764831845f;
            const bool hi = fr >= 8;
            float v[6];
            v[0] = hi ? sc[1][2] : sc[0][0]; v[1] = hi ? sc[1][3] : sc[0][1]; v[2] = hi ? sc[2][0] : sc[0][2];
            v[3] = hi ? sc[2][1] : sc[0][3]; v[4] = hi ? sc[2][2] : sc[1][0]; v[5] = hi ? sc[2][3] : sc[1][1];
            float mx = -INFINITY;
#pragma unroll
            for (int i = 0; i < 6; ++i) { float z = rbf(rbf(v[i]) * scale); if (i >= 2 && hi && fq >= 2) z = -INFINITY; v[i] = z; mx = fmaxf(mx, z); }
            mx = fmaxf(mx, __builtin_bit_cast(float, __builtin_amdgcn_update_dpp(0, __builtin_bit_cast(int, mx), 0x128, 0xf, 0xf, true)));
            mx = xmax16_32(mx);
            float sum = 0.f;
#pragma unroll
            for (int i = 0; i < 6; ++i) { v[i] = __expf(v[i] - mx); sum += v[i]; }
            sum += __builtin_bit_cast(float, __builtin_amdgcn_update_dpp(0, __builtin_bit_cast(int, sum), 0x128, 0xf, 0xf, true));
            sum = xsum16_32(sum);
            const float inv = 1.0f / sum;
            const unsigned w0 = pk_bf16(v[0] * inv, v[1] * inv), w1 = pk_bf16(v[2] * inv, v[3] * inv), w2 = pk_bf16(v[4] * inv, v[5] * inv);
            u32x4 p0, p1;
            p0.x = w0; p0.y = w1; p0.z = w2; p0.w = (unsigned)__builtin_amdgcn_update_dpp(0, (int)w0, 0x128, 0xf, 0xf, true);
            p1.x = (unsigned)__builtin_amdgcn_update_dpp(0, (int)w1, 0x128, 0xf, 0xf, true); p1.y = (unsigned)__builtin_amdgcn_update_dpp(0, (int)w2, 0x128, 0xf, 0xf, true); p1.z = 0u; p1.w = 0u;
#pragma unroll
            for (int et = 0; et < NE; ++et) {
                f32x4 o = (f32x4){0.f, 0.f, 0.f, 0.f};
                o = __builtin_amdgcn_mfma_f32_16x16x32_bf16(vf[et][0], __builtin_bit_cast(bf16x8, p0), o, 0, 0, 0);
                o = __builtin_amdgcn_mfma_f32_16x16x32_bf16(vf[et][1], __builtin_bit_cast(bf16x8, p1), o, 0, 0, 0);
                {
                    const unsigned wx = desent(pk_bf16(o[0], o[1])), wy = desent(pk_bf16(o[2], o[3]));
                    auto sx = __builtin_amdgcn_permlane16_swap(wx, wx, false, false); auto sy = __builtin_amdgcn_permlane16_swap(wy, wy, false, false);
                    const u32x4 w = (u32x4){(unsigned)sx[0], (unsigned)sy[0], (unsigned)sx[1], (unsigned)sy[1]};
                    const int k = h * DH + j * ES + et * 16 + 4 * fq;
                    const int off = O_OM + s * (int)OM_B + (fr / RPG) * (8192 * RPG) + (k >> 5) * (64 * RPG) + ((k >> 3) & 3) * (16 * RPG) + (fr % RPG) * 16;
                    if (fr < 8 && (fq & 1) == 0) __builtin_amdgcn_raw_buffer_store_b128(w, rs, off, 0, AUX_SC1);
                }
            }
            STAMP(4);
        } else {
            const int t2 = tid - 64; const int nb = kb ^ 1; const int row0 = (s + 1) * SEG;
            if (((s + 1) & 7) == 0) { const int blk = (s + 1) >> 3; poll_ge(ws, CNT_KV + blk * 32 + (h >> 1), 8u); poll_ge(ws, CNT_KV + blk * 32 + 16 + (h >> 1), 8u); }
            if (s == FILL0 - 3) poll_ge(ws, CNT_FILL, 32u * CPH * 8u);
            if (s + 1 == 4) { poll_ge(ws, CNT_KV0B + (h >> 1), 16u); poll_ge(ws, CNT_KV0B + 16 + (h >> 1), 16u); }
            for (int i = t2; i < 512 + 32 * ES / 8; i += 448) {
                if (i < 512) { const int r = i >> 4, ch = i & 15;
                    *(LAS u32x4*)(lds + L_KS + nb * 32 * KS_PITCH + r * KS_PITCH + ch * 16) = __builtin_amdgcn_raw_buffer_load_b128(rkv, ((row0 + r) * HD + h * DH + ch * 8) * 2, 0, AUX_SC1); }
                else { const int r2 = (i - 512) / (ES / 8), c2 = (i - 512) % (ES / 8);
                    *(LAS u32x4*)(lds + L_VS + nb * 48 * VP + (8 + r2) * VP + c2 * 16) = __builtin_amdgcn_raw_buffer_load_b128(rkv, O_V + ((row0 + r2) * HD + h * DH + j * ES + c2 * 8) * 2, 0, AUX_SC1); }
            }
        }
        __syncthreads();
        {
            if (WOL) {
#pragma unroll
                for (int i = 8; i < 16; ++i) wo[i] = *(const LAS bf16x8*)(lds + L_WO + (wave * 8 + i - 8) * 1024 + lane * 16);
            }
            __builtin_amdgcn_s_sleep(CH_DELAY3);
            u32x4 a[RPG];
            constexpr int KPG = 16 / RPG;
            const int base = O_OM + s * (int)OM_B + g * (8192 * RPG) + wave * (1024 * RPG) + (fr / RPG) * (64 * RPG) + fq * (16 * RPG) + (fr % RPG) * 16;
            for (unsigned spins = 0;;) {
                asm volatile("" ::: "memory");
#pragma unroll
                for (int i = 0; i < RPG; ++i) a[i] = __builtin_amdgcn_raw_buffer_load_b128(rs, base + i * 1024, 0, AUX_SC1);
                unsigned mxs = 0;
#pragma unroll
                for (int i = 0; i < RPG; ++i) mxs = umax2(mxs, umax4(a[i]));
                if (!__any(mxs == SENT) || dead) break;
                PCOUNT(32);
                if (++spins > SPIN_CAP) { dead = true; if (lane == 0) *(volatile unsigned*)(ws + WS_TMO) = 3u; break; }
            }
            STAMP(5);
            f32x4 acc[KPG];
#pragma unroll
            for (int q = 0; q < KPG; ++q) acc[q] = (f32x4){0.f, 0.f, 0.f, 0.f};
#pragma unroll
            for (int gq = 0; gq < RPG; ++gq)
#pragma unroll
                for (int q = 0; q < KPG; ++q) acc[q] = __builtin_amdgcn_mfma_f32_16x16x32_bf16(wo[gq * KPG + q], __builtin_bit_cast(bf16x8, a[gq]), acc[q], 0, 0, 0);
            f32x4 r = acc[0];
#pragma unroll
            for (int q = 1; q < KPG; ++q) if (fr / RPG == q) r = acc[q];
#pragma unroll
            for (int e = 0; e < 4; ++e) {
                if (RPG <= 2) r[e] += __builtin_bit_cast(float, __builtin_amdgcn_update_dpp(0, __builtin_bit_cast(int, r[e]), 0x122, 0xf, 0xf, true));
                r[e] += __builtin_bit_cast(float, __builtin_amdgcn_update_dpp(0, __builtin_bit_cast(int, r[e]), 0x124, 0xf, 0xf, true));
                r[e] += __builtin_bit_cast(float, __builtin_amdgcn_update_dpp(0, __builtin_bit_cast(int, r[e]), 0x128, 0xf, 0xf, true));
            }
            if (fr < RPG) *(LAS f32x4*)(lds + L_RED2 + ((wave * RPG + fr) * 16 + fq * 4) * 4) = r;
        }
        __syncthreads();
        if (tid < RPG * 8) {
            const int m = tid >> 3, np = (tid & 7) * 2;
            f32x2 sum = (f32x2){0.f, 0.f};
#pragma unroll
            for (int w = 0; w < 8; ++w) sum += *(const LAS f32x2*)(lds + L_RED2 + ((w * RPG + m) * 16 + np) * 4);
            const unsigned pk = desent(pk_bf16(sum.x, sum.y));
            const int off = O_MEM + (s + 1) * (int)MEM_B + (c >> 1) * 512 + ((c & 1) * 2 + (np >> 3)) * 128 + (g * RPG + m) * 16;
            const u32x4 q4 = quad_gather(pk);
            if ((tid & 3) == 0) __builtin_amdgcn_raw_buffer_store_b128(q4, rs, off, 0, AUX_SC1);
        }
        STAMP(6);
    }
}

template <int MT, class Epi>
__device__ __forceinline__ void gemm128_w8(LAS unsigned char* lds, const bf16_t* A, const int lda, const bf16_t* B, const int ldb, const int K, const Epi& E) {
    int tid = threadIdx.x; asm volatile("" : "+v"(tid));
    const int wid = __builtin_amdgcn_readfirstlane(tid >> 6), lane = tid & 63, wr = wid >> 2, wc = wid & 3, fr = lane & 15, fq = lane >> 4;
    constexpr int NA = MT / 2;
    unsigned voffA[NA], voffB[2];
#pragma unroll
    for (int i = 0; i < NA; ++i) { int R, C; g8::stage_rc(tid * 16 + i * 8192, R, C); voffA[i] = (unsigned)(R * lda + C) * 2u; }
#pragma unroll
    for (int i = 0; i < 2; ++i) { const int bb = tid * 16 + i * 8192, row = bb >> 8, ch = (bb & 255) >> 4, seg = (ch >> 1) ^ g8::bsw(row);
        voffB[i] = (unsigned)(row * ldb + seg * 16 + (ch & 1) * 8) * 2u; }
    const char* cA = (const char*)A; const char* cB = (const char*)B;
    const unsigned ldsw = (unsigned)wid * 1024u;
    const int aoff = g8::lds_byte(wr * 16 * MT + fr, fq * 8);
    const int q4 = (lane & 15) >> 2, p4 = lane & 3, sB = q4 | ((fq & 1) << 2);
    const int boff0 = (8 * fq + q4) * 256 + (((wc * 2 + 0) ^ sB) * 32) + 8 * p4;
    const int boff1 = (8 * fq + q4) * 256 + (((wc * 2 + 1) ^ sB) * 32) + 8 * p4;
    f32x4 acc[MT][2];
#pragma unroll
    for (int m = 0; m < MT; ++m) { acc[m][0] = (f32x4){0.f, 0.f, 0.f, 0.f}; acc[m][1] = (f32x4){0.f, 0.f, 0.f, 0.f}; }
    const int nt = K / 64;
#define STG(buf, t) do { _Pragma("unroll") for (int _i = 0; _i < NA; ++_i) \
        __builtin_amdgcn_global_load_lds((const unsigned*)(cA + (size_t)(t) * 128 + voffA[_i]), (LAS unsigned*)(lds + (buf) * 32768 + ldsw + _i * 8192), 16, 0, 0); \
        _Pragma("unroll") for (int _i = 0; _i < 2; ++_i) \
        __builtin_amdgcn_global_load_lds((const unsigned*)(cB + (size_t)(t) * 64 * ldb * 2 + voffB[_i]), (LAS unsigned*)(lds + (buf) * 32768 + 16384 + ldsw + _i * 8192), 16, 0, 0); } while (0)
    STG(0, 0); STG(1, 1); STG(2, 2);
    int cur = 0;
    for (int t = 0; t < nt; ++t) {
        if (MT == 4) { if (t + 2 < nt) asm volatile("s_waitcnt vmcnt(8)" ::: "memory"); else if (t + 1 < nt) asm volatile("s_waitcnt vmcnt(4)" ::: "memory"); else asm volatile("s_waitcnt vmcnt(0)" ::: "memory"); }
        else { if (t + 2 < nt) asm volatile("s_waitcnt vmcnt(6)" ::: "memory"); else if (t + 1 < nt) asm volatile("s_waitcnt vmcnt(3)" ::: "memory"); else asm volatile("s_waitcnt vmcnt(0)" ::: "memory"); }
        __builtin_amdgcn_s_barrier();
        asm volatile("" ::: "memory");
        if (t + 3 < nt) STG((cur + 3) & 3, t + 3);
        bf16x8 At[MT][2], Bt[2][2];
        LAS unsigned char* sa = lds + cur * 32768; LAS unsigned char* sb = sa + 16384;
#pragma unroll
        for (int k = 0; k < 2; ++k) {
            bf16x4 a0 = __builtin_amdgcn_ds_read_tr16_b64_v4i16((LAS bf16x4*)(sb + boff0 + k * 8192)), a1 = __builtin_amdgcn_ds_read_tr16_b64_v4i16((LAS bf16x4*)(sb + boff0 + k * 8192 + 1024));
            bf16x4 c0 = __builtin_amdgcn_ds_read_tr16_b64_v4i16((LAS bf16x4*)(sb + boff1 + k * 8192)), c1 = __builtin_amdgcn_ds_read_tr16_b64_v4i16((LAS bf16x4*)(sb + boff1 + k * 8192 + 1024));
            Bt[0][k] = __builtin_shufflevector(a0, a1, 0, 1, 2, 3, 4, 5, 6, 7); Bt[1][k] = __builtin_shufflevector(c0, c1, 0, 1, 2, 3, 4, 5, 6, 7);
#pragma unroll
            for (int m = 0; m < MT; ++m) At[m][k] = *(const LAS bf16x8*)(sa + aoff + m * 2048 + k * 1024);
        }
        __builtin_amdgcn_s_setprio(1);
#pragma unroll
        for (int k = 0; k < 2; ++k)
#pragma unroll
            for (int m = 0; m < MT; ++m)
#pragma unroll
                for (int n = 0; n < 2; ++n) acc[m][n] = __builtin_amdgcn_mfma_f32_16x16x32_bf16(Bt[n][k], At[m][k], acc[m][n], 0, 0, 0);
        __builtin_amdgcn_s_setprio(0);
        asm volatile("s_waitcnt lgkmcnt(0)" ::: "memory");
        cur = (cur + 1) & 3;
    }
#undef STG
    __builtin_amdgcn_s_barrier();
    E(acc, wr, wc, fr, fq);
}

constexpr int XV_PITCH = 272;
template <bool INK>
__device__ __forceinline__ void xattn_item(LAS unsigned char* vs, char* ws, const int s, const int h) {
    const int lane = threadIdx.x & 63, fr = lane & 15, fq = lane >> 4;
    const bf16_t* Q = (const bf16_t*)(ws + WS_Q) + (size_t)s * SEG * HD + h * DH;
    const bf16_t* K = (const bf16_t*)(ws + WS_K) + (size_t)s * SEG * HD + h * DH;
    const bf16_t* V = (const bf16_t*)(ws + WS_V) + (size_t)s * SEG * HD + h * DH;
    const __amdgpu_buffer_rsrc_t rm = __builtin_amdgcn_make_buffer_rsrc(ws + WS_QKVM, 0, (int)(WS_OM - WS_QKVM), 0x00020000);
    const int kmo = s * (int)QKVM_B + (h * 3 + 1) * 2048, vmo = kmo + 2048;
    u32x4 km[4], vm[2];
    for (unsigned spins = 0;;) {
        asm volatile("" ::: "memory");
#pragma unroll
        for (int i = 0; i < 4; ++i) km[i] = __builtin_amdgcn_raw_buffer_load_b128(rm, kmo + (lane & 7) * 256 + i * 64 + fq * 16, 0, INK ? AUX_SC1 : 0);
#pragma unroll
        for (int i = 0; i < 2; ++i) vm[i] = __builtin_amdgcn_raw_buffer_load_b128(rm, vmo + (i * 4 + fq) * 256 + fr * 16, 0, INK ? AUX_SC1 : 0);
        if (!INK) break;
        unsigned mxs = umax2(umax4(vm[0]), umax4(vm[1]));
#pragma unroll
        for (int i = 0; i < 4; ++i) mxs = umax2(mxs, umax4(km[i]));
        if (!__any(mxs == SENT)) break;
        __builtin_amdgcn_s_sleep(16);
        if (++spins > SPIN_CAP) break;
    }
#pragma unroll
    for (int i = 0; i < 12; ++i) {
        const int r = i * 4 + fq;
        u32x4 v = (u32x4){0u, 0u, 0u, 0u};
        if (i < 2) v = vm[i]; else if (i < 10) v = *(const u32x4*)(V + (size_t)(r - 8) * HD + fr * 8);
        *(LAS u32x4*)(vs + r * XV_PITCH + fr * 16) = v;
    }
    f32x4 sc[3][2];
#pragma unroll
    for (int t = 0; t < 3; ++t) { sc[t][0] = (f32x4){0.f, 0.f, 0.f, 0.f}; sc[t][1] = (f32x4){0.f, 0.f, 0.f, 0.f}; }
#pragma unroll
    for (int i = 0; i < 4; ++i) {
        const bf16x8 q0 = *(const bf16x8*)(Q + (size_t)fr * HD + i * 32 + fq * 8), q1 = *(const bf16x8*)(Q + (size_t)(16 + fr) * HD + i * 32 + fq * 8);
#pragma unroll
        for (int t = 0; t < 3; ++t) {
            int r = t * 16 + fr - 8; r = r < 0 ? 0 : (r > 31 ? 31 : r);
            bf16x8 kf = *(const bf16x8*)(K + (size_t)r * HD + i * 32 + fq * 8);
            if (t == 0 && fr < 8) kf = __builtin_bit_cast(bf16x8, km[i]);
            sc[t][0] = __builtin_amdgcn_mfma_f32_16x16x32_bf16(kf, q0, sc[t][0], 0, 0, 0);
            sc[t][1] = __builtin_amdgcn_mfma_f32_16x16x32_bf16(kf, q1, sc[t][1], 0, 0, 0);
        }
    }
    const float scale = 0.08838834764831845f, NEG = rbf(-1000000000.0f);
    u32x4 p0[2], p1[2];
#pragma unroll
    for (int rt = 0; rt < 2; ++rt) {
        const int row = rt * 16 + fr;
        float v[12]; float mx = -INFINITY;
#pragma unroll
        for (int t = 0; t < 3; ++t)
#pragma unroll
            for (int r = 0; r < 4; ++r) { const int key = t * 16 + fq * 4 + r; float z = rbf(rbf(sc[t][rt][r]) * scale);
                if (key - 8 > row) z = NEG; if (key >= 40) z = -INFINITY; v[t * 4 + r] = z; mx = fmaxf(mx, z); }
        mx = xmax16_32(mx);
        float sum = 0.f;
#pragma unroll
        for (int i = 0; i < 12; ++i) { v[i] = __expf(v[i] - mx); sum += v[i]; }
        sum = xsum16_32(sum);
        const float inv = 1.0f / sum;
        p0[rt].x = pk_bf16(v[0] * inv, v[1] * inv); p0[rt].y = pk_bf16(v[2] * inv, v[3] * inv); p0[rt].z = pk_bf16(v[4] * inv, v[5] * inv); p0[rt].w = pk_bf16(v[6] * inv, v[7] * inv);
        p1[rt].x = pk_bf16(v[8] * inv, v[9] * inv); p1[rt].y = pk_bf16(v[10] * inv, v[11] * inv); p1[rt].z = 0u; p1[rt].w = 0u;
    }
    asm volatile("s_waitcnt lgkmcnt(0)" ::: "memory");
    const __amdgpu_buffer_rsrc_t ro = __builtin_amdgcn_make_buffer_rsrc(ws + WS_O, 0, S_LEN * HD * 2, 0x00020000);
    const int obase = (s * SEG * HD + h * DH) * 2;
    const bf16x4 zz = (bf16x4){0, 0, 0, 0};
    u32x2 ow[8][2];
#pragma unroll
    for (int et = 0; et < 8; ++et) {
        const int voff = (4 * fq + (fr >> 2)) * XV_PITCH + et * 32 + (lane & 3) * 8;
        const bf16x4 v0 = __builtin_amdgcn_ds_read_tr16_b64_v4i16((LAS bf16x4*)(vs + voff));
        const bf16x4 v1 = __builtin_amdgcn_ds_read_tr16_b64_v4i16((LAS bf16x4*)(vs + voff + 16 * XV_PITCH));
        const bf16x4 v2 = __builtin_amdgcn_ds_read_tr16_b64_v4i16((LAS bf16x4*)(vs + voff + 32 * XV_PITCH));
        const bf16x8 va = __builtin_shufflevector(v0, v1, 0, 1, 2, 3, 4, 5, 6, 7), vb = __builtin_shufflevector(v2, zz, 0, 1, 2, 3, 4, 5, 6, 7);
#pragma unroll
        for (int rt = 0; rt < 2; ++rt) {
            f32x4 o = (f32x4){0.f, 0.f, 0.f, 0.f};
            o = __builtin_amdgcn_mfma_f32_16x16x32_bf16(va, __builtin_bit_cast(bf16x8, p0[rt]), o, 0, 0, 0);
            o = __builtin_amdgcn_mfma_f32_16x16x32_bf16(vb, __builtin_bit_cast(bf16x8, p1[rt]), o, 0, 0, 0);
            ow[et][rt].x = pk_bf16(o[0], o[1]); ow[et][rt].y = pk_bf16(o[2], o[3]);
        }
    }
    asm volatile("s_waitcnt lgkmcnt(0)" ::: "memory");
#pragma unroll
    for (int et = 0; et < 8; ++et)
#pragma unroll
        for (int rt = 0; rt < 2; ++rt) *(LAS u32x2*)(vs + (rt * 16 + fr) * XV_PITCH + et * 32 + fq * 8) = ow[et][rt];
    asm volatile("s_waitcnt lgkmcnt(0)" ::: "memory");
#pragma unroll
    for (int i = 0; i < 8; ++i) {
        const int r = i * 4 + fq;
        const u32x4 w = *(const LAS u32x4*)(vs + r * XV_PITCH + fr * 16);
        __builtin_amdgcn_raw_buffer_store_b128(w, ro, obase + (r * HD + fr * 8) * 2, 0, INK ? AUX_SC1 : 0);
    }
    asm volatile("s_waitcnt lgkmcnt(0)" ::: "memory");
}

__device__ __forceinline__ void store_pair_sc1(const __amdgpu_buffer_rsrc_t rs, int off0, u32x2 w0, u32x2 w1, int fq) {
    auto sx = __builtin_amdgcn_permlane16_swap(w0.x, w1.x, false, false); auto sy = __builtin_amdgcn_permlane16_swap(w0.y, w1.y, false, false);
    const u32x4 w = (u32x4){(unsigned)sx[0], (unsigned)sy[0], (unsigned)sx[1], (unsigned)sy[1]};
    __builtin_amdgcn_raw_buffer_store_b128(w, rs, off0 + (fq & 1) * 32 + (fq >> 1) * 16, 0, AUX_SC1);
}
struct SchedOne {
    g8::Unit u; bool valid;
    __device__ __forceinline__ bool next(int i, g8::Unit& o) const { if (i != 0 || !valid) return false; o = u; return true; }
};
struct SchedKVQ {
    const bf16_t* A; const bf16_t* Wq; long dk, dv; int r;
    __device__ __forceinline__ bool next(int i, g8::Unit& u) const {
        const int L = r + 128 * i; if (L >= 352) return false;
        int pm, pn;
        if (L < 224) { pm = 1 + (L >> 5); pn = 16 + (L & 31); } else { pm = (L - 224) & 7; pn = (L - 224) >> 3; }
        const int mat = pn >> 4;
        u.A = A + (size_t)pm * 256 * DM; u.B = Wq + (mat == 1 ? dk : 0L) + (mat == 2 ? dv : 0L) + (pn & 15) * 256; u.pm = pm; u.pn = pn; return true;
    }
};
struct EpiQKV {
    char* ws;
    __device__ __forceinline__ void operator()(const f32x4 (&acc)[2][2][4][2], const g8::Unit& u, int wr, int wc, int fr, int fq) const {
        const __amdgpu_buffer_rsrc_t rq = __builtin_amdgcn_make_buffer_rsrc(ws + WS_Q, 0, (int)(WS_H - WS_Q), 0x00020000);
        const int base = (u.pn >> 4) * (S_LEN * HD * 2) + ((u.pm * 256 + wr * 64 + fr) * HD + (u.pn & 15) * 256 + wc * 32) * 2;
#pragma unroll
        for (int ai = 0; ai < 2; ++ai)
#pragma unroll
            for (int m = 0; m < 4; ++m)
#pragma unroll
                for (int bj = 0; bj < 2; ++bj) { const f32x4 v0 = acc[ai][bj][m][0], v1 = acc[ai][bj][m][1];
                    store_pair_sc1(rq, base + ((ai * 128 + m * 16) * HD + bj * 128) * 2, (u32x2){pk_bf16(v0[0], v0[1]), pk_bf16(v0[2], v0[3])}, (u32x2){pk_bf16(v1[0], v1[1]), pk_bf16(v1[2], v1[3])}, fq); }
        asm volatile("s_waitcnt vmcnt(0)" ::: "memory");
        if ((threadIdx.x & 63) == 0) {
            if (u.pn >= 16) __hip_atomic_fetch_add((unsigned*)(ws + WS_CNT + (size_t)(CNT_KV + u.pm * 32 + u.pn - 16) * 256), 1u, __ATOMIC_RELAXED, __HIP_MEMORY_SCOPE_AGENT);
            __hip_atomic_fetch_add((unsigned*)(ws + WS_CNT + (size_t)(CNT_QKV + u.pm) * 256), 1u, __ATOMIC_RELAXED, __HIP_MEMORY_SCOPE_AGENT);
        }
    }
};
template <int MT>
struct EpiKV0 {
    char* ws; int row0, pn2;
    __device__ __forceinline__ void operator()(const f32x4 (&acc)[MT][2], int wr, int wc, int fr, int fq) const {
        const __amdgpu_buffer_rsrc_t rq = __builtin_amdgcn_make_buffer_rsrc(ws + WS_K, 0, (int)(WS_H - WS_K), 0x00020000);
        const int base = (pn2 >> 5) * (S_LEN * HD * 2) + ((row0 + wr * 16 * MT + fr) * HD + (pn2 & 31) * 128 + wc * 32) * 2;
#pragma unroll
        for (int m = 0; m < MT; ++m) { const f32x4 v0 = acc[m][0], v1 = acc[m][1];
            store_pair_sc1(rq, base + m * 16 * HD * 2, (u32x2){pk_bf16(v0[0], v0[1]), pk_bf16(v0[2], v0[3])}, (u32x2){pk_bf16(v1[0], v1[1]), pk_bf16(v1[2], v1[3])}, fq); }
        asm volatile("s_waitcnt vmcnt(0)" ::: "memory");
        if ((threadIdx.x & 63) == 0) {
            __hip_atomic_fetch_add((unsigned*)(ws + WS_CNT + (size_t)((row0 < 128 ? CNT_KV : CNT_KV0B) + (pn2 >> 1)) * 256), 1u, __ATOMIC_RELAXED, __HIP_MEMORY_SCOPE_AGENT);
            __hip_atomic_fetch_add((unsigned*)(ws + WS_CNT + (size_t)CNT_QKV * 256), 1u, __ATOMIC_RELAXED, __HIP_MEMORY_SCOPE_AGENT);
        }
    }
};
struct EpiMLP1 {
    char* ws; const bf16_t* b1;
    __device__ __forceinline__ void operator()(const f32x4 (&acc)[2][2][4][2], const g8::Unit& u, int wr, int wc, int fr, int fq) const {
        const __amdgpu_buffer_rsrc_t rh = __builtin_amdgcn_make_buffer_rsrc(ws + WS_H, 0, S_LEN * FF * 2, 0x00020000);
        const int row0 = u.pm * 256 + wr * 64 + fr, col0 = u.pn * 256 + wc * 32;
#pragma unroll
        for (int bj = 0; bj < 2; ++bj) {
            const u32x2 ba = *(const u32x2*)(b1 + col0 + bj * 128 + 4 * fq), bb = *(const u32x2*)(b1 + col0 + bj * 128 + 16 + 4 * fq);
#pragma unroll
            for (int ai = 0; ai < 2; ++ai)
#pragma unroll
                for (int m = 0; m < 4; ++m) { const f32x4 v0 = acc[ai][bj][m][0], v1 = acc[ai][bj][m][1];
                    u32x2 w0, w1;
                    w0.x = pk_bf16(gelu_tanh(rbf(rbf(v0[0]) + bf2f(ba.x & 0xffffu))), gelu_tanh(rbf(rbf(v0[1]) + bf2f(ba.x >> 16))));
                    w0.y = pk_bf16(gelu_tanh(rbf(rbf(v0[2]) + bf2f(ba.y & 0xffffu))), gelu_tanh(rbf(rbf(v0[3]) + bf2f(ba.y >> 16))));
                    w1.x = pk_bf16(gelu_tanh(rbf(rbf(v1[0]) + bf2f(bb.x & 0xffffu))), gelu_tanh(rbf(rbf(v1[1]) + bf2f(bb.x >> 16))));
                    w1.y = pk_bf16(gelu_tanh(rbf(rbf(v1[2]) + bf2f(bb.y & 0xffffu))), gelu_tanh(rbf(rbf(v1[3]) + bf2f(bb.y >> 16))));
                    store_pair_sc1(rh, ((row0 + ai * 128 + m * 16) * FF + col0 + bj * 128) * 2, w0, w1, fq);
                    if (m & 1) asm volatile("" ::: "memory"); }
        }
        signal_counter(ws, CNT_H + u.pm);
    }
};
struct EpiMLP2 {
    char* ws; const bf16_t* b2; int row0, col0;
    __device__ __forceinline__ void operator()(const f32x4 (&acc)[4][2], int wr, int wc, int fr, int fq) const {
        const __amdgpu_buffer_rsrc_t rb = __builtin_amdgcn_make_buffer_rsrc(ws + WS_B, 0, S_LEN * DM * 2, 0x00020000);
        const int col = col0 + wc * 32;
        const u32x2 ba = *(const u32x2*)(b2 + col + 4 * fq), bb = *(const u32x2*)(b2 + col + 16 + 4 * fq);
#pragma unroll
        for (int m = 0; m < 4; ++m) { const f32x4 v0 = acc[m][0], v1 = acc[m][1];
            u32x2 w0, w1;
            w0.x = pk_bf16(rbf(v0[0]) + bf2f(ba.x & 0xffffu), rbf(v0[1]) + bf2f(ba.x >> 16)); w0.y = pk_bf16(rbf(v0[2]) + bf2f(ba.y & 0xffffu), rbf(v0[3]) + bf2f(ba.y >> 16));
            w1.x = pk_bf16(rbf(v1[0]) + bf2f(bb.x & 0xffffu), rbf(v1[1]) + bf2f(bb.x >> 16)); w1.y = pk_bf16(rbf(v1[2]) + bf2f(bb.y & 0xffffu), rbf(v1[3]) + bf2f(bb.y >> 16));
            store_pair_sc1(rb, ((row0 + wr * 64 + m * 16 + fr) * DM + col) * 2, w0, w1, fq); }
        signal_counter(ws, CNT_B + (row0 >> 8));
    }
};
__device__ __forceinline__ u32x2 combine4(const f32x4 v, const u32x2 xw, const u32x2 bw) {
    u32x2 w;
    w.x = pk_bf16(rbf(bf2f(xw.x & 0xffffu) + rbf(v[0])) + bf2f(bw.x & 0xffffu), rbf(bf2f(xw.x >> 16) + rbf(v[1])) + bf2f(bw.x >> 16));
    w.y = pk_bf16(rbf(bf2f(xw.y & 0xffffu) + rbf(v[2])) + bf2f(bw.y & 0xffffu), rbf(bf2f(xw.y >> 16) + rbf(v[3])) + bf2f(bw.y >> 16));
    return w;
}
template <int MT>
struct EpiOut {
    const bf16_t* x; const bf16_t* bout; bf16_t* out; int row0, col0;
    __device__ __forceinline__ void operator()(const f32x4 (&acc)[MT][2], int wr, int wc, int fr, int fq) const {
#pragma unroll
        for (int m = 0; m < MT; ++m)
#pragma unroll
            for (int n = 0; n < 2; ++n) {
                const size_t off = (size_t)(row0 + wr * 16 * MT + m * 16 + fr) * DM + col0 + wc * 32 + n * 16 + 4 * fq;
                *(u32x2*)(out + off) = combine4(acc[m][n], *(const u32x2*)(x + off), *(const u32x2*)(bout + off));
            }
    }
};

constexpr int TAIL_BLK0 = 6;
__global__ void __launch_bounds__(512, 2) k_mega(const bf16_t* __restrict__ x, const bf16_t* __restrict__ Wq, const bf16_t* __restrict__ Wk, const bf16_t* __restrict__ Wv,
                                                 const bf16_t* __restrict__ Wo, const bf16_t* __restrict__ w1, const bf16_t* __restrict__ b1, const bf16_t* __restrict__ w2,
                                                 const bf16_t* __restrict__ b2, char* ws, bf16_t* __restrict__ out) {
    extern __shared__ __attribute__((aligned(16))) unsigned char shm_[];
    LAS unsigned char* lds = (LAS unsigned char*)shm_;
    const int b = blockIdx.x;
    const int role = (b >> 3) & 1, r = (b >> 4) * 8 + (b & 7);
    if (role == 0) { chain_body<4>(lds, r, Wq, Wk, Wv, Wo, ws); return; }
#define WSTAMP(i) do {} while (0)
    WSTAMP(0);
    {
        const int row0 = (r >> 6) * 64, pn2 = r & 63;
        EpiKV0<2> E{ws, row0, pn2};
        gemm128_w8<2, EpiKV0<2>>(lds, (const bf16_t*)(ws + WS_XN) + (size_t)row0 * DM, DM, (pn2 >= 32 ? Wv : Wk) + (pn2 & 31) * 128, HD, DM, E);
    }
    if (r < 64) {
        EpiKV0<4> E{ws, 128, r};
        gemm128_w8<4, EpiKV0<4>>(lds, (const bf16_t*)(ws + WS_XN) + (size_t)128 * DM, DM, (r >= 32 ? Wv : Wk) + (r & 31) * 128, HD, DM, E);
    }
    WSTAMP(1);
    {
        SchedKVQ S{(const bf16_t*)(ws + WS_XN), Wq, (long)(Wk - Wq), (long)(Wv - Wq), r ^ 64};
        EpiQKV E{ws}; g8::gemm_phase<EpiQKV, SchedKVQ>(lds, DM, DM, HD, S, E);
    }
    WSTAMP(2);
    {
        const int r2 = r ^ 64, L = r2 >= 96 ? r2 - 96 : 32 + r2;
        SchedOne S; S.valid = (r2 >= 96) || (r2 < 80); S.u.pm = L & 7; S.u.pn = L >> 3; S.u.A = (const bf16_t*)(ws + WS_Y) + (size_t)S.u.pm * 256 * DM; S.u.B = w1 + S.u.pn * 256;
        EpiMLP1 E{ws, b1}; g8::gemm_phase<EpiMLP1, SchedOne>(lds, DM, DM, FF, S, E);
    }
    WSTAMP(3);
    {
        const int pm = r >> 3, pn = r & 7;
        wait_counter(ws, CNT_H + (pm >> 1), 14 * 8);
        EpiMLP2 E{ws, b2, pm * 128, pn * 128};
        gemm128_w8<4, EpiMLP2>(lds, (const bf16_t*)(ws + WS_H) + (size_t)pm * 128 * FF, FF, w2 + pn * 128, DM, FF, E);
    }
    WSTAMP(4);
    if (r < 80) {
        const int blk = r >> 4, t = r & 15;
        {
            if (threadIdx.x < 64) poll_ge(ws, CNT_STEP, (unsigned)(8 * blk + 8));
            wait_counter(ws, CNT_QKV + blk, (blk == 0 ? 192 * 8 : 32 * 8) + 16 * 8);
            WSTAMP(5);
            const int wave = threadIdx.x >> 6;
#pragma unroll 1
            for (int i = 0; i < 2; ++i) { const int item = t * 16 + wave * 2 + i; xattn_item<true>(lds + wave * 48 * XV_PITCH, ws, blk * 8 + (item >> 5), item & 31); }
            signal_counter(ws, CNT_O + blk);
        }
        {
            if (threadIdx.x < 64) poll_ge(ws, CNT_B + blk, 16 * 8);
            wait_counter(ws, CNT_O + blk, 16 * 8);
            WSTAMP(6);
            const int pm = blk * 2 + (t >> 3), pn = t & 7;
            EpiOut<4> E{x, (const bf16_t*)(ws + WS_B), out, pm * 128, pn * 128};
            gemm128_w8<4, EpiOut<4>>(lds, (const bf16_t*)(ws + WS_O) + (size_t)pm * 128 * HD, HD, Wo + pn * 128, DM, HD, E);
        }
    } else if (r < 112) {
        constexpr int blk = 5; const int t = r - 80;
        if (threadIdx.x < 64) poll_ge(ws, CNT_STEP, (unsigned)(8 * blk + 8));
        wait_counter(ws, CNT_QKV + blk, 32 * 8 + 16 * 8);
        WSTAMP(5);
        const int wave = threadIdx.x >> 6, item = t * 8 + wave;
        xattn_item<true>(lds + wave * 48 * XV_PITCH, ws, blk * 8 + (item >> 5), item & 31);
        signal_counter(ws, CNT_O + blk);
        if (threadIdx.x < 64) poll_ge(ws, CNT_B + blk, 16 * 8);
        wait_counter(ws, CNT_O + blk, 32 * 8);
        WSTAMP(6);
        const int pm = blk * 4 + (t >> 3), pn = t & 7;
        EpiOut<2> E{x, (const bf16_t*)(ws + WS_B), out, pm * 64, pn * 128};
        gemm128_w8<2, EpiOut<2>>(lds, (const bf16_t*)(ws + WS_O) + (size_t)pm * 64 * HD, HD, Wo + pn * 128, DM, HD, E);
    }
    WSTAMP(7);
}

__global__ void __launch_bounds__(256) k_tail_attn(char* ws) {
    __shared__ __attribute__((aligned(16))) unsigned char shm_[4 * 48 * XV_PITCH];
    const int wave = threadIdx.x >> 6, item = blockIdx.x * 4 + wave;
    xattn_item<false>((LAS unsigned char*)shm_ + wave * 48 * XV_PITCH, ws, TAIL_BLK0 * 8 + (item >> 5), item & 31);
}
__global__ void __launch_bounds__(256) k_tail_gemm(const bf16_t* __restrict__ x, const bf16_t* __restrict__ Wo, const char* __restrict__ ws, bf16_t* __restrict__ out) {
    extern __shared__ __attribute__((aligned(16))) unsigned char shm_[];
    const int tid = threadIdx.x, wave = __builtin_amdgcn_readfirstlane(tid >> 6), lane = tid & 63, fr = lane & 15, fq = lane >> 4;
    LAS unsigned char* lds = (LAS unsigned char*)shm_ + wave * 36864;
    const int pn = blockIdx.x & 31, pm = blockIdx.x >> 5, row0 = TAIL_BLK0 * 256 + pm * 64, col0 = pn * 32;
    const char* cA = (const char*)((const bf16_t*)(ws + WS_O) + (size_t)row0 * HD + wave * 1024);
    const char* cB = (const char*)(Wo + (size_t)wave * 1024 * DM + col0);
    unsigned voffA[8], voffB[4];
#pragma unroll
    for (int i = 0; i < 8; ++i) { const int c = i * 64 + lane, row = c >> 3, ch = (c & 7) ^ (row & 7); voffA[i] = (unsigned)(row * HD + ch * 8) * 2u; }
#pragma unroll
    for (int i = 0; i < 4; ++i) { const int c = i * 64 + lane, k = c >> 2, cb = c & 3; voffB[i] = (unsigned)(k * DM + cb * 8) * 2u; }
#define STG(buf, t) do { _Pragma("unroll") for (int _i = 0; _i < 8; ++_i) \
        __builtin_amdgcn_global_load_lds((const unsigned*)(cA + (size_t)(t) * 128 + voffA[_i]), (LAS unsigned*)(lds + (buf) * 12288 + _i * 1024), 16, 0, 0); \
        _Pragma("unroll") for (int _i = 0; _i < 4; ++_i) \
        __builtin_amdgcn_global_load_lds((const unsigned*)(cB + (size_t)(t) * 64 * DM * 2 + voffB[_i]), (LAS unsigned*)(lds + (buf) * 12288 + 8192 + _i * 1024), 16, 0, 0); } while (0)
    f32x4 acc[4][2];
#pragma unroll
    for (int m = 0; m < 4; ++m) { acc[m][0] = (f32x4){0.f, 0.f, 0.f, 0.f}; acc[m][1] = (f32x4){0.f, 0.f, 0.f, 0.f}; }
    STG(0, 0); STG(1, 1);
    int cur = 0;
    for (int t = 0; t < 16; ++t) {
        int nx = cur + 2; if (nx >= 3) nx -= 3;
        if (t + 2 < 16) { STG(nx, t + 2); asm volatile("s_waitcnt vmcnt(24)" ::: "memory"); }
        else if (t + 1 < 16) asm volatile("s_waitcnt vmcnt(12)" ::: "memory");
        else asm volatile("s_waitcnt vmcnt(0)" ::: "memory");
        LAS unsigned char* sa = lds + cur * 12288; LAS unsigned char* sb = sa + 8192;
        bf16x8 At[4][2], Bt[2][2];
#pragma unroll
        for (int k = 0; k < 2; ++k) {
#pragma unroll
            for (int m = 0; m < 4; ++m) { const int row = m * 16 + fr; At[m][k] = *(const LAS bf16x8*)(sa + row * 128 + (((k * 4 + fq) ^ (row & 7)) * 16)); }
#pragma unroll
            for (int n = 0; n < 2; ++n) {
                const int off = (k * 32 + 8 * fq + (fr >> 2)) * 64 + n * 32 + (lane & 3) * 8;
                const bf16x4 a0 = __builtin_amdgcn_ds_read_tr16_b64_v4i16((LAS bf16x4*)(sb + off)), a1 = __builtin_amdgcn_ds_read_tr16_b64_v4i16((LAS bf16x4*)(sb + off + 256));
                Bt[n][k] = __builtin_shufflevector(a0, a1, 0, 1, 2, 3, 4, 5, 6, 7); }
        }
#pragma unroll
        for (int k = 0; k < 2; ++k)
#pragma unroll
            for (int m = 0; m < 4; ++m)
#pragma unroll
                for (int n = 0; n < 2; ++n) acc[m][n] = __builtin_amdgcn_mfma_f32_16x16x32_bf16(Bt[n][k], At[m][k], acc[m][n], 0, 0, 0);
        asm volatile("s_waitcnt lgkmcnt(0)" ::: "memory");
        cur = cur + 1 == 3 ? 0 : cur + 1;
    }
#undef STG
    __syncthreads();
    LAS f32x4* red = (LAS f32x4*)shm_;
#pragma unroll
    for (int m = 0; m < 4; ++m)
#pragma unroll
        for (int n = 0; n < 2; ++n) red[((wave * 4 + m) * 2 + n) * 64 + lane] = acc[m][n];
    __syncthreads();
#pragma unroll
    for (int n = 0; n < 2; ++n) {
        const int m = wave;
        const f32x4 v = (red[((0 * 4 + m) * 2 + n) * 64 + lane] + red[((1 * 4 + m) * 2 + n) * 64 + lane]) + (red[((2 * 4 + m) * 2 + n) * 64 + lane] + red[((3 * 4 + m) * 2 + n) * 64 + lane]);
        const size_t off = (size_t)(row0 + m * 16 + fr) * DM + col0 + n * 16 + 4 * fq;
        *(u32x2*)(out + off) = combine4(v, *(const u32x2*)(x + off), *(const u32x2*)((const bf16_t*)(ws + WS_B) + off));
    }
}

extern "C" void kernel_launch(void* const* d_in, const int* in_sizes, int n_in, void* d_out, int out_size, void* d_ws, size_t ws_size, hipStream_t stream) {
    const bf16_t* x = (const bf16_t*)d_in[0];
    const bf16_t* mem0 = (const bf16_t*)d_in[1];
    const bf16_t* Wq = (const bf16_t*)d_in[2];
    const bf16_t* Wk = (const bf16_t*)d_in[3];
    const bf16_t* Wv = (const bf16_t*)d_in[4];
    const bf16_t* Wo = (const bf16_t*)d_in[5];
    const bf16_t* w1 = (const bf16_t*)d_in[8];
    const bf16_t* b1 = (const bf16_t*)d_in[9];
    const bf16_t* w2 = (const bf16_t*)d_in[10];
    const bf16_t* b2 = (const bf16_t*)d_in[11];
    char* ws = (char*)d_ws;
    bf16_t* out = (bf16_t*)d_out;
    (void)hipFuncSetAttribute((const void*)k_mega, hipFuncAttributeMaxDynamicSharedMemorySize, CH_LDS);
    (void)hipFuncSetAttribute((const void*)k_tail_gemm, hipFuncAttributeMaxDynamicSharedMemorySize, 4 * 36864);
    k_prep<<<S_LEN / 4, 256, 0, stream>>>(x, mem0, ws);
    k_mega<<<256, 512, CH_LDS, stream>>>(x, Wq, Wk, Wv, Wo, w1, b1, w2, b2, ws, out);
    k_tail_attn<<<64 * (8 - TAIL_BLK0), 256, 0, stream>>>(ws);
    k_tail_gemm<<<128 * (8 - TAIL_BLK0), 256, 4 * 36864, stream>>>(x, Wo, ws, out);
}
```

```cpp
#include <hip/hip_runtime.h>
#include <hip/hip_bf16.h>

#define LAS __attribute__((address_space(3)))
typedef unsigned short bf16_t;
typedef short bf16x8 __attribute__((ext_vector_type(8)));
typedef short bf16x4 __attribute__((ext_vector_type(4)));
typedef float f32x4 __attribute__((ext_vector_type(4)));
typedef float f32x2 __attribute__((ext_vector_type(2)));
typedef unsigned u32x4 __attribute__((ext_vector_type(4)));
typedef unsigned u32x2 __attribute__((ext_vector_type(2)));

constexpr int S_LEN = 2048, DM = 1024, NH = 32, DH = 128, HD = NH * DH, MM = 8, SEG = 32, NSEG = S_LEN / SEG, FF = 3584;
constexpr int AUX_SC1 = 16;
constexpr unsigned SENT = 0xFFFFFFFFu;

__device__ __forceinline__ float bf2f(unsigned b) { return __builtin_bit_cast(float, b << 16); }
typedef __bf16 bf16x2_t __attribute__((ext_vector_type(2)));
__device__ __forceinline__ unsigned pk_bf16(float lo, float hi) { const f32x2 v = {lo, hi}; return __builtin_bit_cast(unsigned, __builtin_convertvector(v, bf16x2_t)); }
__device__ __forceinline__ float rbf(float x) { return bf2f(pk_bf16(x, x) & 0xffffu); }
__device__ __forceinline__ unsigned desent(unsigned w) { return w == SENT ? 0x7FC07FC0u : w; }
__device__ __forceinline__ unsigned umax2(unsigned a, unsigned b) { return a > b ? a : b; }
__device__ __forceinline__ unsigned umax4(u32x4 v) { return umax2(umax2(v.x, v.y), umax2(v.z, v.w)); }

constexpr size_t WS_XN = 0;
constexpr size_t WS_Y = WS_XN + (size_t)S_LEN * DM * 2;
constexpr size_t WS_Q = WS_Y + (size_t)S_LEN * DM * 2;
constexpr size_t WS_K = WS_Q + (size_t)S_LEN * HD * 2;
constexpr size_t WS_V = WS_K + (size_t)S_LEN * HD * 2;
constexpr size_t WS_H = WS_V + (size_t)S_LEN * HD * 2;
constexpr size_t WS_O = WS_H + (size_t)S_LEN * FF * 2;
constexpr size_t WS_B = WS_O + (size_t)S_LEN * HD * 2;
constexpr size_t WS_SENT0 = WS_B + (size_t)S_LEN * DM * 2;
constexpr size_t MEM_B = (size_t)MM * DM * 2;
constexpr size_t WS_MEM = WS_SENT0;
constexpr size_t QKVM_B = (size_t)NH * 3 * MM * DH * 2;
constexpr size_t WS_QKVM = WS_MEM + (size_t)(NSEG + 1) * MEM_B;
constexpr size_t OM_B = (size_t)MM * HD * 2;
constexpr size_t WS_OM = WS_QKVM + (size_t)NSEG * QKVM_B;
constexpr size_t WS_SENT1 = WS_OM + (size_t)NSEG * OM_B;
constexpr int FILL0 = 16;
constexpr size_t WS_CNT = WS_SENT1;
constexpr int CNT_H = 0;
constexpr int CNT_B = 8;
constexpr int CNT_O = 16;
constexpr int CNT_QKV = 24;
constexpr int CNT_FILL = 33;
constexpr int CNT_STEP = 32;
constexpr int CNT_KV = 40;
constexpr int CNT_KV0B = 40 + 256;
constexpr int CNT_N = 40 + 256 + 32;
constexpr size_t WS_TMO = WS_CNT + (size_t)CNT_N * 256;
constexpr size_t WS_END = WS_TMO + 256;

__device__ __forceinline__ float wave_sum(float v) {
#pragma unroll
    for (int o = 32; o > 0; o >>= 1) v += __shfl_xor(v, o);
    return v;
}
__device__ __forceinline__ void ln_row(float (&v)[16]) {
    float s = 0.f;
#pragma unroll
    for (int i = 0; i < 16; ++i) s += v[i];
    const float mean = wave_sum(s) * (1.0f / DM);
    float q = 0.f;
#pragma unroll
    for (int i = 0; i < 16; ++i) { const float d = v[i] - mean; q += d * d; }
    const float var = rbf(wave_sum(q) * (1.0f / DM));
    const float mb = rbf(mean);
    const float den = rbf(sqrtf(rbf(var + 1e-5f)));
#pragma unroll
    for (int i = 0; i < 16; ++i) v[i] = rbf(rbf(v[i] - mb) / den);
}
__global__ void __launch_bounds__(256) k_prep(const bf16_t* __restrict__ x, const bf16_t* __restrict__ mem0, char* __restrict__ ws) {
    const int wave = threadIdx.x >> 6, lane = threadIdx.x & 63, row = blockIdx.x * 4 + wave;
    const u32x4* src = (const u32x4*)(x + (size_t)row * DM);
    u32x4 a = src[lane], b = src[lane + 64];
    float v[16];
    const unsigned w[8] = {a.x, a.y, a.z, a.w, b.x, b.y, b.z, b.w};
#pragma unroll
    for (int i = 0; i < 8; ++i) { v[2 * i] = bf2f(w[i] & 0xffffu); v[2 * i + 1] = bf2f(w[i] >> 16); }
    ln_row(v);
    u32x4 o0, o1;
    o0.x = pk_bf16(v[0], v[1]); o0.y = pk_bf16(v[2], v[3]); o0.z = pk_bf16(v[4], v[5]); o0.w = pk_bf16(v[6], v[7]);
    o1.x = pk_bf16(v[8], v[9]); o1.y = pk_bf16(v[10], v[11]); o1.z = pk_bf16(v[12], v[13]); o1.w = pk_bf16(v[14], v[15]);
    u32x4* dx = (u32x4*)(ws + WS_XN + (size_t)row * DM * 2);
    dx[lane] = o0; dx[lane + 64] = o1;
    ln_row(v);
    o0.x = pk_bf16(v[0], v[1]); o0.y = pk_bf16(v[2], v[3]); o0.z = pk_bf16(v[4], v[5]); o0.w = pk_bf16(v[6], v[7]);
    o1.x = pk_bf16(v[8], v[9]); o1.y = pk_bf16(v[10], v[11]); o1.z = pk_bf16(v[12], v[13]); o1.w = pk_bf16(v[14], v[15]);
    u32x4* dy = (u32x4*)(ws + WS_Y + (size_t)row * DM * 2);
    dy[lane] = o0; dy[lane + 64] = o1;
    {
        constexpr int nM = FILL0 * (int)MEM_B / 16, nQ = FILL0 * (int)QKVM_B / 16, nO = FILL0 * (int)OM_B / 16;
        const u32x4 f = (u32x4){SENT, SENT, SENT, SENT};
        for (int i = blockIdx.x * 256 + threadIdx.x; i < nM + nQ + nO; i += (S_LEN / 4) * 256) {
            char* dst = i < nM ? ws + WS_MEM + MEM_B + (size_t)i * 16 : (i < nM + nQ ? ws + WS_QKVM + (size_t)(i - nM) * 16 : ws + WS_OM + (size_t)(i - nM - nQ) * 16);
            *(u32x4*)dst = f;
        }
    }
    if (blockIdx.x >= 8 && blockIdx.x < 8 + (CNT_N * 256 / 16 + 255) / 256) {
        const int i = (blockIdx.x - 8) * 256 + threadIdx.x; if (i < CNT_N * 256 / 16) ((u32x4*)(ws + WS_CNT))[i] = (u32x4){0u, 0u, 0u, 0u}; }
    if (blockIdx.x < 4) {
        const int c = blockIdx.x * 256 + threadIdx.x, r = c & 7, kg = c >> 3;
        u32x4 m = *(const u32x4*)(mem0 + (size_t)r * DM + kg * 8);
        m.x = desent(m.x); m.y = desent(m.y); m.z = desent(m.z); m.w = desent(m.w);
        *(u32x4*)(ws + WS_MEM + (size_t)c * 16) = m;
    }
}

namespace g8 {
constexpr int BM = 256, BK = 64, HALF = 128, HTB = HALF * BK * 2, STAGE_BYTES = 8 * HTB;
__host__ __device__ __forceinline__ int lds_byte(int r, int c) { const int st = (r >> 4) * 2 + (c >> 5), rr = r & 15, cc = c & 31, ob = rr * 64 + cc * 2; return st * 1024 + (ob ^ (((ob >> 9) & 1) << 5)); }
__host__ __device__ __forceinline__ void stage_rc(int b, int& R, int& C) { const int st = b / 1024, sb = b % 1024, swz = sb ^ (((sb >> 9) & 1) << 5); R = (st >> 1) * 16 + swz / 64; C = (st & 1) * 32 + (swz % 64) / 2; }
__host__ __device__ __forceinline__ int bsw(int row) { return (row & 3) | (((row >> 3) & 1) << 2); }
struct Unit { const bf16_t* A; const bf16_t* B; int pm, pn; };

template <class Epi, class Sched>
__device__ __forceinline__ void gemm_phase(LAS unsigned char* lds, const int K, const int lda, const int ldb, const Sched& S, const Epi& E) {
    int tid = threadIdx.x; asm volatile("" : "+v"(tid));
    const int wid = __builtin_amdgcn_readfirstlane(tid >> 6), lane = tid & 63, wr = wid >> 2, wc = wid & 3, fr = lane & 15, fq = lane >> 4;
    const int nt = K / BK;
    unsigned voffA[2], voffB[2];
#pragma unroll
    for (int i = 0; i < 2; ++i) { int R, C; stage_rc(tid * 16 + i * 8192, R, C); voffA[i] = (unsigned)(R * lda + C) * 2u;
        const int b = tid * 16 + i * 8192, row = b >> 8, ch = (b & 255) >> 4, seg = (ch >> 1) ^ bsw(row);
        voffB[i] = (unsigned)(row * ldb + seg * 16 + (ch & 1) * 8) * 2u; }
    const size_t kstepA = (size_t)(BK * 2), kstepB = (size_t)BK * ldb * 2;
    const size_t hstepA = (size_t)HALF * lda * 2, hstepB = (size_t)HALF * 2;
    const unsigned ldsw = (unsigned)wid * 1024u;
    const int aoff = lds_byte(wr * 64 + fr, fq * 8);
    const int q4 = (lane & 15) >> 2, p4 = lane & 3, sB = q4 | ((fq & 1) << 2);
    const int boff0 = (8 * fq + q4) * 256 + (((wc * 2 + 0) ^ sB) * 32) + 8 * p4;
    const int boff1 = (8 * fq + q4) * 256 + (((wc * 2 + 1) ^ sB) * 32) + 8 * p4;
#define G8_SA(b, h) (((b) * 2 + (h)) * HTB)
#define G8_SB(b, h) ((4 + (b) * 2 + (h)) * HTB)
#define G8_STAGE(bufoff, gbase, voff) do { _Pragma("unroll") for (int _i = 0; _i < 2; ++_i) \
        __builtin_amdgcn_global_load_lds((const unsigned*)((const char*)(gbase) + (voff)[_i]), (LAS unsigned*)(lds + (bufoff) + ldsw + _i * 8192), 16, 0, 0); } while (0)
#define G8_LDA(dst, b, h) do { _Pragma("unroll") for (int m = 0; m < 4; ++m) _Pragma("unroll") for (int k = 0; k < 2; ++k) dst[m][k] = *(const LAS bf16x8*)(lds + G8_SA(b, h) + aoff + m * 2048 + k * 1024); } while (0)
#define G8_TR(off) __builtin_amdgcn_ds_read_tr16_b64_v4i16((LAS bf16x4*)(lds + (off)))
#define G8_LDB(dst, b, h) do { _Pragma("unroll") for (int k = 0; k < 2; ++k) { \
        bf16x4 a0 = G8_TR(G8_SB(b, h) + boff0 + k * 8192), a1 = G8_TR(G8_SB(b, h) + boff0 + k * 8192 + 1024); \
        bf16x4 c0 = G8_TR(G8_SB(b, h) + boff1 + k * 8192), c1 = G8_TR(G8_SB(b, h) + boff1 + k * 8192 + 1024); \
        dst[0][k] = __builtin_shufflevector(a0, a1, 0, 1, 2, 3, 4, 5, 6, 7); dst[1][k] = __builtin_shufflevector(c0, c1, 0, 1, 2, 3, 4, 5, 6, 7); } } while (0)
#define G8_MMA(ai, bj, At, Bt) do { __builtin_amdgcn_s_setprio(1); _Pragma("unroll") for (int m = 0; m < 4; ++m) _Pragma("unroll") for (int n = 0; n < 2; ++n) _Pragma("unroll") for (int k = 0; k < 2; ++k) \
        acc[ai][bj][m][n] = __builtin_amdgcn_mfma_f32_16x16x32_bf16(Bt[n][k], At[m][k], acc[ai][bj][m][n], 0, 0, 0); __builtin_amdgcn_s_setprio(0); } while (0)
#define G8_WAIT_V(n) asm volatile("s_waitcnt vmcnt(" #n ")" ::: "memory")
#define G8_WAIT_L(n) asm volatile("s_waitcnt lgkmcnt(" #n ")" ::: "memory")
#define G8_BAR __builtin_amdgcn_s_barrier()
#define G8_SCHED __builtin_amdgcn_sched_barrier(0)
    Unit cur, nxt; int ui = 0;
    if (!S.next(0, cur)) return;
    f32x4 acc[2][2][4][2];
#pragma unroll
    for (int a = 0; a < 2; ++a)
#pragma unroll
        for (int b = 0; b < 2; ++b)
#pragma unroll
            for (int m = 0; m < 4; ++m)
#pragma unroll
                for (int n = 0; n < 2; ++n) acc[a][b][m][n] = (f32x4){0.f, 0.f, 0.f, 0.f};
    bf16x8 At[4][2], B0[2][2], B1[2][2];
    const char* cA = (const char*)cur.A; const char* cB = (const char*)cur.B;
    G8_STAGE(G8_SB(0, 0), cB, voffB); G8_STAGE(G8_SA(0, 0), cA, voffA); G8_STAGE(G8_SB(0, 1), cB + hstepB, voffB); G8_STAGE(G8_SA(0, 1), cA + hstepA, voffA);
    if (wr == 1) G8_BAR;
    G8_WAIT_V(4); G8_BAR;
    G8_STAGE(G8_SB(1, 0), cB + kstepB, voffB); G8_STAGE(G8_SA(1, 0), cA + kstepA, voffA); G8_STAGE(G8_SB(1, 1), cB + hstepB + kstepB, voffB);
    G8_WAIT_V(6); G8_BAR;
    for (;;) {
        const bool has_next = S.next(ui + 1, nxt);
        const char* nA = has_next ? (const char*)nxt.A : cA; const char* nB = has_next ? (const char*)nxt.B : cB;
        for (int t = 0; t < nt; t += 2) {
            const bool last = (t == nt - 2);
            const char* a1 = cA + (size_t)(t + 1) * kstepA;
            const char* a2 = last ? nA : cA + (size_t)(t + 2) * kstepA; const char* b2 = last ? nB : cB + (size_t)(t + 2) * kstepB;
            const char* a3 = a2 + kstepA; const char* b3 = b2 + kstepB;
            G8_LDB(B0, 0, 0); G8_SCHED; G8_LDA(At, 0, 0); G8_STAGE(G8_SA(1, 1), a1 + hstepA, voffA);
            G8_WAIT_L(8); G8_BAR; G8_WAIT_L(0); G8_MMA(0, 0, At, B0); G8_BAR; G8_SCHED;
            G8_LDB(B1, 0, 1); G8_STAGE(G8_SB(0, 0), b2, voffB);
            G8_BAR; G8_WAIT_L(0); G8_MMA(0, 1, At, B1); G8_BAR;
            G8_LDA(At, 0, 1); G8_STAGE(G8_SA(0, 0), a2, voffA);
            G8_BAR; G8_WAIT_L(0); G8_MMA(1, 0, At, B0); G8_BAR; G8_SCHED;
            G8_STAGE(G8_SB(0, 1), b2 + hstepB, voffB);
            G8_WAIT_V(6); G8_BAR; G8_MMA(1, 1, At, B1); G8_BAR;
            G8_LDB(B0, 1, 0); G8_SCHED; G8_LDA(At, 1, 0); G8_STAGE(G8_SA(0, 1), a2 + hstepA, voffA);
            G8_WAIT_L(8); G8_BAR; G8_WAIT_L(0); G8_MMA(0, 0, At, B0); G8_BAR; G8_SCHED;
            G8_LDB(B1, 1, 1); G8_STAGE(G8_SB(1, 0), b3, voffB);
            G8_BAR; G8_WAIT_L(0); G8_MMA(0, 1, At, B1); G8_BAR;
            G8_LDA(At, 1, 1); G8_STAGE(G8_SA(1, 0), a3, voffA);
            G8_BAR; G8_WAIT_L(0); G8_MMA(1, 0, At, B0); G8_BAR; G8_SCHED;
            G8_STAGE(G8_SB(1, 1), b3 + hstepB, voffB);
            G8_WAIT_V(6); G8_BAR; G8_MMA(1, 1, At, B1); G8_BAR;
        }
        E(acc, cur, wr, wc, fr, fq);
        if (!has_next) break;
#pragma unroll
        for (int a = 0; a < 2; ++a)
#pragma unroll
            for (int b = 0; b < 2; ++b)
#pragma unroll
                for (int m = 0; m < 4; ++m)
#pragma unroll
                    for (int n = 0; n < 2; ++n) acc[a][b][m][n] = (f32x4){0.f, 0.f, 0.f, 0.f};
        cur = nxt; cA = nA; cB = nB; ++ui;
    }
    G8_WAIT_V(0);
    if (wr == 0) G8_BAR;
    G8_BAR;
#undef G8_SA
#undef G8_SB
#undef G8_STAGE
#undef G8_LDA
#undef G8_LDB
#undef G8_TR
#undef G8_MMA
#undef G8_WAIT_V
#undef G8_WAIT_L
#undef G8_BAR
#undef G8_SCHED
}
}

__device__ __forceinline__ float gelu_tanh(float x) {
    const float u = 0.7978845608028654f * (x + 0.044715f * x * x * x);
    const float th = 1.0f - 2.0f * __builtin_amdgcn_rcpf(__expf(2.0f * u) + 1.0f);
    return 0.5f * x * (1.0f + th);
}

__device__ __forceinline__ void wait_counter(const char* ws, int idx, unsigned need) {
    if (threadIdx.x < 64) {
        const unsigned* p = (const unsigned*)(ws + WS_CNT + (size_t)idx * 256);
        for (unsigned spins = 0; (unsigned)__builtin_amdgcn_readfirstlane(__hip_atomic_load(p, __ATOMIC_RELAXED, __HIP_MEMORY_SCOPE_AGENT)) < need;) {
            __builtin_amdgcn_s_sleep(8);
            if (++spins > 400000u) break;
        }
        __builtin_amdgcn_fence(__ATOMIC_ACQUIRE, "agent");
        asm volatile("s_waitcnt vmcnt(0)" ::: "memory");
    }
    asm volatile("" ::: "memory"); __builtin_amdgcn_s_barrier(); asm volatile("" ::: "memory");
}
__device__ __forceinline__ void signal_counter(char* ws, int idx) {
    asm volatile("s_waitcnt vmcnt(0)" ::: "memory");
    if ((threadIdx.x & 63) == 0) __hip_atomic_fetch_add((unsigned*)(ws + WS_CNT + (size_t)idx * 256), 1u, __ATOMIC_RELAXED, __HIP_MEMORY_SCOPE_AGENT);
}

__device__ __forceinline__ unsigned poll_ge(const char* ws, int idx, unsigned need) {
    const unsigned* p = (const unsigned*)(ws + WS_CNT + (size_t)idx * 256);
    unsigned v = 0;
    for (unsigned spins = 0; (v = (unsigned)__builtin_amdgcn_readfirstlane(__hip_atomic_load(p, __ATOMIC_RELAXED, __HIP_MEMORY_SCOPE_AGENT))) < need;) {
        __builtin_amdgcn_s_sleep(8);
        if (++spins > 400000u) break;
    }
    asm volatile("" ::: "memory");
    return v;
}

constexpr unsigned SPIN_CAP = 200000u;
constexpr int KS_PITCH = 272;
constexpr int CH_LDS = 160 * 1024;

__device__ __forceinline__ float xmax16_32(float v) {
    auto a = __builtin_amdgcn_permlane16_swap(__builtin_bit_cast(unsigned, v), __builtin_bit_cast(unsigned, v), false, false);
    v = fmaxf(__builtin_bit_cast(float, (unsigned)a[0]), __builtin_bit_cast(float, (unsigned)a[1]));
    auto b = __builtin_amdgcn_permlane32_swap(__builtin_bit_cast(unsigned, v), __builtin_bit_cast(unsigned, v), false, false);
    return fmaxf(__builtin_bit_cast(float, (unsigned)b[0]), __builtin_bit_cast(float, (unsigned)b[1]));
}
__device__ __forceinline__ float xsum16_32(float v) {
    auto a = __builtin_amdgcn_permlane16_swap(__builtin_bit_cast(unsigned, v), __builtin_bit_cast(unsigned, v), false, false);
    v = __builtin_bit_cast(float, (unsigned)a[0]) + __builtin_bit_cast(float, (unsigned)a[1]);
    auto b = __builtin_amdgcn_permlane32_swap(__builtin_bit_cast(unsigned, v), __builtin_bit_cast(unsigned, v), false, false);
    return __builtin_bit_cast(float, (unsigned)b[0]) + __builtin_bit_cast(float, (unsigned)b[1]);
}

__device__ __forceinline__ u32x4 quad_gather(unsigned v) {
    u32x4 r;
    r.x = (unsigned)__builtin_amdgcn_update_dpp(0, (int)v, 0x00, 0xf, 0xf, true); r.y = (unsigned)__builtin_amdgcn_update_dpp(0, (int)v, 0x55, 0xf, 0xf, true);
    r.z = (unsigned)__builtin_amdgcn_update_dpp(0, (int)v, 0xAA, 0xf, 0xf, true); r.w = (unsigned)__builtin_amdgcn_update_dpp(0, (int)v, 0xFF, 0xf, 0xf, true);
    return r;
}
#define CH_DELAY1 12
#define CH_DELAY2 6
#define CH_DELAY3 8

template <int CPH>
__device__ __forceinline__ void chain_body(LAS unsigned char* lds, const int b, const bf16_t* __restrict__ Wq, const bf16_t* __restrict__ Wk, const bf16_t* __restrict__ Wv,
                                           const bf16_t* __restrict__ Wo, char* ws) {
    constexpr int ES = 128 / CPH, NE = ES / 16, NT = 3 * NE, RG = CPH / 2, RPG = 8 / RG;
    constexpr int VP = ES * 2;
    constexpr int L_RED = 0;
    constexpr int L_RED2 = L_RED + 8 * NT * 512;
    constexpr int L_KS = L_RED2 + 8 * RPG * 64;
    constexpr int L_VS = L_KS + 2 * 32 * KS_PITCH;
    constexpr int L_OS = L_VS + 2 * 48 * VP;
    constexpr bool WOL = (CPH == 4);
    constexpr int L_WO = L_OS + 8 * 1024 * RPG;
    static_assert(L_WO + (WOL ? 65536 : 0) <= 156 * 1024, "LDS");
    const int tid = threadIdx.x, wave = __builtin_amdgcn_readfirstlane(tid >> 6), lane = tid & 63, fr = lane & 15, fq = lane >> 4;
    const int xc = b & 7, yc = b >> 3;
    const int h = xc + 8 * (yc / CPH), j = yc % CPH, g = yc % RG, c = xc * 8 + yc / RG;
    const __amdgpu_buffer_rsrc_t rs = __builtin_amdgcn_make_buffer_rsrc(ws + WS_SENT0, 0, (int)(WS_SENT1 - WS_SENT0), 0x00020000);
    constexpr int O_MEM = (int)(WS_MEM - WS_SENT0), O_QKVM = (int)(WS_QKVM - WS_SENT0), O_OM = (int)(WS_OM - WS_SENT0);
    const __amdgpu_buffer_rsrc_t rkv = __builtin_amdgcn_make_buffer_rsrc(ws + WS_K, 0, (int)(WS_H - WS_K), 0x00020000);
    constexpr int O_V = (int)(WS_V - WS_K);
    bool dead = false;

    bf16x8 wq[NT][4], wo[16];
    {
        LAS unsigned char* stg = lds + wave * 8192;
        constexpr int CPR = ES / 8;
        static_assert(128 * CPR / 64 == 8, "8 chunks per lane and pass");
        u32x4 t[5][8];
        auto issue = [&](int ps) {
            if (ps < 3) { const bf16_t* W = (ps == 0 ? Wq : (ps == 1 ? Wk : Wv)) + (size_t)wave * 128 * HD + h * DH + j * ES;
#pragma unroll
                for (int i = 0; i < 8; ++i) { const int ci = i * 64 + lane; t[ps][i] = *(const u32x4*)(W + (size_t)(ci / CPR) * HD + (ci % CPR) * 8); } }
            else { const bf16_t* W = Wo + (size_t)(wave * 512 + (ps - 3) * 256) * DM + c * 16;
#pragma unroll
                for (int i = 0; i < 8; ++i) { const int ci = i * 64 + lane; t[ps][i] = *(const u32x4*)(W + (size_t)(ci >> 1) * DM + (ci & 1) * 8); } }
        };
        issue(0); issue(1); issue(2);
        asm volatile("" ::: "memory");
#pragma unroll
        for (int ps = 0; ps < 5; ++ps) {
#pragma unroll
            for (int i = 0; i < 8; ++i) *(LAS u32x4*)(stg + (i * 64 + lane) * 16) = t[ps][i];
            asm volatile("s_waitcnt lgkmcnt(0)" ::: "memory");
            if (ps + 3 < 5) issue(ps + 3);
            asm volatile("" ::: "memory");
            if (ps < 3) {
#pragma unroll
                for (int e = 0; e < NE; ++e)
#pragma unroll
                    for (int i = 0; i < 4; ++i) {
                        const int off = (i * 32 + 8 * fq + (fr >> 2)) * (ES * 2) + e * 32 + (lane & 3) * 8;
                        const bf16x4 a0 = __builtin_amdgcn_ds_read_tr16_b64_v4i16((LAS bf16x4*)(stg + off)), a1 = __builtin_amdgcn_ds_read_tr16_b64_v4i16((LAS bf16x4*)(stg + off + 4 * ES * 2));
                        wq[ps * NE + e][i] = __builtin_shufflevector(a0, a1, 0, 1, 2, 3, 4, 5, 6, 7);
                    }
            } else {
#pragma unroll
                for (int i = 0; i < 8; ++i) {
                    const int off = (i * 32 + 8 * fq + (fr >> 2)) * 32 + (lane & 3) * 8;
                    const bf16x4 a0 = __builtin_amdgcn_ds_read_tr16_b64_v4i16((LAS bf16x4*)(stg + off)), a1 = __builtin_amdgcn_ds_read_tr16_b64_v4i16((LAS bf16x4*)(stg + off + 128));
                    wo[(ps - 3) * 8 + i] = __builtin_shufflevector(a0, a1, 0, 1, 2, 3, 4, 5, 6, 7);
                }
            }
            asm volatile("s_waitcnt lgkmcnt(0)" ::: "memory");
        }
        if (WOL) {
#pragma unroll
            for (int i = 8; i < 16; ++i) *(LAS bf16x8*)(lds + L_WO + (wave * 8 + i - 8) * 1024 + lane * 16) = wo[i];
        }
    }
    __syncthreads();

    {
        constexpr int nM = (NSEG - FILL0) * (int)MEM_B / 16, nQ = (NSEG - FILL0) * (int)QKVM_B / 16, nO = (NSEG - FILL0) * (int)OM_B / 16;
        const u32x4 f = (u32x4){SENT, SENT, SENT, SENT};
        for (int i = b * 512 + tid; i < nM + nQ + nO; i += 32 * CPH * 512) {
            const int off = i < nM ? O_MEM + (FILL0 + 1) * (int)MEM_B + i * 16 : (i < nM + nQ ? O_QKVM + FILL0 * (int)QKVM_B + (i - nM) * 16 : O_OM + FILL0 * (int)OM_B + (i - nM - nQ) * 16);
            __builtin_amdgcn_raw_buffer_store_b128(f, rs, off, 0, AUX_SC1);
        }
        signal_counter(ws, CNT_FILL);
    }
    if (tid == 0) *(LAS unsigned*)(lds + L_OS) = 0u;
    if (tid < 2 * 8 * VP / 16) { const int bf = tid / (8 * VP / 16), r = tid % (8 * VP / 16); *(LAS u32x4*)(lds + L_VS + bf * 48 * VP + 40 * VP + r * 16) = (u32x4){0u, 0u, 0u, 0u}; }
    {
        poll_ge(ws, CNT_KV + (h >> 1), 32u); poll_ge(ws, CNT_KV + 16 + (h >> 1), 32u);
        const int r = tid >> 4, ch = tid & 15;
        *(LAS u32x4*)(lds + L_KS + r * KS_PITCH + ch * 16) = __builtin_amdgcn_raw_buffer_load_b128(rkv, (r * HD + h * DH + ch * 8) * 2, 0, AUX_SC1);
        if (tid < 32 * ES / 8) { const int r2 = tid / (ES / 8), c2 = tid % (ES / 8);
            *(LAS u32x4*)(lds + L_VS + (8 + r2) * VP + c2 * 16) = __builtin_amdgcn_raw_buffer_load_b128(rkv, O_V + (r2 * HD + h * DH + j * ES + c2 * 8) * 2, 0, AUX_SC1); }
    }
    __syncthreads();

    for (int s = 0; s < NSEG; ++s) {
        const int kb = s & 1;
        {
            if (s > 0) __builtin_amdgcn_s_sleep(CH_DELAY1);
            u32x4 a[4];
            const int base = O_MEM + s * (int)MEM_B + wave * 2048 + fq * 128 + (lane & 7) * 16;
            for (unsigned spins = 0;;) {
                asm volatile("" ::: "memory");
#pragma unroll
                for (int i = 0; i < 4; ++i) a[i] = __builtin_amdgcn_raw_buffer_load_b128(rs, base + i * 512, 0, AUX_SC1);
                unsigned mxs = 0;
#pragma unroll
                for (int i = 0; i < 4; ++i) mxs = umax2(mxs, umax4(a[i]));
                if (!__any(mxs == SENT) || dead) break;
                if (++spins > SPIN_CAP) { dead = true; if (lane == 0) *(volatile unsigned*)(ws + WS_TMO) = 1u; break; }
            }
            if (b == 0 && tid == 448 && (s & 7) == 0) __hip_atomic_store((unsigned*)(ws + WS_CNT + (size_t)CNT_STEP * 256), (unsigned)s, __ATOMIC_RELAXED, __HIP_MEMORY_SCOPE_AGENT);
            constexpr int NQK = 2 * NE;
            {
                f32x4 acc[NQK];
#pragma unroll
                for (int t = 0; t < NQK; ++t) acc[t] = (f32x4){0.f, 0.f, 0.f, 0.f};
#pragma unroll
                for (int i = 0; i < 4; ++i)
#pragma unroll
                    for (int t = 0; t < NQK; ++t) acc[t] = __builtin_amdgcn_mfma_f32_16x16x32_bf16(wq[t][i], __builtin_bit_cast(bf16x8, a[i]), acc[t], 0, 0, 0);
                if (fr < 8) {
#pragma unroll
                    for (int t = 0; t < NQK; ++t) *(LAS f32x4*)(lds + L_RED + ((wave * NT + t) * 128 + fr * 16 + fq * 4) * 4) = acc[t];
                }
            }
            __syncthreads();
            if (tid < NQK * 64) {
                const int t = tid >> 6, m = (tid & 63) >> 3, np = (tid & 7) * 2, mat = t / NE, col = (t % NE) * 16 + np;
                f32x2 sum = (f32x2){0.f, 0.f};
#pragma unroll
                for (int w = 0; w < 8; ++w) sum += *(const LAS f32x2*)(lds + L_RED + ((w * NT + t) * 128 + m * 16 + np) * 4);
                const u32x4 q4 = quad_gather(desent(pk_bf16(sum.x, sum.y)));
                if ((tid & 3) == 0) __builtin_amdgcn_raw_buffer_store_b128(q4, rs, O_QKVM + s * (int)QKVM_B + ((h * 3 + mat) * 8 + m) * 256 + (j * ES + col) * 2, 0, AUX_SC1);
            }
            {
                f32x4 acc[NE];
#pragma unroll
                for (int t = 0; t < NE; ++t) acc[t] = (f32x4){0.f, 0.f, 0.f, 0.f};
#pragma unroll
                for (int i = 0; i < 4; ++i)
#pragma unroll
                    for (int t = 0; t < NE; ++t) acc[t] = __builtin_amdgcn_mfma_f32_16x16x32_bf16(wq[NQK + t][i], __builtin_bit_cast(bf16x8, a[i]), acc[t], 0, 0, 0);
                if (fr < 8) {
#pragma unroll
                    for (int t = 0; t < NE; ++t) *(LAS f32x4*)(lds + L_RED + ((wave * NT + NQK + t) * 128 + fr * 16 + fq * 4) * 4) = acc[t];
                }
            }
        }
        __syncthreads();
        if (tid >= 2 * NE * 64 && tid < NT * 64) {
            const int t = tid >> 6, m = (tid & 63) >> 3, np = (tid & 7) * 2, col = (t % NE) * 16 + np;
            f32x2 sum = (f32x2){0.f, 0.f};
#pragma unroll
            for (int w = 0; w < 8; ++w) sum += *(const LAS f32x2*)(lds + L_RED + ((w * NT + t) * 128 + m * 16 + np) * 4);
            const unsigned pk = desent(pk_bf16(sum.x, sum.y));
            const u32x4 q4 = quad_gather(pk);
            if ((tid & 3) == 0) __builtin_amdgcn_raw_buffer_store_b128(q4, rs, O_QKVM + s * (int)QKVM_B + ((h * 3 + 2) * 8 + m) * 256 + (j * ES + col) * 2, 0, AUX_SC1);
            *(LAS unsigned*)(lds + L_VS + kb * 48 * VP + m * VP + col * 2) = pk;
            asm volatile("s_waitcnt lgkmcnt(0)" ::: "memory");
            if (lane == 0) __hip_atomic_fetch_add((LAS unsigned*)(lds + L_OS), 1u, __ATOMIC_RELAXED, __HIP_MEMORY_SCOPE_WORKGROUP);
        }
        if (s == NSEG - 1) break;
        if (wave == 0) {
            bf16x8 kx[3][4];
#pragma unroll
            for (int t = 0; t < 3; ++t) {
                int r = t * 16 + fr - 8; r = r < 0 ? 0 : (r > 31 ? 31 : r);
#pragma unroll
                for (int i = 0; i < 4; ++i) kx[t][i] = *(const LAS bf16x8*)(lds + L_KS + kb * 32 * KS_PITCH + r * KS_PITCH + i * 64 + fq * 16);
            }
            __builtin_amdgcn_s_sleep(CH_DELAY2);
            u32x4 qf[4], km[4];
            const int base = O_QKVM + s * (int)QKVM_B + h * 3 * 2048 + (lane & 7) * 256 + fq * 16;
            for (unsigned spins = 0;;) {
                asm volatile("" ::: "memory");
#pragma unroll
                for (int i = 0; i < 4; ++i) { qf[i] = __builtin_amdgcn_raw_buffer_load_b128(rs, base + i * 64, 0, AUX_SC1); km[i] = __builtin_amdgcn_raw_buffer_load_b128(rs, base + 2048 + i * 64, 0, AUX_SC1); }
                unsigned mxs = 0;
#pragma unroll
                for (int i = 0; i < 4; ++i) mxs = umax2(mxs, umax2(umax4(qf[i]), umax4(km[i])));
                if (!__any(mxs == SENT) || dead) break;
                if (++spins > SPIN_CAP) { dead = true; if (lane == 0) *(volatile unsigned*)(ws + WS_TMO) = 2u; break; }
            }
            f32x4 sc[3];
#pragma unroll
            for (int t = 0; t < 3; ++t) sc[t] = (f32x4){0.f, 0.f, 0.f, 0.f};
#pragma unroll
            for (int i = 0; i < 4; ++i) {
                const bf16x8 k0 = fr < 8 ? __builtin_bit_cast(bf16x8, km[i]) : kx[0][i];
                sc[1] = __builtin_amdgcn_mfma_f32_16x16x32_bf16(kx[1][i], __builtin_bit_cast(bf16x8, qf[i]), sc[1], 0, 0, 0);
                sc[2] = __builtin_amdgcn_mfma_f32_16x16x32_bf16(kx[2][i], __builtin_bit_cast(bf16x8, qf[i]), sc[2], 0, 0, 0);
                sc[0] = __builtin_amdgcn_mfma_f32_16x16x32_bf16(k0, __builtin_bit_cast(bf16x8, qf[i]), sc[0], 0, 0, 0);
            }
            for (unsigned sp = 0; __hip_atomic_load((LAS unsigned*)(lds + L_OS), __ATOMIC_RELAXED, __HIP_MEMORY_SCOPE_WORKGROUP) < (unsigned)(NE * (s + 1)) && sp < SPIN_CAP; ++sp) {}
            asm volatile("" ::: "memory");
            bf16x8 vf[NE][2];
            const bf16x4 zz = (bf16x4){0, 0, 0, 0};
#pragma unroll
            for (int et = 0; et < NE; ++et) {
                const int voff = L_VS + kb * 48 * VP + (4 * fq + (fr >> 2)) * VP + et * 32 + (lane & 3) * 8;
                const bf16x4 v0 = __builtin_amdgcn_ds_read_tr16_b64_v4i16((LAS bf16x4*)(lds + voff));
                const bf16x4 v1 = __builtin_amdgcn_ds_read_tr16_b64_v4i16((LAS bf16x4*)(lds + voff + 16 * VP));
                const bf16x4 v2 = __builtin_amdgcn_ds_read_tr16_b64_v4i16((LAS bf16x4*)(lds + voff + 32 * VP));
                vf[et][0] = __builtin_shufflevector(v0, v1, 0, 1, 2, 3, 4, 5, 6, 7); vf[et][1] = __builtin_shufflevector(v2, zz, 0, 1, 2, 3, 4, 5, 6, 7);
            }
            const float scale = 0.08838834764831845f;
            const bool hi = fr >= 8;
            float v[6];
            v[0] = hi ? sc[1][2] : sc[0][0]; v[1] = hi ? sc[1][3] : sc[0][1]; v[2] = hi ? sc[2][0] : sc[0][2];
            v[3] = hi ? sc[2][1] : sc[0][3]; v[4] = hi ? sc[2][2] : sc[1][0]; v[5] = hi ? sc[2][3] : sc[1][1];
            float mx = -INFINITY;
#pragma unroll
            for (int i = 0; i < 6; ++i) { float z = rbf(rbf(v[i]) * scale); if (i >= 2 && hi && fq >= 2) z = -INFINITY; v[i] = z; mx = fmaxf(mx, z); }
            mx = fmaxf(mx, __builtin_bit_cast(float, __builtin_amdgcn_update_dpp(0, __builtin_bit_cast(int, mx), 0x128, 0xf, 0xf, true)));
            mx = xmax16_32(mx);
            float sum = 0.f;
#pragma unroll
            for (int i = 0; i < 6; ++i) { v[i] = __expf(v[i] - mx); sum += v[i]; }
            sum += __builtin_bit_cast(float, __builtin_amdgcn_update_dpp(0, __builtin_bit_cast(int, sum), 0x128, 0xf, 0xf, true));
            sum = xsum16_32(sum);
            float inv = __builtin_amdgcn_rcpf(sum); inv = inv * (2.0f - sum * inv);
            const unsigned w0 = pk_bf16(v[0] * inv, v[1] * inv), w1 = pk_bf16(v[2] * inv, v[3] * inv), w2 = pk_bf16(v[4] * inv, v[5] * inv);
            u32x4 p0, p1;
            p0.x = w0; p0.y = w1; p0.z = w2; p0.w = (unsigned)__builtin_amdgcn_update_dpp(0, (int)w0, 0x128, 0xf, 0xf, true);
            p1.x = (unsigned)__builtin_amdgcn_update_dpp(0, (int)w1, 0x128, 0xf, 0xf, true); p1.y = (unsigned)__builtin_amdgcn_update_dpp(0, (int)w2, 0x128, 0xf, 0xf, true); p1.z = 0u; p1.w = 0u;
#pragma unroll
            for (int et = 0; et < NE; ++et) {
                f32x4 o = (f32x4){0.f, 0.f, 0.f, 0.f};
                o = __builtin_amdgcn_mfma_f32_16x16x32_bf16(vf[et][0], __builtin_bit_cast(bf16x8, p0), o, 0, 0, 0);
                o = __builtin_amdgcn_mfma_f32_16x16x32_bf16(vf[et][1], __builtin_bit_cast(bf16x8, p1), o, 0, 0, 0);
                {
                    const unsigned wx = desent(pk_bf16(o[0], o[1])), wy = desent(pk_bf16(o[2], o[3]));
                    auto sx = __builtin_amdgcn_permlane16_swap(wx, wx, false, false); auto sy = __builtin_amdgcn_permlane16_swap(wy, wy, false, false);
                    const u32x4 w = (u32x4){(unsigned)sx[0], (unsigned)sy[0], (unsigned)sx[1], (unsigned)sy[1]};
                    const int k = h * DH + j * ES + et * 16 + 4 * fq;
                    const int off = O_OM + s * (int)OM_B + (fr / RPG) * (8192 * RPG) + (k >> 5) * (64 * RPG) + ((k >> 3) & 3) * (16 * RPG) + (fr % RPG) * 16;
                    if (fr < 8 && (fq & 1) == 0) __builtin_amdgcn_raw_buffer_store_b128(w, rs, off, 0, AUX_SC1);
                }
            }
        } else {
            const int t2 = tid - 64; const int nb = kb ^ 1; const int row0 = (s + 1) * SEG;
            if (((s + 1) & 7) == 0) { const int blk = (s + 1) >> 3; poll_ge(ws, CNT_KV + blk * 32 + (h >> 1), 8u); poll_ge(ws, CNT_KV + blk * 32 + 16 + (h >> 1), 8u); }
            if (s == FILL0 - 3) poll_ge(ws, CNT_FILL, 32u * CPH * 8u);
            if (s + 1 == 4) { poll_ge(ws, CNT_KV0B + (h >> 1), 16u); poll_ge(ws, CNT_KV0B + 16 + (h >> 1), 16u); }
            for (int i = t2; i < 512 + 32 * ES / 8; i += 448) {
                if (i < 512) { const int r = i >> 4, ch = i & 15;
                    *(LAS u32x4*)(lds + L_KS + nb * 32 * KS_PITCH + r * KS_PITCH + ch * 16) = __builtin_amdgcn_raw_buffer_load_b128(rkv, ((row0 + r) * HD + h * DH + ch * 8) * 2, 0, AUX_SC1); }
                else { const int r2 = (i - 512) / (ES / 8), c2 = (i - 512) % (ES / 8);
                    *(LAS u32x4*)(lds + L_VS + nb * 48 * VP + (8 + r2) * VP + c2 * 16) = __builtin_amdgcn_raw_buffer_load_b128(rkv, O_V + ((row0 + r2) * HD + h * DH + j * ES + c2 * 8) * 2, 0, AUX_SC1); }
            }
        }
        __syncthreads();
        {
            if (WOL) {
#pragma unroll
                for (int i = 8; i < 16; ++i) wo[i] = *(const LAS bf16x8*)(lds + L_WO + (wave * 8 + i - 8) * 1024 + lane * 16);
            }
            __builtin_amdgcn_s_sleep(CH_DELAY3);
            u32x4 a[RPG];
            constexpr int KPG = 16 / RPG;
            const int base = O_OM + s * (int)OM_B + g * (8192 * RPG) + wave * (1024 * RPG) + (fr / RPG) * (64 * RPG) + fq * (16 * RPG) + (fr % RPG) * 16;
            for (unsigned spins = 0;;) {
                asm volatile("" ::: "memory");
#pragma unroll
                for (int i = 0; i < RPG; ++i) a[i] = __builtin_amdgcn_raw_buffer_load_b128(rs, base + i * 1024, 0, AUX_SC1);
                unsigned mxs = 0;
#pragma unroll
                for (int i = 0; i < RPG; ++i) mxs = umax2(mxs, umax4(a[i]));
                if (!__any(mxs == SENT) || dead) break;
                if (++spins > SPIN_CAP) { dead = true; if (lane == 0) *(volatile unsigned*)(ws + WS_TMO) = 3u; break; }
            }
            f32x4 acc[KPG];
#pragma unroll
            for (int q = 0; q < KPG; ++q) acc[q] = (f32x4){0.f, 0.f, 0.f, 0.f};
#pragma unroll
            for (int gq = 0; gq < RPG; ++gq)
#pragma unroll
                for (int q = 0; q < KPG; ++q) acc[q] = __builtin_amdgcn_mfma_f32_16x16x32_bf16(wo[gq * KPG + q], __builtin_bit_cast(bf16x8, a[gq]), acc[q], 0, 0, 0);
            f32x4 r = acc[0];
#pragma unroll
            for (int q = 1; q < KPG; ++q) if (fr / RPG == q) r = acc[q];
#pragma unroll
            for (int e = 0; e < 4; ++e) {
                if (RPG <= 2) r[e] += __builtin_bit_cast(float, __builtin_amdgcn_update_dpp(0, __builtin_bit_cast(int, r[e]), 0x122, 0xf, 0xf, true));
                r[e] += __builtin_bit_cast(float, __builtin_amdgcn_update_dpp(0, __builtin_bit_cast(int, r[e]), 0x124, 0xf, 0xf, true));
                r[e] += __builtin_bit_cast(float, __builtin_amdgcn_update_dpp(0, __builtin_bit_cast(int, r[e]), 0x128, 0xf, 0xf, true));
            }
            if (fr < RPG) *(LAS f32x4*)(lds + L_RED2 + ((wave * RPG + fr) * 16 + fq * 4) * 4) = r;
        }
        __syncthreads();
        if (tid < RPG * 8) {
            const int m = tid >> 3, np = (tid & 7) * 2;
            f32x2 sum = (f32x2){0.f, 0.f};
#pragma unroll
            for (int w = 0; w < 8; ++w) sum += *(const LAS f32x2*)(lds + L_RED2 + ((w * RPG + m) * 16 + np) * 4);
            const unsigned pk = desent(pk_bf16(sum.x, sum.y));
            const int off = O_MEM + (s + 1) * (int)MEM_B + (c >> 1) * 512 + ((c & 1) * 2 + (np >> 3)) * 128 + (g * RPG + m) * 16;
            const u32x4 q4 = quad_gather(pk);
            if ((tid & 3) == 0) __builtin_amdgcn_raw_buffer_store_b128(q4, rs, off, 0, AUX_SC1);
        }
    }
}

template <int MT, class Epi>
__device__ __forceinline__ void gemm128_w8(LAS unsigned char* lds, const bf16_t* A, const int lda, const bf16_t* B, const int ldb, const int K, const Epi& E) {
    int tid = threadIdx.x; asm volatile("" : "+v"(tid));
    const int wid = __builtin_amdgcn_readfirstlane(tid >> 6), lane = tid & 63, wr = wid >> 2, wc = wid & 3, fr = lane & 15, fq = lane >> 4;
    constexpr int NA = MT / 2;
    unsigned voffA[NA], voffB[2];
#pragma unroll
    for (int i = 0; i < NA; ++i) { int R, C; g8::stage_rc(tid * 16 + i * 8192, R, C); voffA[i] = (unsigned)(R * lda + C) * 2u; }
#pragma unroll
    for (int i = 0; i < 2; ++i) { const int bb = tid * 16 + i * 8192, row = bb >> 8, ch = (bb & 255) >> 4, seg = (ch >> 1) ^ g8::bsw(row);
        voffB[i] = (unsigned)(row * ldb + seg * 16 + (ch & 1) * 8) * 2u; }
    const char* cA = (const char*)A; const char* cB = (const char*)B;
    const unsigned ldsw = (unsigned)wid * 1024u;
    const int aoff = g8::lds_byte(wr * 16 * MT + fr, fq * 8);
    const int q4 = (lane & 15) >> 2, p4 = lane & 3, sB = q4 | ((fq & 1) << 2);
    const int boff0 = (8 * fq + q4) * 256 + (((wc * 2 + 0) ^ sB) * 32) + 8 * p4;
    const int boff1 = (8 * fq + q4) * 256 + (((wc * 2 + 1) ^ sB) * 32) + 8 * p4;
    f32x4 acc[MT][2];
#pragma unroll
    for (int m = 0; m < MT; ++m) { acc[m][0] = (f32x4){0.f, 0.f, 0.f, 0.f}; acc[m][1] = (f32x4){0.f, 0.f, 0.f, 0.f}; }
    const int nt = K / 64;
#define STG(buf, t) do { _Pragma("unroll") for (int _i = 0; _i < NA; ++_i) \
        __builtin_amdgcn_global_load_lds((const unsigned*)(cA + (size_t)(t) * 128 + voffA[_i]), (LAS unsigned*)(lds + (buf) * 32768 + ldsw + _i * 8192), 16, 0, 0); \
        _Pragma("unroll") for (int _i = 0; _i < 2; ++_i) \
        __builtin_amdgcn_global_load_lds((const unsigned*)(cB + (size_t)(t) * 64 * ldb * 2 + voffB[_i]), (LAS unsigned*)(lds + (buf) * 32768 + 16384 + ldsw + _i * 8192), 16, 0, 0); } while (0)
    STG(0, 0); STG(1, 1); STG(2, 2);
    int cur = 0;
    for (int t = 0; t < nt; ++t) {
        if (MT == 4) { if (t + 2 < nt) asm volatile("s_waitcnt vmcnt(8)" ::: "memory"); else if (t + 1 < nt) asm volatile("s_waitcnt vmcnt(4)" ::: "memory"); else asm volatile("s_waitcnt vmcnt(0)" ::: "memory"); }
        else { if (t + 2 < nt) asm volatile("s_waitcnt vmcnt(6)" ::: "memory"); else if (t + 1 < nt) asm volatile("s_waitcnt vmcnt(3)" ::: "memory"); else asm volatile("s_waitcnt vmcnt(0)" ::: "memory"); }
        __builtin_amdgcn_s_barrier();
        asm volatile("" ::: "memory");
        if (t + 3 < nt) STG((cur + 3) & 3, t + 3);
        bf16x8 At[MT][2], Bt[2][2];
        LAS unsigned char* sa = lds + cur * 32768; LAS unsigned char* sb = sa + 16384;
#pragma unroll
        for (int k = 0; k < 2; ++k) {
            bf16x4 a0 = __builtin_amdgcn_ds_read_tr16_b64_v4i16((LAS bf16x4*)(sb + boff0 + k * 8192)), a1 = __builtin_amdgcn_ds_read_tr16_b64_v4i16((LAS bf16x4*)(sb + boff0 + k * 8192 + 1024));
            bf16x4 c0 = __builtin_amdgcn_ds_read_tr16_b64_v4i16((LAS bf16x4*)(sb + boff1 + k * 8192)), c1 = __builtin_amdgcn_ds_read_tr16_b64_v4i16((LAS bf16x4*)(sb + boff1 + k * 8192 + 1024));
            Bt[0][k] = __builtin_shufflevector(a0, a1, 0, 1, 2, 3, 4, 5, 6, 7); Bt[1][k] = __builtin_shufflevector(c0, c1, 0, 1, 2, 3, 4, 5, 6, 7);
#pragma unroll
            for (int m = 0; m < MT; ++m) At[m][k] = *(const LAS bf16x8*)(sa + aoff + m * 2048 + k * 1024);
        }
        __builtin_amdgcn_s_setprio(1);
#pragma unroll
        for (int k = 0; k < 2; ++k)
#pragma unroll
            for (int m = 0; m < MT; ++m)
#pragma unroll
                for (int n = 0; n < 2; ++n) acc[m][n] = __builtin_amdgcn_mfma_f32_16x16x32_bf16(Bt[n][k], At[m][k], acc[m][n], 0, 0, 0);
        __builtin_amdgcn_s_setprio(0);
        asm volatile("s_waitcnt lgkmcnt(0)" ::: "memory");
        cur = (cur + 1) & 3;
    }
#undef STG
    __builtin_amdgcn_s_barrier();
    E(acc, wr, wc, fr, fq);
}

constexpr int XV_PITCH = 272;
template <bool INK>
__device__ __forceinline__ void xattn_item(LAS unsigned char* vs, char* ws, const int s, const int h) {
    const int lane = threadIdx.x & 63, fr = lane & 15, fq = lane >> 4;
    const bf16_t* Q = (const bf16_t*)(ws + WS_Q) + (size_t)s * SEG * HD + h * DH;
    const bf16_t* K = (const bf16_t*)(ws + WS_K) + (size_t)s * SEG * HD + h * DH;
    const bf16_t* V = (const bf16_t*)(ws + WS_V) + (size_t)s * SEG * HD + h * DH;
    const __amdgpu_buffer_rsrc_t rm = __builtin_amdgcn_make_buffer_rsrc(ws + WS_QKVM, 0, (int)(WS_OM - WS_QKVM), 0x00020000);
    const int kmo = s * (int)QKVM_B + (h * 3 + 1) * 2048, vmo = kmo + 2048;
    u32x4 km[4], vm[2];
    for (unsigned spins = 0;;) {
        asm volatile("" ::: "memory");
#pragma unroll
        for (int i = 0; i < 4; ++i) km[i] = __builtin_amdgcn_raw_buffer_load_b128(rm, kmo + (lane & 7) * 256 + i * 64 + fq * 16, 0, INK ? AUX_SC1 : 0);
#pragma unroll
        for (int i = 0; i < 2; ++i) vm[i] = __builtin_amdgcn_raw_buffer_load_b128(rm, vmo + (i * 4 + fq) * 256 + fr * 16, 0, INK ? AUX_SC1 : 0);
        if (!INK) break;
        unsigned mxs = umax2(umax4(vm[0]), umax4(vm[1]));
#pragma unroll
        for (int i = 0; i < 4; ++i) mxs = umax2(mxs, umax4(km[i]));
        if (!__any(mxs == SENT)) break;
        __builtin_amdgcn_s_sleep(16);
        if (++spins > SPIN_CAP) break;
    }
#pragma unroll
    for (int i = 0; i < 12; ++i) {
        const int r = i * 4 + fq;
        u32x4 v = (u32x4){0u, 0u, 0u, 0u};
        if (i < 2) v = vm[i]; else if (i < 10) v = *(const u32x4*)(V + (size_t)(r - 8) * HD + fr * 8);
        *(LAS u32x4*)(vs + r * XV_PITCH + fr * 16) = v;
    }
    f32x4 sc[3][2];
#pragma unroll
    for (int t = 0; t < 3; ++t) { sc[t][0] = (f32x4){0.f, 0.f, 0.f, 0.f}; sc[t][1] = (f32x4){0.f, 0.f, 0.f, 0.f}; }
#pragma unroll
    for (int i = 0; i < 4; ++i) {
        const bf16x8 q0 = *(const bf16x8*)(Q + (size_t)fr * HD + i * 32 + fq * 8), q1 = *(const bf16x8*)(Q + (size_t)(16 + fr) * HD + i * 32 + fq * 8);
#pragma unroll
        for (int t = 0; t < 3; ++t) {
            int r = t * 16 + fr - 8; r = r < 0 ? 0 : (r > 31 ? 31 : r);
            bf16x8 kf = *(const bf16x8*)(K + (size_t)r * HD + i * 32 + fq * 8);
            if (t == 0 && fr < 8) kf = __builtin_bit_cast(bf16x8, km[i]);
            sc[t][0] = __builtin_amdgcn_mfma_f32_16x16x32_bf16(kf, q0, sc[t][0], 0, 0, 0);
            sc[t][1] = __builtin_amdgcn_mfma_f32_16x16x32_bf16(kf, q1, sc[t][1], 0, 0, 0);
        }
    }
    const float scale = 0.08838834764831845f, NEG = rbf(-1000000000.0f);
    u32x4 p0[2], p1[2];
#pragma unroll
    for (int rt = 0; rt < 2; ++rt) {
        const int row = rt * 16 + fr;
        float v[12]; float mx = -INFINITY;
#pragma unroll
        for (int t = 0; t < 3; ++t)
#pragma unroll
            for (int r = 0; r < 4; ++r) { const int key = t * 16 + fq * 4 + r; float z = rbf(rbf(sc[t][rt][r]) * scale);
                if (key - 8 > row) z = NEG; if (key >= 40) z = -INFINITY; v[t * 4 + r] = z; mx = fmaxf(mx, z); }
        mx = xmax16_32(mx);
        float sum = 0.f;
#pragma unroll
        for (int i = 0; i < 12; ++i) { v[i] = __expf(v[i] - mx); sum += v[i]; }
        sum = xsum16_32(sum);
        const float inv = 1.0f / sum;
        p0[rt].x = pk_bf16(v[0] * inv, v[1] * inv); p0[rt].y = pk_bf16(v[2] * inv, v[3] * inv); p0[rt].z = pk_bf16(v[4] * inv, v[5] * inv); p0[rt].w = pk_bf16(v[6] * inv, v[7] * inv);
        p1[rt].x = pk_bf16(v[8] * inv, v[9] * inv); p1[rt].y = pk_bf16(v[10] * inv, v[11] * inv); p1[rt].z = 0u; p1[rt].w = 0u;
    }
    asm volatile("s_waitcnt lgkmcnt(0)" ::: "memory");
    const __amdgpu_buffer_rsrc_t ro = __builtin_amdgcn_make_buffer_rsrc(ws + WS_O, 0, S_LEN * HD * 2, 0x00020000);
    const int obase = (s * SEG * HD + h * DH) * 2;
    const bf16x4 zz = (bf16x4){0, 0, 0, 0};
    u32x2 ow[8][2];
#pragma unroll
    for (int et = 0; et < 8; ++et) {
        const int voff = (4 * fq + (fr >> 2)) * XV_PITCH + et * 32 + (lane & 3) * 8;
        const bf16x4 v0 = __builtin_amdgcn_ds_read_tr16_b64_v4i16((LAS bf16x4*)(vs + voff));
        const bf16x4 v1 = __builtin_amdgcn_ds_read_tr16_b64_v4i16((LAS bf16x4*)(vs + voff + 16 * XV_PITCH));
        const bf16x4 v2 = __builtin_amdgcn_ds_read_tr16_b64_v4i16((LAS bf16x4*)(vs + voff + 32 * XV_PITCH));
        const bf16x8 va = __builtin_shufflevector(v0, v1, 0, 1, 2, 3, 4, 5, 6, 7), vb = __builtin_shufflevector(v2, zz, 0, 1, 2, 3, 4, 5, 6, 7);
#pragma unroll
        for (int rt = 0; rt < 2; ++rt) {
            f32x4 o = (f32x4){0.f, 0.f, 0.f, 0.f};
            o = __builtin_amdgcn_mfma_f32_16x16x32_bf16(va, __builtin_bit_cast(bf16x8, p0[rt]), o, 0, 0, 0);
            o = __builtin_amdgcn_mfma_f32_16x16x32_bf16(vb, __builtin_bit_cast(bf16x8, p1[rt]), o, 0, 0, 0);
            ow[et][rt].x = pk_bf16(o[0], o[1]); ow[et][rt].y = pk_bf16(o[2], o[3]);
        }
    }
    asm volatile("s_waitcnt lgkmcnt(0)" ::: "memory");
#pragma unroll
    for (int et = 0; et < 8; ++et)
#pragma unroll
        for (int rt = 0; rt < 2; ++rt) *(LAS u32x2*)(vs + (rt * 16 + fr) * XV_PITCH + et * 32 + fq * 8) = ow[et][rt];
    asm volatile("s_waitcnt lgkmcnt(0)" ::: "memory");
#pragma unroll
    for (int i = 0; i < 8; ++i) {
        const int r = i * 4 + fq;
        const u32x4 w = *(const LAS u32x4*)(vs + r * XV_PITCH + fr * 16);
        __builtin_amdgcn_raw_buffer_store_b128(w, ro, obase + (r * HD + fr * 8) * 2, 0, INK ? AUX_SC1 : 0);
    }
    asm volatile("s_waitcnt lgkmcnt(0)" ::: "memory");
}

__device__ __forceinline__ void store_pair_sc1(const __amdgpu_buffer_rsrc_t rs, int off0, u32x2 w0, u32x2 w1, int fq) {
    auto sx = __builtin_amdgcn_permlane16_swap(w0.x, w1.x, false, false); auto sy = __builtin_amdgcn_permlane16_swap(w0.y, w1.y, false, false);
    const u32x4 w = (u32x4){(unsigned)sx[0], (unsigned)sy[0], (unsigned)sx[1], (unsigned)sy[1]};
    __builtin_amdgcn_raw_buffer_store_b128(w, rs, off0 + (fq & 1) * 32 + (fq >> 1) * 16, 0, AUX_SC1);
}
struct SchedOne {
    g8::Unit u; bool valid;
    __device__ __forceinline__ bool next(int i, g8::Unit& o) const { if (i != 0 || !valid) return false; o = u; return true; }
};
struct SchedKVQ {
    const bf16_t* A; const bf16_t* Wq; long dk, dv; int r;
    __device__ __forceinline__ bool next(int i, g8::Unit& u) const {
        const int L = r + 128 * i; if (L >= 352) return false;
        int pm, pn;
        if (L < 224) { pm = 1 + (L >> 5); pn = 16 + (L & 31); } else { pm = (L - 224) & 7; pn = (L - 224) >> 3; }
        const int mat = pn >> 4;
        u.A = A + (size_t)pm * 256 * DM; u.B = Wq + (mat == 1 ? dk : 0L) + (mat == 2 ? dv : 0L) + (pn & 15) * 256; u.pm = pm; u.pn = pn; return true;
    }
};
struct EpiQKV {
    char* ws;
    __device__ __forceinline__ void operator()(const f32x4 (&acc)[2][2][4][2], const g8::Unit& u, int wr, int wc, int fr, int fq) const {
        const __amdgpu_buffer_rsrc_t rq = __builtin_amdgcn_make_buffer_rsrc(ws + WS_Q, 0, (int)(WS_H - WS_Q), 0x00020000);
        const int base = (u.pn >> 4) * (S_LEN * HD * 2) + ((u.pm * 256 + wr * 64 + fr) * HD + (u.pn & 15) * 256 + wc * 32) * 2;
#pragma unroll
        for (int ai = 0; ai < 2; ++ai)
#pragma unroll
            for (int m = 0; m < 4; ++m)
#pragma unroll
                for (int bj = 0; bj < 2; ++bj) { const f32x4 v0 = acc[ai][bj][m][0], v1 = acc[ai][bj][m][1];
                    store_pair_sc1(rq, base + ((ai * 128 + m * 16) * HD + bj * 128) * 2, (u32x2){pk_bf16(v0[0], v0[1]), pk_bf16(v0[2], v0[3])}, (u32x2){pk_bf16(v1[0], v1[1]), pk_bf16(v1[2], v1[3])}, fq); }
        asm volatile("s_waitcnt vmcnt(0)" ::: "memory");
        if ((threadIdx.x & 63) == 0) {
            if (u.pn >= 16) __hip_atomic_fetch_add((unsigned*)(ws + WS_CNT + (size_t)(CNT_KV + u.pm * 32 + u.pn - 16) * 256), 1u, __ATOMIC_RELAXED, __HIP_MEMORY_SCOPE_AGENT);
            __hip_atomic_fetch_add((unsigned*)(ws + WS_CNT + (size_t)(CNT_QKV + u.pm) * 256), 1u, __ATOMIC_RELAXED, __HIP_MEMORY_SCOPE_AGENT);
        }
    }
};
template <int MT>
struct EpiKV0 {
    char* ws; int row0, pn2;
    __device__ __forceinline__ void operator()(const f32x4 (&acc)[MT][2], int wr, int wc, int fr, int fq) const {
        const __amdgpu_buffer_rsrc_t rq = __builtin_amdgcn_make_buffer_rsrc(ws + WS_K, 0, (int)(WS_H - WS_K), 0x00020000);
        const int base = (pn2 >> 5) * (S_LEN * HD * 2) + ((row0 + wr * 16 * MT + fr) * HD + (pn2 & 31) * 128 + wc * 32) * 2;
#pragma unroll
        for (int m = 0; m < MT; ++m) { const f32x4 v0 = acc[m][0], v1 = acc[m][1];
            store_pair_sc1(rq, base + m * 16 * HD * 2, (u32x2){pk_bf16(v0[0], v0[1]), pk_bf16(v0[2], v0[3])}, (u32x2){pk_bf16(v1[0], v1[1]), pk_bf16(v1[2], v1[3])}, fq); }
        asm volatile("s_waitcnt vmcnt(0)" ::: "memory");
        if ((threadIdx.x & 63) == 0) {
            __hip_atomic_fetch_add((unsigned*)(ws + WS_CNT + (size_t)((row0 < 128 ? CNT_KV : CNT_KV0B) + (pn2 >> 1)) * 256), 1u, __ATOMIC_RELAXED, __HIP_MEMORY_SCOPE_AGENT);
            __hip_atomic_fetch_add((unsigned*)(ws + WS_CNT + (size_t)CNT_QKV * 256), 1u, __ATOMIC_RELAXED, __HIP_MEMORY_SCOPE_AGENT);
        }
    }
};
struct EpiMLP1 {
    char* ws; const bf16_t* b1;
    __device__ __forceinline__ void operator()(const f32x4 (&acc)[2][2][4][2], const g8::Unit& u, int wr, int wc, int fr, int fq) const {
        const __amdgpu_buffer_rsrc_t rh = __builtin_amdgcn_make_buffer_rsrc(ws + WS_H, 0, S_LEN * FF * 2, 0x00020000);
        const int row0 = u.pm * 256 + wr * 64 + fr, col0 = u.pn * 256 + wc * 32;
#pragma unroll
        for (int bj = 0; bj < 2; ++bj) {
            const u32x2 ba = *(const u32x2*)(b1 + col0 + bj * 128 + 4 * fq), bb = *(const u32x2*)(b1 + col0 + bj * 128 + 16 + 4 * fq);
#pragma unroll
            for (int ai = 0; ai < 2; ++ai)
#pragma unroll
                for (int m = 0; m < 4; ++m) { const f32x4 v0 = acc[ai][bj][m][0], v1 = acc[ai][bj][m][1];
                    u32x2 w0, w1;
                    w0.x = pk_bf16(gelu_tanh(rbf(rbf(v0[0]) + bf2f(ba.x & 0xffffu))), gelu_tanh(rbf(rbf(v0[1]) + bf2f(ba.x >> 16))));
                    w0.y = pk_bf16(gelu_tanh(rbf(rbf(v0[2]) + bf2f(ba.y & 0xffffu))), gelu_tanh(rbf(rbf(v0[3]) + bf2f(ba.y >> 16))));
                    w1.x = pk_bf16(gelu_tanh(rbf(rbf(v1[0]) + bf2f(bb.x & 0xffffu))), gelu_tanh(rbf(rbf(v1[1]) + bf2f(bb.x >> 16))));
                    w1.y = pk_bf16(gelu_tanh(rbf(rbf(v1[2]) + bf2f(bb.y & 0xffffu))), gelu_tanh(rbf(rbf(v1[3]) + bf2f(bb.y >> 16))));
                    store_pair_sc1(rh, ((row0 + ai * 128 + m * 16) * FF + col0 + bj * 128) * 2, w0, w1, fq);
                    if (m & 1) asm volatile("" ::: "memory"); }
        }
        signal_counter(ws, CNT_H + u.pm);
    }
};
struct EpiMLP2 {
    char* ws; const bf16_t* b2; int row0, col0;
    __device__ __forceinline__ void operator()(const f32x4 (&acc)[4][2], int wr, int wc, int fr, int fq) const {
        const __amdgpu_buffer_rsrc_t rb = __builtin_amdgcn_make_buffer_rsrc(ws + WS_B, 0, S_LEN * DM * 2, 0x00020000);
        const int col = col0 + wc * 32;
        const u32x2 ba = *(const u32x2*)(b2 + col + 4 * fq), bb = *(const u32x2*)(b2 + col + 16 + 4 * fq);
#pragma unroll
        for (int m = 0; m < 4; ++m) { const f32x4 v0 = acc[m][0], v1 = acc[m][1];
            u32x2 w0, w1;
            w0.x = pk_bf16(rbf(v0[0]) + bf2f(ba.x & 0xffffu), rbf(v0[1]) + bf2f(ba.x >> 16)); w0.y = pk_bf16(rbf(v0[2]) + bf2f(ba.y & 0xffffu), rbf(v0[3]) + bf2f(ba.y >> 16));
            w1.x = pk_bf16(rbf(v1[0]) + bf2f(bb.x & 0xffffu), rbf(v1[1]) + bf2f(bb.x >> 16)); w1.y = pk_bf16(rbf(v1[2]) + bf2f(bb.y & 0xffffu), rbf(v1[3]) + bf2f(bb.y >> 16));
            store_pair_sc1(rb, ((row0 + wr * 64 + m * 16 + fr) * DM + col) * 2, w0, w1, fq); }
        signal_counter(ws, CNT_B + (row0 >> 8));
    }
};
__device__ __forceinline__ u32x2 combine4(const f32x4 v, const u32x2 xw, const u32x2 bw) {
    u32x2 w;
    w.x = pk_bf16(rbf(bf2f(xw.x & 0xffffu) + rbf(v[0])) + bf2f(bw.x & 0xffffu), rbf(bf2f(xw.x >> 16) + rbf(v[1])) + bf2f(bw.x >> 16));
    w.y = pk_bf16(rbf(bf2f(xw.y & 0xffffu) + rbf(v[2])) + bf2f(bw.y & 0xffffu), rbf(bf2f(xw.y >> 16) + rbf(v[3])) + bf2f(bw.y >> 16));
    return w;
}
template <int MT>
struct EpiOut {
    const bf16_t* x; const bf16_t* bout; bf16_t* out; int row0, col0;
    __device__ __forceinline__ void operator()(const f32x4 (&acc)[MT][2], int wr, int wc, int fr, int fq) const {
#pragma unroll
        for (int m = 0; m < MT; ++m)
#pragma unroll
            for (int n = 0; n < 2; ++n) {
                const size_t off = (size_t)(row0 + wr * 16 * MT + m * 16 + fr) * DM + col0 + wc * 32 + n * 16 + 4 * fq;
                *(u32x2*)(out + off) = combine4(acc[m][n], *(const u32x2*)(x + off), *(const u32x2*)(bout + off));
            }
    }
};

constexpr int TAIL_BLK0 = 6;
__global__ void __launch_bounds__(512, 2) k_mega(const bf16_t* __restrict__ x, const bf16_t* __restrict__ Wq, const bf16_t* __restrict__ Wk, const bf16_t* __restrict__ Wv,
                                                 const bf16_t* __restrict__ Wo, const bf16_t* __restrict__ w1, const bf16_t* __restrict__ b1, const bf16_t* __restrict__ w2,
                                                 const bf16_t* __restrict__ b2, char* ws, bf16_t* __restrict__ out) {
    extern __shared__ __attribute__((aligned(16))) unsigned char shm_[];
    LAS unsigned char* lds = (LAS unsigned char*)shm_;
    const int b = blockIdx.x;
    const int role = (b >> 3) & 1, r = (b >> 4) * 8 + (b & 7);
    if (role == 0) { chain_body<4>(lds, r, Wq, Wk, Wv, Wo, ws); return; }
    {
        const int row0 = (r >> 6) * 64, pn2 = r & 63;
        EpiKV0<2> E{ws, row0, pn2};
        gemm128_w8<2, EpiKV0<2>>(lds, (const bf16_t*)(ws + WS_XN) + (size_t)row0 * DM, DM, (pn2 >= 32 ? Wv : Wk) + (pn2 & 31) * 128, HD, DM, E);
    }
    if (r < 64) {
        EpiKV0<4> E{ws, 128, r};
        gemm128_w8<4, EpiKV0<4>>(lds, (const bf16_t*)(ws + WS_XN) + (size_t)128 * DM, DM, (r >= 32 ? Wv : Wk) + (r & 31) * 128, HD, DM, E);
    }
    {
        SchedKVQ S{(const bf16_t*)(ws + WS_XN), Wq, (long)(Wk - Wq), (long)(Wv - Wq), r ^ 64};
        EpiQKV E{ws}; g8::gemm_phase<EpiQKV, SchedKVQ>(lds, DM, DM, HD, S, E);
    }
    {
        const int r2 = r ^ 64, L = r2 >= 96 ? r2 - 96 : 32 + r2;
        SchedOne S; S.valid = (r2 >= 96) || (r2 < 80); S.u.pm = L & 7; S.u.pn = L >> 3; S.u.A = (const bf16_t*)(ws + WS_Y) + (size_t)S.u.pm * 256 * DM; S.u.B = w1 + S.u.pn * 256;
        EpiMLP1 E{ws, b1}; g8::gemm_phase<EpiMLP1, SchedOne>(lds, DM, DM, FF, S, E);
    }
    {
        const int pm = r >> 3, pn = r & 7;
        wait_counter(ws, CNT_H + (pm >> 1), 14 * 8);
        EpiMLP2 E{ws, b2, pm * 128, pn * 128};
        gemm128_w8<4, EpiMLP2>(lds, (const bf16_t*)(ws + WS_H) + (size_t)pm * 128 * FF, FF, w2 + pn * 128, DM, FF, E);
    }
    if (r < 80) {
        const int blk = r >> 4, t = r & 15;
        {
            if (threadIdx.x < 64) poll_ge(ws, CNT_STEP, (unsigned)(8 * blk + 8));
            wait_counter(ws, CNT_QKV + blk, (blk == 0 ? 192 * 8 : 32 * 8) + 16 * 8);
            const int wave = threadIdx.x >> 6;
#pragma unroll 1
            for (int i = 0; i < 2; ++i) { const int item = t * 16 + wave * 2 + i; xattn_item<true>(lds + wave * 48 * XV_PITCH, ws, blk * 8 + (item >> 5), item & 31); }
            signal_counter(ws, CNT_O + blk);
        }
        {
            if (threadIdx.x < 64) poll_ge(ws, CNT_B + blk, 16 * 8);
            wait_counter(ws, CNT_O + blk, 16 * 8);
            const int pm = blk * 2 + (t >> 3), pn = t & 7;
            EpiOut<4> E{x, (const bf16_t*)(ws + WS_B), out, pm * 128, pn * 128};
            gemm128_w8<4, EpiOut<4>>(lds, (const bf16_t*)(ws + WS_O) + (size_t)pm * 128 * HD, HD, Wo + pn * 128, DM, HD, E);
        }
    } else if (r < 112) {
        constexpr int blk = 5; const int t = r - 80;
        if (threadIdx.x < 64) poll_ge(ws, CNT_STEP, (unsigned)(8 * blk + 8));
        wait_counter(ws, CNT_QKV + blk, 32 * 8 + 16 * 8);
        const int wave = threadIdx.x >> 6, item = t * 8 + wave;
        xattn_item<true>(lds + wave * 48 * XV_PITCH, ws, blk * 8 + (item >> 5), item & 31);
        signal_counter(ws, CNT_O + blk);
        if (threadIdx.x < 64) poll_ge(ws, CNT_B + blk, 16 * 8);
        wait_counter(ws, CNT_O + blk, 32 * 8);
        const int pm = blk * 4 + (t >> 3), pn = t & 7;
        EpiOut<2> E{x, (const bf16_t*)(ws + WS_B), out, pm * 64, pn * 128};
        gemm128_w8<2, EpiOut<2>>(lds, (const bf16_t*)(ws + WS_O) + (size_t)pm * 64 * HD, HD, Wo + pn * 128, DM, HD, E);
    }
}

__global__ void __launch_bounds__(256) k_tail_attn(char* ws) {
    __shared__ __attribute__((aligned(16))) unsigned char shm_[4 * 48 * XV_PITCH];
    const int wave = threadIdx.x >> 6, item = blockIdx.x * 4 + wave;
    xattn_item<false>((LAS unsigned char*)shm_ + wave * 48 * XV_PITCH, ws, TAIL_BLK0 * 8 + (item >> 5), item & 31);
}
__global__ void __launch_bounds__(256) k_tail_gemm(const bf16_t* __restrict__ x, const bf16_t* __restrict__ Wo, const char* __restrict__ ws, bf16_t* __restrict__ out) {
    extern __shared__ __attribute__((aligned(16))) unsigned char shm_[];
    const int tid = threadIdx.x, wave = __builtin_amdgcn_readfirstlane(tid >> 6), lane = tid & 63, fr = lane & 15, fq = lane >> 4;
    LAS unsigned char* lds = (LAS unsigned char*)shm_ + wave * 36864;
    const int pn = blockIdx.x & 31, pm = blockIdx.x >> 5, row0 = TAIL_BLK0 * 256 + pm * 64, col0 = pn * 32;
    const char* cA = (const char*)((const bf16_t*)(ws + WS_O) + (size_t)row0 * HD + wave * 1024);
    const char* cB = (const char*)(Wo + (size_t)wave * 1024 * DM + col0);
    unsigned voffA[8], voffB[4];
#pragma unroll
    for (int i = 0; i < 8; ++i) { const int c = i * 64 + lane, row = c >> 3, ch = (c & 7) ^ (row & 7); voffA[i] = (unsigned)(row * HD + ch * 8) * 2u; }
#pragma unroll
    for (int i = 0; i < 4; ++i) { const int c = i * 64 + lane, k = c >> 2, cb = c & 3; voffB[i] = (unsigned)(k * DM + cb * 8) * 2u; }
#define STG(buf, t) do { _Pragma("unroll") for (int _i = 0; _i < 8; ++_i) \
        __builtin_amdgcn_global_load_lds((const unsigned*)(cA + (size_t)(t) * 128 + voffA[_i]), (LAS unsigned*)(lds + (buf) * 12288 + _i * 1024), 16, 0, 0); \
        _Pragma("unroll") for (int _i = 0; _i < 4; ++_i) \
        __builtin_amdgcn_global_load_lds((const unsigned*)(cB + (size_t)(t) * 64 * DM * 2 + voffB[_i]), (LAS unsigned*)(lds + (buf) * 12288 + 8192 + _i * 1024), 16, 0, 0); } while (0)
    f32x4 acc[4][2];
#pragma unroll
    for (int m = 0; m < 4; ++m) { acc[m][0] = (f32x4){0.f, 0.f, 0.f, 0.f}; acc[m][1] = (f32x4){0.f, 0.f, 0.f, 0.f}; }
    STG(0, 0); STG(1, 1);
    int cur = 0;
    for (int t = 0; t < 16; ++t) {
        int nx = cur + 2; if (nx >= 3) nx -= 3;
        if (t + 2 < 16) { STG(nx, t + 2); asm volatile("s_waitcnt vmcnt(24)" ::: "memory"); }
        else if (t + 1 < 16) asm volatile("s_waitcnt vmcnt(12)" ::: "memory");
        else asm volatile("s_waitcnt vmcnt(0)" ::: "memory");
        LAS unsigned char* sa = lds + cur * 12288; LAS unsigned char* sb = sa + 8192;
        bf16x8 At[4][2], Bt[2][2];
#pragma unroll
        for (int k = 0; k < 2; ++k) {
#pragma unroll
            for (int m = 0; m < 4; ++m) { const int row = m * 16 + fr; At[m][k] = *(const LAS bf16x8*)(sa + row * 128 + (((k * 4 + fq) ^ (row & 7)) * 16)); }
#pragma unroll
            for (int n = 0; n < 2; ++n) {
                const int off = (k * 32 + 8 * fq + (fr >> 2)) * 64 + n * 32 + (lane & 3) * 8;
                const bf16x4 a0 = __builtin_amdgcn_ds_read_tr16_b64_v4i16((LAS bf16x4*)(sb + off)), a1 = __builtin_amdgcn_ds_read_tr16_b64_v4i16((LAS bf16x4*)(sb + off + 256));
                Bt[n][k] = __builtin_shufflevector(a0, a1, 0, 1, 2, 3, 4, 5, 6, 7); }
        }
#pragma unroll
        for (int k = 0; k < 2; ++k)
#pragma unroll
            for (int m = 0; m < 4; ++m)
#pragma unroll
                for (int n = 0; n < 2; ++n) acc[m][n] = __builtin_amdgcn_mfma_f32_16x16x32_bf16(Bt[n][k], At[m][k], acc[m][n], 0, 0, 0);
        asm volatile("s_waitcnt lgkmcnt(0)" ::: "memory");
        cur = cur + 1 == 3 ? 0 : cur + 1;
    }
#undef STG
    __syncthreads();
    LAS f32x4* red = (LAS f32x4*)shm_;
#pragma unroll
    for (int m = 0; m < 4; ++m)
#pragma unroll
        for (int n = 0; n < 2; ++n) red[((wave * 4 + m) * 2 + n) * 64 + lane] = acc[m][n];
    __syncthreads();
#pragma unroll
    for (int n = 0; n < 2; ++n) {
        const int m = wave;
        const f32x4 v = (red[((0 * 4 + m) * 2 + n) * 64 + lane] + red[((1 * 4 + m) * 2 + n) * 64 + lane]) + (red[((2 * 4 + m) * 2 + n) * 64 + lane] + red[((3 * 4 + m) * 2 + n) * 64 + lane]);
        const size_t off = (size_t)(row0 + m * 16 + fr) * DM + col0 + n * 16 + 4 * fq;
        *(u32x2*)(out + off) = combine4(v, *(const u32x2*)(x + off), *(const u32x2*)((const bf16_t*)(ws + WS_B) + off));
    }
}

extern "C" void kernel_launch(void* const* d_in, const int* in_sizes, int n_in, void* d_out, int out_size, void* d_ws, size_t ws_size, hipStream_t stream) {
    const bf16_t* x = (const bf16_t*)d_in[0];
    const bf16_t* mem0 = (const bf16_t*)d_in[1];
    const bf16_t* Wq = (const bf16_t*)d_in[2];
    const bf16_t* Wk = (const bf16_t*)d_in[3];
    const bf16_t* Wv = (const bf16_t*)d_in[4];
    const bf16_t* Wo = (const bf16_t*)d_in[5];
    const bf16_t* w1 = (const bf16_t*)d_in[8];
    const bf16_t* b1 = (const bf16_t*)d_in[9];
    const bf16_t* w2 = (const bf16_t*)d_in[10];
    const bf16_t* b2 = (const bf16_t*)d_in[11];
    char* ws = (char*)d_ws;
    bf16_t* out = (bf16_t*)d_out;
    (void)hipFuncSetAttribute((const void*)k_mega, hipFuncAttributeMaxDynamicSharedMemorySize, CH_LDS);
    (void)hipFuncSetAttribute((const void*)k_tail_gemm, hipFuncAttributeMaxDynamicSharedMemorySize, 4 * 36864);
    k_prep<<<S_LEN / 4, 256, 0, stream>>>(x, mem0, ws);
    k_mega<<<256, 512, CH_LDS, stream>>>(x, Wq, Wk, Wv, Wo, w1, b1, w2, b2, ws, out);
    k_tail_attn<<<64 * (8 - TAIL_BLK0), 256, 0, stream>>>(ws);
    k_tail_gemm<<<128 * (8 - TAIL_BLK0), 256, 4 * 36864, stream>>>(x, Wo, ws, out);
}
```

```cpp
#include <hip/hip_runtime.h>
#include <hip/hip_bf16.h>

#define LAS __attribute__((address_space(3)))
typedef unsigned short bf16_t;
typedef short bf16x8 __attribute__((ext_vector_type(8)));
typedef short bf16x4 __attribute__((ext_vector_type(4)));
typedef float f32x4 __attribute__((ext_vector_type(4)));
typedef float f32x2 __attribute__((ext_vector_type(2)));
typedef unsigned u32x4 __attribute__((ext_vector_type(4)));
typedef unsigned u32x2 __attribute__((ext_vector_type(2)));

constexpr int S_LEN = 2048, DM = 1024, NH = 32, DH = 128, HD = NH * DH, MM = 8, SEG = 32, NSEG = S_LEN / SEG, FF = 3584;
constexpr int AUX_SC1 = 16;
constexpr unsigned SENT = 0xFFFFFFFFu;

__device__ __forceinline__ float bf2f(unsigned b) { return __builtin_bit_cast(float, b << 16); }
typedef __bf16 bf16x2_t __attribute__((ext_vector_type(2)));
__device__ __forceinline__ unsigned pk_bf16(float lo, float hi) { const f32x2 v = {lo, hi}; return __builtin_bit_cast(unsigned, __builtin_convertvector(v, bf16x2_t)); }
__device__ __forceinline__ float rbf(float x) { return bf2f(pk_bf16(x, x) & 0xffffu); }
__device__ __forceinline__ unsigned desent(unsigned w) { return w == SENT ? 0x7FC07FC0u : w; }
__device__ __forceinline__ unsigned umax2(unsigned a, unsigned b) { return a > b ? a : b; }
__device__ __forceinline__ unsigned umax4(u32x4 v) { return umax2(umax2(v.x, v.y), umax2(v.z, v.w)); }

constexpr size_t WS_XN = 0;
constexpr size_t WS_Y = WS_XN + (size_t)S_LEN * DM * 2;
constexpr size_t WS_Q = WS_Y + (size_t)S_LEN * DM * 2;
constexpr size_t WS_K = WS_Q + (size_t)S_LEN * HD * 2;
constexpr size_t WS_V = WS_K + (size_t)S_LEN * HD * 2;
constexpr size_t WS_H = WS_V + (size_t)S_LEN * HD * 2;
constexpr size_t WS_O = WS_H + (size_t)S_LEN * FF * 2;
constexpr size_t WS_B = WS_O + (size_t)S_LEN * HD * 2;
constexpr size_t WS_SENT0 = WS_B + (size_t)S_LEN * DM * 2;
constexpr size_t MEM_B = (size_t)MM * DM * 2;
constexpr size_t WS_MEM = WS_SENT0;
constexpr size_t QKVM_B = (size_t)NH * 3 * MM * DH * 2;
constexpr size_t WS_QKVM = WS_MEM + (size_t)(NSEG + 1) * MEM_B;
constexpr size_t OM_B = (size_t)MM * HD * 2;
constexpr size_t WS_OM = WS_QKVM + (size_t)NSEG * QKVM_B;
constexpr size_t WS_SENT1 = WS_OM + (size_t)NSEG * OM_B;
constexpr int FILL0 = 16;
constexpr size_t WS_CNT = WS_SENT1;
constexpr int CNT_H = 0;
constexpr int CNT_B = 8;
constexpr int CNT_O = 16;
constexpr int CNT_QKV = 24;
constexpr int CNT_FILL = 33;
constexpr int CNT_STEP = 32;
constexpr int CNT_KV = 40;
constexpr int CNT_KV0B = 40 + 256;
constexpr int CNT_N = 40 + 256 + 32;
constexpr size_t WS_TMO = WS_CNT + (size_t)CNT_N * 256;
constexpr size_t WS_END = WS_TMO + 256;

__device__ __forceinline__ float wave_sum(float v) {
#pragma unroll
    for (int o = 32; o > 0; o >>= 1) v += __shfl_xor(v, o);
    return v;
}
__device__ __forceinline__ void ln_row(float (&v)[16]) {
    float s = 0.f;
#pragma unroll
    for (int i = 0; i < 16; ++i) s += v[i];
    const float mean = wave_sum(s) * (1.0f / DM);
    float q = 0.f;
#pragma unroll
    for (int i = 0; i < 16; ++i) { const float d = v[i] - mean; q += d * d; }
    const float var = rbf(wave_sum(q) * (1.0f / DM));
    const float mb = rbf(mean);
    const float den = rbf(sqrtf(rbf(var + 1e-5f)));
#pragma unroll
    for (int i = 0; i < 16; ++i) v[i] = rbf(rbf(v[i] - mb) / den);
}
__global__ void __launch_bounds__(256) k_prep(const bf16_t* __restrict__ x, const bf16_t* __restrict__ mem0, char* __restrict__ ws) {
    const int wave = threadIdx.x >> 6, lane = threadIdx.x & 63, row = blockIdx.x * 4 + wave;
    const u32x4* src = (const u32x4*)(x + (size_t)row * DM);
    u32x4 a = src[lane], b = src[lane + 64];
    float v[16];
    const unsigned w[8] = {a.x, a.y, a.z, a.w, b.x, b.y, b.z, b.w};
#pragma unroll
    for (int i = 0; i < 8; ++i) { v[2 * i] = bf2f(w[i] & 0xffffu); v[2 * i + 1] = bf2f(w[i] >> 16); }
    ln_row(v);
    u32x4 o0, o1;
    o0.x = pk_bf16(v[0], v[1]); o0.y = pk_bf16(v[2], v[3]); o0.z = pk_bf16(v[4], v[5]); o0.w = pk_bf16(v[6], v[7]);
    o1.x = pk_bf16(v[8], v[9]); o1.y = pk_bf16(v[10], v[11]); o1.z = pk_bf16(v[12], v[13]); o1.w = pk_bf16(v[14], v[15]);
    u32x4* dx = (u32x4*)(ws + WS_XN + (size_t)row * DM * 2);
    dx[lane] = o0; dx[lane + 64] = o1;
    ln_row(v);
    o0.x = pk_bf16(v[0], v[1]); o0.y = pk_bf16(v[2], v[3]); o0.z = pk_bf16(v[4], v[5]); o0.w = pk_bf16(v[6], v[7]);
    o1.x = pk_bf16(v[8], v[9]); o1.y = pk_bf16(v[10], v[11]); o1.z = pk_bf16(v[12], v[13]); o1.w = pk_bf16(v[14], v[15]);
    u32x4* dy = (u32x4*)(ws + WS_Y + (size_t)row * DM * 2);
    dy[lane] = o0; dy[lane + 64] = o1;
    {
        constexpr int nM = FILL0 * (int)MEM_B / 16, nQ = FILL0 * (int)QKVM_B / 16, nO = FILL0 * (int)OM_B / 16;
        const u32x4 f = (u32x4){SENT, SENT, SENT, SENT};
        for (int i = blockIdx.x * 256 + threadIdx.x; i < nM + nQ + nO; i += (S_LEN / 4) * 256) {
            char* dst = i < nM ? ws + WS_MEM + MEM_B + (size_t)i * 16 : (i < nM + nQ ? ws + WS_QKVM + (size_t)(i - nM) * 16 : ws + WS_OM + (size_t)(i - nM - nQ) * 16);
            *(u32x4*)dst = f;
        }
    }
    if (blockIdx.x >= 8 && blockIdx.x < 8 + (CNT_N * 256 / 16 + 255) / 256) {
        const int i = (blockIdx.x - 8) * 256 + threadIdx.x; if (i < CNT_N * 256 / 16) ((u32x4*)(ws + WS_CNT))[i] = (u32x4){0u, 0u, 0u, 0u}; }
    if (blockIdx.x < 4) {
        const int c = blockIdx.x * 256 + threadIdx.x, r = c & 7, kg = c >> 3;
        u32x4 m = *(const u32x4*)(mem0 + (size_t)r * DM + kg * 8);
        m.x = desent(m.x); m.y = desent(m.y); m.z = desent(m.z); m.w = desent(m.w);
        *(u32x4*)(ws + WS_MEM + (size_t)c * 16) = m;
    }
}

namespace g8 {
constexpr int BM = 256, BK = 64, HALF = 128, HTB = HALF * BK * 2, STAGE_BYTES = 8 * HTB;
__host__ __device__ __forceinline__ int lds_byte(int r, int c) { const int st = (r >> 4) * 2 + (c >> 5), rr = r & 15, cc = c & 31, ob = rr * 64 + cc * 2; return st * 1024 + (ob ^ (((ob >> 9) & 1) << 5)); }
__host__ __device__ __forceinline__ void stage_rc(int b, int& R, int& C) { const int st = b / 1024, sb = b % 1024, swz = sb ^ (((sb >> 9) & 1) << 5); R = (st >> 1) * 16 + swz / 64; C = (st & 1) * 32 + (swz % 64) / 2; }
__host__ __device__ __forceinline__ int bsw(int row) { return (row & 3) | (((row >> 3) & 1) << 2); }
struct Unit { const bf16_t* A; const bf16_t* B; int pm, pn; };

template <class Epi, class Sched>
__device__ __forceinline__ void gemm_phase(LAS unsigned char* lds, const int K, const int lda, const int ldb, const Sched& S, const Epi& E) {
    int tid = threadIdx.x; asm volatile("" : "+v"(tid));
    const int wid = __builtin_amdgcn_readfirstlane(tid >> 6), lane = tid & 63, wr = wid >> 2, wc = wid & 3, fr = lane & 15, fq = lane >> 4;
    const int nt = K / BK;
    unsigned voffA[2], voffB[2];
#pragma unroll
    for (int i = 0; i < 2; ++i) { int R, C; stage_rc(tid * 16 + i * 8192, R, C); voffA[i] = (unsigned)(R * lda + C) * 2u;
        const int b = tid * 16 + i * 8192, row = b >> 8, ch = (b & 255) >> 4, seg = (ch >> 1) ^ bsw(row);
        voffB[i] = (unsigned)(row * ldb + seg * 16 + (ch & 1) * 8) * 2u; }
    const size_t kstepA = (size_t)(BK * 2), kstepB = (size_t)BK * ldb * 2;
    const size_t hstepA = (size_t)HALF * lda * 2, hstepB = (size_t)HALF * 2;
    const unsigned ldsw = (unsigned)wid * 1024u;
    const int aoff = lds_byte(wr * 64 + fr, fq * 8);
    const int q4 = (lane & 15) >> 2, p4 = lane & 3, sB = q4 | ((fq & 1) << 2);
    const int boff0 = (8 * fq + q4) * 256 + (((wc * 2 + 0) ^ sB) * 32) + 8 * p4;
    const int boff1 = (8 * fq + q4) * 256 + (((wc * 2 + 1) ^ sB) * 32) + 8 * p4;
#define G8_SA(b, h) (((b) * 2 + (h)) * HTB)
#define G8_SB(b, h) ((4 + (b) * 2 + (h)) * HTB)
#define G8_STAGE(bufoff, gbase, voff) do { _Pragma("unroll") for (int _i = 0; _i < 2; ++_i) \
        __builtin_amdgcn_global_load_lds((const unsigned*)((const char*)(gbase) + (voff)[_i]), (LAS unsigned*)(lds + (bufoff) + ldsw + _i * 8192), 16, 0, 0); } while (0)
#define G8_LDA(dst, b, h) do { _Pragma("unroll") for (int m = 0; m < 4; ++m) _Pragma("unroll") for (int k = 0; k < 2; ++k) dst[m][k] = *(const LAS bf16x8*)(lds + G8_SA(b, h) + aoff + m * 2048 + k * 1024); } while (0)
#define G8_TR(off) __builtin_amdgcn_ds_read_tr16_b64_v4i16((LAS bf16x4*)(lds + (off)))
#define G8_LDB(dst, b, h) do { _Pragma("unroll") for (int k = 0; k < 2; ++k) { \
        bf16x4 a0 = G8_TR(G8_SB(b, h) + boff0 + k * 8192), a1 = G8_TR(G8_SB(b, h) + boff0 + k * 8192 + 1024); \
        bf16x4 c0 = G8_TR(G8_SB(b, h) + boff1 + k * 8192), c1 = G8_TR(G8_SB(b, h) + boff1 + k * 8192 + 1024); \
        dst[0][k] = __builtin_shufflevector(a0, a1, 0, 1, 2, 3, 4, 5, 6, 7); dst[1][k] = __builtin_shufflevector(c0, c1, 0, 1, 2, 3, 4, 5, 6, 7); } } while (0)
#define G8_MMA(ai, bj, At, Bt) do { __builtin_amdgcn_s_setprio(1); _Pragma("unroll") for (int m = 0; m < 4; ++m) _Pragma("unroll") for (int n = 0; n < 2; ++n) _Pragma("unroll") for (int k = 0; k < 2; ++k) \
        acc[ai][bj][m][n] = __builtin_amdgcn_mfma_f32_16x16x32_bf16(Bt[n][k], At[m][k], acc[ai][bj][m][n], 0, 0, 0); __builtin_amdgcn_s_setprio(0); } while (0)
#define G8_WAIT_V(n) asm volatile("s_waitcnt vmcnt(" #n ")" ::: "memory")
#define G8_WAIT_L(n) asm volatile("s_waitcnt lgkmcnt(" #n ")" ::: "memory")
#define G8_BAR __builtin_amdgcn_s_barrier()
#define G8_SCHED __builtin_amdgcn_sched_barrier(0)
    Unit cur, nxt; int ui = 0;
    if (!S.next(0, cur)) return;
    f32x4 acc[2][2][4][2];
#pragma unroll
    for (int a = 0; a < 2; ++a)
#pragma unroll
        for (int b = 0; b < 2; ++b)
#pragma unroll
            for (int m = 0; m < 4; ++m)
#pragma unroll
                for (int n = 0; n < 2; ++n) acc[a][b][m][n] = (f32x4){0.f, 0.f, 0.f, 0.f};
    bf16x8 At[4][2], B0[2][2], B1[2][2];
    const char* cA = (const char*)cur.A; const char* cB = (const char*)cur.B;
    G8_STAGE(G8_SB(0, 0), cB, voffB); G8_STAGE(G8_SA(0, 0), cA, voffA); G8_STAGE(G8_SB(0, 1), cB + hstepB, voffB); G8_STAGE(G8_SA(0, 1), cA + hstepA, voffA);
    if (wr == 1) G8_BAR;
    G8_WAIT_V(4); G8_BAR;
    G8_STAGE(G8_SB(1, 0), cB + kstepB, voffB); G8_STAGE(G8_SA(1, 0), cA + kstepA, voffA); G8_STAGE(G8_SB(1, 1), cB + hstepB + kstepB, voffB);
    G8_WAIT_V(6); G8_BAR;
    for (;;) {
        const bool has_next = S.next(ui + 1, nxt);
        const char* nA = has_next ? (const char*)nxt.A : cA; const char* nB = has_next ? (const char*)nxt.B : cB;
        for (int t = 0; t < nt; t += 2) {
            const bool last = (t == nt - 2);
            const char* a1 = cA + (size_t)(t + 1) * kstepA;
            const char* a2 = last ? nA : cA + (size_t)(t + 2) * kstepA; const char* b2 = last ? nB : cB + (size_t)(t + 2) * kstepB;
            const char* a3 = a2 + kstepA; const char* b3 = b2 + kstepB;
            G8_LDB(B0, 0, 0); G8_SCHED; G8_LDA(At, 0, 0); G8_STAGE(G8_SA(1, 1), a1 + hstepA, voffA);
            G8_WAIT_L(8); G8_BAR; G8_WAIT_L(0); G8_MMA(0, 0, At, B0); G8_BAR; G8_SCHED;
            G8_LDB(B1, 0, 1); G8_STAGE(G8_SB(0, 0), b2, voffB);
            G8_BAR; G8_WAIT_L(0); G8_MMA(0, 1, At, B1); G8_BAR;
            G8_LDA(At, 0, 1); G8_STAGE(G8_SA(0, 0), a2, voffA);
            G8_BAR; G8_WAIT_L(0); G8_MMA(1, 0, At, B0); G8_BAR; G8_SCHED;
            G8_STAGE(G8_SB(0, 1), b2 + hstepB, voffB);
            G8_WAIT_V(6); G8_BAR; G8_MMA(1, 1, At, B1); G8_BAR;
            G8_LDB(B0, 1, 0); G8_SCHED; G8_LDA(At, 1, 0); G8_STAGE(G8_SA(0, 1), a2 + hstepA, voffA);
            G8_WAIT_L(8); G8_BAR; G8_WAIT_L(0); G8_MMA(0, 0, At, B0); G8_BAR; G8_SCHED;
            G8_LDB(B1, 1, 1); G8_STAGE(G8_SB(1, 0), b3, voffB);
            G8_BAR; G8_WAIT_L(0); G8_MMA(0, 1, At, B1); G8_BAR;
            G8_LDA(At, 1, 1); G8_STAGE(G8_SA(1, 0), a3, voffA);
            G8_BAR; G8_WAIT_L(0); G8_MMA(1, 0, At, B0); G8_BAR; G8_SCHED;
            G8_STAGE(G8_SB(1, 1), b3 + hstepB, voffB);
            G8_WAIT_V(6); G8_BAR; G8_MMA(1, 1, At, B1); G8_BAR;
        }
        E(acc, cur, wr, wc, fr, fq);
        if (!has_next) break;
#pragma unroll
        for (int a = 0; a < 2; ++a)
#pragma unroll
            for (int b = 0; b < 2; ++b)
#pragma unroll
                for (int m = 0; m < 4; ++m)
#pragma unroll
                    for (int n = 0; n < 2; ++n) acc[a][b][m][n] = (f32x4){0.f, 0.f, 0.f, 0.f};
        cur = nxt; cA = nA; cB = nB; ++ui;
    }
    G8_WAIT_V(0);
    if (wr == 0) G8_BAR;
    G8_BAR;
#undef G8_SA
#undef G8_SB
#undef G8_STAGE
#undef G8_LDA
#undef G8_LDB
#undef G8_TR
#undef G8_MMA
#undef G8_WAIT_V
#undef G8_WAIT_L
#undef G8_BAR
#undef G8_SCHED
}
}

__device__ __forceinline__ float gelu_tanh(float x) {
    const float u = 0.7978845608028654f * (x + 0.044715f * x * x * x);
    const float th = 1.0f - 2.0f * __builtin_amdgcn_rcpf(__expf(2.0f * u) + 1.0f);
    return 0.5f * x * (1.0f + th);
}

__device__ __forceinline__ void wait_counter(const char* ws, int idx, unsigned need) {
    if (threadIdx.x < 64) {
        const unsigned* p = (const unsigned*)(ws + WS_CNT + (size_t)idx * 256);
        for (unsigned spins = 0; (unsigned)__builtin_amdgcn_readfirstlane(__hip_atomic_load(p, __ATOMIC_RELAXED, __HIP_MEMORY_SCOPE_AGENT)) < need;) {
            __builtin_amdgcn_s_sleep(8);
            if (++spins > 400000u) break;
        }
        __builtin_amdgcn_fence(__ATOMIC_ACQUIRE, "agent");
        asm volatile("s_waitcnt vmcnt(0)" ::: "memory");
    }
    asm volatile("" ::: "memory"); __builtin_amdgcn_s_barrier(); asm volatile("" ::: "memory");
}
__device__ __forceinline__ void signal_counter(char* ws, int idx) {
    asm volatile("s_waitcnt vmcnt(0)" ::: "memory");
    if ((threadIdx.x & 63) == 0) __hip_atomic_fetch_add((unsigned*)(ws + WS_CNT + (size_t)idx * 256), 1u, __ATOMIC_RELAXED, __HIP_MEMORY_SCOPE_AGENT);
}

__device__ __forceinline__ unsigned poll_ge(const char* ws, int idx, unsigned need) {
    const unsigned* p = (const unsigned*)(ws + WS_CNT + (size_t)idx * 256);
    unsigned v = 0;
    for (unsigned spins = 0; (v = (unsigned)__builtin_amdgcn_readfirstlane(__hip_atomic_load(p, __ATOMIC_RELAXED, __HIP_MEMORY_SCOPE_AGENT))) < need;) {
        __builtin_amdgcn_s_sleep(8);
        if (++spins > 400000u) break;
    }
    asm volatile("" ::: "memory");
    return v;
}

constexpr unsigned SPIN_CAP = 200000u;
constexpr int KS_PITCH = 272;
constexpr int CH_LDS = 160 * 1024;

__device__ __forceinline__ float xmax16_32(float v) {
    auto a = __builtin_amdgcn_permlane16_swap(__builtin_bit_cast(unsigned, v), __builtin_bit_cast(unsigned, v), false, false);
    v = fmaxf(__builtin_bit_cast(float, (unsigned)a[0]), __builtin_bit_cast(float, (unsigned)a[1]));
    auto b = __builtin_amdgcn_permlane32_swap(__builtin_bit_cast(unsigned, v), __builtin_bit_cast(unsigned, v), false, false);
    return fmaxf(__builtin_bit_cast(float, (unsigned)b[0]), __builtin_bit_cast(float, (unsigned)b[1]));
}
__device__ __forceinline__ float xsum16_32(float v) {
    auto a = __builtin_amdgcn_permlane16_swap(__builtin_bit_cast(unsigned, v), __builtin_bit_cast(unsigned, v), false, false);
    v = __builtin_bit_cast(float, (unsigned)a[0]) + __builtin_bit_cast(float, (unsigned)a[1]);
    auto b = __builtin_amdgcn_permlane32_swap(__builtin_bit_cast(unsigned, v), __builtin_bit_cast(unsigned, v), false, false);
    return __builtin_bit_cast(float, (unsigned)b[0]) + __builtin_bit_cast(float, (unsigned)b[1]);
}

__device__ __forceinline__ u32x4 quad_gather(unsigned v) {
    u32x4 r;
    r.x = (unsigned)__builtin_amdgcn_update_dpp(0, (int)v, 0x00, 0xf, 0xf, true); r.y = (unsigned)__builtin_amdgcn_update_dpp(0, (int)v, 0x55, 0xf, 0xf, true);
    r.z = (unsigned)__builtin_amdgcn_update_dpp(0, (int)v, 0xAA, 0xf, 0xf, true); r.w = (unsigned)__builtin_amdgcn_update_dpp(0, (int)v, 0xFF, 0xf, 0xf, true);
    return r;
}
#define CH_DELAY1 12
#define CH_DELAY2 6
#define CH_DELAY3 8

template <int CPH>
__device__ __forceinline__ void chain_body(LAS unsigned char* lds, const int b, const bf16_t* __restrict__ Wq, const bf16_t* __restrict__ Wk, const bf16_t* __restrict__ Wv,
                                           const bf16_t* __restrict__ Wo, char* ws) {
    constexpr int ES = 128 / CPH, NE = ES / 16, NT = 3 * NE, RG = CPH / 2, RPG = 8 / RG;
    constexpr int VP = ES * 2;
    constexpr int L_RED = 0;
    constexpr int L_RED2 = L_RED + 8 * NT * 512;
    constexpr int L_KS = L_RED2 + 8 * RPG * 64;
    constexpr int L_VS = L_KS + 2 * 32 * KS_PITCH;
    constexpr int L_OS = L_VS + 2 * 48 * VP;
    constexpr bool WOL = (CPH == 4);
    constexpr int L_WO = L_OS + 8 * 1024 * RPG;
    static_assert(L_WO + (WOL ? 65536 : 0) <= 156 * 1024, "LDS");
    const int tid = threadIdx.x, wave = __builtin_amdgcn_readfirstlane(tid >> 6), lane = tid & 63, fr = lane & 15, fq = lane >> 4;
    const int xc = b & 7, yc = b >> 3;
    const int h = xc + 8 * (yc / CPH), j = yc % CPH, g = yc % RG, c = xc * 8 + yc / RG;
    const __amdgpu_buffer_rsrc_t rs = __builtin_amdgcn_make_buffer_rsrc(ws + WS_SENT0, 0, (int)(WS_SENT1 - WS_SENT0), 0x00020000);
    constexpr int O_MEM = (int)(WS_MEM - WS_SENT0), O_QKVM = (int)(WS_QKVM - WS_SENT0), O_OM = (int)(WS_OM - WS_SENT0);
    const __amdgpu_buffer_rsrc_t rkv = __builtin_amdgcn_make_buffer_rsrc(ws + WS_K, 0, (int)(WS_H - WS_K), 0x00020000);
    constexpr int O_V = (int)(WS_V - WS_K);
    bool dead = false;

    bf16x8 wq[NT][4], wo[16];
    {
        LAS unsigned char* stg = lds + wave * 8192;
        constexpr int CPR = ES / 8;
        static_assert(128 * CPR / 64 == 8, "8 chunks per lane and pass");
        u32x4 t[5][8];
        auto issue = [&](int ps) {
            if (ps < 3) { const bf16_t* W = (ps == 0 ? Wq : (ps == 1 ? Wk : Wv)) + (size_t)wave * 128 * HD + h * DH + j * ES;
#pragma unroll
                for (int i = 0; i < 8; ++i) { const int ci = i * 64 + lane; t[ps][i] = *(const u32x4*)(W + (size_t)(ci / CPR) * HD + (ci % CPR) * 8); } }
            else { const bf16_t* W = Wo + (size_t)(wave * 512 + (ps - 3) * 256) * DM + c * 16;
#pragma unroll
                for (int i = 0; i < 8; ++i) { const int ci = i * 64 + lane; t[ps][i] = *(const u32x4*)(W + (size_t)(ci >> 1) * DM + (ci & 1) * 8); } }
        };
        issue(0); issue(1); issue(2);
        asm volatile("" ::: "memory");
#pragma unroll
        for (int ps = 0; ps < 5; ++ps) {
#pragma unroll
            for (int i = 0; i < 8; ++i) *(LAS u32x4*)(stg + (i * 64 + lane) * 16) = t[ps][i];
            asm volatile("s_waitcnt lgkmcnt(0)" ::: "memory");
            if (ps + 3 < 5) issue(ps + 3);
            asm volatile("" ::: "memory");
            if (ps < 3) {
#pragma unroll
                for (int e = 0; e < NE; ++e)
#pragma unroll
                    for (int i = 0; i < 4; ++i) {
                        const int off = (i * 32 + 8 * fq + (fr >> 2)) * (ES * 2) + e * 32 + (lane & 3) * 8;
                        const bf16x4 a0 = __builtin_amdgcn_ds_read_tr16_b64_v4i16((LAS bf16x4*)(stg + off)), a1 = __builtin_amdgcn_ds_read_tr16_b64_v4i16((LAS bf16x4*)(stg + off + 4 * ES * 2));
                        wq[ps * NE + e][i] = __builtin_shufflevector(a0, a1, 0, 1, 2, 3, 4, 5, 6, 7);
                    }
            } else {
#pragma unroll
                for (int i = 0; i < 8; ++i) {
                    const int off = (i * 32 + 8 * fq + (fr >> 2)) * 32 + (lane & 3) * 8;
                    const bf16x4 a0 = __builtin_amdgcn_ds_read_tr16_b64_v4i16((LAS bf16x4*)(stg + off)), a1 = __builtin_amdgcn_ds_read_tr16_b64_v4i16((LAS bf16x4*)(stg + off + 128));
                    wo[(ps - 3) * 8 + i] = __builtin_shufflevector(a0, a1, 0, 1, 2, 3, 4, 5, 6, 7);
                }
            }
            asm volatile("s_waitcnt lgkmcnt(0)" ::: "memory");
        }
        if (WOL) {
#pragma unroll
            for (int i = 8; i < 16; ++i) *(LAS bf16x8*)(lds + L_WO + (wave * 8 + i - 8) * 1024 + lane * 16) = wo[i];
        }
    }
    __syncthreads();

    {
        constexpr int nM = (NSEG - FILL0) * (int)MEM_B / 16, nQ = (NSEG - FILL0) * (int)QKVM_B / 16, nO = (NSEG - FILL0) * (int)OM_B / 16;
        const u32x4 f = (u32x4){SENT, SENT, SENT, SENT};
        for (int i = b * 512 + tid; i < nM + nQ + nO; i += 32 * CPH * 512) {
            const int off = i < nM ? O_MEM + (FILL0 + 1) * (int)MEM_B + i * 16 : (i < nM + nQ ? O_QKVM + FILL0 * (int)QKVM_B + (i - nM) * 16 : O_OM + FILL0 * (int)OM_B + (i - nM - nQ) * 16);
            __builtin_amdgcn_raw_buffer_store_b128(f, rs, off, 0, AUX_SC1);
        }
        signal_counter(ws, CNT_FILL);
    }
    if (tid == 0) *(LAS unsigned*)(lds + L_OS) = 0u;
    if (tid < 2 * 8 * VP / 16) { const int bf = tid / (8 * VP / 16), r = tid % (8 * VP / 16); *(LAS u32x4*)(lds + L_VS + bf * 48 * VP + 40 * VP + r * 16) = (u32x4){0u, 0u, 0u, 0u}; }
    {
        poll_ge(ws, CNT_KV + (h >> 1), 32u); poll_ge(ws, CNT_KV + 16 + (h >> 1), 32u);
        const int r = tid >> 4, ch = tid & 15;
        *(LAS u32x4*)(lds + L_KS + r * KS_PITCH + ch * 16) = __builtin_amdgcn_raw_buffer_load_b128(rkv, (r * HD + h * DH + ch * 8) * 2, 0, AUX_SC1);
        if (tid < 32 * ES / 8) { const int r2 = tid / (ES / 8), c2 = tid % (ES / 8);
            *(LAS u32x4*)(lds + L_VS + (8 + r2) * VP + c2 * 16) = __builtin_amdgcn_raw_buffer_load_b128(rkv, O_V + (r2 * HD + h * DH + j * ES + c2 * 8) * 2, 0, AUX_SC1); }
    }
    __syncthreads();

    for (int s = 0; s < NSEG; ++s) {
        const int kb = s & 1;
        {
            if (s > 0) __builtin_amdgcn_s_sleep(CH_DELAY1);
            u32x4 a[4];
            const int base = O_MEM + s * (int)MEM_B + wave * 2048 + fq * 128 + (lane & 7) * 16;
            for (unsigned spins = 0;;) {
                asm volatile("" ::: "memory");
#pragma unroll
                for (int i = 0; i < 4; ++i) a[i] = __builtin_amdgcn_raw_buffer_load_b128(rs, base + i * 512, 0, AUX_SC1);
                unsigned mxs = 0;
#pragma unroll
                for (int i = 0; i < 4; ++i) mxs = umax2(mxs, umax4(a[i]));
                if (!__any(mxs == SENT) || dead) break;
                if (++spins > SPIN_CAP) { dead = true; if (lane == 0) *(volatile unsigned*)(ws + WS_TMO) = 1u; break; }
            }
            if (b == 0 && tid == 448) __hip_atomic_store((unsigned*)(ws + WS_CNT + (size_t)CNT_STEP * 256), (unsigned)s, __ATOMIC_RELAXED, __HIP_MEMORY_SCOPE_AGENT);
            constexpr int NQK = 2 * NE;
            {
                f32x4 acc[NQK];
#pragma unroll
                for (int t = 0; t < NQK; ++t) acc[t] = (f32x4){0.f, 0.f, 0.f, 0.f};
#pragma unroll
                for (int i = 0; i < 4; ++i)
#pragma unroll
                    for (int t = 0; t < NQK; ++t) acc[t] = __builtin_amdgcn_mfma_f32_16x16x32_bf16(wq[t][i], __builtin_bit_cast(bf16x8, a[i]), acc[t], 0, 0, 0);
                if (fr < 8) {
#pragma unroll
                    for (int t = 0; t < NQK; ++t) *(LAS f32x4*)(lds + L_RED + ((wave * NT + t) * 128 + fr * 16 + fq * 4) * 4) = acc[t];
                }
            }
            __syncthreads();
            if (tid < NQK * 64) {
                const int t = tid >> 6, m = (tid & 63) >> 3, np = (tid & 7) * 2, mat = t / NE, col = (t % NE) * 16 + np;
                f32x2 sum = (f32x2){0.f, 0.f};
#pragma unroll
                for (int w = 0; w < 8; ++w) sum += *(const LAS f32x2*)(lds + L_RED + ((w * NT + t) * 128 + m * 16 + np) * 4);
                const u32x4 q4 = quad_gather(desent(pk_bf16(sum.x, sum.y)));
                if ((tid & 3) == 0) __builtin_amdgcn_raw_buffer_store_b128(q4, rs, O_QKVM + s * (int)QKVM_B + ((h * 3 + mat) * 8 + m) * 256 + (j * ES + col) * 2, 0, AUX_SC1);
            }
            {
                f32x4 acc[NE];
#pragma unroll
                for (int t = 0; t < NE; ++t) acc[t] = (f32x4){0.f, 0.f, 0.f, 0.f};
#pragma unroll
                for (int i = 0; i < 4; ++i)
#pragma unroll
                    for (int t = 0; t < NE; ++t) acc[t] = __builtin_amdgcn_mfma_f32_16x16x32_bf16(wq[NQK + t][i], __builtin_bit_cast(bf16x8, a[i]), acc[t], 0, 0, 0);
                if (fr < 8) {
#pragma unroll
                    for (int t = 0; t < NE; ++t) *(LAS f32x4*)(lds + L_RED + ((wave * NT + NQK + t) * 128 + fr * 16 + fq * 4) * 4) = acc[t];
                }
            }
        }
        __syncthreads();
        if (tid >= 2 * NE * 64 && tid < NT * 64) {
            const int t = tid >> 6, m = (tid & 63) >> 3, np = (tid & 7) * 2, col = (t % NE) * 16 + np;
            f32x2 sum = (f32x2){0.f, 0.f};
#pragma unroll
            for (int w = 0; w < 8; ++w) sum += *(const LAS f32x2*)(lds + L_RED + ((w * NT + t) * 128 + m * 16 + np) * 4);
            const unsigned pk = desent(pk_bf16(sum.x, sum.y));
            const u32x4 q4 = quad_gather(pk);
            if ((tid & 3) == 0) __builtin_amdgcn_raw_buffer_store_b128(q4, rs, O_QKVM + s * (int)QKVM_B + ((h * 3 + 2) * 8 + m) * 256 + (j * ES + col) * 2, 0, AUX_SC1);
            *(LAS unsigned*)(lds + L_VS + kb * 48 * VP + m * VP + col * 2) = pk;
            asm volatile("s_waitcnt lgkmcnt(0)" ::: "memory");
            if (lane == 0) __hip_atomic_fetch_add((LAS unsigned*)(lds + L_OS), 1u, __ATOMIC_RELAXED, __HIP_MEMORY_SCOPE_WORKGROUP);
        }
        if (s == NSEG - 1) break;
        if (wave == 0) {
            bf16x8 kx[3][4];
#pragma unroll
            for (int t = 0; t < 3; ++t) {
                int r = t * 16 + fr - 8; r = r < 0 ? 0 : (r > 31 ? 31 : r);
#pragma unroll
                for (int i = 0; i < 4; ++i) kx[t][i] = *(const LAS bf16x8*)(lds + L_KS + kb * 32 * KS_PITCH + r * KS_PITCH + i * 64 + fq * 16);
            }
            __builtin_amdgcn_s_sleep(CH_DELAY2);
            u32x4 qf[4], km[4];
            const int base = O_QKVM + s * (int)QKVM_B + h * 3 * 2048 + (lane & 7) * 256 + fq * 16;
            for (unsigned spins = 0;;) {
                asm volatile("" ::: "memory");
#pragma unroll
                for (int i = 0; i < 4; ++i) { qf[i] = __builtin_amdgcn_raw_buffer_load_b128(rs, base + i * 64, 0, AUX_SC1); km[i] = __builtin_amdgcn_raw_buffer_load_b128(rs, base + 2048 + i * 64, 0, AUX_SC1); }
                unsigned mxs = 0;
#pragma unroll
                for (int i = 0; i < 4; ++i) mxs = umax2(mxs, umax2(umax4(qf[i]), umax4(km[i])));
                if (!__any(mxs == SENT) || dead) break;
                if (++spins > SPIN_CAP) { dead = true; if (lane == 0) *(volatile unsigned*)(ws + WS_TMO) = 2u; break; }
            }
            f32x4 sc[3];
#pragma unroll
            for (int t = 0; t < 3; ++t) sc[t] = (f32x4){0.f, 0.f, 0.f, 0.f};
#pragma unroll
            for (int i = 0; i < 4; ++i) {
                const bf16x8 k0 = fr < 8 ? __builtin_bit_cast(bf16x8, km[i]) : kx[0][i];
                sc[1] = __builtin_amdgcn_mfma_f32_16x16x32_bf16(kx[1][i], __builtin_bit_cast(bf16x8, qf[i]), sc[1], 0, 0, 0);
                sc[2] = __builtin_amdgcn_mfma_f32_16x16x32_bf16(kx[2][i], __builtin_bit_cast(bf16x8, qf[i]), sc[2], 0, 0, 0);
                sc[0] = __builtin_amdgcn_mfma_f32_16x16x32_bf16(k0, __builtin_bit_cast(bf16x8, qf[i]), sc[0], 0, 0, 0);
            }
            for (unsigned sp = 0; __hip_atomic_load((LAS unsigned*)(lds + L_OS), __ATOMIC_RELAXED, __HIP_MEMORY_SCOPE_WORKGROUP) < (unsigned)(NE * (s + 1)) && sp < SPIN_CAP; ++sp) {}
            asm volatile("" ::: "memory");
            bf16x8 vf[NE][2];
            const bf16x4 zz = (bf16x4){0, 0, 0, 0};
#pragma unroll
            for (int et = 0; et < NE; ++et) {
                const int voff = L_VS + kb * 48 * VP + (4 * fq + (fr >> 2)) * VP + et * 32 + (lane & 3) * 8;
                const bf16x4 v0 = __builtin_amdgcn_ds_read_tr16_b64_v4i16((LAS bf16x4*)(lds + voff));
                const bf16x4 v1 = __builtin_amdgcn_ds_read_tr16_b64_v4i16((LAS bf16x4*)(lds + voff + 16 * VP));
                const bf16x4 v2 = __builtin_amdgcn_ds_read_tr16_b64_v4i16((LAS bf16x4*)(lds + voff + 32 * VP));
                vf[et][0] = __builtin_shufflevector(v0, v1, 0, 1, 2, 3, 4, 5, 6, 7); vf[et][1] = __builtin_shufflevector(v2, zz, 0, 1, 2, 3, 4, 5, 6, 7);
            }
            const float scale = 0.08838834764831845f;
            const bool hi = fr >= 8;
            float v[6];
            v[0] = hi ? sc[1][2] : sc[0][0]; v[1] = hi ? sc[1][3] : sc[0][1]; v[2] = hi ? sc[2][0] : sc[0][2];
            v[3] = hi ? sc[2][1] : sc[0][3]; v[4] = hi ? sc[2][2] : sc[1][0]; v[5] = hi ? sc[2][3] : sc[1][1];
            float mx = -INFINITY;
#pragma unroll
            for (int i = 0; i < 6; ++i) { float z = rbf(rbf(v[i]) * scale); if (i >= 2 && hi && fq >= 2) z = -INFINITY; v[i] = z; mx = fmaxf(mx, z); }
            mx = fmaxf(mx, __builtin_bit_cast(float, __builtin_amdgcn_update_dpp(0, __builtin_bit_cast(int, mx), 0x128, 0xf, 0xf, true)));
            mx = xmax16_32(mx);
            float sum = 0.f;
#pragma unroll
            for (int i = 0; i < 6; ++i) { v[i] = __expf(v[i] - mx); sum += v[i]; }
            sum += __builtin_bit_cast(float, __builtin_amdgcn_update_dpp(0, __builtin_bit_cast(int, sum), 0x128, 0xf, 0xf, true));
            sum = xsum16_32(sum);
            float inv = __builtin_amdgcn_rcpf(sum); inv = inv * (2.0f - sum * inv);
            const unsigned w0 = pk_bf16(v[0] * inv, v[1] * inv), w1 = pk_bf16(v[2] * inv, v[3] * inv), w2 = pk_bf16(v[4] * inv, v[5] * inv);
            u32x4 p0, p1;
            p0.x = w0; p0.y = w1; p0.z = w2; p0.w = (unsigned)__builtin_amdgcn_update_dpp(0, (int)w0, 0x128, 0xf, 0xf, true);
            p1.x = (unsigned)__builtin_amdgcn_update_dpp(0, (int)w1, 0x128, 0xf, 0xf, true); p1.y = (unsigned)__builtin_amdgcn_update_dpp(0, (int)w2, 0x128, 0xf, 0xf, true); p1.z = 0u; p1.w = 0u;
#pragma unroll
            for (int et = 0; et < NE; ++et) {
                f32x4 o = (f32x4){0.f, 0.f, 0.f, 0.f};
                o = __builtin_amdgcn_mfma_f32_16x16x32_bf16(vf[et][0], __builtin_bit_cast(bf16x8, p0), o, 0, 0, 0);
                o = __builtin_amdgcn_mfma_f32_16x16x32_bf16(vf[et][1], __builtin_bit_cast(bf16x8, p1), o, 0, 0, 0);
                {
                    const unsigned wx = desent(pk_bf16(o[0], o[1])), wy = desent(pk_bf16(o[2], o[3]));
                    auto sx = __builtin_amdgcn_permlane16_swap(wx, wx, false, false); auto sy = __builtin_amdgcn_permlane16_swap(wy, wy, false, false);
                    const u32x4 w = (u32x4){(unsigned)sx[0], (unsigned)sy[0], (unsigned)sx[1], (unsigned)sy[1]};
                    const int k = h * DH + j * ES + et * 16 + 4 * fq;
                    const int off = O_OM + s * (int)OM_B + (fr / RPG) * (8192 * RPG) + (k >> 5) * (64 * RPG) + ((k >> 3) & 3) * (16 * RPG) + (fr % RPG) * 16;
                    if (fr < 8 && (fq & 1) == 0) __builtin_amdgcn_raw_buffer_store_b128(w, rs, off, 0, AUX_SC1);
                }
            }
        } else {
            const int t2 = tid - 64; const int nb = kb ^ 1; const int row0 = (s + 1) * SEG;
            if (((s + 1) & 7) == 0) { const int blk = (s + 1) >> 3; poll_ge(ws, CNT_KV + blk * 32 + (h >> 1), 8u); poll_ge(ws, CNT_KV + blk * 32 + 16 + (h >> 1), 8u); }
            if (s == FILL0 - 3) poll_ge(ws, CNT_FILL, 32u * CPH * 8u);
            if (s + 1 == 4) { poll_ge(ws, CNT_KV0B + (h >> 1), 16u); poll_ge(ws, CNT_KV0B + 16 + (h >> 1), 16u); }
            for (int i = t2; i < 512 + 32 * ES / 8; i += 448) {
                if (i < 512) { const int r = i >> 4, ch = i & 15;
                    *(LAS u32x4*)(lds + L_KS + nb * 32 * KS_PITCH + r * KS_PITCH + ch * 16) = __builtin_amdgcn_raw_buffer_load_b128(rkv, ((row0 + r) * HD + h * DH + ch * 8) * 2, 0, AUX_SC1); }
                else { const int r2 = (i - 512) / (ES / 8), c2 = (i - 512) % (ES / 8);
                    *(LAS u32x4*)(lds + L_VS + nb * 48 * VP + (8 + r2) * VP + c2 * 16) = __builtin_amdgcn_raw_buffer_load_b128(rkv, O_V + ((row0 + r2) * HD + h * DH + j * ES + c2 * 8) * 2, 0, AUX_SC1); }
            }
        }
        __syncthreads();
        {
            if (WOL) {
#pragma unroll
                for (int i = 8; i < 16; ++i) wo[i] = *(const LAS bf16x8*)(lds + L_WO + (wave * 8 + i - 8) * 1024 + lane * 16);
            }
            __builtin_amdgcn_s_sleep(CH_DELAY3);
            u32x4 a[RPG];
            constexpr int KPG = 16 / RPG;
            const int base = O_OM + s * (int)OM_B + g * (8192 * RPG) + wave * (1024 * RPG) + (fr / RPG) * (64 * RPG) + fq * (16 * RPG) + (fr % RPG) * 16;
            for (unsigned spins = 0;;) {
                asm volatile("" ::: "memory");
#pragma unroll
                for (int i = 0; i < RPG; ++i) a[i] = __builtin_amdgcn_raw_buffer_load_b128(rs, base + i * 1024, 0, AUX_SC1);
                unsigned mxs = 0;
#pragma unroll
                for (int i = 0; i < RPG; ++i) mxs = umax2(mxs, umax4(a[i]));
                if (!__any(mxs == SENT) || dead) break;
                if (++spins > SPIN_CAP) { dead = true; if (lane == 0) *(volatile unsigned*)(ws + WS_TMO) = 3u; break; }
            }
            f32x4 acc[KPG];
#pragma unroll
            for (int q = 0; q < KPG; ++q) acc[q] = (f32x4){0.f, 0.f, 0.f, 0.f};
#pragma unroll
            for (int gq = 0; gq < RPG; ++gq)
#pragma unroll
                for (int q = 0; q < KPG; ++q) acc[q] = __builtin_amdgcn_mfma_f32_16x16x32_bf16(wo[gq * KPG + q], __builtin_bit_cast(bf16x8, a[gq]), acc[q], 0, 0, 0);
            f32x4 r = acc[0];
#pragma unroll
            for (int q = 1; q < KPG; ++q) if (fr / RPG == q) r = acc[q];
#pragma unroll
            for (int e = 0; e < 4; ++e) {
                if (RPG <= 2) r[e] += __builtin_bit_cast(float, __builtin_amdgcn_update_dpp(0, __builtin_bit_cast(int, r[e]), 0x122, 0xf, 0xf, true));
                r[e] += __builtin_bit_cast(float, __builtin_amdgcn_update_dpp(0, __builtin_bit_cast(int, r[e]), 0x124, 0xf, 0xf, true));
                r[e] += __builtin_bit_cast(float, __builtin_amdgcn_update_dpp(0, __builtin_bit_cast(int, r[e]), 0x128, 0xf, 0xf, true));
            }
            if (fr < RPG) *(LAS f32x4*)(lds + L_RED2 + ((wave * RPG + fr) * 16 + fq * 4) * 4) = r;
        }
        __syncthreads();
        if (tid < RPG * 8) {
            const int m = tid >> 3, np = (tid & 7) * 2;
            f32x2 sum = (f32x2){0.f, 0.f};
#pragma unroll
            for (int w = 0; w < 8; ++w) sum += *(const LAS f32x2*)(lds + L_RED2 + ((w * RPG + m) * 16 + np) * 4);
            const unsigned pk = desent(pk_bf16(sum.x, sum.y));
            const int off = O_MEM + (s + 1) * (int)MEM_B + (c >> 1) * 512 + ((c & 1) * 2 + (np >> 3)) * 128 + (g * RPG + m) * 16;
            const u32x4 q4 = quad_gather(pk);
            if ((tid & 3) == 0) __builtin_amdgcn_raw_buffer_store_b128(q4, rs, off, 0, AUX_SC1);
        }
    }
}

template <int MT, class Epi>
__device__ __forceinline__ void gemm128_w8(LAS unsigned char* lds, const bf16_t* A, const int lda, const bf16_t* B, const int ldb, const int K, const Epi& E) {
    int tid = threadIdx.x; asm volatile("" : "+v"(tid));
    const int wid = __builtin_amdgcn_readfirstlane(tid >> 6), lane = tid & 63, wr = wid >> 2, wc = wid & 3, fr = lane & 15, fq = lane >> 4;
    constexpr int NA = MT / 2;
    unsigned voffA[NA], voffB[2];
#pragma unroll
    for (int i = 0; i < NA; ++i) { int R, C; g8::stage_rc(tid * 16 + i * 8192, R, C); voffA[i] = (unsigned)(R * lda + C) * 2u; }
#pragma unroll
    for (int i = 0; i < 2; ++i) { const int bb = tid * 16 + i * 8192, row = bb >> 8, ch = (bb & 255) >> 4, seg = (ch >> 1) ^ g8::bsw(row);
        voffB[i] = (unsigned)(row * ldb + seg * 16 + (ch & 1) * 8) * 2u; }
    const char* cA = (const char*)A; const char* cB = (const char*)B;
    const unsigned ldsw = (unsigned)wid * 1024u;
    const int aoff = g8::lds_byte(wr * 16 * MT + fr, fq * 8);
    const int q4 = (lane & 15) >> 2, p4 = lane & 3, sB = q4 | ((fq & 1) << 2);
    const int boff0 = (8 * fq + q4) * 256 + (((wc * 2 + 0) ^ sB) * 32) + 8 * p4;
    const int boff1 = (8 * fq + q4) * 256 + (((wc * 2 + 1) ^ sB) * 32) + 8 * p4;
    f32x4 acc[MT][2];
#pragma unroll
    for (int m = 0; m < MT; ++m) { acc[m][0] = (f32x4){0.f, 0.f, 0.f, 0.f}; acc[m][1] = (f32x4){0.f, 0.f, 0.f, 0.f}; }
    const int nt = K / 64;
#define STG(buf, t) do { _Pragma("unroll") for (int _i = 0; _i < NA; ++_i) \
        __builtin_amdgcn_global_load_lds((const unsigned*)(cA + (size_t)(t) * 128 + voffA[_i]), (LAS unsigned*)(lds + (buf) * 32768 + ldsw + _i * 8192), 16, 0, 0); \
        _Pragma("unroll") for (int _i = 0; _i < 2; ++_i) \
        __builtin_amdgcn_global_load_lds((const unsigned*)(cB + (size_t)(t) * 64 * ldb * 2 + voffB[_i]), (LAS unsigned*)(lds + (buf) * 32768 + 16384 + ldsw + _i * 8192), 16, 0, 0); } while (0)
    STG(0, 0); STG(1, 1); STG(2, 2);
    int cur = 0;
    for (int t = 0; t < nt; ++t) {
        if (MT == 4) { if (t + 2 < nt) asm volatile("s_waitcnt vmcnt(8)" ::: "memory"); else if (t + 1 < nt) asm volatile("s_waitcnt vmcnt(4)" ::: "memory"); else asm volatile("s_waitcnt vmcnt(0)" ::: "memory"); }
        else { if (t + 2 < nt) asm volatile("s_waitcnt vmcnt(6)" ::: "memory"); else if (t + 1 < nt) asm volatile("s_waitcnt vmcnt(3)" ::: "memory"); else asm volatile("s_waitcnt vmcnt(0)" ::: "memory"); }
        __builtin_amdgcn_s_barrier();
        asm volatile("" ::: "memory");
        if (t + 3 < nt) STG((cur + 3) & 3, t + 3);
        bf16x8 At[MT][2], Bt[2][2];
        LAS unsigned char* sa = lds + cur * 32768; LAS unsigned char* sb = sa + 16384;
#pragma unroll
        for (int k = 0; k < 2; ++k) {
            bf16x4 a0 = __builtin_amdgcn_ds_read_tr16_b64_v4i16((LAS bf16x4*)(sb + boff0 + k * 8192)), a1 = __builtin_amdgcn_ds_read_tr16_b64_v4i16((LAS bf16x4*)(sb + boff0 + k * 8192 + 1024));
            bf16x4 c0 = __builtin_amdgcn_ds_read_tr16_b64_v4i16((LAS bf16x4*)(sb + boff1 + k * 8192)), c1 = __builtin_amdgcn_ds_read_tr16_b64_v4i16((LAS bf16x4*)(sb + boff1 + k * 8192 + 1024));
            Bt[0][k] = __builtin_shufflevector(a0, a1, 0, 1, 2, 3, 4, 5, 6, 7); Bt[1][k] = __builtin_shufflevector(c0, c1, 0, 1, 2, 3, 4, 5, 6, 7);
#pragma unroll
            for (int m = 0; m < MT; ++m) At[m][k] = *(const LAS bf16x8*)(sa + aoff + m * 2048 + k * 1024);
        }
        __builtin_amdgcn_s_setprio(1);
#pragma unroll
        for (int k = 0; k < 2; ++k)
#pragma unroll
            for (int m = 0; m < MT; ++m)
#pragma unroll
                for (int n = 0; n < 2; ++n) acc[m][n] = __builtin_amdgcn_mfma_f32_16x16x32_bf16(Bt[n][k], At[m][k], acc[m][n], 0, 0, 0);
        __builtin_amdgcn_s_setprio(0);
        asm volatile("s_waitcnt lgkmcnt(0)" ::: "memory");
        cur = (cur + 1) & 3;
    }
#undef STG
    __builtin_amdgcn_s_barrier();
    E(acc, wr, wc, fr, fq);
}

constexpr int XV_PITCH = 272;
template <bool INK>
__device__ __forceinline__ void xattn_item(LAS unsigned char* vs, char* ws, const int s, const int h) {
    const int lane = threadIdx.x & 63, fr = lane & 15, fq = lane >> 4;
    const bf16_t* Q = (const bf16_t*)(ws + WS_Q) + (size_t)s * SEG * HD + h * DH;
    const bf16_t* K = (const bf16_t*)(ws + WS_K) + (size_t)s * SEG * HD + h * DH;
    const bf16_t* V = (const bf16_t*)(ws + WS_V) + (size_t)s * SEG * HD + h * DH;
    const __amdgpu_buffer_rsrc_t rm = __builtin_amdgcn_make_buffer_rsrc(ws + WS_QKVM, 0, (int)(WS_OM - WS_QKVM), 0x00020000);
    const int kmo = s * (int)QKVM_B + (h * 3 + 1) * 2048, vmo = kmo + 2048;
    u32x4 km[4], vm[2];
    for (unsigned spins = 0;;) {
        asm volatile("" ::: "memory");
#pragma unroll
        for (int i = 0; i < 4; ++i) km[i] = __builtin_amdgcn_raw_buffer_load_b128(rm, kmo + (lane & 7) * 256 + i * 64 + fq * 16, 0, INK ? AUX_SC1 : 0);
#pragma unroll
        for (int i = 0; i < 2; ++i) vm[i] = __builtin_amdgcn_raw_buffer_load_b128(rm, vmo + (i * 4 + fq) * 256 + fr * 16, 0, INK ? AUX_SC1 : 0);
        if (!INK) break;
        unsigned mxs = umax2(umax4(vm[0]), umax4(vm[1]));
#pragma unroll
        for (int i = 0; i < 4; ++i) mxs = umax2(mxs, umax4(km[i]));
        if (!__any(mxs == SENT)) break;
        __builtin_amdgcn_s_sleep(16);
        if (++spins > SPIN_CAP) break;
    }
#pragma unroll
    for (int i = 0; i < 12; ++i) {
        const int r = i * 4 + fq;
        u32x4 v = (u32x4){0u, 0u, 0u, 0u};
        if (i < 2) v = vm[i]; else if (i < 10) v = *(const u32x4*)(V + (size_t)(r - 8) * HD + fr * 8);
        *(LAS u32x4*)(vs + r * XV_PITCH + fr * 16) = v;
    }
    f32x4 sc[3][2];
#pragma unroll
    for (int t = 0; t < 3; ++t) { sc[t][0] = (f32x4){0.f, 0.f, 0.f, 0.f}; sc[t][1] = (f32x4){0.f, 0.f, 0.f, 0.f}; }
#pragma unroll
    for (int i = 0; i < 4; ++i) {
        const bf16x8 q0 = *(const bf16x8*)(Q + (size_t)fr * HD + i * 32 + fq * 8), q1 = *(const bf16x8*)(Q + (size_t)(16 + fr) * HD + i * 32 + fq * 8);
#pragma unroll
        for (int t = 0; t < 3; ++t) {
            int r = t * 16 + fr - 8; r = r < 0 ? 0 : (r > 31 ? 31 : r);
            bf16x8 kf = *(const bf16x8*)(K + (size_t)r * HD + i * 32 + fq * 8);
            if (t == 0 && fr < 8) kf = __builtin_bit_cast(bf16x8, km[i]);
            sc[t][0] = __builtin_amdgcn_mfma_f32_16x16x32_bf16(kf, q0, sc[t][0], 0, 0, 0);
            sc[t][1] = __builtin_amdgcn_mfma_f32_16x16x32_bf16(kf, q1, sc[t][1], 0, 0, 0);
        }
    }
    const float scale = 0.08838834764831845f, NEG = rbf(-1000000000.0f);
    u32x4 p0[2], p1[2];
#pragma unroll
    for (int rt = 0; rt < 2; ++rt) {
        const int row = rt * 16 + fr;
        float v[12]; float mx = -INFINITY;
#pragma unroll
        for (int t = 0; t < 3; ++t)
#pragma unroll
            for (int r = 0; r < 4; ++r) { const int key = t * 16 + fq * 4 + r; float z = rbf(rbf(sc[t][rt][r]) * scale);
                if (key - 8 > row) z = NEG; if (key >= 40) z = -INFINITY; v[t * 4 + r] = z; mx = fmaxf(mx, z); }
        mx = xmax16_32(mx);
        float sum = 0.f;
#pragma unroll
        for (int i = 0; i < 12; ++i) { v[i] = __expf(v[i] - mx); sum += v[i]; }
        sum = xsum16_32(sum);
        const float inv = 1.0f / sum;
        p0[rt].x = pk_bf16(v[0] * inv, v[1] * inv); p0[rt].y = pk_bf16(v[2] * inv, v[3] * inv); p0[rt].z = pk_bf16(v[4] * inv, v[5] * inv); p0[rt].w = pk_bf16(v[6] * inv, v[7] * inv);
        p1[rt].x = pk_bf16(v[8] * inv, v[9] * inv); p1[rt].y = pk_bf16(v[10] * inv, v[11] * inv); p1[rt].z = 0u; p1[rt].w = 0u;
    }
    asm volatile("s_waitcnt lgkmcnt(0)" ::: "memory");
    const __amdgpu_buffer_rsrc_t ro = __builtin_amdgcn_make_buffer_rsrc(ws + WS_O, 0, S_LEN * HD * 2, 0x00020000);
    const int obase = (s * SEG * HD + h * DH) * 2;
    const bf16x4 zz = (bf16x4){0, 0, 0, 0};
    u32x2 ow[8][2];
#pragma unroll
    for (int et = 0; et < 8; ++et) {
        const int voff = (4 * fq + (fr >> 2)) * XV_PITCH + et * 32 + (lane & 3) * 8;
        const bf16x4 v0 = __builtin_amdgcn_ds_read_tr16_b64_v4i16((LAS bf16x4*)(vs + voff));
        const bf16x4 v1 = __builtin_amdgcn_ds_read_tr16_b64_v4i16((LAS bf16x4*)(vs + voff + 16 * XV_PITCH));
        const bf16x4 v2 = __builtin_amdgcn_ds_read_tr16_b64_v4i16((LAS bf16x4*)(vs + voff + 32 * XV_PITCH));
        const bf16x8 va = __builtin_shufflevector(v0, v1, 0, 1, 2, 3, 4, 5, 6, 7), vb = __builtin_shufflevector(v2, zz, 0, 1, 2, 3, 4, 5, 6, 7);
#pragma unroll
        for (int rt = 0; rt < 2; ++rt) {
            f32x4 o = (f32x4){0.f, 0.f, 0.f, 0.f};
            o = __builtin_amdgcn_mfma_f32_16x16x32_bf16(va, __builtin_bit_cast(bf16x8, p0[rt]), o, 0, 0, 0);
            o = __builtin_amdgcn_mfma_f32_16x16x32_bf16(vb, __builtin_bit_cast(bf16x8, p1[rt]), o, 0, 0, 0);
            ow[et][rt].x = pk_bf16(o[0], o[1]); ow[et][rt].y = pk_bf16(o[2], o[3]);
        }
    }
    asm volatile("s_waitcnt lgkmcnt(0)" ::: "memory");
#pragma unroll
    for (int et = 0; et < 8; ++et)
#pragma unroll
        for (int rt = 0; rt < 2; ++rt) *(LAS u32x2*)(vs + (rt * 16 + fr) * XV_PITCH + et * 32 + fq * 8) = ow[et][rt];
    asm volatile("s_waitcnt lgkmcnt(0)" ::: "memory");
#pragma unroll
    for (int i = 0; i < 8; ++i) {
        const int r = i * 4 + fq;
        const u32x4 w = *(const LAS u32x4*)(vs + r * XV_PITCH + fr * 16);
        __builtin_amdgcn_raw_buffer_store_b128(w, ro, obase + (r * HD + fr * 8) * 2, 0, INK ? AUX_SC1 : 0);
    }
    asm volatile("s_waitcnt lgkmcnt(0)" ::: "memory");
}

__device__ __forceinline__ void store_pair_sc1(const __amdgpu_buffer_rsrc_t rs, int off0, u32x2 w0, u32x2 w1, int fq) {
    auto sx = __builtin_amdgcn_permlane16_swap(w0.x, w1.x, false, false); auto sy = __builtin_amdgcn_permlane16_swap(w0.y, w1.y, false, false);
    const u32x4 w = (u32x4){(unsigned)sx[0], (unsigned)sy[0], (unsigned)sx[1], (unsigned)sy[1]};
    __builtin_amdgcn_raw_buffer_store_b128(w, rs, off0 + (fq & 1) * 32 + (fq >> 1) * 16, 0, AUX_SC1);
}
struct SchedOne {
    g8::Unit u; bool valid;
    __device__ __forceinline__ bool next(int i, g8::Unit& o) const { if (i != 0 || !valid) return false; o = u; return true; }
};
struct SchedKVQ {
    const bf16_t* A; const bf16_t* Wq; long dk, dv; int r;
    __device__ __forceinline__ bool next(int i, g8::Unit& u) const {
        const int L = r + 128 * i; if (L >= 352) return false;
        int pm, pn;
        if (L < 224) { pm = 1 + (L >> 5); pn = 16 + (L & 31); } else { pm = (L - 224) & 7; pn = (L - 224) >> 3; }
        const int mat = pn >> 4;
        u.A = A + (size_t)pm * 256 * DM; u.B = Wq + (mat == 1 ? dk : 0L) + (mat == 2 ? dv : 0L) + (pn & 15) * 256; u.pm = pm; u.pn = pn; return true;
    }
};
struct EpiQKV {
    char* ws;
    __device__ __forceinline__ void operator()(const f32x4 (&acc)[2][2][4][2], const g8::Unit& u, int wr, int wc, int fr, int fq) const {
        const __amdgpu_buffer_rsrc_t rq = __builtin_amdgcn_make_buffer_rsrc(ws + WS_Q, 0, (int)(WS_H - WS_Q), 0x00020000);
        const int base = (u.pn >> 4) * (S_LEN * HD * 2) + ((u.pm * 256 + wr * 64 + fr) * HD + (u.pn & 15) * 256 + wc * 32) * 2;
#pragma unroll
        for (int ai = 0; ai < 2; ++ai)
#pragma unroll
            for (int m = 0; m < 4; ++m)
#pragma unroll
                for (int bj = 0; bj < 2; ++bj) { const f32x4 v0 = acc[ai][bj][m][0], v1 = acc[ai][bj][m][1];
                    store_pair_sc1(rq, base + ((ai * 128 + m * 16) * HD + bj * 128) * 2, (u32x2){pk_bf16(v0[0], v0[1]), pk_bf16(v0[2], v0[3])}, (u32x2){pk_bf16(v1[0], v1[1]), pk_bf16(v1[2], v1[3])}, fq); }
        asm volatile("s_waitcnt vmcnt(0)" ::: "memory");
        if ((threadIdx.x & 63) == 0) {
            if (u.pn >= 16) __hip_atomic_fetch_add((unsigned*)(ws + WS_CNT + (size_t)(CNT_KV + u.pm * 32 + u.pn - 16) * 256), 1u, __ATOMIC_RELAXED, __HIP_MEMORY_SCOPE_AGENT);
            __hip_atomic_fetch_add((unsigned*)(ws + WS_CNT + (size_t)(CNT_QKV + u.pm) * 256), 1u, __ATOMIC_RELAXED, __HIP_MEMORY_SCOPE_AGENT);
        }
    }
};
template <int MT>
struct EpiKV0 {
    char* ws; int row0, pn2;
    __device__ __forceinline__ void operator()(const f32x4 (&acc)[MT][2], int wr, int wc, int fr, int fq) const {
        const __amdgpu_buffer_rsrc_t rq = __builtin_amdgcn_make_buffer_rsrc(ws + WS_K, 0, (int)(WS_H - WS_K), 0x00020000);
        const int base = (pn2 >> 5) * (S_LEN * HD * 2) + ((row0 + wr * 16 * MT + fr) * HD + (pn2 & 31) * 128 + wc * 32) * 2;
#pragma unroll
        for (int m = 0; m < MT; ++m) { const f32x4 v0 = acc[m][0], v1 = acc[m][1];
            store_pair_sc1(rq, base + m * 16 * HD * 2, (u32x2){pk_bf16(v0[0], v0[1]), pk_bf16(v0[2], v0[3])}, (u32x2){pk_bf16(v1[0], v1[1]), pk_bf16(v1[2], v1[3])}, fq); }
        asm volatile("s_waitcnt vmcnt(0)" ::: "memory");
        if ((threadIdx.x & 63) == 0) {
            __hip_atomic_fetch_add((unsigned*)(ws + WS_CNT + (size_t)((row0 < 128 ? CNT_KV : CNT_KV0B) + (pn2 >> 1)) * 256), 1u, __ATOMIC_RELAXED, __HIP_MEMORY_SCOPE_AGENT);
            __hip_atomic_fetch_add((unsigned*)(ws + WS_CNT + (size_t)CNT_QKV * 256), 1u, __ATOMIC_RELAXED, __HIP_MEMORY_SCOPE_AGENT);
        }
    }
};
struct EpiMLP1 {
    char* ws; const bf16_t* b1;
    __device__ __forceinline__ void operator()(const f32x4 (&acc)[2][2][4][2], const g8::Unit& u, int wr, int wc, int fr, int fq) const {
        const __amdgpu_buffer_rsrc_t rh = __builtin_amdgcn_make_buffer_rsrc(ws + WS_H, 0, S_LEN * FF * 2, 0x00020000);
        const int row0 = u.pm * 256 + wr * 64 + fr, col0 = u.pn * 256 + wc * 32;
#pragma unroll
        for (int bj = 0; bj < 2; ++bj) {
            const u32x2 ba = *(const u32x2*)(b1 + col0 + bj * 128 + 4 * fq), bb = *(const u32x2*)(b1 + col0 + bj * 128 + 16 + 4 * fq);
#pragma unroll
            for (int ai = 0; ai < 2; ++ai)
#pragma unroll
                for (int m = 0; m < 4; ++m) { const f32x4 v0 = acc[ai][bj][m][0], v1 = acc[ai][bj][m][1];
                    u32x2 w0, w1;
                    w0.x = pk_bf16(gelu_tanh(rbf(rbf(v0[0]) + bf2f(ba.x & 0xffffu))), gelu_tanh(rbf(rbf(v0[1]) + bf2f(ba.x >> 16))));
                    w0.y = pk_bf16(gelu_tanh(rbf(rbf(v0[2]) + bf2f(ba.y & 0xffffu))), gelu_tanh(rbf(rbf(v0[3]) + bf2f(ba.y >> 16))));
                    w1.x = pk_bf16(gelu_tanh(rbf(rbf(v1[0]) + bf2f(bb.x & 0xffffu))), gelu_tanh(rbf(rbf(v1[1]) + bf2f(bb.x >> 16))));
                    w1.y = pk_bf16(gelu_tanh(rbf(rbf(v1[2]) + bf2f(bb.y & 0xffffu))), gelu_tanh(rbf(rbf(v1[3]) + bf2f(bb.y >> 16))));
                    store_pair_sc1(rh, ((row0 + ai * 128 + m * 16) * FF + col0 + bj * 128) * 2, w0, w1, fq);
                    if (m & 1) asm volatile("" ::: "memory"); }
        }
        signal_counter(ws, CNT_H + u.pm);
    }
};
struct EpiMLP2 {
    char* ws; const bf16_t* b2; int row0, col0;
    __device__ __forceinline__ void operator()(const f32x4 (&acc)[4][2], int wr, int wc, int fr, int fq) const {
        const __amdgpu_buffer_rsrc_t rb = __builtin_amdgcn_make_buffer_rsrc(ws + WS_B, 0, S_LEN * DM * 2, 0x00020000);
        const int col = col0 + wc * 32;
        const u32x2 ba = *(const u32x2*)(b2 + col + 4 * fq), bb = *(const u32x2*)(b2 + col + 16 + 4 * fq);
#pragma unroll
        for (int m = 0; m < 4; ++m) { const f32x4 v0 = acc[m][0], v1 = acc[m][1];
            u32x2 w0, w1;
            w0.x = pk_bf16(rbf(v0[0]) + bf2f(ba.x & 0xffffu), rbf(v0[1]) + bf2f(ba.x >> 16)); w0.y = pk_bf16(rbf(v0[2]) + bf2f(ba.y & 0xffffu), rbf(v0[3]) + bf2f(ba.y >> 16));
            w1.x = pk_bf16(rbf(v1[0]) + bf2f(bb.x & 0xffffu), rbf(v1[1]) + bf2f(bb.x >> 16)); w1.y = pk_bf16(rbf(v1[2]) + bf2f(bb.y & 0xffffu), rbf(v1[3]) + bf2f(bb.y >> 16));
            store_pair_sc1(rb, ((row0 + wr * 64 + m * 16 + fr) * DM + col) * 2, w0, w1, fq); }
        signal_counter(ws, CNT_B + (row0 >> 8));
    }
};
__device__ __forceinline__ u32x2 combine4(const f32x4 v, const u32x2 xw, const u32x2 bw) {
    u32x2 w;
    w.x = pk_bf16(rbf(bf2f(xw.x & 0xffffu) + rbf(v[0])) + bf2f(bw.x & 0xffffu), rbf(bf2f(xw.x >> 16) + rbf(v[1])) + bf2f(bw.x >> 16));
    w.y = pk_bf16(rbf(bf2f(xw.y & 0xffffu) + rbf(v[2])) + bf2f(bw.y & 0xffffu), rbf(bf2f(xw.y >> 16) + rbf(v[3])) + bf2f(bw.y >> 16));
    return w;
}
template <int MT>
struct EpiOut {
    const bf16_t* x; const bf16_t* bout; bf16_t* out; int row0, col0;
    __device__ __forceinline__ void operator()(const f32x4 (&acc)[MT][2], int wr, int wc, int fr, int fq) const {
#pragma unroll
        for (int m = 0; m < MT; ++m)
#pragma unroll
            for (int n = 0; n < 2; ++n) {
                const size_t off = (size_t)(row0 + wr * 16 * MT + m * 16 + fr) * DM + col0 + wc * 32 + n * 16 + 4 * fq;
                *(u32x2*)(out + off) = combine4(acc[m][n], *(const u32x2*)(x + off), *(const u32x2*)(bout + off));
            }
    }
};

constexpr int TAIL_BLK0 = 6;
__global__ void __launch_bounds__(512, 2) k_mega(const bf16_t* __restrict__ x, const bf16_t* __restrict__ Wq, const bf16_t* __restrict__ Wk, const bf16_t* __restrict__ Wv,
                                                 const bf16_t* __restrict__ Wo, const bf16_t* __restrict__ w1, const bf16_t* __restrict__ b1, const bf16_t* __restrict__ w2,
                                                 const bf16_t* __restrict__ b2, char* ws, bf16_t* __restrict__ out) {
    extern __shared__ __attribute__((aligned(16))) unsigned char shm_[];
    LAS unsigned char* lds = (LAS unsigned char*)shm_;
    const int b = blockIdx.x;
    const int role = (b >> 3) & 1, r = (b >> 4) * 8 + (b & 7);
    if (role == 0) { chain_body<4>(lds, r, Wq, Wk, Wv, Wo, ws); return; }
    {
        const int row0 = (r >> 6) * 64, pn2 = r & 63;
        EpiKV0<2> E{ws, row0, pn2};
        gemm128_w8<2, EpiKV0<2>>(lds, (const bf16_t*)(ws + WS_XN) + (size_t)row0 * DM, DM, (pn2 >= 32 ? Wv : Wk) + (pn2 & 31) * 128, HD, DM, E);
    }
    if (r < 64) {
        EpiKV0<4> E{ws, 128, r};
        gemm128_w8<4, EpiKV0<4>>(lds, (const bf16_t*)(ws + WS_XN) + (size_t)128 * DM, DM, (r >= 32 ? Wv : Wk) + (r & 31) * 128, HD, DM, E);
    }
    {
        SchedKVQ S{(const bf16_t*)(ws + WS_XN), Wq, (long)(Wk - Wq), (long)(Wv - Wq), r ^ 64};
        EpiQKV E{ws}; g8::gemm_phase<EpiQKV, SchedKVQ>(lds, DM, DM, HD, S, E);
    }
    {
        const int r2 = r ^ 64, L = r2 >= 96 ? r2 - 96 : 32 + r2;
        SchedOne S; S.valid = (r2 >= 96) || (r2 < 80); S.u.pm = L & 7; S.u.pn = L >> 3; S.u.A = (const bf16_t*)(ws + WS_Y) + (size_t)S.u.pm * 256 * DM; S.u.B = w1 + S.u.pn * 256;
        EpiMLP1 E{ws, b1}; g8::gemm_phase<EpiMLP1, SchedOne>(lds, DM, DM, FF, S, E);
    }
    {
        const int pm = r >> 3, pn = r & 7;
        wait_counter(ws, CNT_H + (pm >> 1), 14 * 8);
        EpiMLP2 E{ws, b2, pm * 128, pn * 128};
        gemm128_w8<4, EpiMLP2>(lds, (const bf16_t*)(ws + WS_H) + (size_t)pm * 128 * FF, FF, w2 + pn * 128, DM, FF, E);
    }
    if (r < 80) {
        const int blk = r >> 4, t = r & 15;
        {
            if (threadIdx.x < 64) poll_ge(ws, CNT_STEP, (unsigned)(8 * blk + 8));
            wait_counter(ws, CNT_QKV + blk, (blk == 0 ? 192 * 8 : 32 * 8) + 16 * 8);
            const int wave = threadIdx.x >> 6;
#pragma unroll 1
            for (int i = 0; i < 2; ++i) { const int item = t * 16 + wave * 2 + i; xattn_item<true>(lds + wave * 48 * XV_PITCH, ws, blk * 8 + (item >> 5), item & 31); }
            signal_counter(ws, CNT_O + blk);
        }
        {
            if (threadIdx.x < 64) poll_ge(ws, CNT_B + blk, 16 * 8);
            wait_counter(ws, CNT_O + blk, 16 * 8);
            const int pm = blk * 2 + (t >> 3), pn = t & 7;
            EpiOut<4> E{x, (const bf16_t*)(ws + WS_B), out, pm * 128, pn * 128};
            gemm128_w8<4, EpiOut<4>>(lds, (const bf16_t*)(ws + WS_O) + (size_t)pm * 128 * HD, HD, Wo + pn * 128, DM, HD, E);
        }
    } else if (r < 112) {
        constexpr int blk = 5; const int t = r - 80;
        if (threadIdx.x < 64) poll_ge(ws, CNT_STEP, (unsigned)(8 * blk + 8));
        wait_counter(ws, CNT_QKV + blk, 32 * 8 + 16 * 8);
        const int wave = threadIdx.x >> 6, item = t * 8 + wave;
        xattn_item<true>(lds + wave * 48 * XV_PITCH, ws, blk * 8 + (item >> 5), item & 31);
        signal_counter(ws, CNT_O + blk);
        if (threadIdx.x < 64) poll_ge(ws, CNT_B + blk, 16 * 8);
        wait_counter(ws, CNT_O + blk, 32 * 8);
        const int pm = blk * 4 + (t >> 3), pn = t & 7;
        EpiOut<2> E{x, (const bf16_t*)(ws + WS_B), out, pm * 64, pn * 128};
        gemm128_w8<2, EpiOut<2>>(lds, (const bf16_t*)(ws + WS_O) + (size_t)pm * 64 * HD, HD, Wo + pn * 128, DM, HD, E);
    } else {
        if (threadIdx.x < 64) poll_ge(ws, CNT_QKV + 6, 32 * 8 + 16 * 8);
        wait_counter(ws, CNT_QKV + 7, 32 * 8 + 16 * 8);
        const int wave = threadIdx.x >> 6, gw = (r - 112) * 8 + wave;
#pragma unroll 1
        for (int k = 0; k < 4; ++k) {
            const int seg = TAIL_BLK0 * 8 + 4 * k + (gw >> 5);
            poll_ge(ws, CNT_STEP, (unsigned)(seg + 1 < NSEG ? seg + 1 : NSEG - 1));
            xattn_item<true>(lds + wave * 48 * XV_PITCH, ws, seg, gw & 31);
        }
    }
}

__global__ void __launch_bounds__(256) k_tail_gemm(const bf16_t* __restrict__ x, const bf16_t* __restrict__ Wo, const char* __restrict__ ws, bf16_t* __restrict__ out) {
    extern __shared__ __attribute__((aligned(16))) unsigned char shm_[];
    const int tid = threadIdx.x, wave = __builtin_amdgcn_readfirstlane(tid >> 6), lane = tid & 63, fr = lane & 15, fq = lane >> 4;
    LAS unsigned char* lds = (LAS unsigned char*)shm_ + wave * 36864;
    const int pn = blockIdx.x & 31, pm = blockIdx.x >> 5, row0 = TAIL_BLK0 * 256 + pm * 64, col0 = pn * 32;
    const char* cA = (const char*)((const bf16_t*)(ws + WS_O) + (size_t)row0 * HD + wave * 1024);
    const char* cB = (const char*)(Wo + (size_t)wave * 1024 * DM + col0);
    unsigned voffA[8], voffB[4];
#pragma unroll
    for (int i = 0; i < 8; ++i) { const int c = i * 64 + lane, row = c >> 3, ch = (c & 7) ^ (row & 7); voffA[i] = (unsigned)(row * HD + ch * 8) * 2u; }
#pragma unroll
    for (int i = 0; i < 4; ++i) { const int c = i * 64 + lane, k = c >> 2, cb = c & 3; voffB[i] = (unsigned)(k * DM + cb * 8) * 2u; }
#define STG(buf, t) do { _Pragma("unroll") for (int _i = 0; _i < 8; ++_i) \
        __builtin_amdgcn_global_load_lds((const unsigned*)(cA + (size_t)(t) * 128 + voffA[_i]), (LAS unsigned*)(lds + (buf) * 12288 + _i * 1024), 16, 0, 0); \
        _Pragma("unroll") for (int _i = 0; _i < 4; ++_i) \
        __builtin_amdgcn_global_load_lds((const unsigned*)(cB + (size_t)(t) * 64 * DM * 2 + voffB[_i]), (LAS unsigned*)(lds + (buf) * 12288 + 8192 + _i * 1024), 16, 0, 0); } while (0)
    f32x4 acc[4][2];
#pragma unroll
    for (int m = 0; m < 4; ++m) { acc[m][0] = (f32x4){0.f, 0.f, 0.f, 0.f}; acc[m][1] = (f32x4){0.f, 0.f, 0.f, 0.f}; }
    STG(0, 0); STG(1, 1);
    int cur = 0;
    for (int t = 0; t < 16; ++t) {
        int nx = cur + 2; if (nx >= 3) nx -= 3;
        if (t + 2 < 16) { STG(nx, t + 2); asm volatile("s_waitcnt vmcnt(24)" ::: "memory"); }
        else if (t + 1 < 16) asm volatile("s_waitcnt vmcnt(12)" ::: "memory");
        else asm volatile("s_waitcnt vmcnt(0)" ::: "memory");
        LAS unsigned char* sa = lds + cur * 12288; LAS unsigned char* sb = sa + 8192;
        bf16x8 At[4][2], Bt[2][2];
#pragma unroll
        for (int k = 0; k < 2; ++k) {
#pragma unroll
            for (int m = 0; m < 4; ++m) { const int row = m * 16 + fr; At[m][k] = *(const LAS bf16x8*)(sa + row * 128 + (((k * 4 + fq) ^ (row & 7)) * 16)); }
#pragma unroll
            for (int n = 0; n < 2; ++n) {
                const int off = (k * 32 + 8 * fq + (fr >> 2)) * 64 + n * 32 + (lane & 3) * 8;
                const bf16x4 a0 = __builtin_amdgcn_ds_read_tr16_b64_v4i16((LAS bf16x4*)(sb + off)), a1 = __builtin_amdgcn_ds_read_tr16_b64_v4i16((LAS bf16x4*)(sb + off + 256));
                Bt[n][k] = __builtin_shufflevector(a0, a1, 0, 1, 2, 3, 4, 5, 6, 7); }
        }
#pragma unroll
        for (int k = 0; k < 2; ++k)
#pragma unroll
            for (int m = 0; m < 4; ++m)
#pragma unroll
                for (int n = 0; n < 2; ++n) acc[m][n] = __builtin_amdgcn_mfma_f32_16x16x32_bf16(Bt[n][k], At[m][k], acc[m][n], 0, 0, 0);
        asm volatile("s_waitcnt lgkmcnt(0)" ::: "memory");
        cur = cur + 1 == 3 ? 0 : cur + 1;
    }
#undef STG
    __syncthreads();
    LAS f32x4* red = (LAS f32x4*)shm_;
#pragma unroll
    for (int m = 0; m < 4; ++m)
#pragma unroll
        for (int n = 0; n < 2; ++n) red[((wave * 4 + m) * 2 + n) * 64 + lane] = acc[m][n];
    __syncthreads();
#pragma unroll
    for (int n = 0; n < 2; ++n) {
        const int m = wave;
        const f32x4 v = (red[((0 * 4 + m) * 2 + n) * 64 + lane] + red[((1 * 4 + m) * 2 + n) * 64 + lane]) + (red[((2 * 4 + m) * 2 + n) * 64 + lane] + red[((3 * 4 + m) * 2 + n) * 64 + lane]);
        const size_t off = (size_t)(row0 + m * 16 + fr) * DM + col0 + n * 16 + 4 * fq;
        *(u32x2*)(out + off) = combine4(v, *(const u32x2*)(x + off), *(const u32x2*)((const bf16_t*)(ws + WS_B) + off));
    }
}

extern "C" void kernel_launch(void* const* d_in, const int* in_sizes, int n_in, void* d_out, int out_size, void* d_ws, size_t ws_size, hipStream_t stream) {
    const bf16_t* x = (const bf16_t*)d_in[0];
    const bf16_t* mem0 = (const bf16_t*)d_in[1];
    const bf16_t* Wq = (const bf16_t*)d_in[2];
    const bf16_t* Wk = (const bf16_t*)d_in[3];
    const bf16_t* Wv = (const bf16_t*)d_in[4];
    const bf16_t* Wo = (const bf16_t*)d_in[5];
    const bf16_t* w1 = (const bf16_t*)d_in[8];
    const bf16_t* b1 = (const bf16_t*)d_in[9];
    const bf16_t* w2 = (const bf16_t*)d_in[10];
    const bf16_t* b2 = (const bf16_t*)d_in[11];
    char* ws = (char*)d_ws;
    bf16_t* out = (bf16_t*)d_out;
    (void)hipFuncSetAttribute((const void*)k_mega, hipFuncAttributeMaxDynamicSharedMemorySize, CH_LDS);
    (void)hipFuncSetAttribute((const void*)k_tail_gemm, hipFuncAttributeMaxDynamicSharedMemorySize, 4 * 36864);
    k_prep<<<S_LEN / 4, 256, 0, stream>>>(x, mem0, ws);
    k_mega<<<256, 512, CH_LDS, stream>>>(x, Wq, Wk, Wv, Wo, w1, b1, w2, b2, ws, out);
    k_tail_gemm<<<128 * (8 - TAIL_BLK0), 256, 4 * 36864, stream>>>(x, Wo, ws, out);
}
```
